```python
import torch

def _softmax(x, axis=-1):
    return torch.softmax(x, dim=axis)
def _gelu(x):
    return torch.nn.functional.gelu(x, approximate="tanh")
def _silu(x):
    return torch.nn.functional.silu(x)
def _sigmoid(x):
    return torch.sigmoid(x)
def _layernorm(x, axis=-1, eps=1e-5):
    m = x.mean(dim=axis, keepdim=True)
    v = x.var(dim=axis, keepdim=True, unbiased=False)
    return (x - m) / torch.sqrt(v + eps)
def _scatter_add(tgt, idx, src, axis=0):
    return tgt.clone().index_add_(axis, idx, src)
def _segment_sum(src, seg, n):
    out = torch.zeros((n,) + src.shape[1:], dtype=src.dtype)
    return out.index_add_(0, seg, src)
def _pad(x, pw):
    flat = tuple(v for pair in reversed(pw) for v in pair)
    return torch.nn.functional.pad(x, flat)

kernel_name = 'parallel_sum__ssm_conv_h3__glu_gated_bilinear__prompt'

def setup_inputs():

    _w = (lambda a: (lambda t: t.to(torch.bfloat16) if t.is_floating_point() else t)(torch.as_tensor(a)))
    def _A_setup():
        B, S, D = 1, 2048, 576
        ks = 4
        return {
            "q": _w((lambda *s: torch.randn(*s, dtype=torch.bfloat16) * 0.05)(B, S, D)),
            "k": _w((lambda *s: torch.randn(*s, dtype=torch.bfloat16) * 0.2)(B, S, D)),
            "v": _w((lambda *s: torch.randn(*s, dtype=torch.bfloat16) * 0.2)(B, S, D)),
            "shift_k": _w((lambda *s: torch.randn(*s, dtype=torch.bfloat16) * 0.2)(D, ks) * 0.5),
            "ssm_k": _w((lambda *s: torch.randn(*s, dtype=torch.bfloat16) * 0.4)(D, S) * (1.0 / S ** 0.5)),
            "ssm_D": _w((lambda *s: torch.randn(*s, dtype=torch.bfloat16) * 0.2)(D)),
        }
    _A_in = _A_setup()

    def _B_setup():
        B, S, D, F = 1, 2048, 576, 4608
        return {
            "x": _w((lambda *s: torch.randn(*s, dtype=torch.bfloat16) * 0.05)(B, S, D)),
            "w_gate": _w((lambda *s: torch.randn(*s, dtype=torch.bfloat16) * 0.2)(D, F) * (D ** -0.5)),
            "w_up": _w((lambda *s: torch.randn(*s, dtype=torch.bfloat16) * 0.2)(D, F) * (D ** -0.5)),
            "w_down": _w((lambda *s: torch.randn(*s, dtype=torch.bfloat16) * 0.2)(F, D) * (F ** -0.5)),
        }
    _B_in = _B_setup()

    return {
        "A_q": _A_in["q"],
        "A_k": _A_in["k"],
        "A_v": _A_in["v"],
        "A_shift_k": _A_in["shift_k"],
        "A_ssm_k": _A_in["ssm_k"],
        "A_ssm_D": _A_in["ssm_D"],
        "B_x": _B_in["x"],
        "B_w_gate": _B_in["w_gate"],
        "B_w_up": _B_in["w_up"],
        "B_w_down": _B_in["w_down"],
    }

def reference(A_q, A_k, A_v, A_shift_k, A_ssm_k, A_ssm_D, B_x, B_w_gate, B_w_up, B_w_down):
    def _A_ref(q, k, v, shift_k, ssm_k, ssm_D):
        B, S, D = q.shape
        ks = shift_k.shape[-1]
        k_pad = _pad(k, ((0, 0), (ks - 1, 0), (0, 0)))
        k_shift = torch.zeros_like(k)
        for t in range(ks):
            k_shift = k_shift + k_pad[:, t:t + S, :] * shift_k[:, ks - 1 - t][None, None, :]
        kv = k_shift * v
        L = 2 * S
        kv_pad = _pad(kv, ((0, 0), (0, S), (0, 0)))
        kf_pad = _pad(ssm_k, ((0, 0), (0, S)))
        kv_f = (lambda x, axes=(-2,-1): torch.fft.rfft2(x.float(), dim=axes))(kv_pad[:, None, :, :], axes=(1, 2))
        kf_f = (lambda x, axes=(-2,-1): torch.fft.rfft2(x.float(), dim=axes))(kf_pad[:, None, :], axes=(1, 2))
        kf_f = (lambda x, *d: x.permute(*d))(kf_f, 1, 2, 0)
        y_f = kv_f * kf_f[None, :, :, :]
        y = (lambda x, s, axes=(-2,-1): torch.fft.irfft2(x, s=s, dim=axes).to(torch.bfloat16))(y_f, (1, L), axes=(1, 2))[:, 0, :S, :]
        y = y + kv * ssm_D[None, None, :]
        return q * y

    def _B_ref(x, w_gate, w_up, w_down):
        g = torch.einsum("bsd,df->bsf", x, w_gate)
        u = torch.einsum("bsd,df->bsf", x, w_up)
        h = g * u
        return torch.einsum("bsf,fd->bsd", h, w_down)

    x = A_q
    xn = _layernorm(x, axis=-1)
    return x + _A_ref(xn, A_k, A_v, A_shift_k, A_ssm_k, A_ssm_D) + _B_ref(xn, B_w_gate, B_w_up, B_w_down)
```

```cpp
#include <hip/hip_runtime.h>
#include <hip/hip_bf16.h>
#include <stdint.h>
#include <utility>

typedef unsigned short u16;
typedef short bf16x8 __attribute__((ext_vector_type(8)));
typedef short s16x4 __attribute__((ext_vector_type(4)));
typedef float f32x4 __attribute__((ext_vector_type(4)));
typedef float f32x16 __attribute__((ext_vector_type(16)));
typedef unsigned u32x4 __attribute__((ext_vector_type(4)));
typedef unsigned u32x2 __attribute__((ext_vector_type(2)));
typedef __bf16 bf2_t __attribute__((ext_vector_type(2)));
#define LAS __attribute__((address_space(3)))
#define DI __device__ __forceinline__

constexpr int S_ = 2048, D_ = 576, F_ = 4608;

constexpr size_t WS_XFRAG = 0;
constexpr size_t WS_STATS = WS_XFRAG + (size_t)S_ * D_ * 2;
constexpr size_t WS_KVR   = WS_STATS + (size_t)S_ * 8;
constexpr size_t WS_WT    = WS_KVR + (size_t)S_ * D_ * 2;
constexpr size_t WS_H     = WS_WT + (size_t)S_ * D_ * 2;
constexpr size_t WS_SLAB  = WS_H + (size_t)S_ * F_ * 2;
constexpr size_t WS_END   = WS_SLAB + (size_t)4 * S_ * D_ * 4;

DI float bf_lo(unsigned w) { return __uint_as_float(w << 16); }
DI float bf_hi(unsigned w) { return __uint_as_float(w & 0xffff0000u); }
DI float bf2f(u16 b) { return __uint_as_float(((unsigned)b) << 16); }
DI float bfr(float x) { return (float)(__bf16)x; }
DI unsigned pk_bf16(float lo, float hi) { bf2_t v = {(__bf16)lo, (__bf16)hi}; return __builtin_bit_cast(unsigned, v); }

#define WAIT_V(n) asm volatile("s_waitcnt vmcnt(" #n ")" ::: "memory")
#define WAIT_L(n) asm volatile("s_waitcnt lgkmcnt(" #n ")" ::: "memory")
#define CFENCE() asm volatile("" ::: "memory")
#define SCHED0() __builtin_amdgcn_sched_barrier(0)
template <int... I, class Fn> DI void sfor_impl(std::integer_sequence<int, I...>, Fn&& f) { (f(std::integral_constant<int, I>{}), ...); }
template <int N, class Fn> DI void sfor(Fn&& f) { sfor_impl(std::make_integer_sequence<int, N>{}, f); }
#define ALWAYS_INLINE __attribute__((always_inline))
template <int N> DI void wait_vm() { asm volatile("s_waitcnt vmcnt(%0)" ::"i"(N) : "memory"); }

DI void glds4(const void* sbase, unsigned v0, unsigned v1, unsigned v2, unsigned v3, unsigned lds_dst) {
  unsigned keep;
  asm volatile("s_mov_b32 %0, m0\n\ts_mov_b32 m0, %6\n\ts_nop 0\n\t"
               "global_load_lds_dwordx4 %1, %5\n\t"
               "global_load_lds_dwordx4 %2, %5 offset:1024\n\t"
               "global_load_lds_dwordx4 %3, %5 offset:2048\n\t"
               "global_load_lds_dwordx4 %4, %5 offset:3072\n\t"
               "s_mov_b32 m0, %0"
               : "=&s"(keep) : "v"(v0), "v"(v1), "v"(v2), "v"(v3), "s"(sbase), "s"(lds_dst) : "memory");
}
DI void glds1(const void* sbase, unsigned v0, unsigned lds_dst) {
  unsigned keep;
  asm volatile("s_mov_b32 %0, m0\n\ts_mov_b32 m0, %3\n\ts_nop 0\n\t"
               "global_load_lds_dwordx4 %1, %2\n\t"
               "s_mov_b32 m0, %0"
               : "=&s"(keep) : "v"(v0), "s"(sbase), "s"(lds_dst) : "memory");
}

DI void gload16(bf16x8& dst, const void* sbase, unsigned voff) {
  asm volatile("global_load_dwordx4 %0, %1, %2" : "=v"(dst) : "v"(voff), "s"(sbase) : "memory");
}

__global__ __launch_bounds__(256) void k0_prep(const u16* __restrict__ q, const u16* __restrict__ kk, const u16* __restrict__ vv,
                                               const u16* __restrict__ shk, u16* __restrict__ xfrag, float2* __restrict__ stats,
                                               u16* __restrict__ kvr) {
  __shared__ __attribute__((aligned(16))) u16 tile[64 * 72];
  const int bid = blockIdx.x, tid = threadIdx.x;
  if (bid < 128) {
    const int wave = tid >> 6, lane = tid & 63;
    const int token = bid * 16 + wave * 4 + (lane >> 4), j = lane & 15;
    u32x2 d[9];
#pragma unroll
    for (int i = 0; i < 9; ++i) d[i] = *(const u32x2*)(q + (size_t)token * D_ + (j + 16 * i) * 4);
    float s = 0.f;
#pragma unroll
    for (int i = 0; i < 9; ++i) s += (bf_lo(d[i].x) + bf_hi(d[i].x)) + (bf_lo(d[i].y) + bf_hi(d[i].y));
    s += __shfl_xor(s, 1); s += __shfl_xor(s, 2); s += __shfl_xor(s, 4); s += __shfl_xor(s, 8);
    const float mean = s * (1.0f / 576.0f);
    float vs = 0.f;
#pragma unroll
    for (int i = 0; i < 9; ++i) {
      float a = bf_lo(d[i].x) - mean, b = bf_hi(d[i].x) - mean, c = bf_lo(d[i].y) - mean, e = bf_hi(d[i].y) - mean;
      vs += (a * a + b * b) + (c * c + e * e);
    }
    vs += __shfl_xor(vs, 1); vs += __shfl_xor(vs, 2); vs += __shfl_xor(vs, 4); vs += __shfl_xor(vs, 8);
    const float mb = bfr(mean), vb = bfr(vs * (1.0f / 576.0f));
    const float sd = bfr(sqrtf(bfr(vb + 1e-5f)));
    if (j == 0) stats[token] = make_float2(mb, sd);
    const int tl = token >> 5, tr = token & 31;
#pragma unroll
    for (int i = 0; i < 9; ++i) {
      const int p = j + 16 * i;
      float a = bfr(bf_lo(d[i].x) - mb) / sd, b = bfr(bf_hi(d[i].x) - mb) / sd, c = bfr(bf_lo(d[i].y) - mb) / sd, e = bfr(bf_hi(d[i].y) - mb) / sd;
      u32x2 o; o.x = pk_bf16(a, b); o.y = pk_bf16(c, e);
      const int st = p >> 2, kh = (p >> 1) & 1, half = p & 1;
      *(u32x2*)((char*)xfrag + ((size_t)((tl * 36 + st) * 64 + kh * 32 + tr)) * 16 + half * 8) = o;
    }
  } else {
    const int kb = bid - 128, tt = kb / 9, ct = kb - tt * 9;
    const int t0 = tt * 64, c0 = ct * 64;
    const int tr = tid >> 3, cg = tid & 7;
    u32x4 w4[4];
#pragma unroll
    for (int i = 0; i < 4; ++i) w4[i] = *(const u32x4*)(shk + (size_t)(c0 + 8 * cg) * 4 + i * 8);
#pragma unroll
    for (int it = 0; it < 2; ++it) {
      const int tl = tr + 32 * it, t = t0 + tl;
      u32x4 kr[4];
#pragma unroll
      for (int i = 0; i < 4; ++i) {
        const int ts = t + i - 3;
        kr[i] = (u32x4){0u, 0u, 0u, 0u};
        if (ts >= 0) kr[i] = *(const u32x4*)(kk + (size_t)ts * D_ + c0 + 8 * cg);
      }
      const u32x4 vr = *(const u32x4*)(vv + (size_t)t * D_ + c0 + 8 * cg);
#pragma unroll
      for (int e = 0; e < 8; ++e) {
        const unsigned wa = w4[e >> 1][(e & 1) * 2], wb = w4[e >> 1][(e & 1) * 2 + 1];
        const float w0 = bf_lo(wa), w1 = bf_hi(wa), w2 = bf_lo(wb), w3 = bf_hi(wb);
        float kq[4];
#pragma unroll
        for (int i = 0; i < 4; ++i) kq[i] = (e & 1) ? bf_hi(kr[i][e >> 1]) : bf_lo(kr[i][e >> 1]);
        float ks = bfr(kq[0] * w3);
        ks = bfr(ks + bfr(kq[1] * w2));
        ks = bfr(ks + bfr(kq[2] * w1));
        ks = bfr(ks + bfr(kq[3] * w0));
        const float vf = (e & 1) ? bf_hi(vr[e >> 1]) : bf_lo(vr[e >> 1]);
        const float kv = ks * vf;
        tile[(8 * cg + e) * 72 + (63 - tl)] = (u16)(pk_bf16(kv, 0.f) & 0xffffu);
      }
    }
    __syncthreads();
    const int pbase = 1984 - t0;
#pragma unroll
    for (int it = 0; it < 2; ++it) {
      const int pc = tid + 256 * it, ch = pc >> 3, pp = pc & 7;
      const u32x4 o = *(const u32x4*)(tile + ch * 72 + pp * 8);
      *(u32x4*)(kvr + (size_t)(c0 + ch) * S_ + pbase + pp * 8) = o;
    }
  }
}

constexpr int K1_SLOT = 37120;
constexpr int K1_UOFF = 18432 + 128;
constexpr int K1_NSLOT = 4;
constexpr int K1_LDS = K1_NSLOT * K1_SLOT;

DI bf16x8 tr_frag(LAS char* p, int off, int hi_off) {
  s16x4 lo = __builtin_amdgcn_ds_read_tr16_b64_v4i16((LAS s16x4*)(p + off));
  s16x4 hi = __builtin_amdgcn_ds_read_tr16_b64_v4i16((LAS s16x4*)(p + off + hi_off));
  return __builtin_shufflevector(lo, hi, 0, 1, 2, 3, 4, 5, 6, 7);
}

__global__ __launch_bounds__(512, 2) void k1_gemm1(const u16* __restrict__ xfrag, const u16* __restrict__ wg, const u16* __restrict__ wu,
                                                   u16* __restrict__ hout, const u16* __restrict__ ssmk, const u16* __restrict__ ssmd,
                                                   const u16* __restrict__ kvr, u16* __restrict__ wT) {
  extern __shared__ __attribute__((aligned(16))) char smem_g[];
  LAS char* smem = (LAS char*)smem_g;
  const unsigned lds0 = (unsigned)(uintptr_t)smem;
  const int bid = blockIdx.x, tid = threadIdx.x;
  const int w = __builtin_amdgcn_readfirstlane(tid >> 6), lane = tid & 63;
  const int xcd = bid & 7, idx = bid >> 3;
  const int ntile = xcd * 4 + (idx >> 3), mtile = idx & 7;
  const int n0 = ntile * 144;

  const int wq = w & 3, part = w >> 2;
  const char* wbase = (const char*)(part ? wu : wg) + (size_t)n0 * 2;
  unsigned voff[5];
#pragma unroll
  for (int n = 0; n < 5; ++n) {
    const int o = (n < 4) ? ((wq * 4 + n) * 1024 + lane * 16) : (16384 + wq * 512 + (lane & 31) * 16);
    const int k = o / 96, c6 = (o - k * 96) >> 4;
    voff[n] = (unsigned)(k * (F_ * 2) + c6 * 16 - (n < 4 ? n * 1024 : 0));
  }
  const unsigned ldst = lds0 + part * K1_UOFF + wq * 4096;
  const unsigned ldst4 = lds0 + part * K1_UOFF + 16384 + wq * 512;

  auto stage_load = [&](int st) ALWAYS_INLINE {
    const int p = st / 3, ks = st - p * 3, slot = st % K1_NSLOT;
    const char* g = wbase + (size_t)ks * 192 * (F_ * 2) + p * 96;
    glds4(g, voff[0], voff[1], voff[2], voff[3], ldst + slot * K1_SLOT);
    if (lane < 32) glds1(g, voff[4], ldst4 + slot * K1_SLOT);
  };

  bf16x8 xf[36];
  {
    const char* xb = (const char*)xfrag + (size_t)(mtile * 8 + w) * 36 * 1024;
    const unsigned xv = lane * 16;
#pragma unroll
    for (int s = 0; s < 36; ++s) gload16(xf[s], xb + s * 1024, xv);
  }
  stage_load(0);
  stage_load(1);
  stage_load(2);

  const int kh = lane >> 5, prt = (lane >> 4) & 1, qq = (lane & 15) >> 2, p4 = lane & 3;
  LAS char* rbase = smem + (8 * kh + qq) * 96 + prt * K1_UOFF + p4 * 8;

  f32x16 acc[3];
#pragma unroll
  for (int c = 0; c < 3; ++c)
#pragma unroll
    for (int i = 0; i < 16; ++i) acc[c][i] = 0.f;

  const int token = mtile * 256 + w * 32 + (lane & 31);
  char* hb = (char*)hout + ((size_t)(token >> 4) * 144 * 64 + (lane >> 5) * 16 + (token & 15)) * 16;

  asm volatile("s_waitcnt vmcnt(10)"
               : "+v"(xf[0]), "+v"(xf[1]), "+v"(xf[2]), "+v"(xf[3]), "+v"(xf[4]), "+v"(xf[5]), "+v"(xf[6]), "+v"(xf[7]), "+v"(xf[8]),
                 "+v"(xf[9]), "+v"(xf[10]), "+v"(xf[11]), "+v"(xf[12]), "+v"(xf[13]), "+v"(xf[14]), "+v"(xf[15]), "+v"(xf[16]), "+v"(xf[17])::"memory");
  asm volatile(""
               : "+v"(xf[18]), "+v"(xf[19]), "+v"(xf[20]), "+v"(xf[21]), "+v"(xf[22]), "+v"(xf[23]), "+v"(xf[24]), "+v"(xf[25]), "+v"(xf[26]),
                 "+v"(xf[27]), "+v"(xf[28]), "+v"(xf[29]), "+v"(xf[30]), "+v"(xf[31]), "+v"(xf[32]), "+v"(xf[33]), "+v"(xf[34]), "+v"(xf[35])::"memory");
  __builtin_amdgcn_s_barrier();
  CFENCE();

  constexpr int MID = 6;
  bf16x8 af[2][3];
  LAS char* sb = rbase;
#pragma unroll
  for (int c = 0; c < 3; ++c) af[0][c] = tr_frag(sb, c * 32, 4 * 96);
  sfor<108>([&](auto gc) ALWAYS_INLINE {
    constexpr int g = gc;
    constexpr int st = g / 12, s = g - st * 12, p = st / 3, ks = st - p * 3;
    if (s == MID && st < 8) {
      if (st == 0 || st == 1 || st == 2 || st == 5) wait_vm<5>();
      else if (st == 7) wait_vm<3>();
      else wait_vm<8>();
      __builtin_amdgcn_s_barrier();
      CFENCE();
      if (st + 3 < 9) stage_load(st + 3);
    }
    if (g + 1 < 108) {
      constexpr int st1 = (g + 1) / 12, s1 = (g + 1) - st1 * 12;
      if (s1 == 0) { sb = rbase + (st1 % K1_NSLOT) * K1_SLOT; asm volatile("" : "+v"(sb)); }
#pragma unroll
      for (int c = 0; c < 3; ++c) af[(g + 1) & 1][c] = tr_frag(sb, s1 * 16 * 96 + c * 32, 4 * 96);
    }
    SCHED0();
#pragma unroll
    for (int c = 0; c < 3; ++c) acc[c] = __builtin_amdgcn_mfma_f32_32x32x16_bf16(af[g & 1][c], xf[ks * 12 + s], acc[c], 0, 0, 0);
    SCHED0();
    if (ks == 2 && s == 11) {
#pragma unroll
      for (int c = 0; c < 3; ++c) {
        float hv[8];
#pragma unroll
        for (int i = 0; i < 8; ++i) hv[i] = bfr(acc[c][i]) * bfr(acc[c][i + 8]);
        u32x4 o;
        o.x = pk_bf16(hv[0], hv[1]); o.y = pk_bf16(hv[2], hv[3]); o.z = pk_bf16(hv[4], hv[5]); o.w = pk_bf16(hv[6], hv[7]);
        const int c16 = ntile * 9 + p * 3 + c;
        *(u32x4*)(hb + ((size_t)(c16 >> 1) * 64 + (c16 & 1) * 32) * 16) = o;
#pragma unroll
        for (int i = 0; i < 16; ++i) acc[c][i] = 0.f;
      }
      SCHED0();
    }
  });

  __syncthreads();
  const int grp = bid >> 2, qd = bid & 3;
  const int chA = grp * 9 + 2 * qd, chC = grp * 9 + 8;
  constexpr int XR_STRIDE = 4608;
  constexpr int PART_OFF = 16384, PARTC_OFF = 16384 + 65536;
  {
    for (int pc = tid; pc < 768; pc += 512) {
      const int im = pc >> 8, pp = pc & 255;
      const int ch = (im == 2) ? chC : chA + im;
      const u32x4 v = *(const u32x4*)(kvr + (size_t)ch * S_ + pp * 8);
      *(LAS u32x4*)(smem + im * XR_STRIDE + pp * 16) = v;
    }
    if (tid < 96) {
      const int im = tid >> 5, pp = tid & 31;
      *(LAS u32x4*)(smem + im * XR_STRIDE + 4096 + pp * 16) = (u32x4){0u, 0u, 0u, 0u};
    }
  }
  __syncthreads();
  {
    const int t0 = lane & 15, kg = lane >> 4;
    const int aoff = t0 - 15 + 8 * kg;
    const int boff = (2032 - 16 * (lane & 15) + 8 * kg) * 2;
    auto load_a = [&](const u16* hp, int i) -> bf16x8 {
      if (i == 0) {
        bf16x8 r;
#pragma unroll
        for (int j = 0; j < 8; ++j) { const int e = aoff + j; r[j] = (e >= 0) ? (short)hp[e] : (short)0; }
        return r;
      }
      bf16x8 r;
      __builtin_memcpy(&r, hp + aoff + 32 * i, 16);
      return r;
    };
    {
      const int im = w >> 2;
      const u16* hp = ssmk + (size_t)(chA + im) * S_;
      LAS char* xb = smem + im * XR_STRIDE + boff;
      f32x4 ac[8];
#pragma unroll
      for (int j = 0; j < 8; ++j) ac[j] = (f32x4){0.f, 0.f, 0.f, 0.f};
#pragma unroll
      for (int rr = 0; rr < 2; ++rr) {
        const int r = 2 * (w & 3) + rr;
        bf16x8 af[8], bfm[8];
#pragma unroll
        for (int a = 0; a < 8; ++a) af[a] = load_a(hp, 8 * a + r);
#pragma unroll
        for (int b = 0; b < 8; ++b) bfm[b] = *(LAS bf16x8*)(xb - 64 * (8 * b - r));
#pragma unroll
        for (int j = 0; j < 8; ++j)
#pragma unroll
          for (int a = 0; a <= j; ++a) ac[j] = __builtin_amdgcn_mfma_f32_16x16x32_bf16(af[a], bfm[j - a], ac[j], 0, 0, 0);
      }
#pragma unroll
      for (int j = 0; j < 8; ++j)
        *(LAS f32x4*)(smem + PART_OFF + w * 8192 + (256 * j + 16 * (lane & 15) + 4 * kg) * 4) = ac[j];
    }
    {
      const u16* hp = ssmk + (size_t)chC * S_;
      LAS char* xb = smem + 2 * XR_STRIDE + boff;
      const int jA = 7 - qd, jB = qd, r = w;
      f32x4 c0 = (f32x4){0.f, 0.f, 0.f, 0.f}, c1 = c0;
#pragma unroll
      for (int a = 0; a < 8; ++a) {
        if (a <= jA) {
          const bf16x8 af = load_a(hp, 8 * a + r);
          const bf16x8 b0 = *(LAS bf16x8*)(xb - 64 * (8 * (jA - a) - r));
          c0 = __builtin_amdgcn_mfma_f32_16x16x32_bf16(af, b0, c0, 0, 0, 0);
          if (a <= jB) {
            const bf16x8 b1 = *(LAS bf16x8*)(xb - 64 * (8 * (jB - a) - r));
            c1 = __builtin_amdgcn_mfma_f32_16x16x32_bf16(af, b1, c1, 0, 0, 0);
          }
        }
      }
      *(LAS f32x4*)(smem + PARTC_OFF + w * 2048 + (16 * (lane & 15) + 4 * kg) * 4) = c0;
      *(LAS f32x4*)(smem + PARTC_OFF + w * 2048 + 1024 + (16 * (lane & 15) + 4 * kg) * 4) = c1;
    }
  }
  __syncthreads();
  {
    const int im = tid >> 8, t8 = (tid & 255) * 8;
    f32x4 s0 = (f32x4){0.f, 0.f, 0.f, 0.f}, s1 = s0;
#pragma unroll
    for (int ww = 0; ww < 4; ++ww) {
      s0 += *(LAS f32x4*)(smem + PART_OFF + (im * 4 + ww) * 8192 + t8 * 4);
      s1 += *(LAS f32x4*)(smem + PART_OFF + (im * 4 + ww) * 8192 + t8 * 4 + 16);
    }
    const float dd = bf2f(ssmd[chA + im]);
    const u32x4 kvv = *(LAS u32x4*)(smem + im * XR_STRIDE + (2040 - t8) * 2);
    float y[8] = {s0[0], s0[1], s0[2], s0[3], s1[0], s1[1], s1[2], s1[3]};
    float o[8];
#pragma unroll
    for (int e = 0; e < 8; ++e) {
      const int ri = 7 - e;
      const float kvf = (ri & 1) ? bf_hi(kvv[ri >> 1]) : bf_lo(kvv[ri >> 1]);
      o[e] = bfr(y[e]) + bfr(kvf * dd);
    }
    u32x4 ov; ov.x = pk_bf16(o[0], o[1]); ov.y = pk_bf16(o[2], o[3]); ov.z = pk_bf16(o[4], o[5]); ov.w = pk_bf16(o[6], o[7]);
    *(u32x4*)(wT + (size_t)(chA + im) * S_ + t8) = ov;
  }
  if (tid < 64) {
    const int tl = tid >> 5, l8 = (tid & 31) * 8;
    const int jt = tl ? qd : 7 - qd, t8 = 256 * jt + l8;
    f32x4 s0 = (f32x4){0.f, 0.f, 0.f, 0.f}, s1 = s0;
#pragma unroll
    for (int ww = 0; ww < 8; ++ww) {
      s0 += *(LAS f32x4*)(smem + PARTC_OFF + ww * 2048 + tl * 1024 + l8 * 4);
      s1 += *(LAS f32x4*)(smem + PARTC_OFF + ww * 2048 + tl * 1024 + l8 * 4 + 16);
    }
    const float dd = bf2f(ssmd[chC]);
    const u32x4 kvv = *(LAS u32x4*)(smem + 2 * XR_STRIDE + (2040 - t8) * 2);
    float y[8] = {s0[0], s0[1], s0[2], s0[3], s1[0], s1[1], s1[2], s1[3]};
    float o[8];
#pragma unroll
    for (int e = 0; e < 8; ++e) {
      const int ri = 7 - e;
      const float kvf = (ri & 1) ? bf_hi(kvv[ri >> 1]) : bf_lo(kvv[ri >> 1]);
      o[e] = bfr(y[e]) + bfr(kvf * dd);
    }
    u32x4 ov; ov.x = pk_bf16(o[0], o[1]); ov.y = pk_bf16(o[2], o[3]); ov.z = pk_bf16(o[4], o[5]); ov.w = pk_bf16(o[6], o[7]);
    *(u32x4*)(wT + (size_t)chC * S_ + t8) = ov;
  }
}

constexpr int K2_HALF = 18432;
constexpr int K2_SLOT = 2 * K2_HALF;
constexpr int K2_NSLOT = 4;
constexpr int K2_LDS = K2_NSLOT * K2_SLOT;

__global__ __launch_bounds__(512, 2) void k2_gemm2(const u16* __restrict__ hin, const u16* __restrict__ wd, float* __restrict__ slab) {
  extern __shared__ __attribute__((aligned(16))) char smem_g[];
  LAS char* smem = (LAS char*)smem_g;
  const unsigned lds0 = (unsigned)(uintptr_t)smem;
  const int bid = blockIdx.x, tid = threadIdx.x;
  const int w = __builtin_amdgcn_readfirstlane(tid >> 6), lane = tid & 63;
  const int xcd = bid & 7, idx = bid >> 3;
  const int ksl = xcd >> 1, mt = (xcd & 1) * 8 + (idx >> 2), nt = idx & 3;
  const int tq = w & 3, khf = w >> 2;
  const int kbase = ksl * 1152 + khf * 576;

  const char* wbase = (const char*)wd + (size_t)kbase * (D_ * 2) + nt * 288;
  unsigned voff[5];
#pragma unroll
  for (int n = 0; n < 5; ++n) {
    const int o = (n < 4) ? ((tq * 4 + n) * 1024 + lane * 16) : (16384 + tq * 512 + (lane & 31) * 16);
    const int k = o / 288, c = (o - k * 288) >> 4;
    voff[n] = (unsigned)(k * (D_ * 2) + c * 16 - (n < 4 ? n * 1024 : 0));
  }
  const unsigned ldst = lds0 + khf * K2_HALF + tq * 4096, ldst4 = lds0 + khf * K2_HALF + 16384 + tq * 512;
  const char* hbase = (const char*)hin + ((size_t)(mt * 8 + tq * 2) * 144 * 64 + (size_t)(kbase >> 5) * 64) * 16;
  const unsigned hv0 = lane * 16, hv1 = lane * 16 + 144 * 64 * 16;
  bf16x8 hf[K2_NSLOT][2][2];
  auto stage_load = [&](int st) ALWAYS_INLINE {
    const int slot = st % K2_NSLOT;
    const char* g = wbase + (size_t)st * 64 * (D_ * 2);
    glds4(g, voff[0], voff[1], voff[2], voff[3], ldst + slot * K2_SLOT);
    if (lane < 32) glds1(g, voff[4], ldst4 + slot * K2_SLOT);
#pragma unroll
    for (int s = 0; s < 2; ++s) {
      gload16(hf[slot][s][0], hbase + (2 * st + s) * 1024, hv0);
      gload16(hf[slot][s][1], hbase + (2 * st + s) * 1024, hv1);
    }
  };
  stage_load(0);
  stage_load(1);
  stage_load(2);

  const int kq = lane >> 4, qq = (lane & 15) >> 2, p4 = lane & 3;
  LAS char* rbase = smem + khf * K2_HALF + (16 * (kq >> 1) + 4 * (kq & 1) + qq) * 288 + p4 * 8;

  f32x4 acc[2][9];
#pragma unroll
  for (int t = 0; t < 2; ++t)
#pragma unroll
    for (int dt = 0; dt < 9; ++dt) acc[t][dt] = (f32x4){0.f, 0.f, 0.f, 0.f};

  asm volatile("s_waitcnt vmcnt(18)" : "+v"(hf[0][0][0]), "+v"(hf[0][0][1]), "+v"(hf[0][1][0]), "+v"(hf[0][1][1])::"memory");
  __builtin_amdgcn_s_barrier();
  CFENCE();

  constexpr int PD = 3;
  constexpr int MID = 6;
  bf16x8 af[PD + 1];
  LAS char* sb = rbase;
#pragma unroll
  for (int g = 0; g < PD; ++g) af[g] = tr_frag(sb, g * 32, 8 * 288);
  sfor<162>([&](auto gc) ALWAYS_INLINE {
    constexpr int g = gc;
    constexpr int st = g / 18, f = g - st * 18, s = f / 9, dt = f - s * 9, slot = st % K2_NSLOT;
    if (f == MID && st < 8) {
      constexpr int sl1 = (st + 1) % K2_NSLOT;
      if (st < 7) asm volatile("s_waitcnt vmcnt(9)" : "+v"(hf[sl1][0][0]), "+v"(hf[sl1][0][1]), "+v"(hf[sl1][1][0]), "+v"(hf[sl1][1][1])::"memory");
      else        asm volatile("s_waitcnt vmcnt(0)" : "+v"(hf[sl1][0][0]), "+v"(hf[sl1][0][1]), "+v"(hf[sl1][1][0]), "+v"(hf[sl1][1][1])::"memory");
      __builtin_amdgcn_s_barrier();
      CFENCE();
      if (st + 3 < 9) stage_load(st + 3);
    }
    if (g + PD < 162) {
      constexpr int g1 = g + PD, st1 = g1 / 18, f1 = g1 - st1 * 18, s1 = f1 / 9, dt1 = f1 - s1 * 9;
      if (f1 == 0) { sb = rbase + (st1 % K2_NSLOT) * K2_SLOT; asm volatile("" : "+v"(sb)); }
      af[g1 % (PD + 1)] = tr_frag(sb, s1 * 32 * 288 + dt1 * 32, 8 * 288);
    }
    SCHED0();
#pragma unroll
    for (int t = 0; t < 2; ++t) acc[t][dt] = __builtin_amdgcn_mfma_f32_16x16x32_bf16(af[g % (PD + 1)], hf[slot][s][t], acc[t][dt], 0, 0, 0);
    SCHED0();
  });
  __syncthreads();
  if (khf == 1) {
#pragma unroll
    for (int t = 0; t < 2; ++t)
#pragma unroll
      for (int dt = 0; dt < 9; ++dt) *(LAS f32x4*)(smem + ((tq * 18 + t * 9 + dt) * 64 + lane) * 16) = acc[t][dt];
  }
  __syncthreads();
  if (khf == 0) {
    float* sb = slab + (size_t)ksl * S_ * D_;
#pragma unroll
    for (int t = 0; t < 2; ++t)
#pragma unroll
      for (int dt = 0; dt < 9; ++dt) {
        const f32x4 o = acc[t][dt] + *(LAS f32x4*)(smem + ((tq * 18 + t * 9 + dt) * 64 + lane) * 16);
        const int token = mt * 128 + tq * 32 + t * 16 + (lane & 15);
        *(f32x4*)(sb + (size_t)token * D_ + nt * 144 + dt * 16 + 4 * (lane >> 4)) = o;
      }
  }
}

__global__ __launch_bounds__(256) void k3_final(const u16* __restrict__ q, const float2* __restrict__ stats, const u16* __restrict__ wT,
                                                const float* __restrict__ slab, u16* __restrict__ out) {
  const int gid = blockIdx.x * 256 + threadIdx.x;
  const int dq = gid % 144, tqd = gid / 144;
  const int d0 = dq * 4, t0 = tqd * 4;
  u32x2 wv[4], xv[4];
  f32x4 sm[4];
  float2 stt[4];
#pragma unroll
  for (int i = 0; i < 4; ++i) {
    wv[i] = *(const u32x2*)(wT + (size_t)(d0 + i) * S_ + t0);
    xv[i] = *(const u32x2*)(q + (size_t)(t0 + i) * D_ + d0);
    stt[i] = stats[t0 + i];
    f32x4 a = *(const f32x4*)(slab + (size_t)(t0 + i) * D_ + d0);
#pragma unroll
    for (int k = 1; k < 4; ++k) a += *(const f32x4*)(slab + (size_t)k * S_ * D_ + (size_t)(t0 + i) * D_ + d0);
    sm[i] = a;
  }
#pragma unroll
  for (int i = 0; i < 4; ++i) {
    float o[4];
#pragma unroll
    for (int c = 0; c < 4; ++c) {
      const unsigned xw = (c < 2) ? xv[i].x : xv[i].y;
      const float x = (c & 1) ? bf_hi(xw) : bf_lo(xw);
      const unsigned ww = (i < 2) ? wv[c].x : wv[c].y;
      const float wy = (i & 1) ? bf_hi(ww) : bf_lo(ww);
      const float xn = bfr(bfr(x - stt[i].x) / stt[i].y);
      const float a = bfr(xn * wy);
      o[c] = bfr(x + a) + bfr(sm[i][c]);
    }
    u32x2 ov; ov.x = pk_bf16(o[0], o[1]); ov.y = pk_bf16(o[2], o[3]);
    *(u32x2*)(out + (size_t)(t0 + i) * D_ + d0) = ov;
  }
}

extern "C" void kernel_launch(void* const* d_in, const int* in_sizes, int n_in,
                              void* d_out, int out_size, void* d_ws, size_t ws_size,
                              hipStream_t stream) {
  const u16* q = (const u16*)d_in[0];
  const u16* k = (const u16*)d_in[1];
  const u16* v = (const u16*)d_in[2];
  const u16* shk = (const u16*)d_in[3];
  const u16* ssmk = (const u16*)d_in[4];
  const u16* ssmd = (const u16*)d_in[5];
  const u16* wg = (const u16*)d_in[7];
  const u16* wu = (const u16*)d_in[8];
  const u16* wd = (const u16*)d_in[9];
  char* ws = (char*)d_ws;
  u16* xfrag = (u16*)(ws + WS_XFRAG);
  float2* stats = (float2*)(ws + WS_STATS);
  u16* kvr = (u16*)(ws + WS_KVR);
  u16* wT = (u16*)(ws + WS_WT);
  u16* h = (u16*)(ws + WS_H);
  float* slab = (float*)(ws + WS_SLAB);

  (void)hipFuncSetAttribute((const void*)k1_gemm1, hipFuncAttributeMaxDynamicSharedMemorySize, K1_LDS);
  (void)hipFuncSetAttribute((const void*)k2_gemm2, hipFuncAttributeMaxDynamicSharedMemorySize, K2_LDS);

  k0_prep<<<128 + 288, 256, 0, stream>>>(q, k, v, shk, xfrag, stats, kvr);
  k1_gemm1<<<256, 512, K1_LDS, stream>>>(xfrag, wg, wu, h, ssmk, ssmd, kvr, wT);
  k2_gemm2<<<256, 512, K2_LDS, stream>>>(h, wd, slab);
  k3_final<<<(512 * 144) / 256, 256, 0, stream>>>(q, stats, wT, slab, (u16*)d_out);
}
```

```cpp
#include <hip/hip_runtime.h>
#include <hip/hip_bf16.h>
#include <stdint.h>
#include <utility>

typedef unsigned short u16;
typedef short bf16x8 __attribute__((ext_vector_type(8)));
typedef short s16x4 __attribute__((ext_vector_type(4)));
typedef float f32x4 __attribute__((ext_vector_type(4)));
typedef float f32x16 __attribute__((ext_vector_type(16)));
typedef unsigned u32x4 __attribute__((ext_vector_type(4)));
typedef unsigned u32x2 __attribute__((ext_vector_type(2)));
typedef __bf16 bf2_t __attribute__((ext_vector_type(2)));
#define LAS __attribute__((address_space(3)))
#define DI __device__ __forceinline__

constexpr int S_ = 2048, D_ = 576, F_ = 4608;

constexpr size_t WS_XFRAG = 0;
constexpr size_t WS_STATS = WS_XFRAG + (size_t)S_ * D_ * 2;
constexpr size_t WS_KVR   = WS_STATS + (size_t)S_ * 8;
constexpr size_t WS_WT    = WS_KVR + (size_t)S_ * D_ * 2;
constexpr size_t WS_H     = WS_WT + (size_t)S_ * D_ * 2;
constexpr size_t WS_SLAB  = WS_H + (size_t)S_ * F_ * 2;
constexpr size_t WS_CNT   = WS_SLAB + (size_t)4 * S_ * D_ * 4;
constexpr size_t WS_END   = WS_CNT + 64 * 256;

DI float bf_lo(unsigned w) { return __uint_as_float(w << 16); }
DI float bf_hi(unsigned w) { return __uint_as_float(w & 0xffff0000u); }
DI float bf2f(u16 b) { return __uint_as_float(((unsigned)b) << 16); }
DI float bfr(float x) { return (float)(__bf16)x; }
DI unsigned pk_bf16(float lo, float hi) { bf2_t v = {(__bf16)lo, (__bf16)hi}; return __builtin_bit_cast(unsigned, v); }

#define WAIT_V(n) asm volatile("s_waitcnt vmcnt(" #n ")" ::: "memory")
#define WAIT_L(n) asm volatile("s_waitcnt lgkmcnt(" #n ")" ::: "memory")
#define CFENCE() asm volatile("" ::: "memory")
#define STAMP(kid, i)
#define RSTAMP(kid, i)
#define SCHED0() __builtin_amdgcn_sched_barrier(0)
template <int... I, class Fn> DI void sfor_impl(std::integer_sequence<int, I...>, Fn&& f) { (f(std::integral_constant<int, I>{}), ...); }
template <int N, class Fn> DI void sfor(Fn&& f) { sfor_impl(std::make_integer_sequence<int, N>{}, f); }
#define ALWAYS_INLINE __attribute__((always_inline))
template <int N> DI void wait_vm() { asm volatile("s_waitcnt vmcnt(%0)" ::"i"(N) : "memory"); }

DI void glds4(const void* sbase, unsigned v0, unsigned v1, unsigned v2, unsigned v3, unsigned lds_dst) {
  unsigned keep;
  asm volatile("s_mov_b32 %0, m0\n\ts_mov_b32 m0, %6\n\ts_nop 0\n\t"
               "global_load_lds_dwordx4 %1, %5\n\t"
               "global_load_lds_dwordx4 %2, %5 offset:1024\n\t"
               "global_load_lds_dwordx4 %3, %5 offset:2048\n\t"
               "global_load_lds_dwordx4 %4, %5 offset:3072\n\t"
               "s_mov_b32 m0, %0"
               : "=&s"(keep) : "v"(v0), "v"(v1), "v"(v2), "v"(v3), "s"(sbase), "s"(lds_dst) : "memory");
}
DI void glds1(const void* sbase, unsigned v0, unsigned lds_dst) {
  unsigned keep;
  asm volatile("s_mov_b32 %0, m0\n\ts_mov_b32 m0, %3\n\ts_nop 0\n\t"
               "global_load_lds_dwordx4 %1, %2\n\t"
               "s_mov_b32 m0, %0"
               : "=&s"(keep) : "v"(v0), "s"(sbase), "s"(lds_dst) : "memory");
}

DI void gload16(bf16x8& dst, const void* sbase, unsigned voff) {
  asm volatile("global_load_dwordx4 %0, %1, %2" : "=v"(dst) : "v"(voff), "s"(sbase) : "memory");
}

__global__ __launch_bounds__(256) void k0_prep(const u16* __restrict__ q, const u16* __restrict__ kk, const u16* __restrict__ vv,
                                               const u16* __restrict__ shk, u16* __restrict__ xfrag, float2* __restrict__ stats,
                                               u16* __restrict__ kvr, unsigned* __restrict__ cnt) {
  __shared__ __attribute__((aligned(16))) u16 tile[64 * 72];
  const int bid = blockIdx.x, tid = threadIdx.x;
  if (bid == 0 && tid < 64) cnt[tid * 64] = 0u;
  if (bid < 128) {
    const int wave = tid >> 6, lane = tid & 63;
    const int token = bid * 16 + wave * 4 + (lane >> 4), j = lane & 15;
    u32x2 d[9];
#pragma unroll
    for (int i = 0; i < 9; ++i) d[i] = *(const u32x2*)(q + (size_t)token * D_ + (j + 16 * i) * 4);
    float s = 0.f;
#pragma unroll
    for (int i = 0; i < 9; ++i) s += (bf_lo(d[i].x) + bf_hi(d[i].x)) + (bf_lo(d[i].y) + bf_hi(d[i].y));
    s += __shfl_xor(s, 1); s += __shfl_xor(s, 2); s += __shfl_xor(s, 4); s += __shfl_xor(s, 8);
    const float mean = s * (1.0f / 576.0f);
    float vs = 0.f;
#pragma unroll
    for (int i = 0; i < 9; ++i) {
      float a = bf_lo(d[i].x) - mean, b = bf_hi(d[i].x) - mean, c = bf_lo(d[i].y) - mean, e = bf_hi(d[i].y) - mean;
      vs += (a * a + b * b) + (c * c + e * e);
    }
    vs += __shfl_xor(vs, 1); vs += __shfl_xor(vs, 2); vs += __shfl_xor(vs, 4); vs += __shfl_xor(vs, 8);
    const float mb = bfr(mean), vb = bfr(vs * (1.0f / 576.0f));
    const float sd = bfr(sqrtf(bfr(vb + 1e-5f)));
    if (j == 0) stats[token] = make_float2(mb, sd);
    const int tl = token >> 5, tr = token & 31;
#pragma unroll
    for (int i = 0; i < 9; ++i) {
      const int p = j + 16 * i;
      float a = bfr(bf_lo(d[i].x) - mb) / sd, b = bfr(bf_hi(d[i].x) - mb) / sd, c = bfr(bf_lo(d[i].y) - mb) / sd, e = bfr(bf_hi(d[i].y) - mb) / sd;
      u32x2 o; o.x = pk_bf16(a, b); o.y = pk_bf16(c, e);
      const int st = p >> 2, kh = (p >> 1) & 1, half = p & 1;
      *(u32x2*)((char*)xfrag + ((size_t)((tl * 36 + st) * 64 + kh * 32 + tr)) * 16 + half * 8) = o;
    }
  } else {
    const int kb = bid - 128, tt = kb / 9, ct = kb - tt * 9;
    const int t0 = tt * 64, c0 = ct * 64;
    const int tr = tid >> 3, cg = tid & 7;
    u32x4 w4[4];
#pragma unroll
    for (int i = 0; i < 4; ++i) w4[i] = *(const u32x4*)(shk + (size_t)(c0 + 8 * cg) * 4 + i * 8);
#pragma unroll
    for (int it = 0; it < 2; ++it) {
      const int tl = tr + 32 * it, t = t0 + tl;
      u32x4 kr[4];
#pragma unroll
      for (int i = 0; i < 4; ++i) {
        const int ts = t + i - 3;
        kr[i] = (u32x4){0u, 0u, 0u, 0u};
        if (ts >= 0) kr[i] = *(const u32x4*)(kk + (size_t)ts * D_ + c0 + 8 * cg);
      }
      const u32x4 vr = *(const u32x4*)(vv + (size_t)t * D_ + c0 + 8 * cg);
#pragma unroll
      for (int e = 0; e < 8; ++e) {
        const unsigned wa = w4[e >> 1][(e & 1) * 2], wb = w4[e >> 1][(e & 1) * 2 + 1];
        const float w0 = bf_lo(wa), w1 = bf_hi(wa), w2 = bf_lo(wb), w3 = bf_hi(wb);
        float kq[4];
#pragma unroll
        for (int i = 0; i < 4; ++i) kq[i] = (e & 1) ? bf_hi(kr[i][e >> 1]) : bf_lo(kr[i][e >> 1]);
        float ks = bfr(kq[0] * w3);
        ks = bfr(ks + bfr(kq[1] * w2));
        ks = bfr(ks + bfr(kq[2] * w1));
        ks = bfr(ks + bfr(kq[3] * w0));
        const float vf = (e & 1) ? bf_hi(vr[e >> 1]) : bf_lo(vr[e >> 1]);
        const float kv = ks * vf;
        tile[(8 * cg + e) * 72 + (63 - tl)] = (u16)(pk_bf16(kv, 0.f) & 0xffffu);
      }
    }
    __syncthreads();
    const int pbase = 1984 - t0;
#pragma unroll
    for (int it = 0; it < 2; ++it) {
      const int pc = tid + 256 * it, ch = pc >> 3, pp = pc & 7;
      const u32x4 o = *(const u32x4*)(tile + ch * 72 + pp * 8);
      *(u32x4*)(kvr + (size_t)(c0 + ch) * S_ + pbase + pp * 8) = o;
    }
  }
}

constexpr int K1_SLOT = 37120;
constexpr int K1_UOFF = 18432 + 128;
constexpr int K1_NSLOT = 4;
constexpr int K1_LDS = K1_NSLOT * K1_SLOT;
constexpr int XR_OFF = K1_SLOT, XR_STRIDE = 4608;
constexpr int HZ_OFF = K1_SLOT + 3 * XR_STRIDE;
constexpr int PARTC_OFF = K1_SLOT + 16384;
constexpr int PART_OFF = 2 * K1_SLOT;

DI bf16x8 tr_frag(LAS char* p, int off, int hi_off) {
  s16x4 lo = __builtin_amdgcn_ds_read_tr16_b64_v4i16((LAS s16x4*)(p + off));
  s16x4 hi = __builtin_amdgcn_ds_read_tr16_b64_v4i16((LAS s16x4*)(p + off + hi_off));
  return __builtin_shufflevector(lo, hi, 0, 1, 2, 3, 4, 5, 6, 7);
}

constexpr int k1_xidx(int i) { return i < 9 ? 5 + i : (i < 18 ? 10 + i : 15 + i); }
constexpr int k1_dlast(int d) { return d == 0 ? 4 : d == 1 ? 18 : d == 2 ? 32 : d <= 6 ? 55 + 5 * (d - 3) : 79 + 5 * (d - 7); }
constexpr int k1_issued(int st, bool after_mid) {
  int n = 51;
  for (int i = 0; i < 9; ++i) {
    if (i < st || (i == st && after_mid)) { if (i + 3 < 9) n += 5; }
    if (i < st && (i == 3 || i == 7)) n += 4;
  }
  return n;
}

__global__ __launch_bounds__(512, 2) void k1_gemm1(const u16* __restrict__ xfrag, const u16* __restrict__ wg, const u16* __restrict__ wu,
                                                   u16* __restrict__ hout, const u16* __restrict__ ssmk, const u16* __restrict__ ssmd,
                                                   const u16* __restrict__ kvr, u16* __restrict__ wT) {
  extern __shared__ __attribute__((aligned(16))) char smem_g[];
  LAS char* smem = (LAS char*)smem_g;
  const unsigned lds0 = (unsigned)(uintptr_t)smem;
  const int bid = blockIdx.x, tid = threadIdx.x;
  const int w = __builtin_amdgcn_readfirstlane(tid >> 6), lane = tid & 63;
  const int xcd = bid & 7, idx = bid >> 3;
  const int mtile = (xcd & 1) * 4 + (idx & 3);
  const int cgrp = (xcd >> 1) * 2 + (idx >> 4), cq = (idx >> 2) & 3;
  const int colA = 64 * (9 * cgrp + 2 * cq), colQ = 64 * (9 * cgrp + 8) + 16 * cq;
  RSTAMP(1, 14); STAMP(1, 0);

  const int wq = w & 3, part = w >> 2;
  const char* wbase = (const char*)(part ? wu : wg);
  unsigned voff[5];
#pragma unroll
  for (int n = 0; n < 5; ++n) {
    const int o = (n < 4) ? ((wq * 4 + n) * 1024 + lane * 16) : (16384 + wq * 512 + (lane & 31) * 16);
    const int k = o >> 7, pc = (o >> 4) & 7;
    const int j = (pc >> 1) ^ (((k >> 1) & 1) | (part << 1));
    voff[n] = (unsigned)(k * (F_ * 2) + (2 * j + (pc & 1)) * 16 - (n < 4 ? n * 1024 : 0));
  }
  const unsigned ldst = lds0 + part * K1_UOFF + wq * 4096;
  const unsigned ldst4 = lds0 + part * K1_UOFF + 16384 + wq * 512;

  auto stage_load = [&](int st) ALWAYS_INLINE {
    const int slot = st % K1_NSLOT;
    if (st < 8) {
      const char* g = wbase + (size_t)(st & 3) * 144 * (F_ * 2) + (colA + (st >> 2) * 64) * 2;
      glds4(g, voff[0], voff[1], voff[2], voff[3], ldst + slot * K1_SLOT);
      if (lane < 32) glds1(g, voff[4], ldst4 + slot * K1_SLOT);
    } else {
      unsigned vq[5];
#pragma unroll
      for (int n = 0; n < 5; ++n) {
        const int o = (n < 4) ? ((wq * 4 + n) * 1024 + lane * 16) : (16384 + wq * 512 + (lane & 31) * 16);
        vq[n] = (unsigned)((o >> 5) * (F_ * 2) + ((o >> 4) & 1) * 16 - (n < 4 ? n * 1024 : 0));
      }
      const char* g = wbase + colQ * 2;
      glds4(g, vq[0], vq[1], vq[2], vq[3], ldst + slot * K1_SLOT);
      if (lane < 32) glds1(g, vq[4], ldst4 + slot * K1_SLOT);
    }
  };

  bf16x8 xf[36];
  {
    const char* xb = (const char*)xfrag + (size_t)(mtile * 8 + w) * 36 * 1024;
    const unsigned xv = lane * 16;
    stage_load(0);
#pragma unroll
    for (int s = 0; s < 9; ++s) gload16(xf[s], xb + s * 1024, xv);
    stage_load(1);
#pragma unroll
    for (int s = 9; s < 18; ++s) gload16(xf[s], xb + s * 1024, xv);
    stage_load(2);
#pragma unroll
    for (int s = 18; s < 36; ++s) gload16(xf[s], xb + s * 1024, xv);
  }

  const int kh = lane >> 5, prt = (lane >> 4) & 1, qq = (lane & 15) >> 2, p4 = lane & 3;
  const unsigned rbU = lds0 + (8 * kh + qq) * 128 + prt * K1_UOFF + ((((qq >> 1) & 1) | (prt << 1)) << 5) + p4 * 8;

  f32x16 acc[4];
#pragma unroll
  for (int c = 0; c < 4; ++c)
#pragma unroll
    for (int i = 0; i < 16; ++i) acc[c][i] = 0.f;

  const int token = mtile * 256 + w * 32 + (lane & 31);
  char* hb = (char*)hout + ((size_t)(token >> 4) * 144 * 64 + (lane >> 5) * 16 + (token & 15)) * 16;
  auto store_h = [&](const f32x16& a, int c16) ALWAYS_INLINE {
    float hv[8];
#pragma unroll
    for (int i = 0; i < 8; ++i) hv[i] = bfr(a[i]) * bfr(a[i + 8]);
    u32x4 o;
    o.x = pk_bf16(hv[0], hv[1]); o.y = pk_bf16(hv[2], hv[3]); o.z = pk_bf16(hv[4], hv[5]); o.w = pk_bf16(hv[6], hv[7]);
    *(u32x4*)(hb + ((size_t)(c16 >> 1) * 64 + (c16 & 1) * 32) * 16) = o;
  };

  const int grp = bid >> 2, qd = bid & 3;
  const int chA = grp * 9 + 2 * qd, chC = grp * 9 + 8;
  const int ct0 = lane & 15, ckg = lane >> 4;
  const char* hpA = (const char*)(ssmk + (size_t)(chA + (w >> 2)) * S_ + 64 * (w & 3));
  const char* hpC = (const char*)(ssmk + (size_t)chC * S_ + 32 * w);
  const bool zA = (w & 3) == 0, zC = w == 0;
  bf16x8 caf[24];
  int cvo = 0;

  wait_vm<51 - 1 - 4>();
  __builtin_amdgcn_s_barrier();
  CFENCE();
  STAMP(1, 1);

  constexpr int PD = 3, MID = 16;
  bf16x8 af[PD + 1];
  unsigned rb[4];
#pragma unroll
  for (int j = 0; j < 4; ++j) rb[j] = rbU ^ (j << 5);
#pragma unroll
  for (int g = 0; g < PD; ++g) af[g] = tr_frag((LAS char*)(uintptr_t)rb[g], 0, 512);
  sfor<324>([&](auto gc) ALWAYS_INLINE {
    constexpr int g = gc;
    constexpr int st = g / 36, f = g - st * 36;
    constexpr int s = st < 8 ? f / 4 : f, c = st < 8 ? f % 4 : 0;
    constexpr int xi = st < 8 ? (st & 3) * 9 + s : s;
    if constexpr (f == MID && st < 8) {
      wait_vm<k1_issued(st, false) - 1 - k1_dlast(st + 1)>();
      __builtin_amdgcn_s_barrier();
      CFENCE();
      if constexpr (st + 3 < 9) stage_load(st + 3);
      if constexpr (st == 6) {
        if (w < 3) {
          const int ch = (w == 2) ? chC : chA + w;
          const unsigned lv = lane * 16;
          glds4((const char*)kvr + (size_t)ch * (S_ * 2), lv, lv, lv, lv, lds0 + XR_OFF + w * XR_STRIDE);
          if (lane < 4) glds1((const char*)ssmk + (size_t)ch * (S_ * 2), lv, lds0 + HZ_OFF + w * 96 + 32);
        } else if (w == 3) {
          if (lane < 32) {
#pragma unroll
            for (int im = 0; im < 3; ++im) *(LAS u32x4*)(smem + XR_OFF + im * XR_STRIDE + 4096 + lane * 16) = (u32x4){0u, 0u, 0u, 0u};
          } else if (lane < 38) {
            const int im = (lane - 32) >> 1, hh = lane & 1;
            *(LAS u32x4*)(smem + HZ_OFF + im * 96 + hh * 16) = (u32x4){0u, 0u, 0u, 0u};
          }
        }
      }
    }
    if constexpr (g + PD < 324) {
      constexpr int g1 = g + PD, st1 = g1 / 36, f1 = g1 - st1 * 36;
      if constexpr (f1 == 0) {
        if constexpr (st1 < 8) {
          unsigned b2 = rbU; asm volatile("" : "+v"(b2));
#pragma unroll
          for (int j = 0; j < 4; ++j) { rb[j] = (b2 + (st1 % K1_NSLOT) * K1_SLOT) ^ (j << 5); asm volatile("" : "+v"(rb[j])); }
        } else {
          int l2 = lane; asm volatile("" : "+v"(l2));
          rb[0] = lds0 + (8 * (l2 >> 5) + ((l2 & 15) >> 2)) * 32 + ((l2 >> 4) & 1) * K1_UOFF + (l2 & 3) * 8 + (st1 % K1_NSLOT) * K1_SLOT;
          asm volatile("" : "+v"(rb[0]));
        }
      }
      if constexpr (st1 < 8) af[g1 % (PD + 1)] = tr_frag((LAS char*)(uintptr_t)rb[f1 % 4], (f1 / 4) * 2048, 512);
      else af[g1 % (PD + 1)] = tr_frag((LAS char*)(uintptr_t)rb[0], f1 * 512, 128);
    }
    if constexpr (st < 4 && c == 0) {
      asm volatile("s_waitcnt vmcnt(%1)" : "+v"(xf[xi]) : "i"(k1_issued(st, f >= MID) - 1 - k1_xidx(xi)) : "memory");
    }
    if constexpr (st == 8 && f >= 10 && f < 34) {
      constexpr int ci = f - 10;
      if constexpr (ci == 0) { int l2 = lane; asm volatile("" : "+v"(l2)); cvo = ((l2 & 15) - 15 + 8 * (l2 >> 4)) * 2; }
      if constexpr (ci == 0) __builtin_memcpy(&caf[ci], hpA + (zA ? 32 : 0) + cvo, 16);
      else if constexpr (ci < 16) __builtin_memcpy(&caf[ci], hpA + 64 * (ci >> 3) + 512 * (ci & 7) + cvo, 16);
      else if constexpr (ci == 16) __builtin_memcpy(&caf[ci], hpC + (zC ? 32 : 0) + cvo, 16);
      else __builtin_memcpy(&caf[ci], hpC + 512 * (ci - 16) + cvo, 16);
    }
    SCHED0();
    acc[c] = __builtin_amdgcn_mfma_f32_32x32x16_bf16(af[g % (PD + 1)], xf[xi], acc[c], 0, 0, 0);
    SCHED0();
    if constexpr ((st == 3 || st == 7) && f == 35) {
#pragma unroll
      for (int cc = 0; cc < 4; ++cc) {
        store_h(acc[cc], (colA >> 4) + (st >> 2) * 4 + cc);
#pragma unroll
        for (int i = 0; i < 16; ++i) acc[cc][i] = 0.f;
      }
      SCHED0();
    }
  });
  store_h(acc[0], colQ >> 4);

  STAMP(1, 2);
  __syncthreads();
  STAMP(1, 3);
  {
    const int boff = (2032 - 16 * (lane & 15) + 8 * ckg) * 2;
    auto head_frag = [&](int im) ALWAYS_INLINE -> bf16x8 {
      bf16x8 r;
      LAS u16* hz = (LAS u16*)(smem + HZ_OFF + im * 96) + 16 + (ct0 - 15 + 8 * ckg);
#pragma unroll
      for (int j = 0; j < 8; ++j) r[j] = (short)hz[j];
      return r;
    };
    if (zA) caf[0] = head_frag(w >> 2);
    if (zC) caf[16] = head_frag(2);
    {
      const int im = w >> 2;
      LAS char* xb = smem + XR_OFF + im * XR_STRIDE + boff + 128 * (w & 3);
      f32x4 ac[8];
#pragma unroll
      for (int j = 0; j < 8; ++j) ac[j] = (f32x4){0.f, 0.f, 0.f, 0.f};
#pragma unroll
      for (int rr = 1; rr >= 0; --rr) {
        bf16x8 bfm[8];
#pragma unroll
        for (int b = 0; b < 8; ++b) bfm[b] = *(LAS bf16x8*)(xb - 512 * b + 64 * rr);
#pragma unroll
        for (int a = 7; a >= 0; --a)
#pragma unroll
          for (int j = a; j < 8; ++j) ac[j] = __builtin_amdgcn_mfma_f32_16x16x32_bf16(caf[rr * 8 + a], bfm[j - a], ac[j], 0, 0, 0);
      }
#pragma unroll
      for (int j = 0; j < 8; ++j)
        *(LAS f32x4*)(smem + PART_OFF + w * 8192 + (256 * j + 16 * (lane & 15) + 4 * ckg) * 4) = ac[j];
    }
    {
      LAS char* xb = smem + XR_OFF + 2 * XR_STRIDE + boff + 64 * w;
      const int jA = 7 - qd, jB = qd;
      f32x4 c0 = (f32x4){0.f, 0.f, 0.f, 0.f}, c1 = c0;
#pragma unroll
      for (int a = 7; a >= 0; --a) {
        if (a <= jA) {
          const bf16x8 b0 = *(LAS bf16x8*)(xb - 512 * (jA - a));
          c0 = __builtin_amdgcn_mfma_f32_16x16x32_bf16(caf[16 + a], b0, c0, 0, 0, 0);
        }
        if (a <= jB) {
          const bf16x8 b1 = *(LAS bf16x8*)(xb - 512 * (jB - a));
          c1 = __builtin_amdgcn_mfma_f32_16x16x32_bf16(caf[16 + a], b1, c1, 0, 0, 0);
        }
      }
      *(LAS f32x4*)(smem + PARTC_OFF + w * 2048 + (16 * (lane & 15) + 4 * ckg) * 4) = c0;
      *(LAS f32x4*)(smem + PARTC_OFF + w * 2048 + 1024 + (16 * (lane & 15) + 4 * ckg) * 4) = c1;
    }
  }
  STAMP(1, 4);
  __syncthreads();
  STAMP(1, 5);
  auto finalize = [&](f32x4 s0, f32x4 s1, int im, int ch, int t8) ALWAYS_INLINE {
    const float dd = bf2f(ssmd[ch]);
    const u32x4 kvv = *(LAS u32x4*)(smem + XR_OFF + im * XR_STRIDE + (2040 - t8) * 2);
    float y[8] = {s0[0], s0[1], s0[2], s0[3], s1[0], s1[1], s1[2], s1[3]};
    float o[8];
#pragma unroll
    for (int e = 0; e < 8; ++e) {
      const int ri = 7 - e;
      const float kvf = (ri & 1) ? bf_hi(kvv[ri >> 1]) : bf_lo(kvv[ri >> 1]);
      o[e] = bfr(y[e]) + bfr(kvf * dd);
    }
    u32x4 ov; ov.x = pk_bf16(o[0], o[1]); ov.y = pk_bf16(o[2], o[3]); ov.z = pk_bf16(o[4], o[5]); ov.w = pk_bf16(o[6], o[7]);
    *(u32x4*)(wT + (size_t)ch * S_ + t8) = ov;
  };
  {
    const int im = tid >> 8, t8 = (tid & 255) * 8;
    f32x4 s0 = (f32x4){0.f, 0.f, 0.f, 0.f}, s1 = s0;
#pragma unroll
    for (int ww = 0; ww < 4; ++ww) {
      s0 += *(LAS f32x4*)(smem + PART_OFF + (im * 4 + ww) * 8192 + t8 * 4);
      s1 += *(LAS f32x4*)(smem + PART_OFF + (im * 4 + ww) * 8192 + t8 * 4 + 16);
    }
    finalize(s0, s1, im, chA + im, t8);
  }
  if (tid < 64) {
    const int tl = tid >> 5, l8 = (tid & 31) * 8;
    const int jt = tl ? qd : 7 - qd, t8 = 256 * jt + l8;
    f32x4 s0 = (f32x4){0.f, 0.f, 0.f, 0.f}, s1 = s0;
#pragma unroll
    for (int ww = 0; ww < 8; ++ww) {
      s0 += *(LAS f32x4*)(smem + PARTC_OFF + ww * 2048 + tl * 1024 + l8 * 4);
      s1 += *(LAS f32x4*)(smem + PARTC_OFF + ww * 2048 + tl * 1024 + l8 * 4 + 16);
    }
    finalize(s0, s1, 2, chC, t8);
  }
  STAMP(1, 6); RSTAMP(1, 15);
}

constexpr int K2_HALF = 18432;
constexpr int K2_SLOT = 2 * K2_HALF;
constexpr int K2_NSLOT = 4;
constexpr int K2_WT_OFF = K2_NSLOT * K2_SLOT;
constexpr int K2_LDS = K2_WT_OFF + 9216;
constexpr int K2_OWN_OFF = 73728;

__global__ __launch_bounds__(512, 2) void k2_gemm2(const u16* __restrict__ hin, const u16* __restrict__ wd, float* __restrict__ slab,
                                                   unsigned* __restrict__ cnt, const u16* __restrict__ q, const float2* __restrict__ stats,
                                                   const u16* __restrict__ wT, u16* __restrict__ out) {
  extern __shared__ __attribute__((aligned(16))) char smem_g[];
  LAS char* smem = (LAS char*)smem_g;
  const unsigned lds0 = (unsigned)(uintptr_t)smem;
  const int bid = blockIdx.x, tid = threadIdx.x;
  const int w = __builtin_amdgcn_readfirstlane(tid >> 6), lane = tid & 63;
  const int xcd = bid & 7, idx = bid >> 3;
  const int ksl = xcd >> 1, mt = (xcd & 1) * 8 + (idx >> 2), nt = idx & 3;
  const int tq = w & 3, khf = w >> 2;
  const int kbase = ksl * 1152 + khf * 576;
  RSTAMP(2, 14); STAMP(2, 0);

  {
    const char* g = (const char*)(wT + (size_t)(nt * 144) * S_ + mt * 128 + ksl * 32);
    const unsigned v = (unsigned)((w * 16 + (lane >> 2)) * (S_ * 2) + (lane & 3) * 16);
    glds1(g, v, lds0 + K2_WT_OFF + w * 1024);
    if (w == 0) glds1(g, v + 128u * (S_ * 2), lds0 + K2_WT_OFF + 8192);
  }

  const char* wbase = (const char*)wd + (size_t)kbase * (D_ * 2) + nt * 288;
  unsigned voff[5];
#pragma unroll
  for (int n = 0; n < 5; ++n) {
    const int o = (n < 4) ? ((tq * 4 + n) * 1024 + lane * 16) : (16384 + tq * 512 + (lane & 31) * 16);
    const int k = o / 288, c = (o - k * 288) >> 4;
    voff[n] = (unsigned)(k * (D_ * 2) + c * 16 - (n < 4 ? n * 1024 : 0));
  }
  const unsigned ldst = lds0 + khf * K2_HALF + tq * 4096, ldst4 = lds0 + khf * K2_HALF + 16384 + tq * 512;
  const char* hbase = (const char*)hin + ((size_t)(mt * 8 + tq * 2) * 144 * 64 + (size_t)(kbase >> 5) * 64) * 16;
  const unsigned hv0 = lane * 16, hv1 = lane * 16 + 144 * 64 * 16;
  bf16x8 hf[K2_NSLOT][2][2];
  auto stage_load = [&](int st) ALWAYS_INLINE {
    const int slot = st % K2_NSLOT;
    const char* g = wbase + (size_t)st * 64 * (D_ * 2);
    glds4(g, voff[0], voff[1], voff[2], voff[3], ldst + slot * K2_SLOT);
    if (lane < 32) glds1(g, voff[4], ldst4 + slot * K2_SLOT);
#pragma unroll
    for (int s = 0; s < 2; ++s) {
      gload16(hf[slot][s][0], hbase + (2 * st + s) * 1024, hv0);
      gload16(hf[slot][s][1], hbase + (2 * st + s) * 1024, hv1);
    }
  };
  stage_load(0);
  stage_load(1);
  stage_load(2);

  const int kq = lane >> 4, qq = (lane & 15) >> 2, p4 = lane & 3;
  const unsigned rbase = lds0 + khf * K2_HALF + (16 * (kq >> 1) + 4 * (kq & 1) + qq) * 288 + p4 * 8;

  f32x4 acc[2][9];
#pragma unroll
  for (int t = 0; t < 2; ++t)
#pragma unroll
    for (int dt = 0; dt < 9; ++dt) acc[t][dt] = (f32x4){0.f, 0.f, 0.f, 0.f};

  asm volatile("s_waitcnt vmcnt(18)" : "+v"(hf[0][0][0]), "+v"(hf[0][0][1]), "+v"(hf[0][1][0]), "+v"(hf[0][1][1])::"memory");
  __builtin_amdgcn_s_barrier();
  CFENCE();
  STAMP(2, 1);

  constexpr int PD = 3;
  constexpr int MID = 6;
  bf16x8 af[PD + 1];
  unsigned sb = rbase;
#pragma unroll
  for (int g = 0; g < PD; ++g) af[g] = tr_frag((LAS char*)(uintptr_t)sb, g * 32, 8 * 288);
  sfor<162>([&](auto gc) ALWAYS_INLINE {
    constexpr int g = gc;
    constexpr int st = g / 18, f = g - st * 18, s = f / 9, dt = f - s * 9, slot = st % K2_NSLOT;
    if constexpr (f == MID && st < 8) {
      constexpr int sl1 = (st + 1) % K2_NSLOT;
      asm volatile("s_waitcnt vmcnt(%4)" : "+v"(hf[sl1][0][0]), "+v"(hf[sl1][0][1]), "+v"(hf[sl1][1][0]), "+v"(hf[sl1][1][1]) : "i"(st < 7 ? 9 : 0) : "memory");
      __builtin_amdgcn_s_barrier();
      CFENCE();
      if constexpr (st + 3 < 9) stage_load(st + 3);
    }
    if constexpr (g + PD < 162) {
      constexpr int g1 = g + PD, st1 = g1 / 18, f1 = g1 - st1 * 18, s1 = f1 / 9, dt1 = f1 - s1 * 9;
      if constexpr (f1 == 0) { unsigned b2 = rbase; asm volatile("" : "+v"(b2)); sb = b2 + (st1 % K2_NSLOT) * K2_SLOT; asm volatile("" : "+v"(sb)); }
      af[g1 % (PD + 1)] = tr_frag((LAS char*)(uintptr_t)sb, s1 * 32 * 288 + dt1 * 32, 8 * 288);
    }
    SCHED0();
#pragma unroll
    for (int t = 0; t < 2; ++t) acc[t][dt] = __builtin_amdgcn_mfma_f32_16x16x32_bf16(af[g % (PD + 1)], hf[slot][s][t], acc[t][dt], 0, 0, 0);
    SCHED0();
  });
  STAMP(2, 2);

  u32x2 xq[3];
  float2 stt[3];
#pragma unroll
  for (int i = 0; i < 3; ++i) {
    const int fi = (w + 8 * i < 18) ? w + 8 * i : w;
    const int t = fi >= 9 ? 1 : 0, dt = fi - 9 * t;
    const int token = mt * 128 + ksl * 32 + t * 16 + (lane & 15);
    xq[i] = *(const u32x2*)(q + (size_t)token * D_ + nt * 144 + dt * 16 + 4 * (lane >> 4));
    stt[i] = stats[token];
  }

  __syncthreads();
#pragma unroll
  for (int dt = 0; dt < 9; ++dt) *(LAS f32x4*)(smem + ((w * 9 + dt) * 64 + lane) * 16) = khf ? acc[0][dt] : acc[1][dt];
  __syncthreads();
  const int tile = mt * 4 + nt;
  const __amdgpu_buffer_rsrc_t rs = __builtin_amdgcn_make_buffer_rsrc((char*)slab + (size_t)tile * (16 * 18432), 0, 16 * 18432, 0x00020000);
  {
    f32x4 sm[9];
#pragma unroll
    for (int dt = 0; dt < 9; ++dt) sm[dt] = (khf ? acc[1][dt] : acc[0][dt]) + *(LAS f32x4*)(smem + (((w ^ 4) * 9 + dt) * 64 + lane) * 16);
    if (tq != ksl) {
#pragma unroll
      for (int dt = 0; dt < 9; ++dt)
        __builtin_amdgcn_raw_buffer_store_b128(__builtin_bit_cast(u32x4, sm[dt]), rs, (unsigned)(((tq * 4 + ksl) * 18 + khf * 9 + dt) * 1024 + lane * 16), 0, 16);
    } else {
#pragma unroll
      for (int dt = 0; dt < 9; ++dt) *(LAS f32x4*)(smem + K2_OWN_OFF + ((khf * 9 + dt) * 64 + lane) * 16) = sm[dt];
    }
  }
  asm volatile("s_waitcnt vmcnt(0)" ::: "memory");
  __syncthreads();
  STAMP(2, 3);
  if (tid == 0) {
    unsigned* c = cnt + tile * 64;
    __hip_atomic_fetch_add(c, 1u, __ATOMIC_RELAXED, __HIP_MEMORY_SCOPE_AGENT);
    for (unsigned spins = 0; spins < (1u << 22); ++spins) {
      if (__hip_atomic_load(c, __ATOMIC_RELAXED, __HIP_MEMORY_SCOPE_AGENT) >= 4u) break;
      __builtin_amdgcn_s_sleep(1);
    }
  }
  __syncthreads();
  STAMP(2, 4);
  __builtin_amdgcn_fence(__ATOMIC_ACQUIRE, "wavefront");
  {
    f32x4 pv[3][4];
#pragma unroll
    for (int i = 0; i < 3; ++i) {
      const int fi = (w + 8 * i < 18) ? w + 8 * i : w;
#pragma unroll
      for (int k = 1; k < 4; ++k)
        pv[i][k] = __builtin_bit_cast(f32x4, __builtin_amdgcn_raw_buffer_load_b128(rs, (unsigned)(((ksl * 4 + ((ksl + k) & 3)) * 18 + fi) * 1024 + lane * 16), 0, 16));
      pv[i][0] = *(LAS f32x4*)(smem + K2_OWN_OFF + (fi * 64 + lane) * 16);
    }
#pragma unroll
    for (int i = 0; i < 3; ++i) {
      if (w + 8 * i < 18) {
        const int fi = w + 8 * i;
        const int t = fi >= 9 ? 1 : 0, dt = fi - 9 * t;
        const int tl = t * 16 + (lane & 15), dl = dt * 16 + 4 * (lane >> 4);
        const f32x4 bs = ((pv[i][0] + pv[i][1]) + pv[i][2]) + pv[i][3];
        float o[4];
#pragma unroll
        for (int c = 0; c < 4; ++c) {
          const unsigned xw = (c < 2) ? xq[i].x : xq[i].y;
          const float x = (c & 1) ? bf_hi(xw) : bf_lo(xw);
          const float wy = bf2f(*(LAS u16*)(smem + K2_WT_OFF + (dl + c) * 64 + tl * 2));
          const float xn = bfr(bfr(x - stt[i].x) / stt[i].y);
          const float a = bfr(xn * wy);
          o[c] = bfr(x + a) + bfr(bs[c]);
        }
        u32x2 ov; ov.x = pk_bf16(o[0], o[1]); ov.y = pk_bf16(o[2], o[3]);
        *(u32x2*)(out + (size_t)(mt * 128 + ksl * 32 + tl) * D_ + nt * 144 + dl) = ov;
      }
    }
  }
  STAMP(2, 5); RSTAMP(2, 15);
}

extern "C" void kernel_launch(void* const* d_in, const int* in_sizes, int n_in,
                              void* d_out, int out_size, void* d_ws, size_t ws_size,
                              hipStream_t stream) {
  const u16* q = (const u16*)d_in[0];
  const u16* k = (const u16*)d_in[1];
  const u16* v = (const u16*)d_in[2];
  const u16* shk = (const u16*)d_in[3];
  const u16* ssmk = (const u16*)d_in[4];
  const u16* ssmd = (const u16*)d_in[5];
  const u16* wg = (const u16*)d_in[7];
  const u16* wu = (const u16*)d_in[8];
  const u16* wd = (const u16*)d_in[9];
  char* ws = (char*)d_ws;
  u16* xfrag = (u16*)(ws + WS_XFRAG);
  float2* stats = (float2*)(ws + WS_STATS);
  u16* kvr = (u16*)(ws + WS_KVR);
  u16* wT = (u16*)(ws + WS_WT);
  u16* h = (u16*)(ws + WS_H);
  float* slab = (float*)(ws + WS_SLAB);
  unsigned* cnt = (unsigned*)(ws + WS_CNT);

  (void)hipFuncSetAttribute((const void*)k1_gemm1, hipFuncAttributeMaxDynamicSharedMemorySize, K1_LDS);
  (void)hipFuncSetAttribute((const void*)k2_gemm2, hipFuncAttributeMaxDynamicSharedMemorySize, K2_LDS);

  k0_prep<<<128 + 288, 256, 0, stream>>>(q, k, v, shk, xfrag, stats, kvr, cnt);
  k1_gemm1<<<256, 512, K1_LDS, stream>>>(xfrag, wg, wu, h, ssmk, ssmd, kvr, wT);
  k2_gemm2<<<256, 512, K2_LDS, stream>>>(h, wd, slab, cnt, q, stats, wT, (u16*)d_out);
}
```

```cpp
#include <hip/hip_runtime.h>
#include <hip/hip_bf16.h>
#include <stdint.h>
#include <utility>

typedef unsigned short u16;
typedef short bf16x8 __attribute__((ext_vector_type(8)));
typedef short s16x4 __attribute__((ext_vector_type(4)));
typedef float f32x4 __attribute__((ext_vector_type(4)));
typedef float f32x16 __attribute__((ext_vector_type(16)));
typedef unsigned u32x4 __attribute__((ext_vector_type(4)));
typedef unsigned u32x2 __attribute__((ext_vector_type(2)));
typedef __bf16 bf2_t __attribute__((ext_vector_type(2)));
#define LAS __attribute__((address_space(3)))
#define DI __device__ __forceinline__

constexpr int S_ = 2048, D_ = 576, F_ = 4608;

constexpr size_t WS_XFRAG = 0;
constexpr size_t WS_STATS = WS_XFRAG + (size_t)S_ * D_ * 2;
constexpr size_t WS_KVR   = WS_STATS + (size_t)S_ * 8;
constexpr size_t WS_WT    = WS_KVR + (size_t)S_ * D_ * 2;
constexpr size_t WS_H     = WS_WT + (size_t)S_ * D_ * 2;
constexpr size_t WS_SLAB  = WS_H + (size_t)S_ * F_ * 2;
constexpr size_t WS_CNT   = WS_SLAB + (size_t)4 * S_ * D_ * 4;
constexpr size_t WS_END   = WS_CNT + 64 * 256;

DI float bf_lo(unsigned w) { return __uint_as_float(w << 16); }
DI float bf_hi(unsigned w) { return __uint_as_float(w & 0xffff0000u); }
DI float bf2f(u16 b) { return __uint_as_float(((unsigned)b) << 16); }
DI float bfr(float x) { return (float)(__bf16)x; }
DI unsigned pk_bf16(float lo, float hi) { bf2_t v = {(__bf16)lo, (__bf16)hi}; return __builtin_bit_cast(unsigned, v); }

#define WAIT_V(n) asm volatile("s_waitcnt vmcnt(" #n ")" ::: "memory")
#define WAIT_L(n) asm volatile("s_waitcnt lgkmcnt(" #n ")" ::: "memory")
#define CFENCE() asm volatile("" ::: "memory")
#define STAMP(kid, i)
#define RSTAMP(kid, i)
#define STAMP_DECL
#define STAMP_FLUSH(kid)
#define SCHED0() __builtin_amdgcn_sched_barrier(0)
template <int... I, class Fn> DI void sfor_impl(std::integer_sequence<int, I...>, Fn&& f) { (f(std::integral_constant<int, I>{}), ...); }
template <int N, class Fn> DI void sfor(Fn&& f) { sfor_impl(std::make_integer_sequence<int, N>{}, f); }
#define ALWAYS_INLINE __attribute__((always_inline))
template <int N> DI void wait_vm() { asm volatile("s_waitcnt vmcnt(%0)" ::"i"(N) : "memory"); }

DI void glds4(const void* sbase, unsigned v0, unsigned v1, unsigned v2, unsigned v3, unsigned lds_dst) {
  unsigned keep;
  asm volatile("s_mov_b32 %0, m0\n\ts_mov_b32 m0, %6\n\ts_nop 0\n\t"
               "global_load_lds_dwordx4 %1, %5\n\t"
               "global_load_lds_dwordx4 %2, %5 offset:1024\n\t"
               "global_load_lds_dwordx4 %3, %5 offset:2048\n\t"
               "global_load_lds_dwordx4 %4, %5 offset:3072\n\t"
               "s_mov_b32 m0, %0"
               : "=&s"(keep) : "v"(v0), "v"(v1), "v"(v2), "v"(v3), "s"(sbase), "s"(lds_dst) : "memory");
}
DI void glds3(const void* sbase, unsigned v0, unsigned v1, unsigned v2, unsigned lds_dst) {
  unsigned keep;
  asm volatile("s_mov_b32 %0, m0\n\ts_mov_b32 m0, %5\n\ts_nop 0\n\t"
               "global_load_lds_dwordx4 %1, %4\n\t"
               "global_load_lds_dwordx4 %2, %4 offset:1024\n\t"
               "global_load_lds_dwordx4 %3, %4 offset:2048\n\t"
               "s_mov_b32 m0, %0"
               : "=&s"(keep) : "v"(v0), "v"(v1), "v"(v2), "s"(sbase), "s"(lds_dst) : "memory");
}
DI void glds2(const void* sbase, unsigned v0, unsigned v1, unsigned lds_dst) {
  unsigned keep;
  asm volatile("s_mov_b32 %0, m0\n\ts_mov_b32 m0, %4\n\ts_nop 0\n\t"
               "global_load_lds_dwordx4 %1, %3\n\t"
               "global_load_lds_dwordx4 %2, %3 offset:1024\n\t"
               "s_mov_b32 m0, %0"
               : "=&s"(keep) : "v"(v0), "v"(v1), "s"(sbase), "s"(lds_dst) : "memory");
}
DI void glds1(const void* sbase, unsigned v0, unsigned lds_dst) {
  unsigned keep;
  asm volatile("s_mov_b32 %0, m0\n\ts_mov_b32 m0, %3\n\ts_nop 0\n\t"
               "global_load_lds_dwordx4 %1, %2\n\t"
               "s_mov_b32 m0, %0"
               : "=&s"(keep) : "v"(v0), "s"(sbase), "s"(lds_dst) : "memory");
}

template <int OFF> DI void gload16(bf16x8& dst, const void* sbase, unsigned voff) {
  asm volatile("global_load_dwordx4 %0, %1, %2 offset:%3" : "=v"(dst) : "v"(voff), "s"(sbase), "i"(OFF) : "memory");
}

__global__ __launch_bounds__(256) void k0_prep(const u16* __restrict__ q, const u16* __restrict__ kk, const u16* __restrict__ vv,
                                               const u16* __restrict__ shk, u16* __restrict__ xfrag, float2* __restrict__ stats,
                                               u16* __restrict__ kvr, unsigned* __restrict__ cnt) {
  __shared__ __attribute__((aligned(16))) u16 tile[64 * 72];
  const int bid = blockIdx.x, tid = threadIdx.x;
  if (bid == 0 && tid < 64) cnt[tid * 64] = 0u;
  if (bid < 128) {
    const int wave = tid >> 6, lane = tid & 63;
    const int token = bid * 16 + wave * 4 + (lane >> 4), j = lane & 15;
    u32x2 d[9];
#pragma unroll
    for (int i = 0; i < 9; ++i) d[i] = *(const u32x2*)(q + (size_t)token * D_ + (j + 16 * i) * 4);
    float s = 0.f;
#pragma unroll
    for (int i = 0; i < 9; ++i) s += (bf_lo(d[i].x) + bf_hi(d[i].x)) + (bf_lo(d[i].y) + bf_hi(d[i].y));
    s += __shfl_xor(s, 1); s += __shfl_xor(s, 2); s += __shfl_xor(s, 4); s += __shfl_xor(s, 8);
    const float mean = s * (1.0f / 576.0f);
    float vs = 0.f;
#pragma unroll
    for (int i = 0; i < 9; ++i) {
      float a = bf_lo(d[i].x) - mean, b = bf_hi(d[i].x) - mean, c = bf_lo(d[i].y) - mean, e = bf_hi(d[i].y) - mean;
      vs += (a * a + b * b) + (c * c + e * e);
    }
    vs += __shfl_xor(vs, 1); vs += __shfl_xor(vs, 2); vs += __shfl_xor(vs, 4); vs += __shfl_xor(vs, 8);
    const float mb = bfr(mean), vb = bfr(vs * (1.0f / 576.0f));
    const float sd = bfr(sqrtf(bfr(vb + 1e-5f)));
    if (j == 0) stats[token] = make_float2(mb, sd);
    const int tl = token >> 5, tr = token & 31;
#pragma unroll
    for (int i = 0; i < 9; ++i) {
      const int p = j + 16 * i;
      float a = bfr(bf_lo(d[i].x) - mb) / sd, b = bfr(bf_hi(d[i].x) - mb) / sd, c = bfr(bf_lo(d[i].y) - mb) / sd, e = bfr(bf_hi(d[i].y) - mb) / sd;
      u32x2 o; o.x = pk_bf16(a, b); o.y = pk_bf16(c, e);
      const int st = p >> 2, kh = (p >> 1) & 1, half = p & 1;
      *(u32x2*)((char*)xfrag + ((size_t)((tl * 36 + st) * 64 + kh * 32 + tr)) * 16 + half * 8) = o;
    }
  } else {
    const int kb = bid - 128, tt = kb / 9, ct = kb - tt * 9;
    const int t0 = tt * 64, c0 = ct * 64;
    const int tr = tid >> 3, cg = tid & 7;
    u32x4 w4[4];
#pragma unroll
    for (int i = 0; i < 4; ++i) w4[i] = *(const u32x4*)(shk + (size_t)(c0 + 8 * cg) * 4 + i * 8);
#pragma unroll
    for (int it = 0; it < 2; ++it) {
      const int tl = tr + 32 * it, t = t0 + tl;
      u32x4 kr[4];
#pragma unroll
      for (int i = 0; i < 4; ++i) {
        const int ts = t + i - 3;
        kr[i] = (u32x4){0u, 0u, 0u, 0u};
        if (ts >= 0) kr[i] = *(const u32x4*)(kk + (size_t)ts * D_ + c0 + 8 * cg);
      }
      const u32x4 vr = *(const u32x4*)(vv + (size_t)t * D_ + c0 + 8 * cg);
#pragma unroll
      for (int e = 0; e < 8; ++e) {
        const unsigned wa = w4[e >> 1][(e & 1) * 2], wb = w4[e >> 1][(e & 1) * 2 + 1];
        const float w0 = bf_lo(wa), w1 = bf_hi(wa), w2 = bf_lo(wb), w3 = bf_hi(wb);
        float kq[4];
#pragma unroll
        for (int i = 0; i < 4; ++i) kq[i] = (e & 1) ? bf_hi(kr[i][e >> 1]) : bf_lo(kr[i][e >> 1]);
        float ks = bfr(kq[0] * w3);
        ks = bfr(ks + bfr(kq[1] * w2));
        ks = bfr(ks + bfr(kq[2] * w1));
        ks = bfr(ks + bfr(kq[3] * w0));
        const float vf = (e & 1) ? bf_hi(vr[e >> 1]) : bf_lo(vr[e >> 1]);
        const float kv = ks * vf;
        tile[(8 * cg + e) * 72 + (63 - tl)] = (u16)(pk_bf16(kv, 0.f) & 0xffffu);
      }
    }
    __syncthreads();
    const int pbase = 1984 - t0;
#pragma unroll
    for (int it = 0; it < 2; ++it) {
      const int pc = tid + 256 * it, ch = pc >> 3, pp = pc & 7;
      const u32x4 o = *(const u32x4*)(tile + ch * 72 + pp * 8);
      *(u32x4*)(kvr + (size_t)(c0 + ch) * S_ + pbase + pp * 8) = o;
    }
  }
}

constexpr int K1_SLOT = 37120;
constexpr int K1_UOFF = 18432 + 128;
constexpr int K1_NSLOT = 4;
constexpr int K1_LDS = K1_NSLOT * K1_SLOT;
constexpr int XR_OFF = K1_SLOT, XR_STRIDE = 4608;
constexpr int HZ_OFF = K1_SLOT + 3 * XR_STRIDE;
constexpr int PARTC_OFF = K1_SLOT + 16384;
constexpr int PART_OFF = 2 * K1_SLOT;

DI bf16x8 tr_frag(LAS char* p, int off, int hi_off) {
  s16x4 lo = __builtin_amdgcn_ds_read_tr16_b64_v4i16((LAS s16x4*)(p + off));
  s16x4 hi = __builtin_amdgcn_ds_read_tr16_b64_v4i16((LAS s16x4*)(p + off + hi_off));
  return __builtin_shufflevector(lo, hi, 0, 1, 2, 3, 4, 5, 6, 7);
}

constexpr int K1_MID = 16;
constexpr int k1_xkey(int i) { return i < 9 ? -100 + i : (i < 18 ? 4 * (1 + 2 * (i - 9)) + 2 : (i < 27 ? 4 * (21 + 2 * (i - 18)) + 2 : 4 * (43 + 2 * (i - 27)) + 2)); }
constexpr int k1_dkey(int d) { return d == 0 ? -200 : d == 1 ? -50 : d == 2 ? 4 * 19 + 1 : d == 3 ? 4 * 40 + 1 : 4 * (36 * (d - 3) + K1_MID) + 1; }
constexpr int k1_younger(int okey, int wkey) {
  int n = 0;
  for (int i = 0; i < 36; ++i) if (k1_xkey(i) > okey && k1_xkey(i) < wkey) n += 1;
  for (int d = 0; d < 9; ++d) if (k1_dkey(d) > okey && k1_dkey(d) < wkey) n += 5;
  if (4 * 143 + 3 > okey && 4 * 143 + 3 < wkey) n += 4;
  if (4 * 287 + 3 > okey && 4 * 287 + 3 < wkey) n += 4;
  return n;
}

__global__ __launch_bounds__(512, 2) void k1_gemm1(const u16* __restrict__ xfrag, const u16* __restrict__ wg, const u16* __restrict__ wu,
                                                   u16* __restrict__ hout, const u16* __restrict__ ssmk, const u16* __restrict__ ssmd,
                                                   const u16* __restrict__ kvr, u16* __restrict__ wT) {
  extern __shared__ __attribute__((aligned(16))) char smem_g[];
  LAS char* smem = (LAS char*)smem_g;
  const unsigned lds0 = (unsigned)(uintptr_t)smem;
  const int bid = blockIdx.x, tid = threadIdx.x;
  const int w = __builtin_amdgcn_readfirstlane(tid >> 6), lane = tid & 63;
  const int xcd = bid & 7, idx = bid >> 3;
  const int mtile = (xcd & 1) * 4 + (idx & 3);
  const int cgrp = (xcd >> 1) * 2 + (idx >> 4), cq = (idx >> 2) & 3;
  const int colA = 64 * (9 * cgrp + 2 * cq), colQ = 64 * (9 * cgrp + 8) + 16 * cq;
  STAMP_DECL; RSTAMP(1, 14); STAMP(1, 0);

  const int wq = w & 3, part = w >> 2;
  const char* wbase = (const char*)(part ? wu : wg);
  unsigned voff[5];
#pragma unroll
  for (int n = 0; n < 5; ++n) {
    const int o = (n < 4) ? ((wq * 4 + n) * 1024 + lane * 16) : (16384 + wq * 512 + (lane & 31) * 16);
    const int k = o >> 7, pc = (o >> 4) & 7;
    const int j = (pc >> 1) ^ (((k >> 1) & 1) | (part << 1));
    voff[n] = (unsigned)(k * (F_ * 2) + (2 * j + (pc & 1)) * 16 - (n < 4 ? n * 1024 : 0));
  }
  const unsigned ldst = lds0 + part * K1_UOFF + wq * 4096;
  const unsigned ldst4 = lds0 + part * K1_UOFF + 16384 + wq * 512;

  auto stage_load = [&](int st) ALWAYS_INLINE {
    const int slot = st % K1_NSLOT;
    if (st < 8) {
      const char* g = wbase + (size_t)(st & 3) * 144 * (F_ * 2) + (colA + (st >> 2) * 64) * 2;
      glds4(g, voff[0], voff[1], voff[2], voff[3], ldst + slot * K1_SLOT);
      if (lane < 32) glds1(g, voff[4], ldst4 + slot * K1_SLOT);
    } else {
      unsigned vq[5];
#pragma unroll
      for (int n = 0; n < 5; ++n) {
        const int o = (n < 4) ? ((wq * 4 + n) * 1024 + lane * 16) : (16384 + wq * 512 + (lane & 31) * 16);
        vq[n] = (unsigned)((o >> 5) * (F_ * 2) + ((o >> 4) & 1) * 16 - (n < 4 ? n * 1024 : 0));
      }
      const char* g = wbase + colQ * 2;
      glds4(g, vq[0], vq[1], vq[2], vq[3], ldst + slot * K1_SLOT);
      if (lane < 32) glds1(g, vq[4], ldst4 + slot * K1_SLOT);
    }
  };

  bf16x8 xf[36];
  const char* xb = (const char*)xfrag + (size_t)(mtile * 8 + w) * 36 * 1024;
  const unsigned xv = lane * 16;
  auto x_load = [&](auto ic) ALWAYS_INLINE {
    constexpr int i = ic;
    gload16<(i & 3) * 1024>(xf[i], xb + (i >> 2) * 4096, xv);
  };
  stage_load(0);
  sfor<9>([&](auto ic) ALWAYS_INLINE { x_load(ic); });
  stage_load(1);

  const int kh = lane >> 5, prt = (lane >> 4) & 1, qq = (lane & 15) >> 2, p4 = lane & 3;
  const unsigned rbU = lds0 + (8 * kh + qq) * 128 + prt * K1_UOFF + ((((qq >> 1) & 1) | (prt << 1)) << 5) + p4 * 8;

  f32x16 acc[4];
#pragma unroll
  for (int c = 0; c < 4; ++c)
#pragma unroll
    for (int i = 0; i < 16; ++i) acc[c][i] = 0.f;

  const int token = mtile * 256 + w * 32 + (lane & 31);
  char* hb = (char*)hout + ((size_t)(token >> 4) * 144 * 64 + (lane >> 5) * 16 + (token & 15)) * 16;
  auto store_h = [&](const f32x16& a, int c16) ALWAYS_INLINE {
    float hv[8];
#pragma unroll
    for (int i = 0; i < 8; ++i) hv[i] = bfr(a[i]) * bfr(a[i + 8]);
    u32x4 o;
    o.x = pk_bf16(hv[0], hv[1]); o.y = pk_bf16(hv[2], hv[3]); o.z = pk_bf16(hv[4], hv[5]); o.w = pk_bf16(hv[6], hv[7]);
    *(u32x4*)(hb + ((size_t)(c16 >> 1) * 64 + (c16 & 1) * 32) * 16) = o;
  };

  const int grp = bid >> 2, qd = bid & 3;
  const int chA = grp * 9 + 2 * qd, chC = grp * 9 + 8;
  const int ct0 = lane & 15, ckg = lane >> 4;
  const char* hpA = (const char*)(ssmk + (size_t)(chA + (w >> 2)) * S_ + 64 * (w & 3));
  const char* hpC = (const char*)(ssmk + (size_t)chC * S_ + 32 * w);
  const bool zA = (w & 3) == 0, zC = w == 0;
  u32x4 caf[24];
  unsigned cax[24];
  int cvo = 0, cvx = 0;

  wait_vm<k1_younger(k1_dkey(0), 0)>();
  __builtin_amdgcn_s_barrier();
  CFENCE();
  STAMP(1, 1);

  constexpr int PD = 3, MID = K1_MID;
  bf16x8 af[PD + 1];
  unsigned rb[4];
#pragma unroll
  for (int j = 0; j < 4; ++j) rb[j] = rbU ^ (j << 5);
#pragma unroll
  for (int g = 0; g < PD; ++g) af[g] = tr_frag((LAS char*)(uintptr_t)rb[g], 0, 512);
  sfor<324>([&](auto gc) ALWAYS_INLINE {
    constexpr int g = gc;
    constexpr int st = g / 36, f = g - st * 36;
    constexpr int s = st < 8 ? f / 4 : f, c = st < 8 ? f % 4 : 0;
    constexpr int xi = st < 8 ? (st & 3) * 9 + s : s;
    if constexpr (f == MID && st < 8) {
      STAMP(3, 2 * st);
      wait_vm<k1_younger(k1_dkey(st + 1), 4 * g)>();
      __builtin_amdgcn_s_barrier();
      CFENCE();
      STAMP(3, 2 * st + 1);
      if constexpr (st == 6) {
        if (w < 3) {
          const int ch = (w == 2) ? chC : chA + w;
          const unsigned lv = lane * 16;
          glds4((const char*)kvr + (size_t)ch * (S_ * 2), lv, lv, lv, lv, lds0 + XR_OFF + w * XR_STRIDE);
          if (lane < 4) glds1((const char*)ssmk + (size_t)ch * (S_ * 2), lv, lds0 + HZ_OFF + w * 96 + 32);
        } else if (w == 3) {
          if (lane < 32) {
#pragma unroll
            for (int im = 0; im < 3; ++im) *(LAS u32x4*)(smem + XR_OFF + im * XR_STRIDE + 4096 + lane * 16) = (u32x4){0u, 0u, 0u, 0u};
          } else if (lane < 38) {
            const int im = (lane - 32) >> 1, hh = lane & 1;
            *(LAS u32x4*)(smem + HZ_OFF + im * 96 + hh * 16) = (u32x4){0u, 0u, 0u, 0u};
          }
        }
      }
    }
    sfor<7>([&](auto dc) ALWAYS_INLINE { if constexpr (k1_dkey(dc + 2) == 4 * g + 1) stage_load(dc + 2); });
    sfor<27>([&](auto ic) ALWAYS_INLINE { if constexpr (k1_xkey(ic + 9) == 4 * g + 2) x_load(std::integral_constant<int, ic + 9>{}); });
    if constexpr (g + PD < 324) {
      constexpr int g1 = g + PD, st1 = g1 / 36, f1 = g1 - st1 * 36;
      if constexpr (f1 == 0) {
        if constexpr (st1 < 8) {
          unsigned b2 = rbU; asm volatile("" : "+v"(b2));
#pragma unroll
          for (int j = 0; j < 4; ++j) { rb[j] = (b2 + (st1 % K1_NSLOT) * K1_SLOT) ^ (j << 5); asm volatile("" : "+v"(rb[j])); }
        } else {
          int l2 = lane; asm volatile("" : "+v"(l2));
          rb[0] = lds0 + (8 * (l2 >> 5) + ((l2 & 15) >> 2)) * 32 + ((l2 >> 4) & 1) * K1_UOFF + (l2 & 3) * 8 + (st1 % K1_NSLOT) * K1_SLOT;
          asm volatile("" : "+v"(rb[0]));
        }
      }
      if constexpr (st1 < 8) af[g1 % (PD + 1)] = tr_frag((LAS char*)(uintptr_t)rb[f1 % 4], (f1 / 4) * 2048, 512);
      else af[g1 % (PD + 1)] = tr_frag((LAS char*)(uintptr_t)rb[0], f1 * 512, 128);
    }
    if constexpr (st < 4 && c == 0) {
      asm volatile("s_waitcnt vmcnt(%1)" : "+v"(xf[xi]) : "i"(k1_younger(k1_xkey(xi), 4 * g + 3)) : "memory");
    }
    if constexpr (st == 8 && f >= 10 && f < 34) {
      constexpr int ci = f - 10;
      if constexpr (ci == 0) {
        int l2 = lane; asm volatile("" : "+v"(l2));
        const int e = (l2 & 15) - 15 + 8 * (l2 >> 4);
        cvo = (e & ~1) * 2; cvx = cvo + ((e & 1) ? 16 : 0);
      }
      constexpr int cb = ci < 16 ? 64 * (ci >> 3) + 512 * (ci & 7) : 512 * (ci - 16);
      const char* hp = (ci < 16 ? hpA : hpC) + cb + ((ci == 0 && zA) || (ci == 16 && zC) ? 32 : 0);
      __builtin_memcpy(&caf[ci], __builtin_assume_aligned(hp + cvo, 4), 16);
      cax[ci] = *(const unsigned*)(hp + cvx);
    }
    SCHED0();
    acc[c] = __builtin_amdgcn_mfma_f32_32x32x16_bf16(af[g % (PD + 1)], xf[xi], acc[c], 0, 0, 0);
    SCHED0();
    if constexpr ((st == 3 || st == 7) && f == 35) {
#pragma unroll
      for (int cc = 0; cc < 4; ++cc) {
        store_h(acc[cc], (colA >> 4) + (st >> 2) * 4 + cc);
#pragma unroll
        for (int i = 0; i < 16; ++i) acc[cc][i] = 0.f;
      }
      SCHED0();
    }
  });
  store_h(acc[0], colQ >> 4);

  STAMP(1, 2);
  __syncthreads();
  STAMP(1, 3);
  {
    const int boff = (2032 - 16 * (lane & 15) + 8 * ckg) * 2;
    auto head_frag = [&](int im) ALWAYS_INLINE -> bf16x8 {
      bf16x8 r;
      LAS u16* hz = (LAS u16*)(smem + HZ_OFF + im * 96) + 16 + (ct0 - 15 + 8 * ckg);
#pragma unroll
      for (int j = 0; j < 8; ++j) r[j] = (short)hz[j];
      return r;
    };
    bf16x8 cf[24];
    {
      const unsigned sh = ((ct0 + 1) & 1) * 2;
#pragma unroll
      for (int i = 0; i < 24; ++i) {
        u32x4 r;
        r.x = __builtin_amdgcn_alignbyte(caf[i].y, caf[i].x, sh);
        r.y = __builtin_amdgcn_alignbyte(caf[i].z, caf[i].y, sh);
        r.z = __builtin_amdgcn_alignbyte(caf[i].w, caf[i].z, sh);
        r.w = __builtin_amdgcn_alignbyte(cax[i], caf[i].w, sh);
        cf[i] = __builtin_bit_cast(bf16x8, r);
      }
    }
    if (zA) cf[0] = head_frag(w >> 2);
    if (zC) cf[16] = head_frag(2);
    {
      const int im = w >> 2;
      LAS char* xb = smem + XR_OFF + im * XR_STRIDE + boff + 128 * (w & 3);
      f32x4 ac[8];
#pragma unroll
      for (int j = 0; j < 8; ++j) ac[j] = (f32x4){0.f, 0.f, 0.f, 0.f};
#pragma unroll
      for (int rr = 1; rr >= 0; --rr) {
        bf16x8 bfm[8];
#pragma unroll
        for (int b = 0; b < 8; ++b) bfm[b] = *(LAS bf16x8*)(xb - 512 * b + 64 * rr);
#pragma unroll
        for (int a = 7; a >= 0; --a)
#pragma unroll
          for (int j = a; j < 8; ++j) ac[j] = __builtin_amdgcn_mfma_f32_16x16x32_bf16(cf[rr * 8 + a], bfm[j - a], ac[j], 0, 0, 0);
      }
#pragma unroll
      for (int j = 0; j < 8; ++j)
        *(LAS f32x4*)(smem + PART_OFF + w * 8192 + (256 * j + 16 * (lane & 15) + 4 * ckg) * 4) = ac[j];
    }
    {
      LAS char* xb = smem + XR_OFF + 2 * XR_STRIDE + boff + 64 * w;
      const int jA = 7 - qd, jB = qd;
      bf16x8 b0[8], b1[4];
#pragma unroll
      for (int a = 0; a < 8; ++a) { const int bb = jA - a; b0[a] = *(LAS bf16x8*)(xb - 512 * (bb < 0 ? 0 : bb)); }
#pragma unroll
      for (int a = 0; a < 4; ++a) { const int bb = jB - a; b1[a] = *(LAS bf16x8*)(xb - 512 * (bb < 0 ? 0 : bb)); }
      f32x4 c0 = (f32x4){0.f, 0.f, 0.f, 0.f}, c1 = c0;
#pragma unroll
      for (int a = 7; a >= 0; --a) {
        if (a <= jA) c0 = __builtin_amdgcn_mfma_f32_16x16x32_bf16(cf[16 + a], b0[a], c0, 0, 0, 0);
        if (a < 4 && a <= jB) c1 = __builtin_amdgcn_mfma_f32_16x16x32_bf16(cf[16 + a], b1[a < 4 ? a : 0], c1, 0, 0, 0);
      }
      *(LAS f32x4*)(smem + PARTC_OFF + w * 2048 + (16 * (lane & 15) + 4 * ckg) * 4) = c0;
      *(LAS f32x4*)(smem + PARTC_OFF + w * 2048 + 1024 + (16 * (lane & 15) + 4 * ckg) * 4) = c1;
    }
  }
  STAMP(1, 4);
  __syncthreads();
  STAMP(1, 5);
  auto finalize = [&](f32x4 s0, f32x4 s1, int im, int ch, int t8) ALWAYS_INLINE {
    const float dd = bf2f(ssmd[ch]);
    const u32x4 kvv = *(LAS u32x4*)(smem + XR_OFF + im * XR_STRIDE + (2040 - t8) * 2);
    float y[8] = {s0[0], s0[1], s0[2], s0[3], s1[0], s1[1], s1[2], s1[3]};
    float o[8];
#pragma unroll
    for (int e = 0; e < 8; ++e) {
      const int ri = 7 - e;
      const float kvf = (ri & 1) ? bf_hi(kvv[ri >> 1]) : bf_lo(kvv[ri >> 1]);
      o[e] = bfr(y[e]) + bfr(kvf * dd);
    }
    u32x4 ov; ov.x = pk_bf16(o[0], o[1]); ov.y = pk_bf16(o[2], o[3]); ov.z = pk_bf16(o[4], o[5]); ov.w = pk_bf16(o[6], o[7]);
    *(u32x4*)(wT + (size_t)ch * S_ + t8) = ov;
  };
  {
    const int im = tid >> 8, t8 = (tid & 255) * 8;
    f32x4 s0 = (f32x4){0.f, 0.f, 0.f, 0.f}, s1 = s0;
#pragma unroll
    for (int ww = 0; ww < 4; ++ww) {
      s0 += *(LAS f32x4*)(smem + PART_OFF + (im * 4 + ww) * 8192 + t8 * 4);
      s1 += *(LAS f32x4*)(smem + PART_OFF + (im * 4 + ww) * 8192 + t8 * 4 + 16);
    }
    finalize(s0, s1, im, chA + im, t8);
  }
  if (tid < 64) {
    const int tl = tid >> 5, l8 = (tid & 31) * 8;
    const int jt = tl ? qd : 7 - qd, t8 = 256 * jt + l8;
    f32x4 s0 = (f32x4){0.f, 0.f, 0.f, 0.f}, s1 = s0;
#pragma unroll
    for (int ww = 0; ww < 8; ++ww) {
      s0 += *(LAS f32x4*)(smem + PARTC_OFF + ww * 2048 + tl * 1024 + l8 * 4);
      s1 += *(LAS f32x4*)(smem + PARTC_OFF + ww * 2048 + tl * 1024 + l8 * 4 + 16);
    }
    finalize(s0, s1, 2, chC, t8);
  }
  STAMP(1, 6); RSTAMP(1, 15); STAMP_FLUSH(1); STAMP_FLUSH(3);
}

constexpr int K2_HALF = 12288;
constexpr int K2_SLOT = 2 * K2_HALF;
constexpr int K2_NSLOT = 4;
constexpr int K2_XB_OFF = K2_NSLOT * K2_SLOT;
constexpr int K2_OWN_OFF = K2_XB_OFF + 32768;
constexpr int K2_WT_OFF = K2_OWN_OFF + 18432;
constexpr int K2_LDS = K2_WT_OFF + 9216;
constexpr int K2_MID = 4;
constexpr int k2_g0(int st) { return st <= 12 ? 12 * st : 144 + 9 * (st - 12); }
constexpr int k2_stage(int g) { return g < 144 ? g / 12 : 12 + (g - 144) / 9; }
constexpr int k2_dkey(int d) { return d == 0 ? -300 : d == 1 ? -200 : d == 2 ? 4 * 1 + 2 : 4 * (k2_g0(d - 3) + K2_MID) + 2; }
constexpr int k2_hkey(int j) { return j == 0 ? -250 : j == 1 ? -150 : j == 2 ? 4 * 2 + 3 : 4 * (k2_g0(j - 3) + K2_MID) + 3; }
constexpr int k2_skey(int p) { return 4 * (k2_g0(6 * p + 6) + K2_MID) + 1; }
constexpr int k2_younger(int okey, int wkey, bool stores) {
  int n = 0;
  for (int d = 0; d < 14; ++d) if (k2_dkey(d) > okey && k2_dkey(d) < wkey) n += 3;
  for (int j = 0; j < 6; ++j) if (k2_hkey(j) > okey && k2_hkey(j) < wkey) n += 6;
  if (stores) for (int p = 0; p < 2; ++p) if (k2_skey(p) > okey && k2_skey(p) < wkey) n += 4;
  return n;
}
constexpr int cmax(int a, int b) { return a > b ? a : b; }

__global__ __launch_bounds__(512, 2) void k2_gemm2(const u16* __restrict__ hin, const u16* __restrict__ wd, float* __restrict__ slab,
                                                   unsigned* __restrict__ cnt, const u16* __restrict__ q, const float2* __restrict__ stats,
                                                   const u16* __restrict__ wT, u16* __restrict__ out) {
  extern __shared__ __attribute__((aligned(16))) char smem_g[];
  LAS char* smem = (LAS char*)smem_g;
  const unsigned lds0 = (unsigned)(uintptr_t)smem;
  const int bid = blockIdx.x, tid = threadIdx.x;
  const int w = __builtin_amdgcn_readfirstlane(tid >> 6), lane = tid & 63;
  const int xcd = bid & 7, idx = bid >> 3;
  const int ksl = xcd >> 1, mt = (xcd & 1) * 8 + (idx >> 2), nt = idx & 3;
  const int tq = w & 3, khf = w >> 2;
  const int kbase = ksl * 1152 + khf * 576;
  const int colA = 128 * nt, colQ = 512 + 16 * nt;
  STAMP_DECL; RSTAMP(2, 14); STAMP(2, 0);

  {
    const char* g = (const char*)(wT + mt * 128 + ksl * 32);
    const unsigned v = (unsigned)((colA + w * 16 + (lane >> 2)) * (S_ * 2) + (lane & 3) * 16);
    glds1(g, v, lds0 + K2_WT_OFF + w * 1024);
    if (w == 0) glds1(g, (unsigned)((colQ + (lane >> 2)) * (S_ * 2) + (lane & 3) * 16), lds0 + K2_WT_OFF + 8192);
  }

  const char* wbase = (const char*)wd + (size_t)kbase * (D_ * 2);
  unsigned voff[3];
#pragma unroll
  for (int n = 0; n < 3; ++n) {
    const int o = (tq * 3 + n) * 1024 + lane * 16;
    const int k = o >> 7, pc = (o >> 4) & 7;
    const int j = (pc >> 1) ^ ((k >> 1) & 3);
    voff[n] = (unsigned)(k * (D_ * 2) + (2 * j + (pc & 1)) * 16 - n * 1024);
  }
  const unsigned ldst = lds0 + khf * K2_HALF;
  auto stage_load = [&](int st) ALWAYS_INLINE {
    const int slot = st % K2_NSLOT;
    if (st < 12) {
      const int p = st / 6, ks = st - p * 6;
      const char* g = wbase + (size_t)ks * 96 * (D_ * 2) + (colA + p * 64) * 2;
      glds3(g, voff[0], voff[1], voff[2], ldst + slot * K2_SLOT + tq * 3072);
    } else {
      int l2 = lane; asm volatile("" : "+v"(l2));
      const char* g = wbase + (size_t)(st - 12) * 288 * (D_ * 2) + colQ * 2;
      const int o0 = tq * 2048 + l2 * 16, o2 = 8192 + tq * 256 + (l2 & 15) * 16;
      const unsigned v0 = (unsigned)((o0 >> 5) * (D_ * 2) + ((o0 >> 4) & 1) * 16);
      glds2(g, v0, v0 + 32u * (D_ * 2) - 1024u, ldst + slot * K2_SLOT + tq * 2048);
      if (l2 < 16) glds1(g, (unsigned)((o2 >> 5) * (D_ * 2) + ((o2 >> 4) & 1) * 16), ldst + slot * K2_SLOT + 8192 + tq * 256);
    }
  };
  const char* hbase = (const char*)hin + ((size_t)(mt * 8 + tq * 2) * 144 * 64 + (size_t)(kbase >> 5) * 64) * 16;
  const unsigned hv0 = lane * 16, hv1 = lane * 16 + 144 * 64 * 16;
  bf16x8 hf[18][2];
  auto h_load = [&](auto jc) ALWAYS_INLINE {
    constexpr int j = jc;
    const char* g = hbase + j * 3072;
    gload16<0>(hf[3 * j][0], g, hv0);        gload16<0>(hf[3 * j][1], g, hv1);
    gload16<1024>(hf[3 * j + 1][0], g, hv0); gload16<1024>(hf[3 * j + 1][1], g, hv1);
    gload16<2048>(hf[3 * j + 2][0], g, hv0); gload16<2048>(hf[3 * j + 2][1], g, hv1);
  };
  stage_load(0); h_load(std::integral_constant<int, 0>{});
  stage_load(1); h_load(std::integral_constant<int, 1>{});

  const int kq = lane >> 4, qq = (lane & 15) >> 2, p4 = lane & 3;
  const unsigned rbU = lds0 + khf * K2_HALF + (16 * (kq >> 1) + 4 * (kq & 1) + qq) * 128 + ((((qq >> 1) & 1) | ((kq & 1) << 1)) << 5) + p4 * 8;

  f32x4 acc[2][4], keep[4];
#pragma unroll
  for (int t = 0; t < 2; ++t)
#pragma unroll
    for (int dt = 0; dt < 4; ++dt) acc[t][dt] = (f32x4){0.f, 0.f, 0.f, 0.f};

  const int tile = mt * 4 + nt;
  const __amdgpu_buffer_rsrc_t rs = __builtin_amdgcn_make_buffer_rsrc((char*)slab + (size_t)tile * (16 * 18432), 0, 16 * 18432, 0x00020000);
  const bool foreign = tq != ksl;
  auto panel_end = [&](int nd) ALWAYS_INLINE {
#pragma unroll
    for (int dt = 0; dt < 4; ++dt) if (dt < nd) {
      *(LAS f32x4*)(smem + K2_XB_OFF + ((w * 4 + dt) * 64 + lane) * 16) = khf ? acc[0][dt] : acc[1][dt];
      keep[dt] = khf ? acc[1][dt] : acc[0][dt];
      acc[0][dt] = (f32x4){0.f, 0.f, 0.f, 0.f}; acc[1][dt] = (f32x4){0.f, 0.f, 0.f, 0.f};
    }
  };
  auto publish = [&](int p, int nd) ALWAYS_INLINE {
    f32x4 sm[4];
#pragma unroll
    for (int dt = 0; dt < 4; ++dt) if (dt < nd) sm[dt] = keep[dt] + *(LAS f32x4*)(smem + K2_XB_OFF + (((w ^ 4) * 4 + dt) * 64 + lane) * 16);
    if (foreign) {
#pragma unroll
      for (int dt = 0; dt < 4; ++dt) if (dt < nd)
        __builtin_amdgcn_raw_buffer_store_b128(__builtin_bit_cast(u32x4, sm[dt]), rs, (unsigned)(((tq * 4 + ksl) * 18 + khf * 9 + p * 4 + dt) * 1024 + lane * 16), 0, 16);
    } else {
#pragma unroll
      for (int dt = 0; dt < 4; ++dt) if (dt < nd) *(LAS f32x4*)(smem + K2_OWN_OFF + ((khf * 9 + p * 4 + dt) * 64 + lane) * 16) = sm[dt];
    }
  };

  asm volatile("s_waitcnt vmcnt(%6)" : "+v"(hf[0][0]), "+v"(hf[0][1]), "+v"(hf[1][0]), "+v"(hf[1][1]), "+v"(hf[2][0]), "+v"(hf[2][1])
               : "i"(k2_younger(k2_hkey(0), 0, false)) : "memory");
  __builtin_amdgcn_s_barrier();
  CFENCE();
  STAMP(2, 1);

  constexpr int PD = 3;
  bf16x8 af[PD + 1];
  unsigned rb[4];
#pragma unroll
  for (int j = 0; j < 4; ++j) rb[j] = rbU ^ (j << 5);
#pragma unroll
  for (int g = 0; g < PD; ++g) af[g] = tr_frag((LAS char*)(uintptr_t)rb[g], 0, 1024);
  sfor<162>([&](auto gc) ALWAYS_INLINE {
    constexpr int g = gc;
    constexpr int st = k2_stage(g), f = g - k2_g0(st);
    constexpr int ks = st < 12 ? (st % 6) * 3 + f / 4 : (st - 12) * 9 + f, dt = st < 12 ? f % 4 : 0;
    if constexpr (f == K2_MID && st < 13) {
      constexpr int okey = cmax(k2_dkey(st + 1), st + 1 < 6 ? k2_hkey(st + 1) : -1000);
      constexpr int n0 = k2_younger(okey, 4 * g, false), n1 = k2_younger(okey, 4 * g, true);
      if constexpr (st < 8) { STAMP(0, 2 * st); }
      if constexpr (n0 != n1) { if (foreign) wait_vm<n1>(); else wait_vm<n0>(); }
      else wait_vm<n0>();
      if constexpr (st + 1 < 6) {
        constexpr int k0 = 3 * (st + 1);
        asm volatile("" : "+v"(hf[k0][0]), "+v"(hf[k0][1]), "+v"(hf[k0 + 1][0]), "+v"(hf[k0 + 1][1]), "+v"(hf[k0 + 2][0]), "+v"(hf[k0 + 2][1])::"memory");
      }
      if constexpr (st == 6 || st == 12) WAIT_L(0);
      __builtin_amdgcn_s_barrier();
      CFENCE();
      if constexpr (st < 8) { STAMP(0, 2 * st + 1); }
      if constexpr (st == 6 || st == 12) publish(st / 6 - 1, 4);
      if constexpr (st + 3 < 14) stage_load(st + 3);
      if constexpr (st + 3 < 6) h_load(std::integral_constant<int, st + 3>{});
    }
    if constexpr (g == 1) stage_load(2);
    if constexpr (g == 2) h_load(std::integral_constant<int, 2>{});
    if constexpr (g + PD < 162) {
      constexpr int g1 = g + PD, st1 = k2_stage(g1), f1 = g1 - k2_g0(st1);
      if constexpr (f1 == 0) {
        if constexpr (st1 < 12) {
          unsigned b2 = rbU; asm volatile("" : "+v"(b2));
#pragma unroll
          for (int j = 0; j < 4; ++j) { rb[j] = (b2 + (st1 % K2_NSLOT) * K2_SLOT) ^ (j << 5); asm volatile("" : "+v"(rb[j])); }
        } else {
          int l2 = lane; asm volatile("" : "+v"(l2));
          const int kq2 = l2 >> 4;
          rb[0] = lds0 + khf * K2_HALF + (16 * (kq2 >> 1) + 4 * (kq2 & 1) + ((l2 & 15) >> 2)) * 32 + (l2 & 3) * 8 + (st1 % K2_NSLOT) * K2_SLOT;
          asm volatile("" : "+v"(rb[0]));
        }
      }
      if constexpr (st1 < 12) af[g1 % (PD + 1)] = tr_frag((LAS char*)(uintptr_t)rb[f1 % 4], (f1 / 4) * 4096, 1024);
      else af[g1 % (PD + 1)] = tr_frag((LAS char*)(uintptr_t)rb[0], f1 * 1024, 256);
    }
    SCHED0();
#pragma unroll
    for (int t = 0; t < 2; ++t) acc[t][dt] = __builtin_amdgcn_mfma_f32_16x16x32_bf16(af[g % (PD + 1)], hf[ks][t], acc[t][dt], 0, 0, 0);
    SCHED0();
    if constexpr (g == 71 || g == 143) { panel_end(4); SCHED0(); }
  });
  STAMP(2, 2);
  panel_end(1);

  u32x2 xq[3];
  float2 stt[3];
#pragma unroll
  for (int i = 0; i < 3; ++i) {
    const int fi = (w + 8 * i < 18) ? w + 8 * i : w;
    const int t = fi >= 9 ? 1 : 0, dt = fi - 9 * t;
    const int token = mt * 128 + ksl * 32 + t * 16 + (lane & 15);
    xq[i] = *(const u32x2*)(q + (size_t)token * D_ + (dt < 8 ? colA + dt * 16 : colQ) + 4 * (lane >> 4));
    stt[i] = stats[token];
  }
  WAIT_L(0);
  __builtin_amdgcn_s_barrier();
  CFENCE();
  publish(2, 1);
  asm volatile("s_waitcnt vmcnt(0)" ::: "memory");
  __syncthreads();
  STAMP(2, 3);
  if (tid == 0) {
    unsigned* c = cnt + tile * 64;
    __hip_atomic_fetch_add(c, 1u, __ATOMIC_RELAXED, __HIP_MEMORY_SCOPE_AGENT);
    for (unsigned spins = 0; spins < (1u << 22); ++spins) {
      if (__hip_atomic_load(c, __ATOMIC_RELAXED, __HIP_MEMORY_SCOPE_AGENT) >= 4u) break;
      __builtin_amdgcn_s_sleep(1);
    }
  }
  __syncthreads();
  STAMP(2, 4);
  __builtin_amdgcn_fence(__ATOMIC_ACQUIRE, "wavefront");
  {
    f32x4 pv[3][4];
#pragma unroll
    for (int i = 0; i < 3; ++i) {
      const int fi = (w + 8 * i < 18) ? w + 8 * i : w;
#pragma unroll
      for (int k = 1; k < 4; ++k)
        pv[i][k] = __builtin_bit_cast(f32x4, __builtin_amdgcn_raw_buffer_load_b128(rs, (unsigned)(((ksl * 4 + ((ksl + k) & 3)) * 18 + fi) * 1024 + lane * 16), 0, 16));
      pv[i][0] = *(LAS f32x4*)(smem + K2_OWN_OFF + (fi * 64 + lane) * 16);
    }
#pragma unroll
    for (int i = 0; i < 3; ++i) {
      if (w + 8 * i < 18) {
        const int fi = w + 8 * i;
        const int t = fi >= 9 ? 1 : 0, dt = fi - 9 * t;
        const int tl = t * 16 + (lane & 15), dl = dt * 16 + 4 * (lane >> 4);
        const f32x4 bs = ((pv[i][0] + pv[i][1]) + pv[i][2]) + pv[i][3];
        float o[4];
#pragma unroll
        for (int c = 0; c < 4; ++c) {
          const unsigned xw = (c < 2) ? xq[i].x : xq[i].y;
          const float x = (c & 1) ? bf_hi(xw) : bf_lo(xw);
          const float wy = bf2f(*(LAS u16*)(smem + K2_WT_OFF + (dl + c) * 64 + tl * 2));
          const float xn = bfr(bfr(x - stt[i].x) / stt[i].y);
          const float a = bfr(xn * wy);
          o[c] = bfr(x + a) + bfr(bs[c]);
        }
        u32x2 ov; ov.x = pk_bf16(o[0], o[1]); ov.y = pk_bf16(o[2], o[3]);
        *(u32x2*)(out + (size_t)(mt * 128 + ksl * 32 + tl) * D_ + (dt < 8 ? colA + dt * 16 : colQ) + 4 * (lane >> 4)) = ov;
      }
    }
  }
  STAMP(2, 5); RSTAMP(2, 15); STAMP_FLUSH(2); STAMP_FLUSH(0);
}

extern "C" void kernel_launch(void* const* d_in, const int* in_sizes, int n_in,
                              void* d_out, int out_size, void* d_ws, size_t ws_size,
                              hipStream_t stream) {
  const u16* q = (const u16*)d_in[0];
  const u16* k = (const u16*)d_in[1];
  const u16* v = (const u16*)d_in[2];
  const u16* shk = (const u16*)d_in[3];
  const u16* ssmk = (const u16*)d_in[4];
  const u16* ssmd = (const u16*)d_in[5];
  const u16* wg = (const u16*)d_in[7];
  const u16* wu = (const u16*)d_in[8];
  const u16* wd = (const u16*)d_in[9];
  char* ws = (char*)d_ws;
  u16* xfrag = (u16*)(ws + WS_XFRAG);
  float2* stats = (float2*)(ws + WS_STATS);
  u16* kvr = (u16*)(ws + WS_KVR);
  u16* wT = (u16*)(ws + WS_WT);
  u16* h = (u16*)(ws + WS_H);
  float* slab = (float*)(ws + WS_SLAB);
  unsigned* cnt = (unsigned*)(ws + WS_CNT);

  (void)hipFuncSetAttribute((const void*)k1_gemm1, hipFuncAttributeMaxDynamicSharedMemorySize, K1_LDS);
  (void)hipFuncSetAttribute((const void*)k2_gemm2, hipFuncAttributeMaxDynamicSharedMemorySize, K2_LDS);

  k0_prep<<<128 + 288, 256, 0, stream>>>(q, k, v, shk, xfrag, stats, kvr, cnt);
  k1_gemm1<<<256, 512, K1_LDS, stream>>>(xfrag, wg, wu, h, ssmk, ssmd, kvr, wT);
  k2_gemm2<<<256, 512, K2_LDS, stream>>>(h, wd, slab, cnt, q, stats, wT, (u16*)d_out);
}
```

```cpp
#include <hip/hip_runtime.h>
#include <hip/hip_bf16.h>
#include <stdint.h>
#include <utility>

typedef unsigned short u16;
typedef short bf16x8 __attribute__((ext_vector_type(8)));
typedef short s16x4 __attribute__((ext_vector_type(4)));
typedef float f32x4 __attribute__((ext_vector_type(4)));
typedef float f32x16 __attribute__((ext_vector_type(16)));
typedef unsigned u32x4 __attribute__((ext_vector_type(4)));
typedef unsigned u32x2 __attribute__((ext_vector_type(2)));
typedef __bf16 bf2_t __attribute__((ext_vector_type(2)));
#define LAS __attribute__((address_space(3)))
#define DI __device__ __forceinline__

constexpr int S_ = 2048, D_ = 576, F_ = 4608;

constexpr size_t WS_XFRAG = 0;
constexpr size_t WS_STATS = WS_XFRAG + (size_t)S_ * D_ * 2;
constexpr size_t WS_KVR   = WS_STATS + (size_t)S_ * 8;
constexpr size_t WS_WT    = WS_KVR + (size_t)S_ * D_ * 2;
constexpr size_t WS_H     = WS_WT + (size_t)S_ * D_ * 2;
constexpr size_t WS_SLAB  = WS_H + (size_t)S_ * F_ * 2;
constexpr size_t WS_CNT   = WS_SLAB + (size_t)4 * S_ * D_ * 4;
constexpr size_t WS_END   = WS_CNT + 64 * 256;

DI float bf_lo(unsigned w) { return __uint_as_float(w << 16); }
DI float bf_hi(unsigned w) { return __uint_as_float(w & 0xffff0000u); }
DI float bf2f(u16 b) { return __uint_as_float(((unsigned)b) << 16); }
DI float bfr(float x) { return (float)(__bf16)x; }
DI unsigned pk_bf16(float lo, float hi) { bf2_t v = {(__bf16)lo, (__bf16)hi}; return __builtin_bit_cast(unsigned, v); }

#define WAIT_V(n) asm volatile("s_waitcnt vmcnt(" #n ")" ::: "memory")
#define WAIT_L(n) asm volatile("s_waitcnt lgkmcnt(" #n ")" ::: "memory")
#define CFENCE() asm volatile("" ::: "memory")
#define STAMP(kid, i)
#define RSTAMP(kid, i)
#define STAMP_DECL
#define STAMP_FLUSH(kid)
#define K0STAMP(i)
#define SCHED0() __builtin_amdgcn_sched_barrier(0)
template <int... I, class Fn> DI void sfor_impl(std::integer_sequence<int, I...>, Fn&& f) { (f(std::integral_constant<int, I>{}), ...); }
template <int N, class Fn> DI void sfor(Fn&& f) { sfor_impl(std::make_integer_sequence<int, N>{}, f); }
#define ALWAYS_INLINE __attribute__((always_inline))
template <int N> DI void wait_vm() { asm volatile("s_waitcnt vmcnt(%0)" ::"i"(N) : "memory"); }

DI void glds4(const void* sbase, unsigned v0, unsigned v1, unsigned v2, unsigned v3, unsigned lds_dst) {
  unsigned keep;
  asm volatile("s_mov_b32 %0, m0\n\ts_mov_b32 m0, %6\n\ts_nop 0\n\t"
               "global_load_lds_dwordx4 %1, %5\n\t"
               "global_load_lds_dwordx4 %2, %5 offset:1024\n\t"
               "global_load_lds_dwordx4 %3, %5 offset:2048\n\t"
               "global_load_lds_dwordx4 %4, %5 offset:3072\n\t"
               "s_mov_b32 m0, %0"
               : "=&s"(keep) : "v"(v0), "v"(v1), "v"(v2), "v"(v3), "s"(sbase), "s"(lds_dst) : "memory");
}
DI void glds3(const void* sbase, unsigned v0, unsigned v1, unsigned v2, unsigned lds_dst) {
  unsigned keep;
  asm volatile("s_mov_b32 %0, m0\n\ts_mov_b32 m0, %5\n\ts_nop 0\n\t"
               "global_load_lds_dwordx4 %1, %4\n\t"
               "global_load_lds_dwordx4 %2, %4 offset:1024\n\t"
               "global_load_lds_dwordx4 %3, %4 offset:2048\n\t"
               "s_mov_b32 m0, %0"
               : "=&s"(keep) : "v"(v0), "v"(v1), "v"(v2), "s"(sbase), "s"(lds_dst) : "memory");
}
DI void glds2(const void* sbase, unsigned v0, unsigned v1, unsigned lds_dst) {
  unsigned keep;
  asm volatile("s_mov_b32 %0, m0\n\ts_mov_b32 m0, %4\n\ts_nop 0\n\t"
               "global_load_lds_dwordx4 %1, %3\n\t"
               "global_load_lds_dwordx4 %2, %3 offset:1024\n\t"
               "s_mov_b32 m0, %0"
               : "=&s"(keep) : "v"(v0), "v"(v1), "s"(sbase), "s"(lds_dst) : "memory");
}
DI void glds1(const void* sbase, unsigned v0, unsigned lds_dst) {
  unsigned keep;
  asm volatile("s_mov_b32 %0, m0\n\ts_mov_b32 m0, %3\n\ts_nop 0\n\t"
               "global_load_lds_dwordx4 %1, %2\n\t"
               "s_mov_b32 m0, %0"
               : "=&s"(keep) : "v"(v0), "s"(sbase), "s"(lds_dst) : "memory");
}

template <bool NT> DI void glds1p(const void* sbase, unsigned v0, unsigned lds_dst) {
  unsigned keep;
  if constexpr (NT)
    asm volatile("s_mov_b32 %0, m0\n\ts_mov_b32 m0, %3\n\ts_nop 0\n\tglobal_load_lds_dwordx4 %1, %2 nt\n\ts_mov_b32 m0, %0"
                 : "=&s"(keep) : "v"(v0), "s"(sbase), "s"(lds_dst) : "memory");
  else
    asm volatile("s_mov_b32 %0, m0\n\ts_mov_b32 m0, %3\n\ts_nop 0\n\tglobal_load_lds_dwordx4 %1, %2\n\ts_mov_b32 m0, %0"
                 : "=&s"(keep) : "v"(v0), "s"(sbase), "s"(lds_dst) : "memory");
}
DI void touch_lines(const void* sbase, unsigned v0, unsigned lds_dst) {
  unsigned keep;
  asm volatile("s_mov_b32 %0, m0\n\ts_mov_b32 m0, %3\n\ts_nop 0\n\tglobal_load_lds_dword %1, %2\n\ts_mov_b32 m0, %0"
               : "=&s"(keep) : "v"(v0), "s"(sbase), "s"(lds_dst) : "memory");
}
template <int OFF> DI void gload16(bf16x8& dst, const void* sbase, unsigned voff) {
  asm volatile("global_load_dwordx4 %0, %1, %2 offset:%3" : "=v"(dst) : "v"(voff), "s"(sbase), "i"(OFF) : "memory");
}

__global__ __launch_bounds__(256) void k0_prep(const u16* __restrict__ q, const u16* __restrict__ kk, const u16* __restrict__ vv,
                                               const u16* __restrict__ shk, u16* __restrict__ xfrag, float2* __restrict__ stats,
                                               u16* __restrict__ kvr, unsigned* __restrict__ cnt) {
  __shared__ __attribute__((aligned(16))) u16 tile[64 * 72];
  const int bid = blockIdx.x, tid = threadIdx.x;
  K0STAMP(0);
  if (bid == 0 && tid < 64) cnt[tid * 64] = 0u;
  if (bid < 128) {
    const int wave = tid >> 6, lane = tid & 63;
    const int token = bid * 16 + wave * 4 + (lane >> 4), j = lane & 15;
    u32x2 d[9];
#pragma unroll
    for (int i = 0; i < 9; ++i) d[i] = *(const u32x2*)(q + (size_t)token * D_ + (j + 16 * i) * 4);
    float s = 0.f;
#pragma unroll
    for (int i = 0; i < 9; ++i) s += (bf_lo(d[i].x) + bf_hi(d[i].x)) + (bf_lo(d[i].y) + bf_hi(d[i].y));
    s += __shfl_xor(s, 1); s += __shfl_xor(s, 2); s += __shfl_xor(s, 4); s += __shfl_xor(s, 8);
    const float mean = s * (1.0f / 576.0f);
    float vs = 0.f;
#pragma unroll
    for (int i = 0; i < 9; ++i) {
      float a = bf_lo(d[i].x) - mean, b = bf_hi(d[i].x) - mean, c = bf_lo(d[i].y) - mean, e = bf_hi(d[i].y) - mean;
      vs += (a * a + b * b) + (c * c + e * e);
    }
    vs += __shfl_xor(vs, 1); vs += __shfl_xor(vs, 2); vs += __shfl_xor(vs, 4); vs += __shfl_xor(vs, 8);
    const float mb = bfr(mean), vb = bfr(vs * (1.0f / 576.0f));
    const float sd = bfr(sqrtf(bfr(vb + 1e-5f)));
    if (j == 0) stats[token] = make_float2(mb, sd);
    const int tl = token >> 5, tr = token & 31;
#pragma unroll
    for (int i = 0; i < 9; ++i) {
      const int p = j + 16 * i;
      float a = bfr(bf_lo(d[i].x) - mb) / sd, b = bfr(bf_hi(d[i].x) - mb) / sd, c = bfr(bf_lo(d[i].y) - mb) / sd, e = bfr(bf_hi(d[i].y) - mb) / sd;
      u32x2 o; o.x = pk_bf16(a, b); o.y = pk_bf16(c, e);
      const int st = p >> 2, kh = (p >> 1) & 1, half = p & 1;
      *(u32x2*)((char*)xfrag + ((size_t)((tl * 36 + st) * 64 + kh * 32 + tr)) * 16 + half * 8) = o;
    }
  } else {
    const int kb = bid - 128, tt = kb / 9, ct = kb - tt * 9;
    const int t0 = tt * 64, c0 = ct * 64;
    const int tr = tid >> 3, cg = tid & 7;
    u32x4 w4[4];
#pragma unroll
    for (int i = 0; i < 4; ++i) w4[i] = *(const u32x4*)(shk + (size_t)(c0 + 8 * cg) * 4 + i * 8);
#pragma unroll
    for (int it = 0; it < 2; ++it) {
      const int tl = tr + 32 * it, t = t0 + tl;
      u32x4 kr[4];
#pragma unroll
      for (int i = 0; i < 4; ++i) {
        const int ts = t + i - 3;
        kr[i] = (u32x4){0u, 0u, 0u, 0u};
        if (ts >= 0) kr[i] = *(const u32x4*)(kk + (size_t)ts * D_ + c0 + 8 * cg);
      }
      const u32x4 vr = *(const u32x4*)(vv + (size_t)t * D_ + c0 + 8 * cg);
#pragma unroll
      for (int e = 0; e < 8; ++e) {
        const unsigned wa = w4[e >> 1][(e & 1) * 2], wb = w4[e >> 1][(e & 1) * 2 + 1];
        const float w0 = bf_lo(wa), w1 = bf_hi(wa), w2 = bf_lo(wb), w3 = bf_hi(wb);
        float kq[4];
#pragma unroll
        for (int i = 0; i < 4; ++i) kq[i] = (e & 1) ? bf_hi(kr[i][e >> 1]) : bf_lo(kr[i][e >> 1]);
        float ks = bfr(kq[0] * w3);
        ks = bfr(ks + bfr(kq[1] * w2));
        ks = bfr(ks + bfr(kq[2] * w1));
        ks = bfr(ks + bfr(kq[3] * w0));
        const float vf = (e & 1) ? bf_hi(vr[e >> 1]) : bf_lo(vr[e >> 1]);
        const float kv = ks * vf;
        tile[(8 * cg + e) * 72 + (63 - tl)] = (u16)(pk_bf16(kv, 0.f) & 0xffffu);
      }
    }
    __syncthreads();
    const int pbase = 1984 - t0;
#pragma unroll
    for (int it = 0; it < 2; ++it) {
      const int pc = tid + 256 * it, ch = pc >> 3, pp = pc & 7;
      const u32x4 o = *(const u32x4*)(tile + ch * 72 + pp * 8);
      *(u32x4*)(kvr + (size_t)(c0 + ch) * S_ + pbase + pp * 8) = o;
    }
  }
  K0STAMP(1);
}

constexpr int K1_SLOT = 37120;
constexpr int K1_UOFF = 18432 + 128;
constexpr int K1_NSLOT = 4;
constexpr int K1_PF_OFF = K1_NSLOT * K1_SLOT;
constexpr int K1_LDS = K1_PF_OFF + 256;
constexpr int XR_OFF = K1_SLOT, XR_STRIDE = 4608;
constexpr int HZ_OFF = K1_SLOT + 3 * XR_STRIDE;
constexpr int PARTC_OFF = K1_SLOT + 16384;
constexpr int PART_OFF = 2 * K1_SLOT;

DI bf16x8 tr_frag(LAS char* p, int off, int hi_off) {
  s16x4 lo = __builtin_amdgcn_ds_read_tr16_b64_v4i16((LAS s16x4*)(p + off));
  s16x4 hi = __builtin_amdgcn_ds_read_tr16_b64_v4i16((LAS s16x4*)(p + off + hi_off));
  return __builtin_shufflevector(lo, hi, 0, 1, 2, 3, 4, 5, 6, 7);
}

constexpr int K1_MID = 16;
constexpr int k1_xkey(int i) { return i < 9 ? -100 + i : (i < 18 ? 4 * (1 + 2 * (i - 9)) + 2 : (i < 27 ? 4 * (21 + 2 * (i - 18)) + 2 : 4 * (43 + 2 * (i - 27)) + 2)); }
constexpr int k1_dstep(int d) { return d == 2 ? 18 : d == 3 ? 40 : 36 * (d - 3) + K1_MID; }
constexpr int k1_dkey(int d, int n) { return d == 0 ? -200 + n : d == 1 ? -50 + n : 4 * (k1_dstep(d) + 2 * n) + 1; }
constexpr int K1_NPF = 1;
constexpr int k1_pkey(int i) { return 4 * (150 + 12 * i) + 2; }
constexpr int k1_younger(int okey, int wkey) {
  int n = 0;
  for (int i = 0; i < K1_NPF; ++i) if (k1_pkey(i) > okey && k1_pkey(i) < wkey) n += 1;
  for (int i = 0; i < 36; ++i) if (k1_xkey(i) > okey && k1_xkey(i) < wkey) n += 1;
  for (int d = 0; d < 9; ++d) for (int pc = 0; pc < 5; ++pc) if (k1_dkey(d, pc) > okey && k1_dkey(d, pc) < wkey) n += 1;
  if (4 * 143 + 3 > okey && 4 * 143 + 3 < wkey) n += 4;
  if (4 * 287 + 3 > okey && 4 * 287 + 3 < wkey) n += 4;
  return n;
}
#define HSTORE 1
#define K1_NT false

__global__ __launch_bounds__(512, 2) void k1_gemm1(const u16* __restrict__ xfrag, const u16* __restrict__ wg, const u16* __restrict__ wu,
                                                   u16* __restrict__ hout, const u16* __restrict__ ssmk, const u16* __restrict__ ssmd,
                                                   const u16* __restrict__ kvr, u16* __restrict__ wT, const u16* __restrict__ wd) {
  extern __shared__ __attribute__((aligned(16))) char smem_g[];
  LAS char* smem = (LAS char*)smem_g;
  const unsigned lds0 = (unsigned)(uintptr_t)smem;
  const int bid = blockIdx.x, tid = threadIdx.x;
  const int w = __builtin_amdgcn_readfirstlane(tid >> 6), lane = tid & 63;
  const int xcd = bid & 7, idx = bid >> 3;
  const int mtile = (xcd & 1) * 4 + (idx & 3);
  const int cgrp = (xcd >> 1) * 2 + (idx >> 4), cq = (idx >> 2) & 3;
  const int colA = 64 * (9 * cgrp + 2 * cq), colQ = 64 * (9 * cgrp + 8) + 16 * cq;
  STAMP_DECL; RSTAMP(1, 14); STAMP(1, 0);

  const int wq = w & 3, part = w >> 2;
  const char* wbase = (const char*)(part ? wu : wg);
  unsigned voff[5];
#pragma unroll
  for (int n = 0; n < 5; ++n) {
    const int o = (n < 4) ? ((wq * 4 + n) * 1024 + lane * 16) : (16384 + wq * 512 + (lane & 31) * 16);
    const int k = o >> 7, pc = (o >> 4) & 7;
    const int j = (pc >> 1) ^ (((k >> 1) & 1) | (part << 1));
    voff[n] = (unsigned)(k * (F_ * 2) + (2 * j + (pc & 1)) * 16);
  }
  const unsigned ldst = lds0 + part * K1_UOFF + wq * 4096;
  const unsigned ldst4 = lds0 + part * K1_UOFF + 16384 + wq * 512;

  auto piece_load = [&](int st, int n) ALWAYS_INLINE {
    const int slot = st % K1_NSLOT;
    const unsigned dst = (n < 4 ? ldst + n * 1024 : ldst4) + slot * K1_SLOT;
    if (st < 8) {
      const char* g = wbase + (size_t)(st & 3) * 144 * (F_ * 2) + (colA + (st >> 2) * 64) * 2;
      if (n < 4 || lane < 32) glds1p<K1_NT>(g, voff[n], dst);
    } else {
      int l2 = lane; asm volatile("" : "+v"(l2));
      const int o = (n < 4) ? ((wq * 4 + n) * 1024 + l2 * 16) : (16384 + wq * 512 + (l2 & 31) * 16);
      const unsigned vq = (unsigned)((o >> 5) * (F_ * 2) + ((o >> 4) & 1) * 16);
      if (n < 4 || lane < 32) glds1p<K1_NT>(wbase + colQ * 2, vq, dst);
    }
  };
  auto stage_load = [&](int st) ALWAYS_INLINE {
#pragma unroll
    for (int n = 0; n < 5; ++n) piece_load(st, n);
  };

  bf16x8 xf[36];
  const char* xb = (const char*)xfrag + (size_t)(mtile * 8 + w) * 36 * 1024;
  const unsigned xv = lane * 16;
  auto x_load = [&](auto ic) ALWAYS_INLINE {
    constexpr int i = ic;
    gload16<(i & 3) * 1024>(xf[i], xb + (i >> 2) * 4096, xv);
  };
  stage_load(0);
  sfor<9>([&](auto ic) ALWAYS_INLINE { x_load(ic); });
  stage_load(1);

  const int kh = lane >> 5, prt = (lane >> 4) & 1, qq = (lane & 15) >> 2, p4 = lane & 3;
  const unsigned rbU = lds0 + (8 * kh + qq) * 128 + prt * K1_UOFF + ((((qq >> 1) & 1) | (prt << 1)) << 5) + p4 * 8;

  f32x16 acc[4];
#pragma unroll
  for (int c = 0; c < 4; ++c)
#pragma unroll
    for (int i = 0; i < 16; ++i) acc[c][i] = 0.f;

  const int token = mtile * 256 + w * 32 + (lane & 31);
  char* hb = (char*)hout + ((size_t)(token >> 4) * 144 * 64 + (lane >> 5) * 16 + (token & 15)) * 16;
  auto store_h = [&](const f32x16& a, int c16) ALWAYS_INLINE {
    float hv[8];
#pragma unroll
    for (int i = 0; i < 8; ++i) hv[i] = bfr(a[i]) * bfr(a[i + 8]);
    u32x4 o;
    o.x = pk_bf16(hv[0], hv[1]); o.y = pk_bf16(hv[2], hv[3]); o.z = pk_bf16(hv[4], hv[5]); o.w = pk_bf16(hv[6], hv[7]);
    asm volatile("global_store_dwordx4 %0, %1, off sc1\n\ts_nop 1" ::"v"(hb + ((size_t)(c16 >> 1) * 64 + (c16 & 1) * 32) * 16), "v"(o) : "memory");
  };

  const int grp = bid >> 2, qd = bid & 3;
  const int chA = grp * 9 + 2 * qd, chC = grp * 9 + 8;
  const int ct0 = lane & 15, ckg = lane >> 4;
  const char* hpA = (const char*)(ssmk + (size_t)(chA + (w >> 2)) * S_ + 64 * (w & 3));
  const char* hpC = (const char*)(ssmk + (size_t)chC * S_ + 32 * w);
  const bool zA = (w & 3) == 0, zC = w == 0;
  const char* pf_wd = (const char*)wd + (size_t)(xcd >> 1) * 1152 * 1152;
  const char* pf_sk = (const char*)ssmk;
  u32x4 caf[24];
  unsigned cax[24];
  int cvo = 0, cvx = 0;

  wait_vm<k1_younger(k1_dkey(0, 4), 0)>();
  __builtin_amdgcn_s_barrier();
  CFENCE();
  STAMP(1, 1);

  constexpr int PD = 3, MID = K1_MID;
  bf16x8 af[PD + 1];
  unsigned rb[4];
#pragma unroll
  for (int j = 0; j < 4; ++j) rb[j] = rbU ^ (j << 5);
#pragma unroll
  for (int g = 0; g < PD; ++g) af[g] = tr_frag((LAS char*)(uintptr_t)rb[g], 0, 512);
  sfor<324>([&](auto gc) ALWAYS_INLINE {
    constexpr int g = gc;
    constexpr int st = g / 36, f = g - st * 36;
    constexpr int s = st < 8 ? f / 4 : f, c = st < 8 ? f % 4 : 0;
    constexpr int xi = st < 8 ? (st & 3) * 9 + s : s;
    if constexpr (f == MID && st < 8) {
      STAMP(3, 2 * st);
      wait_vm<k1_younger(k1_dkey(st + 1, 4), 4 * g)>();
      __builtin_amdgcn_s_barrier();
      CFENCE();
      STAMP(3, 2 * st + 1);
      if constexpr (st == 6) {
        if (w < 3) {
          const int ch = (w == 2) ? chC : chA + w;
          const unsigned lv = lane * 16;
          glds4((const char*)kvr + (size_t)ch * (S_ * 2), lv, lv, lv, lv, lds0 + XR_OFF + w * XR_STRIDE);
          if (lane < 4) glds1((const char*)ssmk + (size_t)ch * (S_ * 2), lv, lds0 + HZ_OFF + w * 96 + 32);
        } else if (w == 3) {
          if (lane < 32) {
#pragma unroll
            for (int im = 0; im < 3; ++im) *(LAS u32x4*)(smem + XR_OFF + im * XR_STRIDE + 4096 + lane * 16) = (u32x4){0u, 0u, 0u, 0u};
          } else if (lane < 38) {
            const int im = (lane - 32) >> 1, hh = lane & 1;
            *(LAS u32x4*)(smem + HZ_OFF + im * 96 + hh * 16) = (u32x4){0u, 0u, 0u, 0u};
          }
        }
      }
    }
    sfor<35>([&](auto dc) ALWAYS_INLINE { if constexpr (k1_dkey(dc / 5 + 2, dc % 5) == 4 * g + 1) piece_load(dc / 5 + 2, dc % 5); });
    sfor<27>([&](auto ic) ALWAYS_INLINE { if constexpr (k1_xkey(ic + 9) == 4 * g + 2) x_load(std::integral_constant<int, ic + 9>{}); });
    if constexpr (k1_pkey(0) == 4 * g + 2) {
      int l2 = lane; asm volatile("" : "+v"(l2));
      if (w < 6) {
        const unsigned line = (unsigned)(idx * 324 + w * 64 + l2 < (idx + 1) * 324 ? idx * 324 + w * 64 + l2 : idx * 324);
        touch_lines(pf_wd, line * 128u, lds0 + K1_PF_OFF);
      } else {
        const int li = (w - 6) * 64 + l2;
        const int ch = li < 32 ? chA : (li < 64 ? chA + 1 : chC);
        touch_lines(pf_sk, (unsigned)(ch * (S_ * 2) + (li & 31) * 128), lds0 + K1_PF_OFF);
      }
    }
    if constexpr (g + PD < 324) {
      constexpr int g1 = g + PD, st1 = g1 / 36, f1 = g1 - st1 * 36;
      if constexpr (f1 == 0) {
        if constexpr (st1 < 8) {
          unsigned b2 = rbU; asm volatile("" : "+v"(b2));
#pragma unroll
          for (int j = 0; j < 4; ++j) { rb[j] = (b2 + (st1 % K1_NSLOT) * K1_SLOT) ^ (j << 5); asm volatile("" : "+v"(rb[j])); }
        } else {
          int l2 = lane; asm volatile("" : "+v"(l2));
          rb[0] = lds0 + (8 * (l2 >> 5) + ((l2 & 15) >> 2)) * 32 + ((l2 >> 4) & 1) * K1_UOFF + (l2 & 3) * 8 + (st1 % K1_NSLOT) * K1_SLOT;
          asm volatile("" : "+v"(rb[0]));
        }
      }
      if constexpr (st1 < 8) af[g1 % (PD + 1)] = tr_frag((LAS char*)(uintptr_t)rb[f1 % 4], (f1 / 4) * 2048, 512);
      else af[g1 % (PD + 1)] = tr_frag((LAS char*)(uintptr_t)rb[0], f1 * 512, 128);
    }
    if constexpr (st < 4 && c == 0) {
      asm volatile("s_waitcnt vmcnt(%1)" : "+v"(xf[xi]) : "i"(k1_younger(k1_xkey(xi), 4 * g + 3)) : "memory");
    }
    if constexpr (st == 8 && f >= 10 && f < 34) {
      constexpr int ci = f - 10;
      if constexpr (ci == 0) {
        int l2 = lane; asm volatile("" : "+v"(l2));
        const int e = (l2 & 15) - 15 + 8 * (l2 >> 4);
        cvo = (e & ~1) * 2; cvx = cvo + ((e & 1) ? 16 : 0);
      }
      constexpr int cb = ci < 16 ? 64 * (ci >> 3) + 512 * (ci & 7) : 512 * (ci - 16);
      const char* hp = (ci < 16 ? hpA : hpC) + cb + ((ci == 0 && zA) || (ci == 16 && zC) ? 32 : 0);
      __builtin_memcpy(&caf[ci], __builtin_assume_aligned(hp + cvo, 4), 16);
      cax[ci] = *(const unsigned*)(hp + cvx);
    }
    SCHED0();
    acc[c] = __builtin_amdgcn_mfma_f32_32x32x16_bf16(af[g % (PD + 1)], xf[xi], acc[c], 0, 0, 0);
    SCHED0();
    if constexpr ((st == 3 || st == 7) && f == 35) {
#pragma unroll
      for (int cc = 0; cc < 4; ++cc) {
        store_h(acc[cc], (colA >> 4) + (st >> 2) * 4 + cc);
#pragma unroll
        for (int i = 0; i < 16; ++i) acc[cc][i] = 0.f;
      }
      SCHED0();
    }
  });
  store_h(acc[0], colQ >> 4);

  STAMP(1, 2);
  __syncthreads();
  STAMP(1, 3);
  {
    const int boff = (2032 - 16 * (lane & 15) + 8 * ckg) * 2;
    auto head_frag = [&](int im) ALWAYS_INLINE -> bf16x8 {
      bf16x8 r;
      LAS u16* hz = (LAS u16*)(smem + HZ_OFF + im * 96) + 16 + (ct0 - 15 + 8 * ckg);
#pragma unroll
      for (int j = 0; j < 8; ++j) r[j] = (short)hz[j];
      return r;
    };
    bf16x8 cf[24];
    {
      const unsigned sh = ((ct0 + 1) & 1) * 2;
#pragma unroll
      for (int i = 0; i < 24; ++i) {
        u32x4 r;
        r.x = __builtin_amdgcn_alignbyte(caf[i].y, caf[i].x, sh);
        r.y = __builtin_amdgcn_alignbyte(caf[i].z, caf[i].y, sh);
        r.z = __builtin_amdgcn_alignbyte(caf[i].w, caf[i].z, sh);
        r.w = __builtin_amdgcn_alignbyte(cax[i], caf[i].w, sh);
        cf[i] = __builtin_bit_cast(bf16x8, r);
      }
    }
    if (zA) cf[0] = head_frag(w >> 2);
    if (zC) cf[16] = head_frag(2);
    {
      const int im = w >> 2;
      LAS char* xb = smem + XR_OFF + im * XR_STRIDE + boff + 128 * (w & 3);
      f32x4 ac[8];
#pragma unroll
      for (int j = 0; j < 8; ++j) ac[j] = (f32x4){0.f, 0.f, 0.f, 0.f};
#pragma unroll
      for (int rr = 1; rr >= 0; --rr) {
        bf16x8 bfm[8];
#pragma unroll
        for (int b = 0; b < 8; ++b) bfm[b] = *(LAS bf16x8*)(xb - 512 * b + 64 * rr);
#pragma unroll
        for (int a = 7; a >= 0; --a)
#pragma unroll
          for (int j = a; j < 8; ++j) ac[j] = __builtin_amdgcn_mfma_f32_16x16x32_bf16(cf[rr * 8 + a], bfm[j - a], ac[j], 0, 0, 0);
      }
#pragma unroll
      for (int j = 0; j < 8; ++j)
        *(LAS f32x4*)(smem + PART_OFF + w * 8192 + (256 * j + 16 * (lane & 15) + 4 * ckg) * 4) = ac[j];
    }
    {
      LAS char* xb = smem + XR_OFF + 2 * XR_STRIDE + boff + 64 * w;
      const int jA = 7 - qd, jB = qd;
      bf16x8 b0[8], b1[4];
#pragma unroll
      for (int a = 0; a < 8; ++a) { const int bb = jA - a; b0[a] = *(LAS bf16x8*)(xb - 512 * (bb < 0 ? 0 : bb)); }
#pragma unroll
      for (int a = 0; a < 4; ++a) { const int bb = jB - a; b1[a] = *(LAS bf16x8*)(xb - 512 * (bb < 0 ? 0 : bb)); }
      f32x4 c0 = (f32x4){0.f, 0.f, 0.f, 0.f}, c1 = c0;
#pragma unroll
      for (int a = 7; a >= 0; --a) {
        if (a <= jA) c0 = __builtin_amdgcn_mfma_f32_16x16x32_bf16(cf[16 + a], b0[a], c0, 0, 0, 0);
        if (a < 4 && a <= jB) c1 = __builtin_amdgcn_mfma_f32_16x16x32_bf16(cf[16 + a], b1[a < 4 ? a : 0], c1, 0, 0, 0);
      }
      *(LAS f32x4*)(smem + PARTC_OFF + w * 2048 + (16 * (lane & 15) + 4 * ckg) * 4) = c0;
      *(LAS f32x4*)(smem + PARTC_OFF + w * 2048 + 1024 + (16 * (lane & 15) + 4 * ckg) * 4) = c1;
    }
  }
  STAMP(1, 4);
  __syncthreads();
  STAMP(1, 5);
  auto finalize = [&](f32x4 s0, f32x4 s1, int im, int ch, int t8) ALWAYS_INLINE {
    const float dd = bf2f(ssmd[ch]);
    const u32x4 kvv = *(LAS u32x4*)(smem + XR_OFF + im * XR_STRIDE + (2040 - t8) * 2);
    float y[8] = {s0[0], s0[1], s0[2], s0[3], s1[0], s1[1], s1[2], s1[3]};
    float o[8];
#pragma unroll
    for (int e = 0; e < 8; ++e) {
      const int ri = 7 - e;
      const float kvf = (ri & 1) ? bf_hi(kvv[ri >> 1]) : bf_lo(kvv[ri >> 1]);
      o[e] = bfr(y[e]) + bfr(kvf * dd);
    }
    u32x4 ov; ov.x = pk_bf16(o[0], o[1]); ov.y = pk_bf16(o[2], o[3]); ov.z = pk_bf16(o[4], o[5]); ov.w = pk_bf16(o[6], o[7]);
    *(u32x4*)(wT + (size_t)ch * S_ + t8) = ov;
  };
  {
    const int im = tid >> 8, t8 = (tid & 255) * 8;
    f32x4 s0 = (f32x4){0.f, 0.f, 0.f, 0.f}, s1 = s0;
#pragma unroll
    for (int ww = 0; ww < 4; ++ww) {
      s0 += *(LAS f32x4*)(smem + PART_OFF + (im * 4 + ww) * 8192 + t8 * 4);
      s1 += *(LAS f32x4*)(smem + PART_OFF + (im * 4 + ww) * 8192 + t8 * 4 + 16);
    }
    finalize(s0, s1, im, chA + im, t8);
  }
  if (tid < 64) {
    const int tl = tid >> 5, l8 = (tid & 31) * 8;
    const int jt = tl ? qd : 7 - qd, t8 = 256 * jt + l8;
    f32x4 s0 = (f32x4){0.f, 0.f, 0.f, 0.f}, s1 = s0;
#pragma unroll
    for (int ww = 0; ww < 8; ++ww) {
      s0 += *(LAS f32x4*)(smem + PARTC_OFF + ww * 2048 + tl * 1024 + l8 * 4);
      s1 += *(LAS f32x4*)(smem + PARTC_OFF + ww * 2048 + tl * 1024 + l8 * 4 + 16);
    }
    finalize(s0, s1, 2, chC, t8);
  }
  STAMP(1, 6); RSTAMP(1, 15); STAMP_FLUSH(1); STAMP_FLUSH(3);
}

constexpr int K2_HALF = 12288;
constexpr int K2_SLOT = 2 * K2_HALF;
constexpr int K2_NSLOT = 4;
constexpr int K2_XB_OFF = K2_NSLOT * K2_SLOT;
constexpr int K2_OWN_OFF = K2_XB_OFF + 32768;
constexpr int K2_WT_OFF = K2_OWN_OFF + 18432;
constexpr int K2_LDS = K2_WT_OFF + 9216;
constexpr int K2_MID = 4;
constexpr int k2_g0(int st) { return st <= 12 ? 12 * st : 144 + 9 * (st - 12); }
constexpr int k2_stage(int g) { return g < 144 ? g / 12 : 12 + (g - 144) / 9; }
constexpr int k2_dkey(int d, int n) { return d == 0 ? -300 + n : d == 1 ? -200 + n : 4 * ((d == 2 ? 1 : k2_g0(d - 3) + K2_MID) + n) + 2; }
constexpr int k2_hkey(int j, int m) { return j == 0 ? -250 + m : j == 1 ? -150 + m : 4 * ((j == 2 ? 5 : k2_g0(j - 3) + K2_MID + 4) + m) + 3; }
constexpr int k2_skey(int p) { return 4 * (k2_g0(6 * p + 6) + K2_MID) + 1; }
constexpr int k2_younger(int okey, int wkey, bool stores) {
  int n = 0;
  for (int d = 0; d < 14; ++d) for (int pc = 0; pc < 3; ++pc) if (k2_dkey(d, pc) > okey && k2_dkey(d, pc) < wkey) n += 1;
  for (int j = 0; j < 6; ++j) for (int m = 0; m < 3; ++m) if (k2_hkey(j, m) > okey && k2_hkey(j, m) < wkey) n += 2;
  if (stores) for (int p = 0; p < 2; ++p) if (k2_skey(p) > okey && k2_skey(p) < wkey) n += 4;
  return n;
}
constexpr int cmax(int a, int b) { return a > b ? a : b; }

__global__ __launch_bounds__(512, 2) void k2_gemm2(const u16* __restrict__ hin, const u16* __restrict__ wd, float* __restrict__ slab,
                                                   unsigned* __restrict__ cnt, const u16* __restrict__ q, const float2* __restrict__ stats,
                                                   const u16* __restrict__ wT, u16* __restrict__ out) {
  extern __shared__ __attribute__((aligned(16))) char smem_g[];
  LAS char* smem = (LAS char*)smem_g;
  const unsigned lds0 = (unsigned)(uintptr_t)smem;
  const int bid = blockIdx.x, tid = threadIdx.x;
  const int w = __builtin_amdgcn_readfirstlane(tid >> 6), lane = tid & 63;
  const int xcd = bid & 7, idx = bid >> 3;
  const int ksl = xcd >> 1, mt = (xcd & 1) * 8 + (idx >> 2), nt = idx & 3;
  const int tq = w & 3, khf = w >> 2;
  const int kbase = ksl * 1152 + khf * 576;
  const int colA = 128 * nt, colQ = 512 + 16 * nt;
  STAMP_DECL; RSTAMP(2, 14); STAMP(2, 0);

  {
    const char* g = (const char*)(wT + mt * 128 + ksl * 32);
    const unsigned v = (unsigned)((colA + w * 16 + (lane >> 2)) * (S_ * 2) + (lane & 3) * 16);
    glds1(g, v, lds0 + K2_WT_OFF + w * 1024);
    if (w == 0) glds1(g, (unsigned)((colQ + (lane >> 2)) * (S_ * 2) + (lane & 3) * 16), lds0 + K2_WT_OFF + 8192);
  }

  const char* wbase = (const char*)wd + (size_t)kbase * (D_ * 2);
  unsigned voff[3];
#pragma unroll
  for (int n = 0; n < 3; ++n) {
    const int o = (tq * 3 + n) * 1024 + lane * 16;
    const int k = o >> 7, pc = (o >> 4) & 7;
    const int j = (pc >> 1) ^ ((k >> 1) & 3);
    voff[n] = (unsigned)(k * (D_ * 2) + (2 * j + (pc & 1)) * 16);
  }
  const unsigned ldst = lds0 + khf * K2_HALF;
  auto piece_load = [&](int st, int n) ALWAYS_INLINE {
    const int slot = st % K2_NSLOT;
    if (st < 12) {
      const int p = st / 6, ks = st - p * 6;
      const char* g = wbase + (size_t)ks * 96 * (D_ * 2) + (colA + p * 64) * 2;
      glds1p<false>(g, voff[n], ldst + slot * K2_SLOT + tq * 3072 + n * 1024);
    } else {
      int l2 = lane; asm volatile("" : "+v"(l2));
      const char* g = wbase + (size_t)(st - 12) * 288 * (D_ * 2) + colQ * 2;
      const int o = n < 2 ? tq * 2048 + n * 1024 + l2 * 16 : 8192 + tq * 256 + (l2 & 15) * 16;
      const unsigned v0 = (unsigned)((o >> 5) * (D_ * 2) + ((o >> 4) & 1) * 16);
      if (n < 2 || l2 < 16) glds1p<false>(g, v0, ldst + slot * K2_SLOT + (n < 2 ? tq * 2048 + n * 1024 : 8192 + tq * 256));
    }
  };
  const char* hbase = (const char*)hin + ((size_t)(mt * 8 + tq * 2) * 144 * 64 + (size_t)(kbase >> 5) * 64) * 16;
  const unsigned hv0 = lane * 16, hv1 = lane * 16 + 144 * 64 * 16;
  bf16x8 hf[18][2];
  auto h_load = [&](auto kc) ALWAYS_INLINE {
    constexpr int k = kc;
    const char* g = hbase + (k >> 2) * 4096;
    gload16<(k & 3) * 1024>(hf[k][0], g, hv0);
    gload16<(k & 3) * 1024>(hf[k][1], g, hv1);
  };
#pragma unroll
  for (int n = 0; n < 3; ++n) piece_load(0, n);
  sfor<3>([&](auto kc) ALWAYS_INLINE { h_load(kc); });
#pragma unroll
  for (int n = 0; n < 3; ++n) piece_load(1, n);
  sfor<3>([&](auto kc) ALWAYS_INLINE { h_load(std::integral_constant<int, kc + 3>{}); });

  const int kq = lane >> 4, qq = (lane & 15) >> 2, p4 = lane & 3;
  const unsigned rbU = lds0 + khf * K2_HALF + (16 * (kq >> 1) + 4 * (kq & 1) + qq) * 128 + ((((qq >> 1) & 1) | ((kq & 1) << 1)) << 5) + p4 * 8;

  f32x4 acc[2][4], keep[4];
#pragma unroll
  for (int t = 0; t < 2; ++t)
#pragma unroll
    for (int dt = 0; dt < 4; ++dt) acc[t][dt] = (f32x4){0.f, 0.f, 0.f, 0.f};

  const int tile = mt * 4 + nt;
  const __amdgpu_buffer_rsrc_t rs = __builtin_amdgcn_make_buffer_rsrc((char*)slab + (size_t)tile * (16 * 18432), 0, 16 * 18432, 0x00020000);
  const bool foreign = tq != ksl;
  auto panel_end = [&](int nd) ALWAYS_INLINE {
#pragma unroll
    for (int dt = 0; dt < 4; ++dt) if (dt < nd) {
      *(LAS f32x4*)(smem + K2_XB_OFF + ((w * 4 + dt) * 64 + lane) * 16) = khf ? acc[0][dt] : acc[1][dt];
      keep[dt] = khf ? acc[1][dt] : acc[0][dt];
      acc[0][dt] = (f32x4){0.f, 0.f, 0.f, 0.f}; acc[1][dt] = (f32x4){0.f, 0.f, 0.f, 0.f};
    }
  };
  auto publish = [&](int p, int nd) ALWAYS_INLINE {
    f32x4 sm[4];
#pragma unroll
    for (int dt = 0; dt < 4; ++dt) if (dt < nd) sm[dt] = keep[dt] + *(LAS f32x4*)(smem + K2_XB_OFF + (((w ^ 4) * 4 + dt) * 64 + lane) * 16);
    if (foreign) {
#pragma unroll
      for (int dt = 0; dt < 4; ++dt) if (dt < nd)
        __builtin_amdgcn_raw_buffer_store_b128(__builtin_bit_cast(u32x4, sm[dt]), rs, (unsigned)(((tq * 4 + ksl) * 18 + khf * 9 + p * 4 + dt) * 1024 + lane * 16), 0, 16);
    } else {
#pragma unroll
      for (int dt = 0; dt < 4; ++dt) if (dt < nd) *(LAS f32x4*)(smem + K2_OWN_OFF + ((khf * 9 + p * 4 + dt) * 64 + lane) * 16) = sm[dt];
    }
  };

  asm volatile("s_waitcnt vmcnt(%6)" : "+v"(hf[0][0]), "+v"(hf[0][1]), "+v"(hf[1][0]), "+v"(hf[1][1]), "+v"(hf[2][0]), "+v"(hf[2][1])
               : "i"(k2_younger(k2_hkey(0, 2), 0, false)) : "memory");
  __builtin_amdgcn_s_barrier();
  CFENCE();
  STAMP(2, 1);

  constexpr int PD = 3;
  bf16x8 af[PD + 1];
  unsigned rb[4];
#pragma unroll
  for (int j = 0; j < 4; ++j) rb[j] = rbU ^ (j << 5);
#pragma unroll
  for (int g = 0; g < PD; ++g) af[g] = tr_frag((LAS char*)(uintptr_t)rb[g], 0, 1024);
  sfor<162>([&](auto gc) ALWAYS_INLINE {
    constexpr int g = gc;
    constexpr int st = k2_stage(g), f = g - k2_g0(st);
    constexpr int ks = st < 12 ? (st % 6) * 3 + f / 4 : (st - 12) * 9 + f, dt = st < 12 ? f % 4 : 0;
    if constexpr (f == K2_MID && st < 13) {
      constexpr int okey = cmax(k2_dkey(st + 1, 2), st + 1 < 6 ? k2_hkey(st + 1, 2) : -1000);
      constexpr int n0 = k2_younger(okey, 4 * g, false), n1 = k2_younger(okey, 4 * g, true);
      if constexpr (st < 8) { STAMP(0, 2 * st); }
      if constexpr (n0 != n1) { if (foreign) wait_vm<n1>(); else wait_vm<n0>(); }
      else wait_vm<n0>();
      if constexpr (st + 1 < 6) {
        constexpr int k0 = 3 * (st + 1);
        asm volatile("" : "+v"(hf[k0][0]), "+v"(hf[k0][1]), "+v"(hf[k0 + 1][0]), "+v"(hf[k0 + 1][1]), "+v"(hf[k0 + 2][0]), "+v"(hf[k0 + 2][1])::"memory");
      }
      if constexpr (st == 6 || st == 12) WAIT_L(0);
      __builtin_amdgcn_s_barrier();
      CFENCE();
      if constexpr (st < 8) { STAMP(0, 2 * st + 1); }
      if constexpr (st == 6 || st == 12) publish(st / 6 - 1, 4);
    }
    sfor<36>([&](auto dc) ALWAYS_INLINE { if constexpr (k2_dkey(dc / 3 + 2, dc % 3) == 4 * g + 2) piece_load(dc / 3 + 2, dc % 3); });
    sfor<12>([&](auto hc) ALWAYS_INLINE { if constexpr (k2_hkey(hc / 3 + 2, hc % 3) == 4 * g + 3) h_load(std::integral_constant<int, hc + 6>{}); });
    if constexpr (g + PD < 162) {
      constexpr int g1 = g + PD, st1 = k2_stage(g1), f1 = g1 - k2_g0(st1);
      if constexpr (f1 == 0) {
        if constexpr (st1 < 12) {
          unsigned b2 = rbU; asm volatile("" : "+v"(b2));
#pragma unroll
          for (int j = 0; j < 4; ++j) { rb[j] = (b2 + (st1 % K2_NSLOT) * K2_SLOT) ^ (j << 5); asm volatile("" : "+v"(rb[j])); }
        } else {
          int l2 = lane; asm volatile("" : "+v"(l2));
          const int kq2 = l2 >> 4;
          rb[0] = lds0 + khf * K2_HALF + (16 * (kq2 >> 1) + 4 * (kq2 & 1) + ((l2 & 15) >> 2)) * 32 + (l2 & 3) * 8 + (st1 % K2_NSLOT) * K2_SLOT;
          asm volatile("" : "+v"(rb[0]));
        }
      }
      if constexpr (st1 < 12) af[g1 % (PD + 1)] = tr_frag((LAS char*)(uintptr_t)rb[f1 % 4], (f1 / 4) * 4096, 1024);
      else af[g1 % (PD + 1)] = tr_frag((LAS char*)(uintptr_t)rb[0], f1 * 1024, 256);
    }
    SCHED0();
#pragma unroll
    for (int t = 0; t < 2; ++t) acc[t][dt] = __builtin_amdgcn_mfma_f32_16x16x32_bf16(af[g % (PD + 1)], hf[ks][t], acc[t][dt], 0, 0, 0);
    SCHED0();
    if constexpr (g == 71 || g == 143) { panel_end(4); SCHED0(); }
  });
  STAMP(2, 2);
  panel_end(1);

  u32x2 xq[3];
  float2 stt[3];
#pragma unroll
  for (int i = 0; i < 3; ++i) {
    const int fi = (w + 8 * i < 18) ? w + 8 * i : w;
    const int t = fi >= 9 ? 1 : 0, dt = fi - 9 * t;
    const int token = mt * 128 + ksl * 32 + t * 16 + (lane & 15);
    xq[i] = *(const u32x2*)(q + (size_t)token * D_ + (dt < 8 ? colA + dt * 16 : colQ) + 4 * (lane >> 4));
    stt[i] = stats[token];
  }
  WAIT_L(0);
  __builtin_amdgcn_s_barrier();
  CFENCE();
  publish(2, 1);
  asm volatile("s_waitcnt vmcnt(0)" ::: "memory");
  __syncthreads();
  STAMP(2, 3);
  if (tid == 0) {
    unsigned* c = cnt + tile * 64;
    __hip_atomic_fetch_add(c, 1u, __ATOMIC_RELAXED, __HIP_MEMORY_SCOPE_AGENT);
    for (unsigned spins = 0; spins < (1u << 22); ++spins) {
      if (__hip_atomic_load(c, __ATOMIC_RELAXED, __HIP_MEMORY_SCOPE_AGENT) >= 4u) break;
      __builtin_amdgcn_s_sleep(1);
    }
  }
  __syncthreads();
  STAMP(2, 4);
  __builtin_amdgcn_fence(__ATOMIC_ACQUIRE, "wavefront");
  {
    f32x4 pv[3][4];
#pragma unroll
    for (int i = 0; i < 3; ++i) {
      const int fi = (w + 8 * i < 18) ? w + 8 * i : w;
#pragma unroll
      for (int k = 1; k < 4; ++k)
        pv[i][k] = __builtin_bit_cast(f32x4, __builtin_amdgcn_raw_buffer_load_b128(rs, (unsigned)(((ksl * 4 + ((ksl + k) & 3)) * 18 + fi) * 1024 + lane * 16), 0, 16));
      pv[i][0] = *(LAS f32x4*)(smem + K2_OWN_OFF + (fi * 64 + lane) * 16);
    }
#pragma unroll
    for (int i = 0; i < 3; ++i) {
      if (w + 8 * i < 18) {
        const int fi = w + 8 * i;
        const int t = fi >= 9 ? 1 : 0, dt = fi - 9 * t;
        const int tl = t * 16 + (lane & 15), dl = dt * 16 + 4 * (lane >> 4);
        const f32x4 bs = ((pv[i][0] + pv[i][1]) + pv[i][2]) + pv[i][3];
        float o[4];
#pragma unroll
        for (int c = 0; c < 4; ++c) {
          const unsigned xw = (c < 2) ? xq[i].x : xq[i].y;
          const float x = (c & 1) ? bf_hi(xw) : bf_lo(xw);
          const float wy = bf2f(*(LAS u16*)(smem + K2_WT_OFF + (dl + c) * 64 + tl * 2));
          const float xn = bfr(bfr(x - stt[i].x) / stt[i].y);
          const float a = bfr(xn * wy);
          o[c] = bfr(x + a) + bfr(bs[c]);
        }
        u32x2 ov; ov.x = pk_bf16(o[0], o[1]); ov.y = pk_bf16(o[2], o[3]);
        *(u32x2*)(out + (size_t)(mt * 128 + ksl * 32 + tl) * D_ + (dt < 8 ? colA + dt * 16 : colQ) + 4 * (lane >> 4)) = ov;
      }
    }
  }
  STAMP(2, 5); RSTAMP(2, 15); STAMP_FLUSH(2); STAMP_FLUSH(0);
}

extern "C" void kernel_launch(void* const* d_in, const int* in_sizes, int n_in,
                              void* d_out, int out_size, void* d_ws, size_t ws_size,
                              hipStream_t stream) {
  const u16* q = (const u16*)d_in[0];
  const u16* k = (const u16*)d_in[1];
  const u16* v = (const u16*)d_in[2];
  const u16* shk = (const u16*)d_in[3];
  const u16* ssmk = (const u16*)d_in[4];
  const u16* ssmd = (const u16*)d_in[5];
  const u16* wg = (const u16*)d_in[7];
  const u16* wu = (const u16*)d_in[8];
  const u16* wd = (const u16*)d_in[9];
  char* ws = (char*)d_ws;
  u16* xfrag = (u16*)(ws + WS_XFRAG);
  float2* stats = (float2*)(ws + WS_STATS);
  u16* kvr = (u16*)(ws + WS_KVR);
  u16* wT = (u16*)(ws + WS_WT);
  u16* h = (u16*)(ws + WS_H);
  float* slab = (float*)(ws + WS_SLAB);
  unsigned* cnt = (unsigned*)(ws + WS_CNT);

  (void)hipFuncSetAttribute((const void*)k1_gemm1, hipFuncAttributeMaxDynamicSharedMemorySize, K1_LDS);
  (void)hipFuncSetAttribute((const void*)k2_gemm2, hipFuncAttributeMaxDynamicSharedMemorySize, K2_LDS);

  k0_prep<<<128 + 288, 256, 0, stream>>>(q, k, v, shk, xfrag, stats, kvr, cnt);
  k1_gemm1<<<256, 512, K1_LDS, stream>>>(xfrag, wg, wu, h, ssmk, ssmd, kvr, wT, wd);
  k2_gemm2<<<256, 512, K2_LDS, stream>>>(h, wd, slab, cnt, q, stats, wT, (u16*)d_out);
}
```

```cpp
#include <hip/hip_runtime.h>
#include <hip/hip_bf16.h>
#include <stdint.h>
#include <utility>

typedef unsigned short u16;
typedef short bf16x8 __attribute__((ext_vector_type(8)));
typedef short s16x4 __attribute__((ext_vector_type(4)));
typedef float f32x4 __attribute__((ext_vector_type(4)));
typedef float f32x16 __attribute__((ext_vector_type(16)));
typedef unsigned u32x4 __attribute__((ext_vector_type(4)));
typedef unsigned u32x2 __attribute__((ext_vector_type(2)));
typedef __bf16 bf2_t __attribute__((ext_vector_type(2)));
#define LAS __attribute__((address_space(3)))
#define DI __device__ __forceinline__

constexpr int S_ = 2048, D_ = 576, F_ = 4608;

constexpr size_t WS_XFRAG = 0;
constexpr size_t WS_STATS = WS_XFRAG + (size_t)S_ * D_ * 2;
constexpr size_t WS_KVR   = WS_STATS + (size_t)S_ * 8;
constexpr size_t WS_WT    = WS_KVR + (size_t)S_ * D_ * 2;
constexpr size_t WS_H     = WS_WT + (size_t)S_ * D_ * 2;
constexpr size_t WS_SLAB  = WS_H + (size_t)S_ * F_ * 2;
constexpr size_t WS_CNT   = WS_SLAB + (size_t)4 * S_ * D_ * 4;
constexpr int NCNT = 97;
constexpr size_t WS_END   = WS_CNT + NCNT * 256;

DI float bf_lo(unsigned w) { return __uint_as_float(w << 16); }
DI float bf_hi(unsigned w) { return __uint_as_float(w & 0xffff0000u); }
DI float bf2f(u16 b) { return __uint_as_float(((unsigned)b) << 16); }
DI float bfr(float x) { return (float)(__bf16)x; }
DI unsigned pk_bf16(float lo, float hi) { bf2_t v = {(__bf16)lo, (__bf16)hi}; return __builtin_bit_cast(unsigned, v); }

#define WAIT_V(n) asm volatile("s_waitcnt vmcnt(" #n ")" ::: "memory")
#define WAIT_L(n) asm volatile("s_waitcnt lgkmcnt(" #n ")" ::: "memory")
#define CFENCE() asm volatile("" ::: "memory")
#define STAMP(kid, i)
#define RSTAMP(kid, i)
#define STAMP_DECL
#define STAMP_FLUSH(kid)
#define K0STAMP(i)
#define SCHED0() __builtin_amdgcn_sched_barrier(0)
template <int... I, class Fn> DI void sfor_impl(std::integer_sequence<int, I...>, Fn&& f) { (f(std::integral_constant<int, I>{}), ...); }
template <int N, class Fn> DI void sfor(Fn&& f) { sfor_impl(std::make_integer_sequence<int, N>{}, f); }
#define ALWAYS_INLINE __attribute__((always_inline))
template <int N> DI void wait_vm() { asm volatile("s_waitcnt vmcnt(%0)" ::"i"(N) : "memory"); }

DI void glds4(const void* sbase, unsigned v0, unsigned v1, unsigned v2, unsigned v3, unsigned lds_dst) {
  unsigned keep;
  asm volatile("s_mov_b32 %0, m0\n\ts_mov_b32 m0, %6\n\ts_nop 0\n\t"
               "global_load_lds_dwordx4 %1, %5\n\t"
               "global_load_lds_dwordx4 %2, %5 offset:1024\n\t"
               "global_load_lds_dwordx4 %3, %5 offset:2048\n\t"
               "global_load_lds_dwordx4 %4, %5 offset:3072\n\t"
               "s_mov_b32 m0, %0"
               : "=&s"(keep) : "v"(v0), "v"(v1), "v"(v2), "v"(v3), "s"(sbase), "s"(lds_dst) : "memory");
}
DI void glds3(const void* sbase, unsigned v0, unsigned v1, unsigned v2, unsigned lds_dst) {
  unsigned keep;
  asm volatile("s_mov_b32 %0, m0\n\ts_mov_b32 m0, %5\n\ts_nop 0\n\t"
               "global_load_lds_dwordx4 %1, %4\n\t"
               "global_load_lds_dwordx4 %2, %4 offset:1024\n\t"
               "global_load_lds_dwordx4 %3, %4 offset:2048\n\t"
               "s_mov_b32 m0, %0"
               : "=&s"(keep) : "v"(v0), "v"(v1), "v"(v2), "s"(sbase), "s"(lds_dst) : "memory");
}
DI void glds2(const void* sbase, unsigned v0, unsigned v1, unsigned lds_dst) {
  unsigned keep;
  asm volatile("s_mov_b32 %0, m0\n\ts_mov_b32 m0, %4\n\ts_nop 0\n\t"
               "global_load_lds_dwordx4 %1, %3\n\t"
               "global_load_lds_dwordx4 %2, %3 offset:1024\n\t"
               "s_mov_b32 m0, %0"
               : "=&s"(keep) : "v"(v0), "v"(v1), "s"(sbase), "s"(lds_dst) : "memory");
}
DI void glds1(const void* sbase, unsigned v0, unsigned lds_dst) {
  unsigned keep;
  asm volatile("s_mov_b32 %0, m0\n\ts_mov_b32 m0, %3\n\ts_nop 0\n\t"
               "global_load_lds_dwordx4 %1, %2\n\t"
               "s_mov_b32 m0, %0"
               : "=&s"(keep) : "v"(v0), "s"(sbase), "s"(lds_dst) : "memory");
}

template <bool NT> DI void glds1p(const void* sbase, unsigned v0, unsigned lds_dst) {
  unsigned keep;
  if constexpr (NT)
    asm volatile("s_mov_b32 %0, m0\n\ts_mov_b32 m0, %3\n\ts_nop 0\n\tglobal_load_lds_dwordx4 %1, %2 nt\n\ts_mov_b32 m0, %0"
                 : "=&s"(keep) : "v"(v0), "s"(sbase), "s"(lds_dst) : "memory");
  else
    asm volatile("s_mov_b32 %0, m0\n\ts_mov_b32 m0, %3\n\ts_nop 0\n\tglobal_load_lds_dwordx4 %1, %2\n\ts_mov_b32 m0, %0"
                 : "=&s"(keep) : "v"(v0), "s"(sbase), "s"(lds_dst) : "memory");
}
DI void touch_lines(const void* sbase, unsigned v0, unsigned lds_dst) {
  unsigned keep;
  asm volatile("s_mov_b32 %0, m0\n\ts_mov_b32 m0, %3\n\ts_nop 0\n\tglobal_load_lds_dword %1, %2\n\ts_mov_b32 m0, %0"
               : "=&s"(keep) : "v"(v0), "s"(sbase), "s"(lds_dst) : "memory");
}
template <int OFF> DI void gload16(bf16x8& dst, const void* sbase, unsigned voff) {
  asm volatile("global_load_dwordx4 %0, %1, %2 offset:%3" : "=v"(dst) : "v"(voff), "s"(sbase), "i"(OFF) : "memory");
}
template <int OFF> DI void gload16_sc1(bf16x8& dst, const void* sbase, unsigned voff) {
  asm volatile("global_load_dwordx4 %0, %1, %2 offset:%3 sc1" : "=v"(dst) : "v"(voff), "s"(sbase), "i"(OFF) : "memory");
}
DI void gstore16_sc1(void* p, const u32x4& v) { asm volatile("global_store_dwordx4 %0, %1, off sc1\n\ts_nop 1" ::"v"(p), "v"(v) : "memory"); }
DI void wait_count(unsigned* c, unsigned want) {
  for (unsigned spins = 0; spins < (1u << 22); ++spins) {
    if (__hip_atomic_load(c, __ATOMIC_RELAXED, __HIP_MEMORY_SCOPE_AGENT) >= want) break;
    __builtin_amdgcn_s_sleep(1);
  }
}

__global__ __launch_bounds__(256) void k0_prep(const u16* __restrict__ q, const u16* __restrict__ kk, const u16* __restrict__ vv,
                                               const u16* __restrict__ shk, u16* __restrict__ xfrag, float2* __restrict__ stats,
                                               u16* __restrict__ kvr, unsigned* __restrict__ cnt) {
  __shared__ __attribute__((aligned(16))) u16 tile[64 * 72];
  const int bid = blockIdx.x, tid = threadIdx.x;
  K0STAMP(0);
  if (bid == 0 && tid < NCNT) cnt[tid * 64] = 0u;
  if (bid < 128) {
    const int wave = tid >> 6, lane = tid & 63;
    const int token = bid * 16 + wave * 4 + (lane >> 4), j = lane & 15;
    u32x2 d[9];
#pragma unroll
    for (int i = 0; i < 9; ++i) d[i] = *(const u32x2*)(q + (size_t)token * D_ + (j + 16 * i) * 4);
    float s = 0.f;
#pragma unroll
    for (int i = 0; i < 9; ++i) s += (bf_lo(d[i].x) + bf_hi(d[i].x)) + (bf_lo(d[i].y) + bf_hi(d[i].y));
    s += __shfl_xor(s, 1); s += __shfl_xor(s, 2); s += __shfl_xor(s, 4); s += __shfl_xor(s, 8);
    const float mean = s * (1.0f / 576.0f);
    float vs = 0.f;
#pragma unroll
    for (int i = 0; i < 9; ++i) {
      float a = bf_lo(d[i].x) - mean, b = bf_hi(d[i].x) - mean, c = bf_lo(d[i].y) - mean, e = bf_hi(d[i].y) - mean;
      vs += (a * a + b * b) + (c * c + e * e);
    }
    vs += __shfl_xor(vs, 1); vs += __shfl_xor(vs, 2); vs += __shfl_xor(vs, 4); vs += __shfl_xor(vs, 8);
    const float mb = bfr(mean), vb = bfr(vs * (1.0f / 576.0f));
    const float sd = bfr(sqrtf(bfr(vb + 1e-5f)));
    if (j == 0) stats[token] = make_float2(mb, sd);
    const int tl = token >> 5, tr = token & 31;
#pragma unroll
    for (int i = 0; i < 9; ++i) {
      const int p = j + 16 * i;
      float a = bfr(bf_lo(d[i].x) - mb) / sd, b = bfr(bf_hi(d[i].x) - mb) / sd, c = bfr(bf_lo(d[i].y) - mb) / sd, e = bfr(bf_hi(d[i].y) - mb) / sd;
      u32x2 o; o.x = pk_bf16(a, b); o.y = pk_bf16(c, e);
      const int st = p >> 2, kh = (p >> 1) & 1, half = p & 1;
      *(u32x2*)((char*)xfrag + ((size_t)((tl * 36 + st) * 64 + kh * 32 + tr)) * 16 + half * 8) = o;
    }
  } else {
    const int kb = bid - 128, tt = kb / 9, ct = kb - tt * 9;
    const int t0 = tt * 64, c0 = ct * 64;
    const int tr = tid >> 3, cg = tid & 7;
    u32x4 w4[4];
#pragma unroll
    for (int i = 0; i < 4; ++i) w4[i] = *(const u32x4*)(shk + (size_t)(c0 + 8 * cg) * 4 + i * 8);
#pragma unroll
    for (int it = 0; it < 2; ++it) {
      const int tl = tr + 32 * it, t = t0 + tl;
      u32x4 kr[4];
#pragma unroll
      for (int i = 0; i < 4; ++i) {
        const int ts = t + i - 3;
        kr[i] = (u32x4){0u, 0u, 0u, 0u};
        if (ts >= 0) kr[i] = *(const u32x4*)(kk + (size_t)ts * D_ + c0 + 8 * cg);
      }
      const u32x4 vr = *(const u32x4*)(vv + (size_t)t * D_ + c0 + 8 * cg);
#pragma unroll
      for (int e = 0; e < 8; ++e) {
        const unsigned wa = w4[e >> 1][(e & 1) * 2], wb = w4[e >> 1][(e & 1) * 2 + 1];
        const float w0 = bf_lo(wa), w1 = bf_hi(wa), w2 = bf_lo(wb), w3 = bf_hi(wb);
        float kq[4];
#pragma unroll
        for (int i = 0; i < 4; ++i) kq[i] = (e & 1) ? bf_hi(kr[i][e >> 1]) : bf_lo(kr[i][e >> 1]);
        float ks = bfr(kq[0] * w3);
        ks = bfr(ks + bfr(kq[1] * w2));
        ks = bfr(ks + bfr(kq[2] * w1));
        ks = bfr(ks + bfr(kq[3] * w0));
        const float vf = (e & 1) ? bf_hi(vr[e >> 1]) : bf_lo(vr[e >> 1]);
        const float kv = ks * vf;
        tile[(8 * cg + e) * 72 + (63 - tl)] = (u16)(pk_bf16(kv, 0.f) & 0xffffu);
      }
    }
    __syncthreads();
    const int pbase = 1984 - t0;
#pragma unroll
    for (int it = 0; it < 2; ++it) {
      const int pc = tid + 256 * it, ch = pc >> 3, pp = pc & 7;
      const u32x4 o = *(const u32x4*)(tile + ch * 72 + pp * 8);
      *(u32x4*)(kvr + (size_t)(c0 + ch) * S_ + pbase + pp * 8) = o;
    }
  }
  K0STAMP(1);
}

constexpr int K1_SLOT = 37120;
constexpr int K1_UOFF = 18432 + 128;
constexpr int K1_NSLOT = 4;
constexpr int K1_PF_OFF = K1_NSLOT * K1_SLOT;
constexpr int XR_OFF = K1_PF_OFF + 256, XR_STRIDE = 4608;
constexpr int HZ_OFF = XR_OFF + 3 * XR_STRIDE;
constexpr int K1_LDS = HZ_OFF + 288;
constexpr int PART_OFF = 49152;
constexpr int PARTC_OFF = PART_OFF + 65536;
constexpr int HCNT_OFF = K1_PF_OFF;

DI bf16x8 tr_frag(LAS char* p, int off, int hi_off) {
  s16x4 lo = __builtin_amdgcn_ds_read_tr16_b64_v4i16((LAS s16x4*)(p + off));
  s16x4 hi = __builtin_amdgcn_ds_read_tr16_b64_v4i16((LAS s16x4*)(p + off + hi_off));
  return __builtin_shufflevector(lo, hi, 0, 1, 2, 3, 4, 5, 6, 7);
}

constexpr int K1_MID = 16;
constexpr int k1_xkey(int i) { return i < 9 ? -100 + i : (i < 18 ? 4 * (1 + 2 * (i - 9)) + 2 : (i < 27 ? 4 * (21 + 2 * (i - 18)) + 2 : 4 * (43 + 2 * (i - 27)) + 2)); }
constexpr int k1_dstep(int d, int n) { return d == 2 ? 18 + 2 * n : d == 3 ? 40 + 2 * n : d == 4 ? 62 + 6 * n : d == 5 ? 94 + 3 * n : 36 * (d - 3) + K1_MID + 3 * n; }
constexpr int k1_dkey(int d, int n) { return d == 0 ? -200 + n : d == 1 ? -50 + n : 4 * k1_dstep(d, n) + 1; }
constexpr int k1_cstep(int i) { return i < 7 ? 218 + 4 * i : (i < 14 ? 254 + 4 * (i - 7) : (i < 21 ? 289 + 2 * (i - 14) : 306 + 4 * (i - 21))); }
constexpr int k1_sstep(int c) { return c == 0 ? 143 : (c < 4 ? 144 + 6 * c : 182); }
constexpr int k1_younger(int okey, int wkey) {
  int n = 0;
  for (int i = 0; i < 36; ++i) if (k1_xkey(i) > okey && k1_xkey(i) < wkey) n += 1;
  for (int d = 0; d < 9; ++d) for (int pc = 0; pc < 5; ++pc) if (k1_dkey(d, pc) > okey && k1_dkey(d, pc) < wkey) n += 1;
  for (int i = 0; i < 24; ++i) if (4 * k1_cstep(i) + 2 > okey && 4 * k1_cstep(i) + 2 < wkey) n += 2;
  for (int c = 0; c < 5; ++c) if (4 * k1_sstep(c) + 3 > okey && 4 * k1_sstep(c) + 3 < wkey) n += 1;
  return n;
}
#define HSTORE 1

constexpr int K2_HALF = 12288;
constexpr int K2_SLOT = 2 * K2_HALF;
constexpr int K2_NSLOT = 4;
constexpr int K2_XB_OFF = K2_NSLOT * K2_SLOT;
constexpr int K2_OWN_OFF = K2_XB_OFF + 32768;
constexpr int K2_WT_OFF = K2_OWN_OFF + 18432;
constexpr int K2_LDS = K2_WT_OFF + 9216;
constexpr int K2_MID = 4;
constexpr int k2_g0(int st) { return st <= 12 ? 12 * st : 144 + 9 * (st - 12); }
constexpr int k2_stage(int g) { return g < 144 ? g / 12 : 12 + (g - 144) / 9; }
constexpr int k2_dkey(int d, int n) { return d == 0 ? -300 + n : d == 1 ? -290 + n : 4 * ((d == 2 ? 1 : k2_g0(d - 3) + K2_MID) + n) + 2; }
constexpr int k2_hkey(int j, int m) { return j == 0 ? -250 + m : j == 1 ? -150 + m : 4 * ((j == 2 ? 5 : k2_g0(j - 3) + K2_MID + 4) + m) + 3; }
constexpr int k2_skey(int p) { return 4 * (k2_g0(6 * p + 6) + K2_MID) + 1; }
constexpr int K2_WST = 9;
constexpr int k2_wkey() { return 4 * (k2_g0(K2_WST) + K2_MID + 4) + 3; }
constexpr int k2_younger(int okey, int wkey, bool stores) {
  int n = 0;
  if (-100 > okey && -100 < wkey) n += 1;
  if (k2_wkey() > okey && k2_wkey() < wkey) n += 2;
  for (int d = 0; d < 14; ++d) for (int pc = 0; pc < 3; ++pc) if (k2_dkey(d, pc) > okey && k2_dkey(d, pc) < wkey) n += 1;
  for (int j = 0; j < 6; ++j) for (int m = 0; m < 3; ++m) if (k2_hkey(j, m) > okey && k2_hkey(j, m) < wkey) n += 2;
  if (stores) for (int p = 0; p < 2; ++p) if (k2_skey(p) > okey && k2_skey(p) < wkey) n += 4;
  return n;
}
constexpr int cmax(int a, int b) { return a > b ? a : b; }

__global__ __launch_bounds__(512, 2) void k1_mlp(const u16* __restrict__ xfrag, const u16* __restrict__ wg, const u16* __restrict__ wu,
                                                 u16* hout, const u16* __restrict__ ssmk, const u16* __restrict__ ssmd,
                                                 const u16* __restrict__ kvr, u16* wT, const u16* __restrict__ wd,
                                                 float* slab, unsigned* cnt, const u16* __restrict__ q, const float2* __restrict__ stats,
                                                 u16* __restrict__ out) {
  extern __shared__ __attribute__((aligned(16))) char smem_g[];
  LAS char* smem = (LAS char*)smem_g;
  const unsigned lds0 = (unsigned)(uintptr_t)smem;
  const int bid = blockIdx.x, tid = threadIdx.x;
  const int w = __builtin_amdgcn_readfirstlane(tid >> 6), lane = tid & 63;
  const int xcd = bid & 7, idx = bid >> 3;
  STAMP_DECL; RSTAMP(1, 14); STAMP(1, 0);
  const int ksl = xcd >> 1, mt = (xcd & 1) * 8 + (idx >> 2), nt = idx & 3;
  const int tq = w & 3, khf = w >> 2;
  const int kbase = ksl * 1152 + khf * 576;
  const int colA2 = 128 * nt, colQ2 = 512 + 16 * nt;
  const int mtile = (xcd & 1) * 4 + (idx & 3);
  const int cgrp = (xcd >> 1) * 2 + (idx >> 4), cq = (idx >> 2) & 3;
  const int colA = 64 * (9 * cgrp + 2 * cq), colQ = 64 * (9 * cgrp + 8) + 16 * cq;

  const int grp = bid >> 2, qd = bid & 3;
  const int chA = grp * 9 + 2 * qd, chC = grp * 9 + 8;
  float ddA, ddC;
  {
    const int c1 = chA + (w >> 2);
    const unsigned d1 = ((const unsigned*)ssmd)[c1 >> 1], d2 = ((const unsigned*)ssmd)[chC >> 1];
    ddA = (c1 & 1) ? bf_hi(d1) : bf_lo(d1);
    ddC = (chC & 1) ? bf_hi(d2) : bf_lo(d2);
  }

  const int wq = w & 3, part = w >> 2;
  const char* wbase = (const char*)(part ? wu : wg);
  unsigned voff[5];
#pragma unroll
  for (int n = 0; n < 5; ++n) {
    const int o = (n < 4) ? ((wq * 4 + n) * 1024 + lane * 16) : (16384 + wq * 512 + (lane & 31) * 16);
    const int k = o >> 7, pc = (o >> 4) & 7;
    const int j = (pc >> 1) ^ (((k >> 1) & 1) | (part << 1));
    voff[n] = (unsigned)(k * (F_ * 2) + (2 * j + (pc & 1)) * 16);
  }
  const unsigned ldst = lds0 + part * K1_UOFF + wq * 4096;
  const unsigned ldst4 = lds0 + part * K1_UOFF + 16384 + wq * 512;

  auto piece_load = [&](int st, int n) ALWAYS_INLINE {
    const int slot = st % K1_NSLOT;
    const unsigned dst = (n < 4 ? ldst + n * 1024 : ldst4) + slot * K1_SLOT;
    if (st != 4) {
      const int ks = st < 4 ? st : st - 5;
      const char* g = wbase + (size_t)ks * 144 * (F_ * 2) + (colA + (st > 4 ? 64 : 0)) * 2;
      if (n < 4 || lane < 32) glds1p<false>(g, voff[n], dst);
    } else {
      int l2 = lane; asm volatile("" : "+v"(l2));
      const int o = (n < 4) ? ((wq * 4 + n) * 1024 + l2 * 16) : (16384 + wq * 512 + (l2 & 31) * 16);
      const unsigned vq = (unsigned)((o >> 5) * (F_ * 2) + ((o >> 4) & 1) * 16);
      if (n < 4 || lane < 32) glds1p<false>(wbase + colQ * 2, vq, dst);
    }
  };

  bf16x8 xf[36];
  const char* xb = (const char*)xfrag + (size_t)(mtile * 8 + w) * 36 * 1024;
  const unsigned xv = lane * 16;
  auto x_load = [&](auto ic) ALWAYS_INLINE {
    constexpr int i = ic;
    gload16<(i & 3) * 1024>(xf[i], xb + (i >> 2) * 4096, xv);
  };
#pragma unroll
  for (int n = 0; n < 5; ++n) piece_load(0, n);
  sfor<9>([&](auto ic) ALWAYS_INLINE { x_load(ic); });
#pragma unroll
  for (int n = 0; n < 5; ++n) piece_load(1, n);

  const int kh = lane >> 5, prt = (lane >> 4) & 1, qq = (lane & 15) >> 2, p4 = lane & 3;
  const unsigned rbU = lds0 + (8 * kh + qq) * 128 + prt * K1_UOFF + ((((qq >> 1) & 1) | (prt << 1)) << 5) + p4 * 8;

  f32x16 acc[4];
  const f32x16 zero16 = {0.f, 0.f, 0.f, 0.f, 0.f, 0.f, 0.f, 0.f, 0.f, 0.f, 0.f, 0.f, 0.f, 0.f, 0.f, 0.f};

  const int token = mtile * 256 + w * 32 + (lane & 31);
  char* hb = (char*)hout + ((size_t)(token >> 4) * 144 * 64 + (lane >> 5) * 16 + (token & 15)) * 16;
  auto h_word = [&](const f32x16& a, int i) ALWAYS_INLINE -> unsigned {
    return pk_bf16(bfr(a[2 * i]) * bfr(a[2 * i + 8]), bfr(a[2 * i + 1]) * bfr(a[2 * i + 9]));
  };
  auto h_store = [&](const u32x4& o, int c16) ALWAYS_INLINE {
    gstore16_sc1(hb + ((size_t)(c16 >> 1) * 64 + (c16 & 1) * 32) * 16, o);
  };
  u32x4 ho;

  const char* hpA = (const char*)(ssmk + (size_t)(chA + (w >> 2)) * S_ + 64 * (w & 3));
  const char* hpC = (const char*)(ssmk + (size_t)chC * S_ + 32 * w);
  const bool zA = (w & 3) == 0, zC = w == 0;
  const char* pf_sk = (const char*)ssmk;
  const char* kvb = (const char*)kvr;
  u32x4 caf[24];
  unsigned cax[24];
  int cvo = 0, cvx = 0;

  wait_vm<k1_younger(k1_dkey(0, 4), 0)>();
  __builtin_amdgcn_s_barrier();
  CFENCE();
  STAMP(1, 1);

  constexpr int PD = 3, MID = K1_MID;
  bf16x8 af[PD + 1];
  unsigned rb[4];
#pragma unroll
  for (int j = 0; j < 4; ++j) rb[j] = rbU ^ (j << 5);
#pragma unroll
  for (int g = 0; g < PD; ++g) af[g] = tr_frag((LAS char*)(uintptr_t)rb[g], 0, 512);
  sfor<324>([&](auto gc) ALWAYS_INLINE {
    constexpr int g = gc;
    constexpr int st = g / 36, f = g - st * 36;
    constexpr int s = st != 4 ? f / 4 : f, c = st != 4 ? f % 4 : 0;
    constexpr int xi = st < 4 ? st * 9 + s : (st == 4 ? s : (st - 5) * 9 + s);
    constexpr int ai = st < 4 ? c : (st == 4 ? 0 : (c + 1) & 3);
    constexpr bool first = (st == 0 || st == 5) ? s == 0 : (st == 4 && f == 0);
    if constexpr (f == MID) {
      if constexpr (st < 8) { STAMP(3, 2 * st); }
      if constexpr (st < 8) wait_vm<k1_younger(k1_dkey(st + 1, 4), 4 * g)>();
      else { wait_vm<k1_younger(4 * (7 * 36 + MID + 1) + 1, 4 * g)>(); if (w == 3) WAIT_L(0); }
      __builtin_amdgcn_s_barrier();
      CFENCE();
      if constexpr (st < 8) { STAMP(3, 2 * st + 1); }
    }
    if constexpr (((f + 36 - MID) % 36) % 9 == 0) __builtin_amdgcn_s_setprio(3 - ((f + 36 - MID) % 36) / 9);
    sfor<35>([&](auto dc) ALWAYS_INLINE { if constexpr (k1_dkey(dc / 5 + 2, dc % 5) == 4 * g + 1) piece_load(dc / 5 + 2, dc % 5); });
    sfor<27>([&](auto ic) ALWAYS_INLINE { if constexpr (k1_xkey(ic + 9) == 4 * g + 2) x_load(std::integral_constant<int, ic + 9>{}); });
    if constexpr (g == 7 * 36 + MID + 1) {
      if (w < 3) {
        const int ch = (w == 2) ? chC : chA + w;
        const unsigned lv = lane * 16;
        glds4(kvb + (size_t)ch * (S_ * 2), lv, lv, lv, lv, lds0 + XR_OFF + w * XR_STRIDE);
        if (lane < 4) glds1(pf_sk + (size_t)ch * (S_ * 2), lv, lds0 + HZ_OFF + w * 96 + 32);
      } else if (w == 3) {
        if (lane < 32) {
#pragma unroll
          for (int im = 0; im < 3; ++im) *(LAS u32x4*)(smem + XR_OFF + im * XR_STRIDE + 4096 + lane * 16) = (u32x4){0u, 0u, 0u, 0u};
        } else if (lane < 38) {
          const int im = (lane - 32) >> 1, hh = lane & 1;
          *(LAS u32x4*)(smem + HZ_OFF + im * 96 + hh * 16) = (u32x4){0u, 0u, 0u, 0u};
        } else if (lane == 38) *(LAS unsigned*)(smem + HCNT_OFF) = 0u;
      }
    }
    sfor<24>([&](auto cc) ALWAYS_INLINE {
      constexpr int ci = cc;
      if constexpr (k1_cstep(ci) == g) {
        if constexpr (ci == 0) {
          int l2 = lane; asm volatile("" : "+v"(l2));
          const int e = (l2 & 15) - 15 + 8 * (l2 >> 4);
          cvo = (e & ~1) * 2; cvx = cvo + ((e & 1) ? 16 : 0);
        }
        constexpr int cb = ci < 16 ? 64 * (ci >> 3) + 512 * (ci & 7) : 512 * (ci - 16);
        const char* hp = (ci < 16 ? hpA : hpC) + cb + ((ci == 0 && zA) || (ci == 16 && zC) ? 32 : 0);
        __builtin_memcpy(&caf[ci], __builtin_assume_aligned(hp + cvo, 4), 16);
        cax[ci] = *(const unsigned*)(hp + cvx);
      }
    });
    if constexpr (g + PD < 324) {
      constexpr int g1 = g + PD, st1 = g1 / 36, f1 = g1 - st1 * 36;
      if constexpr (f1 == 0) {
        if constexpr (st1 != 4) {
          unsigned b2 = rbU; asm volatile("" : "+v"(b2));
#pragma unroll
          for (int j = 0; j < 4; ++j) { rb[j] = (b2 + (st1 % K1_NSLOT) * K1_SLOT) ^ (j << 5); asm volatile("" : "+v"(rb[j])); }
        } else {
          int l2 = lane; asm volatile("" : "+v"(l2));
          rb[0] = lds0 + (8 * (l2 >> 5) + ((l2 & 15) >> 2)) * 32 + ((l2 >> 4) & 1) * K1_UOFF + (l2 & 3) * 8 + (st1 % K1_NSLOT) * K1_SLOT;
          asm volatile("" : "+v"(rb[0]));
        }
      }
      if constexpr (st1 != 4) af[g1 % (PD + 1)] = tr_frag((LAS char*)(uintptr_t)rb[f1 % 4], (f1 / 4) * 2048, 512);
      else af[g1 % (PD + 1)] = tr_frag((LAS char*)(uintptr_t)rb[0], f1 * 512, 128);
    }
    if constexpr (st < 4 && c == 0) {
      asm volatile("s_waitcnt vmcnt(%1)" : "+v"(xf[xi]) : "i"(k1_younger(k1_xkey(xi), 4 * g + 3)) : "memory");
    }
    SCHED0();
    acc[ai] = __builtin_amdgcn_mfma_f32_32x32x16_bf16(af[g % (PD + 1)], xf[xi], first ? zero16 : acc[ai], 0, 0, 0);
    SCHED0();
    if constexpr (g == 143) {
#pragma unroll
      for (int i = 0; i < 4; ++i) ho[i] = h_word(acc[0], i);
      h_store(ho, colA >> 4);
      SCHED0();
    }
    if constexpr (g > 144 && g <= 162) {
      constexpr int cc = (g - 145) / 6 + 1, pi = (g - 145) % 6;
      if constexpr (pi < 4) ho[pi] = h_word(acc[cc], pi);
      if constexpr (pi == 5) h_store(ho, (colA >> 4) + cc);
      SCHED0();
    }
    if constexpr (g >= 180 && g <= 182) {
      if constexpr (g == 180) { ho[0] = h_word(acc[0], 0); ho[1] = h_word(acc[0], 1); }
      if constexpr (g == 181) { ho[2] = h_word(acc[0], 2); ho[3] = h_word(acc[0], 3); }
      if constexpr (g == 182) h_store(ho, colQ >> 4);
      SCHED0();
    }
  });
  const char* wbase2 = (const char*)wd + (size_t)kbase * (D_ * 2);
  unsigned voff2[3];
  int lane2 = lane; asm volatile("" : "+v"(lane2));
#pragma unroll
  for (int n = 0; n < 3; ++n) {
    const int o = (tq * 3 + n) * 1024 + lane2 * 16;
    const int k = o >> 7, pc = (o >> 4) & 7;
    const int j = (pc >> 1) ^ ((k >> 1) & 3);
    voff2[n] = (unsigned)(k * (D_ * 2) + (2 * j + (pc & 1)) * 16);
  }
  const unsigned ldst2 = lds0 + khf * K2_HALF;
  auto piece_load2 = [&](int st, int n) ALWAYS_INLINE {
    const int slot = st % K2_NSLOT;
    if (st < 12) {
      const int p = st / 6, ks = st - p * 6;
      const char* g = wbase2 + (size_t)ks * 96 * (D_ * 2) + (colA2 + p * 64) * 2;
      glds1p<false>(g, voff2[n], ldst2 + slot * K2_SLOT + tq * 3072 + n * 1024);
    } else {
      int l2 = lane; asm volatile("" : "+v"(l2));
      const char* g = wbase2 + (size_t)(st - 12) * 288 * (D_ * 2) + colQ2 * 2;
      const int o = n < 2 ? tq * 2048 + n * 1024 + l2 * 16 : 8192 + tq * 256 + (l2 & 15) * 16;
      const unsigned v0 = (unsigned)((o >> 5) * (D_ * 2) + ((o >> 4) & 1) * 16);
      if (n < 2 || l2 < 16) glds1p<false>(g, v0, ldst2 + slot * K2_SLOT + (n < 2 ? tq * 2048 + n * 1024 : 8192 + tq * 256));
    }
  };
  const char* hbase = (const char*)hout + ((size_t)(mt * 8 + tq * 2) * 144 * 64 + (size_t)(kbase >> 5) * 64) * 16;
  const unsigned hv0 = lane2 * 16, hv1 = lane2 * 16 + 144 * 64 * 16;
  bf16x8 hf[18][2];
  auto h_load = [&](auto kc) ALWAYS_INLINE {
    constexpr int k = kc;
    const char* g = hbase + (k >> 2) * 4096;
    gload16_sc1<(k & 3) * 1024>(hf[k][0], g, hv0);
    gload16_sc1<(k & 3) * 1024>(hf[k][1], g, hv1);
  };
  STAMP(1, 2);
  __builtin_amdgcn_s_setprio(0);
  bf16x8 cf[24];
  {
    const unsigned sh = ((lane + 1) & 1) * 2;
#pragma unroll
    for (int i = 0; i < 24; ++i) {
      u32x4 r;
      r.x = __builtin_amdgcn_alignbyte(caf[i].y, caf[i].x, sh);
      r.y = __builtin_amdgcn_alignbyte(caf[i].z, caf[i].y, sh);
      r.z = __builtin_amdgcn_alignbyte(caf[i].w, caf[i].z, sh);
      r.w = __builtin_amdgcn_alignbyte(cax[i], caf[i].w, sh);
      cf[i] = __builtin_bit_cast(bf16x8, r);
      asm volatile("" : "+v"(cf[i]));
    }
  }

#pragma unroll
  for (int cc = 0; cc < 4; ++cc) {
    u32x4 o;
#pragma unroll
    for (int i = 0; i < 4; ++i) o[i] = h_word(acc[(cc + 1) & 3], i);
    h_store(o, (colA >> 4) + 4 + cc);
  }

  __builtin_amdgcn_s_barrier();
  CFENCE();
#pragma unroll
  for (int n = 0; n < 3; ++n) piece_load2(0, n);
#pragma unroll
  for (int n = 0; n < 3; ++n) piece_load2(1, n);

  STAMP(1, 3);
  const int ct0 = lane & 15, ckg = lane >> 4;
  {
    const int boff = (2032 - 16 * (lane & 15) + 8 * ckg) * 2;
    auto head_frag = [&](int im) ALWAYS_INLINE -> bf16x8 {
      bf16x8 r;
      LAS u16* hz = (LAS u16*)(smem + HZ_OFF + im * 96) + 16 + (ct0 - 15 + 8 * ckg);
#pragma unroll
      for (int j = 0; j < 8; ++j) r[j] = (short)hz[j];
      return r;
    };
    if (zA) cf[0] = head_frag(w >> 2);
    if (zC) cf[16] = head_frag(2);
    {
      const int im = w >> 2;
      LAS char* xr = smem + XR_OFF + im * XR_STRIDE + boff + 128 * (w & 3);
      f32x4 ac[8];
#pragma unroll
      for (int j = 0; j < 8; ++j) ac[j] = (f32x4){0.f, 0.f, 0.f, 0.f};
#pragma unroll
      for (int rr = 1; rr >= 0; --rr) {
        bf16x8 bfm[8];
#pragma unroll
        for (int b = 0; b < 8; ++b) bfm[b] = *(LAS bf16x8*)(xr - 512 * b + 64 * rr);
#pragma unroll
        for (int a = 7; a >= 0; --a)
#pragma unroll
          for (int j = a; j < 8; ++j) ac[j] = __builtin_amdgcn_mfma_f32_16x16x32_bf16(cf[rr * 8 + a], bfm[j - a], ac[j], 0, 0, 0);
      }
#pragma unroll
      for (int j = 0; j < 8; ++j)
        *(LAS f32x4*)(smem + PART_OFF + w * 8192 + (256 * j + 16 * (lane & 15) + 4 * ckg) * 4) = ac[j];
    }
    wait_vm<6>();
    if (lane == 0) {
      const unsigned old = __hip_atomic_fetch_add((LAS unsigned*)(smem + HCNT_OFF), 1u, __ATOMIC_RELAXED, __HIP_MEMORY_SCOPE_WORKGROUP);
      if (old == 7u) __hip_atomic_fetch_add(cnt + (64 + mtile * 4 + (cgrp >> 1)) * 64, 1u, __ATOMIC_RELAXED, __HIP_MEMORY_SCOPE_AGENT);
    }
    {
      LAS char* xr = smem + XR_OFF + 2 * XR_STRIDE + boff + 64 * w;
      const int jA = 7 - qd, jB = qd;
      bf16x8 b0[8], b1[4];
#pragma unroll
      for (int a = 0; a < 8; ++a) { const int bb = jA - a; b0[a] = *(LAS bf16x8*)(xr - 512 * (bb < 0 ? 0 : bb)); }
#pragma unroll
      for (int a = 0; a < 4; ++a) { const int bb = jB - a; b1[a] = *(LAS bf16x8*)(xr - 512 * (bb < 0 ? 0 : bb)); }
      f32x4 c0 = (f32x4){0.f, 0.f, 0.f, 0.f}, c1 = c0;
#pragma unroll
      for (int a = 7; a >= 0; --a) {
        if (a <= jA) c0 = __builtin_amdgcn_mfma_f32_16x16x32_bf16(cf[16 + a], b0[a], c0, 0, 0, 0);
        if (a < 4 && a <= jB) c1 = __builtin_amdgcn_mfma_f32_16x16x32_bf16(cf[16 + a], b1[a < 4 ? a : 0], c1, 0, 0, 0);
      }
      *(LAS f32x4*)(smem + PARTC_OFF + w * 2048 + (16 * (lane & 15) + 4 * ckg) * 4) = c0;
      *(LAS f32x4*)(smem + PARTC_OFF + w * 2048 + 1024 + (16 * (lane & 15) + 4 * ckg) * 4) = c1;
    }
  }
  STAMP(1, 4);
  if (tid == 0) wait_count(cnt + (64 + (mt >> 1) * 4 + ksl) * 64, 8u);
  WAIT_L(0);
  __builtin_amdgcn_s_barrier();
  CFENCE();
  STAMP(1, 5);
  sfor<6>([&](auto kc) ALWAYS_INLINE { h_load(kc); });
  auto finalize = [&](f32x4 s0, f32x4 s1, int im, int ch, int t8, float dd) ALWAYS_INLINE {
    const u32x4 kvv = *(LAS u32x4*)(smem + XR_OFF + im * XR_STRIDE + (2040 - t8) * 2);
    float y[8] = {s0[0], s0[1], s0[2], s0[3], s1[0], s1[1], s1[2], s1[3]};
    float o[8];
#pragma unroll
    for (int e = 0; e < 8; ++e) {
      const int ri = 7 - e;
      const float kvf = (ri & 1) ? bf_hi(kvv[ri >> 1]) : bf_lo(kvv[ri >> 1]);
      o[e] = bfr(y[e]) + bfr(kvf * dd);
    }
    u32x4 ov; ov.x = pk_bf16(o[0], o[1]); ov.y = pk_bf16(o[2], o[3]); ov.z = pk_bf16(o[4], o[5]); ov.w = pk_bf16(o[6], o[7]);
    gstore16_sc1(wT + (size_t)ch * S_ + t8, ov);
  };
  {
    const int im = tid >> 8, t8 = (tid & 255) * 8;
    f32x4 s0 = (f32x4){0.f, 0.f, 0.f, 0.f}, s1 = s0;
#pragma unroll
    for (int ww = 0; ww < 4; ++ww) {
      s0 += *(LAS f32x4*)(smem + PART_OFF + (im * 4 + ww) * 8192 + t8 * 4);
      s1 += *(LAS f32x4*)(smem + PART_OFF + (im * 4 + ww) * 8192 + t8 * 4 + 16);
    }
    finalize(s0, s1, im, chA + im, t8, ddA);
  }
  if (tid >= 448) {
    const int t2 = tid - 448;
    const int tl = t2 >> 5, l8 = (t2 & 31) * 8;
    const int jt = tl ? qd : 7 - qd, t8 = 256 * jt + l8;
    f32x4 s0 = (f32x4){0.f, 0.f, 0.f, 0.f}, s1 = s0;
#pragma unroll
    for (int ww = 0; ww < 8; ++ww) {
      s0 += *(LAS f32x4*)(smem + PARTC_OFF + ww * 2048 + tl * 1024 + l8 * 4);
      s1 += *(LAS f32x4*)(smem + PARTC_OFF + ww * 2048 + tl * 1024 + l8 * 4 + 16);
    }
    finalize(s0, s1, 2, chC, t8, ddC);
  }
  STAMP(1, 6);

  STAMP(2, 0);
  {
  const int kq = lane2 >> 4, qq = (lane2 & 15) >> 2, p4 = lane2 & 3;
  const unsigned rbU2 = lds0 + khf * K2_HALF + (16 * (kq >> 1) + 4 * (kq & 1) + qq) * 128 + ((((qq >> 1) & 1) | ((kq & 1) << 1)) << 5) + p4 * 8;

  bf16x8 wt0, wt1;
  const char* wtb = (const char*)(wT + mt * 128 + ksl * 32);
  f32x4 acc[2][4], keep[4];
#pragma unroll
  for (int t = 0; t < 2; ++t)
#pragma unroll
    for (int dt = 0; dt < 4; ++dt) acc[t][dt] = (f32x4){0.f, 0.f, 0.f, 0.f};

  const int tile = mt * 4 + nt;
  const __amdgpu_buffer_rsrc_t rs = __builtin_amdgcn_make_buffer_rsrc((char*)slab + (size_t)tile * (16 * 18432), 0, 16 * 18432, 0x00020000);
  const bool foreign = tq != ksl;
  auto panel_end = [&](int nd) ALWAYS_INLINE {
#pragma unroll
    for (int dt = 0; dt < 4; ++dt) if (dt < nd) {
      *(LAS f32x4*)(smem + K2_XB_OFF + ((w * 4 + dt) * 64 + lane) * 16) = khf ? acc[0][dt] : acc[1][dt];
      keep[dt] = khf ? acc[1][dt] : acc[0][dt];
      acc[0][dt] = (f32x4){0.f, 0.f, 0.f, 0.f}; acc[1][dt] = (f32x4){0.f, 0.f, 0.f, 0.f};
    }
  };
  auto publish = [&](int p, int nd) ALWAYS_INLINE {
    f32x4 sm[4];
#pragma unroll
    for (int dt = 0; dt < 4; ++dt) if (dt < nd) sm[dt] = keep[dt] + *(LAS f32x4*)(smem + K2_XB_OFF + (((w ^ 4) * 4 + dt) * 64 + lane) * 16);
    if (foreign) {
#pragma unroll
      for (int dt = 0; dt < 4; ++dt) if (dt < nd)
        __builtin_amdgcn_raw_buffer_store_b128(__builtin_bit_cast(u32x4, sm[dt]), rs, (unsigned)(((tq * 4 + ksl) * 18 + khf * 9 + p * 4 + dt) * 1024 + lane * 16), 0, 16);
    } else {
#pragma unroll
      for (int dt = 0; dt < 4; ++dt) if (dt < nd) *(LAS f32x4*)(smem + K2_OWN_OFF + ((khf * 9 + p * 4 + dt) * 64 + lane) * 16) = sm[dt];
    }
  };

  asm volatile("s_waitcnt vmcnt(%6)" : "+v"(hf[0][0]), "+v"(hf[0][1]), "+v"(hf[1][0]), "+v"(hf[1][1]), "+v"(hf[2][0]), "+v"(hf[2][1])
               : "i"(k2_younger(k2_hkey(0, 2), 0, false)) : "memory");
  WAIT_L(0);
  __builtin_amdgcn_s_barrier();
  CFENCE();
  STAMP(2, 1);

  constexpr int PD = 3;
  bf16x8 af[PD + 1];
  unsigned rb[4];
#pragma unroll
  for (int j = 0; j < 4; ++j) rb[j] = rbU2 ^ (j << 5);
#pragma unroll
  for (int g = 0; g < PD; ++g) af[g] = tr_frag((LAS char*)(uintptr_t)rb[g], 0, 1024);
  sfor<162>([&](auto gc) ALWAYS_INLINE {
    constexpr int g = gc;
    constexpr int st = k2_stage(g), f = g - k2_g0(st);
    constexpr int ks = st < 12 ? (st % 6) * 3 + f / 4 : (st - 12) * 9 + f, dt = st < 12 ? f % 4 : 0;
    if constexpr (f == K2_MID && st < 13) {
      constexpr int okey = cmax(k2_dkey(st + 1, 2), st + 1 < 6 ? k2_hkey(st + 1, 2) : -1000);
      constexpr int n0 = k2_younger(okey, 4 * g, false), n1 = k2_younger(okey, 4 * g, true);
      if constexpr (st < 8) { STAMP(0, 2 * st); }
      if constexpr (st == K2_WST) { if (tid == 0) wait_count(cnt + 96 * 64, 256u); }
      if constexpr (n0 != n1) { if (foreign) wait_vm<n1>(); else wait_vm<n0>(); }
      else if constexpr (st == 0) { if (w == 7) wait_vm<n0 + 1>(); else wait_vm<n0>(); }
      else if constexpr (st == K2_WST + 3) {
        asm volatile("s_waitcnt vmcnt(%2)" : "+v"(wt0), "+v"(wt1) : "i"(n0) : "memory");
        *(LAS bf16x8*)(smem + K2_WT_OFF + w * 1024 + lane * 16) = wt0;
        if (w == 0) *(LAS bf16x8*)(smem + K2_WT_OFF + 8192 + lane * 16) = wt1;
      }
      else wait_vm<n0>();
      if constexpr (st + 1 < 6) {
        constexpr int k0 = 3 * (st + 1);
        asm volatile("" : "+v"(hf[k0][0]), "+v"(hf[k0][1]), "+v"(hf[k0 + 1][0]), "+v"(hf[k0 + 1][1]), "+v"(hf[k0 + 2][0]), "+v"(hf[k0 + 2][1])::"memory");
      }
      if constexpr (st == 6 || st == 12) WAIT_L(0);
      __builtin_amdgcn_s_barrier();
      CFENCE();
      if constexpr (st < 8) { STAMP(0, 2 * st + 1); }
      if constexpr (st == 1) { if (tid == 0) __hip_atomic_fetch_add(cnt + 96 * 64, 1u, __ATOMIC_RELAXED, __HIP_MEMORY_SCOPE_AGENT); }
      if constexpr (st == 6 || st == 12) publish(st / 6 - 1, 4);
    }
    if constexpr (st < 12 && ((f + 12 - K2_MID) % 12) % 3 == 0) __builtin_amdgcn_s_setprio(3 - ((f + 12 - K2_MID) % 12) / 3);
    sfor<36>([&](auto dc) ALWAYS_INLINE { if constexpr (k2_dkey(dc / 3 + 2, dc % 3) == 4 * g + 2) piece_load2(dc / 3 + 2, dc % 3); });
    sfor<12>([&](auto hc) ALWAYS_INLINE { if constexpr (k2_hkey(hc / 3 + 2, hc % 3) == 4 * g + 3) h_load(std::integral_constant<int, hc + 6>{}); });
    if constexpr (k2_wkey() == 4 * g + 3) {
      int l2 = lane; asm volatile("" : "+v"(l2));
      gload16_sc1<0>(wt0, wtb, (unsigned)((colA2 + w * 16 + (l2 >> 2)) * (S_ * 2) + (l2 & 3) * 16));
      gload16_sc1<0>(wt1, wtb, (unsigned)((colQ2 + (l2 >> 2)) * (S_ * 2) + (l2 & 3) * 16));
    }
    if constexpr (g + PD < 162) {
      constexpr int g1 = g + PD, st1 = k2_stage(g1), f1 = g1 - k2_g0(st1);
      if constexpr (f1 == 0) {
        if constexpr (st1 < 12) {
          unsigned b2 = rbU2; asm volatile("" : "+v"(b2));
#pragma unroll
          for (int j = 0; j < 4; ++j) { rb[j] = (b2 + (st1 % K2_NSLOT) * K2_SLOT) ^ (j << 5); asm volatile("" : "+v"(rb[j])); }
        } else {
          int l2 = lane; asm volatile("" : "+v"(l2));
          const int kq2 = l2 >> 4;
          rb[0] = lds0 + khf * K2_HALF + (16 * (kq2 >> 1) + 4 * (kq2 & 1) + ((l2 & 15) >> 2)) * 32 + (l2 & 3) * 8 + (st1 % K2_NSLOT) * K2_SLOT;
          asm volatile("" : "+v"(rb[0]));
        }
      }
      if constexpr (st1 < 12) af[g1 % (PD + 1)] = tr_frag((LAS char*)(uintptr_t)rb[f1 % 4], (f1 / 4) * 4096, 1024);
      else af[g1 % (PD + 1)] = tr_frag((LAS char*)(uintptr_t)rb[0], f1 * 1024, 256);
    }
    SCHED0();
#pragma unroll
    for (int t = 0; t < 2; ++t) acc[t][dt] = __builtin_amdgcn_mfma_f32_16x16x32_bf16(af[g % (PD + 1)], hf[ks][t], acc[t][dt], 0, 0, 0);
    SCHED0();
    if constexpr (g == 71 || g == 143) { panel_end(4); SCHED0(); }
  });
  STAMP(2, 2);
  __builtin_amdgcn_s_setprio(0);
  panel_end(1);

  u32x2 xq[3];
  float2 stt[3];
#pragma unroll
  for (int i = 0; i < 3; ++i) {
    const int fi = (w + 8 * i < 18) ? w + 8 * i : w;
    const int t = fi >= 9 ? 1 : 0, dt = fi - 9 * t;
    const int token = mt * 128 + ksl * 32 + t * 16 + (lane & 15);
    xq[i] = *(const u32x2*)(q + (size_t)token * D_ + (dt < 8 ? colA2 + dt * 16 : colQ2) + 4 * (lane >> 4));
    stt[i] = stats[token];
  }
  WAIT_L(0);
  __builtin_amdgcn_s_barrier();
  CFENCE();
  publish(2, 1);
  asm volatile("s_waitcnt vmcnt(0)" ::: "memory");
  __syncthreads();
  STAMP(2, 3);
  if (tid == 0) {
    unsigned* c = cnt + tile * 64;
    __hip_atomic_fetch_add(c, 1u, __ATOMIC_RELAXED, __HIP_MEMORY_SCOPE_AGENT);
    wait_count(c, 4u);
  }
  __syncthreads();
  STAMP(2, 4);
  __builtin_amdgcn_fence(__ATOMIC_ACQUIRE, "wavefront");
  {
    f32x4 pv[3][4];
#pragma unroll
    for (int i = 0; i < 3; ++i) {
      const int fi = (w + 8 * i < 18) ? w + 8 * i : w;
#pragma unroll
      for (int k = 1; k < 4; ++k)
        pv[i][k] = __builtin_bit_cast(f32x4, __builtin_amdgcn_raw_buffer_load_b128(rs, (unsigned)(((ksl * 4 + ((ksl + k) & 3)) * 18 + fi) * 1024 + lane * 16), 0, 16));
      pv[i][0] = *(LAS f32x4*)(smem + K2_OWN_OFF + (fi * 64 + lane) * 16);
    }
#pragma unroll
    for (int i = 0; i < 3; ++i) {
      if (w + 8 * i < 18) {
        const int fi = w + 8 * i;
        const int t = fi >= 9 ? 1 : 0, dt = fi - 9 * t;
        const int tl = t * 16 + (lane & 15), dl = dt * 16 + 4 * (lane >> 4);
        const f32x4 bs = ((pv[i][0] + pv[i][1]) + pv[i][2]) + pv[i][3];
        float o[4];
#pragma unroll
        for (int c = 0; c < 4; ++c) {
          const unsigned xw = (c < 2) ? xq[i].x : xq[i].y;
          const float x = (c & 1) ? bf_hi(xw) : bf_lo(xw);
          const float wy = bf2f(*(LAS u16*)(smem + K2_WT_OFF + (dl + c) * 64 + tl * 2));
          const float xn = bfr(bfr(x - stt[i].x) / stt[i].y);
          const float a = bfr(xn * wy);
          o[c] = bfr(x + a) + bfr(bs[c]);
        }
        u32x2 ov; ov.x = pk_bf16(o[0], o[1]); ov.y = pk_bf16(o[2], o[3]);
        *(u32x2*)(out + (size_t)(mt * 128 + ksl * 32 + tl) * D_ + (dt < 8 ? colA2 + dt * 16 : colQ2) + 4 * (lane >> 4)) = ov;
      }
    }
  }
  STAMP(2, 5); RSTAMP(1, 15); STAMP_FLUSH(1); STAMP_FLUSH(2); STAMP_FLUSH(0); STAMP_FLUSH(3);
  }
}

extern "C" void kernel_launch(void* const* d_in, const int* in_sizes, int n_in,
                              void* d_out, int out_size, void* d_ws, size_t ws_size,
                              hipStream_t stream) {
  const u16* q = (const u16*)d_in[0];
  const u16* k = (const u16*)d_in[1];
  const u16* v = (const u16*)d_in[2];
  const u16* shk = (const u16*)d_in[3];
  const u16* ssmk = (const u16*)d_in[4];
  const u16* ssmd = (const u16*)d_in[5];
  const u16* wg = (const u16*)d_in[7];
  const u16* wu = (const u16*)d_in[8];
  const u16* wd = (const u16*)d_in[9];
  char* ws = (char*)d_ws;
  u16* xfrag = (u16*)(ws + WS_XFRAG);
  float2* stats = (float2*)(ws + WS_STATS);
  u16* kvr = (u16*)(ws + WS_KVR);
  u16* wT = (u16*)(ws + WS_WT);
  u16* h = (u16*)(ws + WS_H);
  float* slab = (float*)(ws + WS_SLAB);
  unsigned* cnt = (unsigned*)(ws + WS_CNT);

  (void)hipFuncSetAttribute((const void*)k1_mlp, hipFuncAttributeMaxDynamicSharedMemorySize, K1_LDS);

  k0_prep<<<128 + 288, 256, 0, stream>>>(q, k, v, shk, xfrag, stats, kvr, cnt);
  k1_mlp<<<256, 512, K1_LDS, stream>>>(xfrag, wg, wu, h, ssmk, ssmd, kvr, wT, wd, slab, cnt, q, stats, (u16*)d_out);
}
```

```cpp
#include <hip/hip_runtime.h>
#include <hip/hip_bf16.h>
#include <stdint.h>
#include <utility>

typedef unsigned short u16;
typedef short bf16x8 __attribute__((ext_vector_type(8)));
typedef short s16x4 __attribute__((ext_vector_type(4)));
typedef float f32x4 __attribute__((ext_vector_type(4)));
typedef float f32x16 __attribute__((ext_vector_type(16)));
typedef unsigned u32x4 __attribute__((ext_vector_type(4)));
typedef unsigned u32x2 __attribute__((ext_vector_type(2)));
typedef __bf16 bf2_t __attribute__((ext_vector_type(2)));
#define LAS __attribute__((address_space(3)))
#define DI __device__ __forceinline__

constexpr int S_ = 2048, D_ = 576, F_ = 4608;

constexpr size_t WS_XFRAG = 0;
constexpr size_t WS_STATS = WS_XFRAG + (size_t)S_ * D_ * 2;
constexpr size_t WS_KVR   = WS_STATS + (size_t)S_ * 8;
constexpr size_t WS_WT    = WS_KVR + (size_t)S_ * D_ * 2;
constexpr size_t WS_H     = WS_WT + (size_t)S_ * D_ * 2;
constexpr size_t WS_SLAB  = WS_H + (size_t)S_ * F_ * 2;
constexpr size_t WS_FLG   = WS_SLAB + (size_t)4 * S_ * D_ * 4;
constexpr int FL_GEN = 0, FL_X = 32, FL_K = FL_X + 512, FL_H = FL_K + 256, FL_W = FL_H + 256, FL_S = FL_W + 256, FL_END = FL_S + 256;
constexpr size_t WS_END   = WS_FLG + FL_END * 8;
typedef unsigned long long u64;

DI float bf_lo(unsigned w) { return __uint_as_float(w << 16); }
DI float bf_hi(unsigned w) { return __uint_as_float(w & 0xffff0000u); }
DI float bf2f(u16 b) { return __uint_as_float(((unsigned)b) << 16); }
DI float bfr(float x) { return (float)(__bf16)x; }
DI unsigned pk_bf16(float lo, float hi) { bf2_t v = {(__bf16)lo, (__bf16)hi}; return __builtin_bit_cast(unsigned, v); }

#define WAIT_V(n) asm volatile("s_waitcnt vmcnt(" #n ")" ::: "memory")
#define WAIT_L(n) asm volatile("s_waitcnt lgkmcnt(" #n ")" ::: "memory")
#define CFENCE() asm volatile("" ::: "memory")
#define STAMP(kid, i)
#define RSTAMP(kid, i)
#define STAMP_DECL
#define STAMP_FLUSH(kid)
#define K0STAMP(i)
#define SCHED0() __builtin_amdgcn_sched_barrier(0)
template <int... I, class Fn> DI void sfor_impl(std::integer_sequence<int, I...>, Fn&& f) { (f(std::integral_constant<int, I>{}), ...); }
template <int N, class Fn> DI void sfor(Fn&& f) { sfor_impl(std::make_integer_sequence<int, N>{}, f); }
#define ALWAYS_INLINE __attribute__((always_inline))
template <int N> DI void wait_vm() { asm volatile("s_waitcnt vmcnt(%0)" ::"i"(N) : "memory"); }

DI void glds4(const void* sbase, unsigned v0, unsigned v1, unsigned v2, unsigned v3, unsigned lds_dst) {
  unsigned keep;
  asm volatile("s_mov_b32 %0, m0\n\ts_mov_b32 m0, %6\n\ts_nop 0\n\t"
               "global_load_lds_dwordx4 %1, %5\n\t"
               "global_load_lds_dwordx4 %2, %5 offset:1024\n\t"
               "global_load_lds_dwordx4 %3, %5 offset:2048\n\t"
               "global_load_lds_dwordx4 %4, %5 offset:3072\n\t"
               "s_mov_b32 m0, %0"
               : "=&s"(keep) : "v"(v0), "v"(v1), "v"(v2), "v"(v3), "s"(sbase), "s"(lds_dst) : "memory");
}
DI void glds3(const void* sbase, unsigned v0, unsigned v1, unsigned v2, unsigned lds_dst) {
  unsigned keep;
  asm volatile("s_mov_b32 %0, m0\n\ts_mov_b32 m0, %5\n\ts_nop 0\n\t"
               "global_load_lds_dwordx4 %1, %4\n\t"
               "global_load_lds_dwordx4 %2, %4 offset:1024\n\t"
               "global_load_lds_dwordx4 %3, %4 offset:2048\n\t"
               "s_mov_b32 m0, %0"
               : "=&s"(keep) : "v"(v0), "v"(v1), "v"(v2), "s"(sbase), "s"(lds_dst) : "memory");
}
DI void glds2(const void* sbase, unsigned v0, unsigned v1, unsigned lds_dst) {
  unsigned keep;
  asm volatile("s_mov_b32 %0, m0\n\ts_mov_b32 m0, %4\n\ts_nop 0\n\t"
               "global_load_lds_dwordx4 %1, %3\n\t"
               "global_load_lds_dwordx4 %2, %3 offset:1024\n\t"
               "s_mov_b32 m0, %0"
               : "=&s"(keep) : "v"(v0), "v"(v1), "s"(sbase), "s"(lds_dst) : "memory");
}
DI void glds1(const void* sbase, unsigned v0, unsigned lds_dst) {
  unsigned keep;
  asm volatile("s_mov_b32 %0, m0\n\ts_mov_b32 m0, %3\n\ts_nop 0\n\t"
               "global_load_lds_dwordx4 %1, %2\n\t"
               "s_mov_b32 m0, %0"
               : "=&s"(keep) : "v"(v0), "s"(sbase), "s"(lds_dst) : "memory");
}

template <bool NT> DI void glds1p(const void* sbase, unsigned v0, unsigned lds_dst) {
  unsigned keep;
  if constexpr (NT)
    asm volatile("s_mov_b32 %0, m0\n\ts_mov_b32 m0, %3\n\ts_nop 0\n\tglobal_load_lds_dwordx4 %1, %2 nt\n\ts_mov_b32 m0, %0"
                 : "=&s"(keep) : "v"(v0), "s"(sbase), "s"(lds_dst) : "memory");
  else
    asm volatile("s_mov_b32 %0, m0\n\ts_mov_b32 m0, %3\n\ts_nop 0\n\tglobal_load_lds_dwordx4 %1, %2\n\ts_mov_b32 m0, %0"
                 : "=&s"(keep) : "v"(v0), "s"(sbase), "s"(lds_dst) : "memory");
}
DI void touch_lines(const void* sbase, unsigned v0, unsigned lds_dst) {
  unsigned keep;
  asm volatile("s_mov_b32 %0, m0\n\ts_mov_b32 m0, %3\n\ts_nop 0\n\tglobal_load_lds_dword %1, %2\n\ts_mov_b32 m0, %0"
               : "=&s"(keep) : "v"(v0), "s"(sbase), "s"(lds_dst) : "memory");
}
template <int OFF> DI void gload16(bf16x8& dst, const void* sbase, unsigned voff) {
  asm volatile("global_load_dwordx4 %0, %1, %2 offset:%3" : "=v"(dst) : "v"(voff), "s"(sbase), "i"(OFF) : "memory");
}
template <int OFF> DI void gload16_sc1(bf16x8& dst, const void* sbase, unsigned voff) {
  asm volatile("global_load_dwordx4 %0, %1, %2 offset:%3 sc1" : "=v"(dst) : "v"(voff), "s"(sbase), "i"(OFF) : "memory");
}
DI void gstore16_sc1(void* p, const u32x4& v) { asm volatile("global_store_dwordx4 %0, %1, off sc1\n\ts_nop 1" ::"v"(p), "v"(v) : "memory"); }
DI void set_flag(u64* f, u64 tag) { __hip_atomic_store(f, tag, __ATOMIC_RELAXED, __HIP_MEMORY_SCOPE_AGENT); }
template <int PER> DI void poll_flags(u64* f, int n, u64 tag, int lane) {
  const int i = lane < n ? lane : 0;
  for (unsigned spins = 0; spins < (1u << 20); ++spins) {
    bool ok = true;
#pragma unroll
    for (int k = 0; k < PER; ++k) ok &= __hip_atomic_load(f + i + 64 * k, __ATOMIC_RELAXED, __HIP_MEMORY_SCOPE_AGENT) == tag;
    if (__all(ok)) break;
    __builtin_amdgcn_s_sleep(1);
  }
}

constexpr int K1_SLOT = 37120;
constexpr int K1_UOFF = 18432 + 128;
constexpr int K1_NSLOT = 4;
constexpr int K1_PF_OFF = K1_NSLOT * K1_SLOT;
constexpr int XR_OFF = K1_PF_OFF + 288, XR_STRIDE = 4608;
constexpr int HZ_OFF = XR_OFF + 3 * XR_STRIDE;
constexpr int K1_LDS = HZ_OFF + 288;
constexpr int PART_OFF = 49152;
constexpr int PARTC_OFF = PART_OFF + 65536;
constexpr int HCNT_OFF = K1_PF_OFF;

DI bf16x8 tr_frag(LAS char* p, int off, int hi_off) {
  s16x4 lo = __builtin_amdgcn_ds_read_tr16_b64_v4i16((LAS s16x4*)(p + off));
  s16x4 hi = __builtin_amdgcn_ds_read_tr16_b64_v4i16((LAS s16x4*)(p + off + hi_off));
  return __builtin_shufflevector(lo, hi, 0, 1, 2, 3, 4, 5, 6, 7);
}

constexpr int K1_MID = 16;
constexpr int k1_xkey(int i) { return i < 9 ? -100 + i : 4 * (1 + 2 * (i - 9)) + 2; }
constexpr int k1_dstep(int d, int n) { return d == 2 ? 8 + 3 * n : d == 3 ? 38 + 3 * n : d == 4 ? 58 + 6 * n : d == 5 ? 94 + 3 * n : 36 * (d - 3) + K1_MID + 3 * n; }
constexpr int k1_dkey(int d, int n) { return d < 4 ? -400 + 10 * d + n : 4 * k1_dstep(d, n) + 1; }
constexpr int k1_cstep(int i) { return i < 7 ? 218 + 4 * i : (i < 14 ? 254 + 4 * (i - 7) : (i < 21 ? 289 + 2 * (i - 14) : 306 + 4 * (i - 21))); }
constexpr int k1_sstep(int c) { return c == 0 ? 143 : (c < 4 ? 144 + 6 * c : 182); }
constexpr int K1_TOUCH = 7 * 36 + K1_MID + 2;
constexpr int k1_younger(int okey, int wkey) {
  int n = 0;
  if (4 * K1_TOUCH + 1 > okey && 4 * K1_TOUCH + 1 < wkey) n += 1;
  for (int i = 0; i < 36; ++i) if (k1_xkey(i) > okey && k1_xkey(i) < wkey) n += 1;
  for (int d = 0; d < 9; ++d) for (int pc = 0; pc < 5; ++pc) if (k1_dkey(d, pc) > okey && k1_dkey(d, pc) < wkey) n += 1;
  for (int i = 0; i < 24; ++i) if (4 * k1_cstep(i) + 2 > okey && 4 * k1_cstep(i) + 2 < wkey) n += 2;
  for (int c = 0; c < 5; ++c) if (4 * k1_sstep(c) + 3 > okey && 4 * k1_sstep(c) + 3 < wkey) n += 1;
  return n;
}
#define HSTORE 1

constexpr int K2_HALF = 12288;
constexpr int K2_SLOT = 2 * K2_HALF;
constexpr int K2_NSLOT = 4;
constexpr int K2_XB_OFF = K2_NSLOT * K2_SLOT;
constexpr int K2_OWN_OFF = K2_XB_OFF + 32768;
constexpr int K2_WT_OFF = K2_OWN_OFF + 18432;
constexpr int K2_LDS = K2_WT_OFF + 9216;
constexpr int K2_MID = 4;
constexpr int k2_g0(int st) { return st <= 12 ? 12 * st : 144 + 9 * (st - 12); }
constexpr int k2_stage(int g) { return g < 144 ? g / 12 : 12 + (g - 144) / 9; }
constexpr int k2_dkey(int d, int n) { return d == 0 ? -300 + n : d == 1 ? -290 + n : 4 * ((d == 2 ? 1 : k2_g0(d - 3) + K2_MID) + n) + 2; }
constexpr int k2_hkey(int j, int m) { return j == 0 ? -250 + m : j == 1 ? -150 + m : 4 * (1 + 5 * (j - 2) + m) + 3; }
constexpr int k2_skey(int p) { return 4 * (k2_g0(6 * p + 6) + K2_MID) + 1; }
constexpr int K2_WST = 9;
constexpr int k2_wkey() { return 4 * (k2_g0(K2_WST) + K2_MID + 4) + 3; }
constexpr int k2_younger(int okey, int wkey, bool stores) {
  int n = 0;
  if (-100 > okey && -100 < wkey) n += 1;
  if (k2_wkey() > okey && k2_wkey() < wkey) n += 2;
  for (int d = 0; d < 14; ++d) for (int pc = 0; pc < 3; ++pc) if (k2_dkey(d, pc) > okey && k2_dkey(d, pc) < wkey) n += 1;
  for (int j = 0; j < 6; ++j) for (int m = 0; m < 3; ++m) if (k2_hkey(j, m) > okey && k2_hkey(j, m) < wkey) n += 2;
  if (stores) for (int p = 0; p < 2; ++p) if (k2_skey(p) > okey && k2_skey(p) < wkey) n += 4;
  return n;
}
constexpr int cmax(int a, int b) { return a > b ? a : b; }

__global__ __launch_bounds__(512, 2) void k_fused(const u16* __restrict__ q, const u16* __restrict__ kk, const u16* __restrict__ vv,
                                                  const u16* __restrict__ shk, const u16* __restrict__ ssmk, const u16* __restrict__ ssmd,
                                                  const u16* __restrict__ wg, const u16* __restrict__ wu, const u16* __restrict__ wd,
                                                  char* ws, u16* __restrict__ out) {
  u16* xfrag = (u16*)(ws + WS_XFRAG);
  u64* stats = (u64*)(ws + WS_STATS);
  u16* kvr = (u16*)(ws + WS_KVR);
  u16* wT = (u16*)(ws + WS_WT);
  u16* hout = (u16*)(ws + WS_H);
  float* slab = (float*)(ws + WS_SLAB);
  u64* flg = (u64*)(ws + WS_FLG);
  extern __shared__ __attribute__((aligned(16))) char smem_g[];
  LAS char* smem = (LAS char*)smem_g;
  const unsigned lds0 = (unsigned)(uintptr_t)smem;
  const int bid = blockIdx.x, tid = threadIdx.x;
  const int w = __builtin_amdgcn_readfirstlane(tid >> 6), lane = tid & 63;
  const int xcd = bid & 7, idx = bid >> 3;
  STAMP_DECL; RSTAMP(1, 14); STAMP(1, 0);
  const u64 genv = __hip_atomic_load(flg + FL_GEN, __ATOMIC_RELAXED, __HIP_MEMORY_SCOPE_AGENT);
  u64 tag;
  auto make_tag = [&]() ALWAYS_INLINE {
    tag = (((u64)(unsigned)__builtin_amdgcn_readfirstlane((unsigned)(genv >> 32)) << 32) | (unsigned)__builtin_amdgcn_readfirstlane((unsigned)genv)) + 1ull;
  };
  const int ksl = xcd >> 1, mt = (xcd & 1) * 8 + (idx >> 2), nt = idx & 3;
  const int tq = w & 3, khf = w >> 2;
  const int kbase = ksl * 1152 + khf * 576;
  const int colA2 = 128 * nt, colQ2 = 512 + 16 * nt;
  const int mtile = (xcd & 1) * 4 + (idx & 3);
  const int cgrp = (xcd >> 1) * 2 + (idx >> 4), cq = (idx >> 2) & 3;
  const int colA = 64 * (9 * cgrp + 2 * cq), colQ = 64 * (9 * cgrp + 8) + 16 * cq;

  const int grp = bid >> 2, qd = bid & 3;
  const int chA = grp * 9 + 2 * qd, chC = grp * 9 + 8;
  float ddA, ddC;
  {
    const int c1 = chA + (w >> 2);
    const unsigned d1 = ((const unsigned*)ssmd)[c1 >> 1], d2 = ((const unsigned*)ssmd)[chC >> 1];
    ddA = (c1 & 1) ? bf_hi(d1) : bf_lo(d1);
    ddC = (chC & 1) ? bf_hi(d2) : bf_lo(d2);
  }

  const int wq = w & 3, part = w >> 2;
  const char* wbase = (const char*)(part ? wu : wg);
  unsigned voff[5];
#pragma unroll
  for (int n = 0; n < 5; ++n) {
    const int o = (n < 4) ? ((wq * 4 + n) * 1024 + lane * 16) : (16384 + wq * 512 + (lane & 31) * 16);
    const int k = o >> 7, pc = (o >> 4) & 7;
    const int j = (pc >> 1) ^ (((k >> 1) & 1) | (part << 1));
    voff[n] = (unsigned)(k * (F_ * 2) + (2 * j + (pc & 1)) * 16);
  }
  const unsigned ldst = lds0 + part * K1_UOFF + wq * 4096;
  const unsigned ldst4 = lds0 + part * K1_UOFF + 16384 + wq * 512;

  auto piece_load = [&](int st, int n) ALWAYS_INLINE {
    const int slot = st % K1_NSLOT;
    const unsigned dst = (n < 4 ? ldst + n * 1024 : ldst4) + slot * K1_SLOT;
    if (st != 4) {
      const int ks = st < 4 ? st : st - 5;
      const char* g = wbase + (size_t)ks * 144 * (F_ * 2) + (colA + (st > 4 ? 64 : 0)) * 2;
      if (n < 4 || lane < 32) glds1p<false>(g, voff[n], dst);
    } else {
      int l2 = lane; asm volatile("" : "+v"(l2));
      const int o = (n < 4) ? ((wq * 4 + n) * 1024 + l2 * 16) : (16384 + wq * 512 + (l2 & 31) * 16);
      const unsigned vq = (unsigned)((o >> 5) * (F_ * 2) + ((o >> 4) & 1) * 16);
      if (n < 4 || lane < 32) glds1p<false>(wbase + colQ * 2, vq, dst);
    }
  };

  bf16x8 xf[36];
  const char* xb = (const char*)xfrag + (size_t)(mtile * 8 + w) * 36 * 1024;
  const unsigned xv = lane * 16;
  auto x_load = [&](auto ic) ALWAYS_INLINE {
    constexpr int i = ic;
    gload16_sc1<(i & 3) * 1024>(xf[i], xb + (i >> 2) * 4096, xv);
  };
  {
    LAS u16* tile = (LAS u16*)(smem + XR_OFF);
    const int tt = bid >> 3, ct = bid & 7, t0 = tt * 64, c0 = ct * 72;
    if (w < 2) {
      const int token = mtile * 256 + (cgrp * 4 + cq) * 8 + w * 4 + (lane >> 4), j = lane & 15;
      u32x2 d[9];
#pragma unroll
      for (int i = 0; i < 9; ++i) d[i] = *(const u32x2*)(q + (size_t)token * D_ + (j + 16 * i) * 4);
      float sm = 0.f;
#pragma unroll
      for (int i = 0; i < 9; ++i) sm += (bf_lo(d[i].x) + bf_hi(d[i].x)) + (bf_lo(d[i].y) + bf_hi(d[i].y));
      sm += __shfl_xor(sm, 1); sm += __shfl_xor(sm, 2); sm += __shfl_xor(sm, 4); sm += __shfl_xor(sm, 8);
      const float mean = sm * (1.0f / 576.0f);
      float vs = 0.f;
#pragma unroll
      for (int i = 0; i < 9; ++i) {
        float a = bf_lo(d[i].x) - mean, b = bf_hi(d[i].x) - mean, c = bf_lo(d[i].y) - mean, e = bf_hi(d[i].y) - mean;
        vs += (a * a + b * b) + (c * c + e * e);
      }
      vs += __shfl_xor(vs, 1); vs += __shfl_xor(vs, 2); vs += __shfl_xor(vs, 4); vs += __shfl_xor(vs, 8);
      const float mb = bfr(mean), vb = bfr(vs * (1.0f / 576.0f));
      const float sd = bfr(sqrtf(bfr(vb + 1e-5f)));
      if (j == 0) __hip_atomic_store(stats + token, (u64)__float_as_uint(mb) | ((u64)__float_as_uint(sd) << 32), __ATOMIC_RELAXED, __HIP_MEMORY_SCOPE_AGENT);
      u32x2 o[10];
#pragma unroll
      for (int i = 0; i < 9; ++i) {
        float a = bfr(bf_lo(d[i].x) - mb) / sd, b = bfr(bf_hi(d[i].x) - mb) / sd, c = bfr(bf_lo(d[i].y) - mb) / sd, e = bfr(bf_hi(d[i].y) - mb) / sd;
        o[i].x = pk_bf16(a, b); o[i].y = pk_bf16(c, e);
      }
      o[9] = o[8];
      const bool odd = j & 1;
      char* xo = (char*)xfrag + (size_t)((token >> 5) * 36 * 64 + (token & 31)) * 16;
#pragma unroll
      for (int i2 = 0; i2 < 5; ++i2) {
        const u32x2 mine = odd ? o[2 * i2 + 1] : o[2 * i2], give = odd ? o[2 * i2] : o[2 * i2 + 1];
        u32x2 got; got.x = __shfl_xor(give.x, 1); got.y = __shfl_xor(give.y, 1);
        const u32x4 v = odd ? (u32x4){got.x, got.y, mine.x, mine.y} : (u32x4){mine.x, mine.y, got.x, got.y};
        const int i = 2 * i2 + (odd ? 1 : 0), p16 = (j >> 1) + 8 * i;
        if (i < 9) gstore16_sc1(xo + ((p16 >> 1) * 64 + (p16 & 1) * 32) * 16, v);
      }
      make_tag();
      asm volatile("s_waitcnt vmcnt(0)" ::: "memory");
      if (lane == 0) set_flag(flg + FL_X + mtile * 64 + (cgrp * 4 + cq) * 2 + w, tag);
#pragma unroll
      for (int n = 0; n < 5; ++n) piece_load(0, n);
#pragma unroll
      for (int n = 0; n < 5; ++n) piece_load(1, n);
    } else {
#pragma unroll
      for (int n = 0; n < 5; ++n) piece_load(0, n);
#pragma unroll
      for (int n = 0; n < 5; ++n) piece_load(1, n);
#pragma unroll
      for (int it = 0; it < 2; ++it) {
        const int item = tid - 128 + 384 * it;
        if (item < 576) {
          const int tl = item / 9, cg = item - tl * 9, t = t0 + tl;
          u32x4 w4[4], kr[4];
#pragma unroll
          for (int i = 0; i < 4; ++i) w4[i] = *(const u32x4*)(shk + (size_t)(c0 + 8 * cg) * 4 + i * 8);
#pragma unroll
          for (int i = 0; i < 4; ++i) {
            const int ts = t + i - 3;
            kr[i] = *(const u32x4*)(kk + (size_t)(ts < 0 ? 0 : ts) * D_ + c0 + 8 * cg);
            if (ts < 0) kr[i] = (u32x4){0u, 0u, 0u, 0u};
          }
          const u32x4 vr = *(const u32x4*)(vv + (size_t)t * D_ + c0 + 8 * cg);
#pragma unroll
          for (int e = 0; e < 8; ++e) {
            const unsigned wa = w4[e >> 1][(e & 1) * 2], wb = w4[e >> 1][(e & 1) * 2 + 1];
            const float w0 = bf_lo(wa), w1 = bf_hi(wa), w2 = bf_lo(wb), w3 = bf_hi(wb);
            float kq4[4];
#pragma unroll
            for (int i = 0; i < 4; ++i) kq4[i] = (e & 1) ? bf_hi(kr[i][e >> 1]) : bf_lo(kr[i][e >> 1]);
            float ks = bfr(kq4[0] * w3);
            ks = bfr(ks + bfr(kq4[1] * w2));
            ks = bfr(ks + bfr(kq4[2] * w1));
            ks = bfr(ks + bfr(kq4[3] * w0));
            const float vf = (e & 1) ? bf_hi(vr[e >> 1]) : bf_lo(vr[e >> 1]);
            tile[(8 * cg + e) * 72 + (63 - tl)] = (u16)(pk_bf16(ks * vf, 0.f) & 0xffffu);
          }
        }
      }
      make_tag();
    }
    WAIT_L(0);
    __builtin_amdgcn_s_barrier();
    CFENCE();
#pragma unroll
    for (int it = 0; it < 2; ++it) {
      const int pc = tid + 512 * it;
      if (pc < 576) {
        const int ch = pc >> 3, pp = pc & 7;
        const u32x4 o = *(LAS u32x4*)(tile + ch * 72 + pp * 8);
        gstore16_sc1(kvr + (size_t)(c0 + ch) * S_ + (1984 - t0) + pp * 8, o);
      }
    }
#pragma unroll
    for (int n = 0; n < 5; ++n) piece_load(2, n);
#pragma unroll
    for (int n = 0; n < 5; ++n) piece_load(3, n);
    STAMP(1, 7);
    if (w == 0) poll_flags<1>(flg + FL_X + mtile * 64, 64, tag, lane);
    __builtin_amdgcn_s_barrier();
    CFENCE();
    STAMP(1, 8);
  }
  sfor<9>([&](auto ic) ALWAYS_INLINE { x_load(ic); });

  const int kh = lane >> 5, prt = (lane >> 4) & 1, qq = (lane & 15) >> 2, p4 = lane & 3;
  const unsigned rbU = lds0 + (8 * kh + qq) * 128 + prt * K1_UOFF + ((((qq >> 1) & 1) | (prt << 1)) << 5) + p4 * 8;

  f32x16 acc[4];
  const f32x16 zero16 = {0.f, 0.f, 0.f, 0.f, 0.f, 0.f, 0.f, 0.f, 0.f, 0.f, 0.f, 0.f, 0.f, 0.f, 0.f, 0.f};

  const int token = mtile * 256 + w * 32 + (lane & 31);
  char* hb = (char*)hout + ((size_t)(token >> 4) * 144 * 64 + (lane >> 5) * 16 + (token & 15)) * 16;
  auto h_word = [&](const f32x16& a, int i) ALWAYS_INLINE -> unsigned {
    return pk_bf16(bfr(a[2 * i]) * bfr(a[2 * i + 8]), bfr(a[2 * i + 1]) * bfr(a[2 * i + 9]));
  };
  auto h_store = [&](const u32x4& o, int c16) ALWAYS_INLINE {
    gstore16_sc1(hb + ((size_t)(c16 >> 1) * 64 + (c16 & 1) * 32) * 16, o);
  };
  u32x4 ho;

  const char* hpA = (const char*)(ssmk + (size_t)(chA + (w >> 2)) * S_ + 64 * (w & 3));
  const char* hpC = (const char*)(ssmk + (size_t)chC * S_ + 32 * w);
  const bool zA = (w & 3) == 0, zC = w == 0;
  const char* pf_sk = (const char*)ssmk;
  const char* pf_wd = (const char*)wd + (size_t)(xcd >> 1) * 1152 * 1152;
  const char* kvb = (const char*)kvr;
  u32x4 caf[24];
  unsigned cax[24];
  int cvo = 0, cvx = 0;

  wait_vm<19>();
  __builtin_amdgcn_s_barrier();
  CFENCE();
  if (tid == 0) set_flag(flg + FL_K + (bid & 7) * 32 + (bid >> 3), tag);
  STAMP(1, 1);

  constexpr int PD = 3, MID = K1_MID;
  bf16x8 af[PD + 1];
  unsigned rb[4];
#pragma unroll
  for (int j = 0; j < 4; ++j) rb[j] = rbU ^ (j << 5);
#pragma unroll
  for (int g = 0; g < PD; ++g) af[g] = tr_frag((LAS char*)(uintptr_t)rb[g], 0, 512);
  sfor<324>([&](auto gc) ALWAYS_INLINE {
    constexpr int g = gc;
    constexpr int st = g / 36, f = g - st * 36;
    constexpr int s = st != 4 ? f / 4 : f, c = st != 4 ? f % 4 : 0;
    constexpr int xi = st < 4 ? st * 9 + s : (st == 4 ? s : (st - 5) * 9 + s);
    constexpr int ai = st < 4 ? c : (st == 4 ? 0 : (c + 1) & 3);
    constexpr bool first = (st == 0 || st == 5) ? s == 0 : (st == 4 && f == 0);
    if constexpr (f == MID) {
      if constexpr (st < 8) { STAMP(3, 2 * st); }
      if constexpr (st < 8) wait_vm<k1_younger(k1_dkey(st + 1, 4), 4 * g)>();
      else { wait_vm<k1_younger(4 * (7 * 36 + MID + 1) + 1, 4 * g)>(); if (w <= 3) WAIT_L(0); }
      __builtin_amdgcn_s_barrier();
      CFENCE();
      if constexpr (st < 8) { STAMP(3, 2 * st + 1); }
    }
    if constexpr (((f + 36 - MID) % 36) % 9 == 0) __builtin_amdgcn_s_setprio(3 - ((f + 36 - MID) % 36) / 9);
    sfor<25>([&](auto dc) ALWAYS_INLINE { if constexpr (k1_dkey(dc / 5 + 4, dc % 5) == 4 * g + 1) piece_load(dc / 5 + 4, dc % 5); });
    sfor<27>([&](auto ic) ALWAYS_INLINE { if constexpr (k1_xkey(ic + 9) == 4 * g + 2) x_load(std::integral_constant<int, ic + 9>{}); });
    if constexpr (g == 7 * 36 + MID + 1) {
      if (w < 3) {
        const int ch = (w == 2) ? chC : chA + w;
        const unsigned lv = lane * 16;
        poll_flags<1>(flg + FL_K + (grp >> 3) * 32, 32, tag, lane);
        const __amdgpu_buffer_rsrc_t rk = __builtin_amdgcn_make_buffer_rsrc((char*)kvb + (size_t)ch * (S_ * 2), 0, S_ * 2, 0x00020000);
#pragma unroll
        for (int i = 0; i < 4; ++i)
          *(LAS u32x4*)(smem + XR_OFF + w * XR_STRIDE + i * 1024 + lv) = __builtin_amdgcn_raw_buffer_load_b128(rk, lv + i * 1024, 0, 16);
        if (lane < 4) glds1(pf_sk + (size_t)ch * (S_ * 2), lv, lds0 + HZ_OFF + w * 96 + 32);
      } else if (w == 3) {
        if (lane < 32) {
#pragma unroll
          for (int im = 0; im < 3; ++im) *(LAS u32x4*)(smem + XR_OFF + im * XR_STRIDE + 4096 + lane * 16) = (u32x4){0u, 0u, 0u, 0u};
        } else if (lane < 38) {
          const int im = (lane - 32) >> 1, hh = lane & 1;
          *(LAS u32x4*)(smem + HZ_OFF + im * 96 + hh * 16) = (u32x4){0u, 0u, 0u, 0u};
        } else if (lane == 38) *(LAS unsigned*)(smem + HCNT_OFF) = 0u;
      }
    }
    if constexpr (g == K1_TOUCH) {
      int l2 = lane; asm volatile("" : "+v"(l2));
      const unsigned line = (unsigned)(w * 64 + l2 < 324 ? idx * 324 + w * 64 + l2 : idx * 324);
      touch_lines(pf_wd, line * 128u, lds0 + K1_PF_OFF + 16);
    }
    sfor<24>([&](auto cc) ALWAYS_INLINE {
      constexpr int ci = cc;
      if constexpr (k1_cstep(ci) == g) {
        if constexpr (ci == 0) {
          int l2 = lane; asm volatile("" : "+v"(l2));
          const int e = (l2 & 15) - 15 + 8 * (l2 >> 4);
          cvo = (e & ~1) * 2; cvx = cvo + ((e & 1) ? 16 : 0);
        }
        constexpr int cb = ci < 16 ? 64 * (ci >> 3) + 512 * (ci & 7) : 512 * (ci - 16);
        const char* hp = (ci < 16 ? hpA : hpC) + cb + ((ci == 0 && zA) || (ci == 16 && zC) ? 32 : 0);
        __builtin_memcpy(&caf[ci], __builtin_assume_aligned(hp + cvo, 4), 16);
        cax[ci] = *(const unsigned*)(hp + cvx);
      }
    });
    if constexpr (g + PD < 324) {
      constexpr int g1 = g + PD, st1 = g1 / 36, f1 = g1 - st1 * 36;
      if constexpr (f1 == 0) {
        if constexpr (st1 != 4) {
          unsigned b2 = rbU; asm volatile("" : "+v"(b2));
#pragma unroll
          for (int j = 0; j < 4; ++j) { rb[j] = (b2 + (st1 % K1_NSLOT) * K1_SLOT) ^ (j << 5); asm volatile("" : "+v"(rb[j])); }
        } else {
          int l2 = lane; asm volatile("" : "+v"(l2));
          rb[0] = lds0 + (8 * (l2 >> 5) + ((l2 & 15) >> 2)) * 32 + ((l2 >> 4) & 1) * K1_UOFF + (l2 & 3) * 8 + (st1 % K1_NSLOT) * K1_SLOT;
          asm volatile("" : "+v"(rb[0]));
        }
      }
      if constexpr (st1 != 4) af[g1 % (PD + 1)] = tr_frag((LAS char*)(uintptr_t)rb[f1 % 4], (f1 / 4) * 2048, 512);
      else af[g1 % (PD + 1)] = tr_frag((LAS char*)(uintptr_t)rb[0], f1 * 512, 128);
    }
    if constexpr (st < 4 && c == 0) {
      asm volatile("s_waitcnt vmcnt(%1)" : "+v"(xf[xi]) : "i"(k1_younger(k1_xkey(xi), 4 * g + 3)) : "memory");
    }
    SCHED0();
    acc[ai] = __builtin_amdgcn_mfma_f32_32x32x16_bf16(af[g % (PD + 1)], xf[xi], first ? zero16 : acc[ai], 0, 0, 0);
    SCHED0();
    if constexpr (g == 143) {
#pragma unroll
      for (int i = 0; i < 4; ++i) ho[i] = h_word(acc[0], i);
      h_store(ho, colA >> 4);
      SCHED0();
    }
    if constexpr (g > 144 && g <= 162) {
      constexpr int cc = (g - 145) / 6 + 1, pi = (g - 145) % 6;
      if constexpr (pi < 4) ho[pi] = h_word(acc[cc], pi);
      if constexpr (pi == 5) h_store(ho, (colA >> 4) + cc);
      SCHED0();
    }
    if constexpr (g >= 180 && g <= 182) {
      if constexpr (g == 180) { ho[0] = h_word(acc[0], 0); ho[1] = h_word(acc[0], 1); }
      if constexpr (g == 181) { ho[2] = h_word(acc[0], 2); ho[3] = h_word(acc[0], 3); }
      if constexpr (g == 182) h_store(ho, colQ >> 4);
      SCHED0();
    }
  });
  const char* wbase2 = (const char*)wd + (size_t)kbase * (D_ * 2);
  unsigned voff2[3];
  int lane2 = lane; asm volatile("" : "+v"(lane2));
#pragma unroll
  for (int n = 0; n < 3; ++n) {
    const int o = (tq * 3 + n) * 1024 + lane2 * 16;
    const int k = o >> 7, pc = (o >> 4) & 7;
    const int j = (pc >> 1) ^ ((k >> 1) & 3);
    voff2[n] = (unsigned)(k * (D_ * 2) + (2 * j + (pc & 1)) * 16);
  }
  const unsigned ldst2 = lds0 + khf * K2_HALF;
  auto piece_load2 = [&](int st, int n) ALWAYS_INLINE {
    const int slot = st % K2_NSLOT;
    if (st < 12) {
      const int p = st / 6, ks = st - p * 6;
      const char* g = wbase2 + (size_t)ks * 96 * (D_ * 2) + (colA2 + p * 64) * 2;
      glds1p<false>(g, voff2[n], ldst2 + slot * K2_SLOT + tq * 3072 + n * 1024);
    } else {
      int l2 = lane; asm volatile("" : "+v"(l2));
      const char* g = wbase2 + (size_t)(st - 12) * 288 * (D_ * 2) + colQ2 * 2;
      const int o = n < 2 ? tq * 2048 + n * 1024 + l2 * 16 : 8192 + tq * 256 + (l2 & 15) * 16;
      const unsigned v0 = (unsigned)((o >> 5) * (D_ * 2) + ((o >> 4) & 1) * 16);
      if (n < 2 || l2 < 16) glds1p<false>(g, v0, ldst2 + slot * K2_SLOT + (n < 2 ? tq * 2048 + n * 1024 : 8192 + tq * 256));
    }
  };
  const char* hbase = (const char*)hout + ((size_t)(mt * 8 + tq * 2) * 144 * 64 + (size_t)(kbase >> 5) * 64) * 16;
  const unsigned hv0 = lane2 * 16, hv1 = lane2 * 16 + 144 * 64 * 16;
  bf16x8 hf[18][2];
  auto h_load = [&](auto kc) ALWAYS_INLINE {
    constexpr int k = kc;
    const char* g = hbase + (k >> 2) * 4096;
    gload16_sc1<(k & 3) * 1024>(hf[k][0], g, hv0);
    gload16_sc1<(k & 3) * 1024>(hf[k][1], g, hv1);
  };
  STAMP(1, 2);
  __builtin_amdgcn_s_setprio(0);
  bf16x8 cf[24];
  {
    const unsigned sh = ((lane + 1) & 1) * 2;
#pragma unroll
    for (int i = 0; i < 24; ++i) {
      u32x4 r;
      r.x = __builtin_amdgcn_alignbyte(caf[i].y, caf[i].x, sh);
      r.y = __builtin_amdgcn_alignbyte(caf[i].z, caf[i].y, sh);
      r.z = __builtin_amdgcn_alignbyte(caf[i].w, caf[i].z, sh);
      r.w = __builtin_amdgcn_alignbyte(cax[i], caf[i].w, sh);
      cf[i] = __builtin_bit_cast(bf16x8, r);
      asm volatile("" : "+v"(cf[i]));
    }
  }

#pragma unroll
  for (int cc = 0; cc < 4; ++cc) {
    u32x4 o;
#pragma unroll
    for (int i = 0; i < 4; ++i) o[i] = h_word(acc[(cc + 1) & 3], i);
    h_store(o, (colA >> 4) + 4 + cc);
  }

  __builtin_amdgcn_s_barrier();
  CFENCE();
#pragma unroll
  for (int n = 0; n < 3; ++n) piece_load2(0, n);
#pragma unroll
  for (int n = 0; n < 3; ++n) piece_load2(1, n);

  STAMP(1, 3);
  const int ct0 = lane & 15, ckg = lane >> 4;
  {
    const int boff = (2032 - 16 * (lane & 15) + 8 * ckg) * 2;
    auto head_frag = [&](int im) ALWAYS_INLINE -> bf16x8 {
      bf16x8 r;
      LAS u16* hz = (LAS u16*)(smem + HZ_OFF + im * 96) + 16 + (ct0 - 15 + 8 * ckg);
#pragma unroll
      for (int j = 0; j < 8; ++j) r[j] = (short)hz[j];
      return r;
    };
    if (zA) cf[0] = head_frag(w >> 2);
    if (zC) cf[16] = head_frag(2);
    {
      const int im = w >> 2;
      LAS char* xr = smem + XR_OFF + im * XR_STRIDE + boff + 128 * (w & 3);
      f32x4 ac[8];
#pragma unroll
      for (int j = 0; j < 8; ++j) ac[j] = (f32x4){0.f, 0.f, 0.f, 0.f};
#pragma unroll
      for (int rr = 1; rr >= 0; --rr) {
        bf16x8 bfm[8];
#pragma unroll
        for (int b = 0; b < 8; ++b) bfm[b] = *(LAS bf16x8*)(xr - 512 * b + 64 * rr);
#pragma unroll
        for (int a = 7; a >= 0; --a)
#pragma unroll
          for (int j = a; j < 8; ++j) ac[j] = __builtin_amdgcn_mfma_f32_16x16x32_bf16(cf[rr * 8 + a], bfm[j - a], ac[j], 0, 0, 0);
      }
#pragma unroll
      for (int j = 0; j < 8; ++j)
        *(LAS f32x4*)(smem + PART_OFF + w * 8192 + (256 * j + 16 * (lane & 15) + 4 * ckg) * 4) = ac[j];
    }
    wait_vm<6>();
    if (lane == 0) {
      const unsigned old = __hip_atomic_fetch_add((LAS unsigned*)(smem + HCNT_OFF), 1u, __ATOMIC_RELAXED, __HIP_MEMORY_SCOPE_WORKGROUP);
      if (old == 7u) set_flag(flg + FL_H + (mtile * 4 + (cgrp >> 1)) * 8 + (cgrp & 1) * 4 + cq, tag);
    }
    {
      LAS char* xr = smem + XR_OFF + 2 * XR_STRIDE + boff + 64 * w;
      const int jA = 7 - qd, jB = qd;
      bf16x8 b0[8], b1[4];
#pragma unroll
      for (int a = 0; a < 8; ++a) { const int bb = jA - a; b0[a] = *(LAS bf16x8*)(xr - 512 * (bb < 0 ? 0 : bb)); }
#pragma unroll
      for (int a = 0; a < 4; ++a) { const int bb = jB - a; b1[a] = *(LAS bf16x8*)(xr - 512 * (bb < 0 ? 0 : bb)); }
      f32x4 c0 = (f32x4){0.f, 0.f, 0.f, 0.f}, c1 = c0;
#pragma unroll
      for (int a = 7; a >= 0; --a) {
        if (a <= jA) c0 = __builtin_amdgcn_mfma_f32_16x16x32_bf16(cf[16 + a], b0[a], c0, 0, 0, 0);
        if (a < 4 && a <= jB) c1 = __builtin_amdgcn_mfma_f32_16x16x32_bf16(cf[16 + a], b1[a < 4 ? a : 0], c1, 0, 0, 0);
      }
      *(LAS f32x4*)(smem + PARTC_OFF + w * 2048 + (16 * (lane & 15) + 4 * ckg) * 4) = c0;
      *(LAS f32x4*)(smem + PARTC_OFF + w * 2048 + 1024 + (16 * (lane & 15) + 4 * ckg) * 4) = c1;
    }
  }
  STAMP(1, 4);
  if (w == 0) poll_flags<1>(flg + FL_H + ((mt >> 1) * 4 + ksl) * 8, 8, tag, lane);
  WAIT_L(0);
  __builtin_amdgcn_s_barrier();
  CFENCE();
  STAMP(1, 5);
  sfor<6>([&](auto kc) ALWAYS_INLINE { h_load(kc); });
  auto finalize = [&](f32x4 s0, f32x4 s1, int im, int ch, int t8, float dd) ALWAYS_INLINE {
    const u32x4 kvv = *(LAS u32x4*)(smem + XR_OFF + im * XR_STRIDE + (2040 - t8) * 2);
    float y[8] = {s0[0], s0[1], s0[2], s0[3], s1[0], s1[1], s1[2], s1[3]};
    float o[8];
#pragma unroll
    for (int e = 0; e < 8; ++e) {
      const int ri = 7 - e;
      const float kvf = (ri & 1) ? bf_hi(kvv[ri >> 1]) : bf_lo(kvv[ri >> 1]);
      o[e] = bfr(y[e]) + bfr(kvf * dd);
    }
    u32x4 ov; ov.x = pk_bf16(o[0], o[1]); ov.y = pk_bf16(o[2], o[3]); ov.z = pk_bf16(o[4], o[5]); ov.w = pk_bf16(o[6], o[7]);
    gstore16_sc1(wT + (size_t)ch * S_ + t8, ov);
  };
  {
    const int im = tid >> 8, t8 = (tid & 255) * 8;
    f32x4 s0 = (f32x4){0.f, 0.f, 0.f, 0.f}, s1 = s0;
#pragma unroll
    for (int ww = 0; ww < 4; ++ww) {
      s0 += *(LAS f32x4*)(smem + PART_OFF + (im * 4 + ww) * 8192 + t8 * 4);
      s1 += *(LAS f32x4*)(smem + PART_OFF + (im * 4 + ww) * 8192 + t8 * 4 + 16);
    }
    finalize(s0, s1, im, chA + im, t8, ddA);
  }
  if (tid >= 448) {
    const int t2 = tid - 448;
    const int tl = t2 >> 5, l8 = (t2 & 31) * 8;
    const int jt = tl ? qd : 7 - qd, t8 = 256 * jt + l8;
    f32x4 s0 = (f32x4){0.f, 0.f, 0.f, 0.f}, s1 = s0;
#pragma unroll
    for (int ww = 0; ww < 8; ++ww) {
      s0 += *(LAS f32x4*)(smem + PARTC_OFF + ww * 2048 + tl * 1024 + l8 * 4);
      s1 += *(LAS f32x4*)(smem + PARTC_OFF + ww * 2048 + tl * 1024 + l8 * 4 + 16);
    }
    finalize(s0, s1, 2, chC, t8, ddC);
  }
  STAMP(1, 6);

  STAMP(2, 0);
  {
  const int kq = lane2 >> 4, qq = (lane2 & 15) >> 2, p4 = lane2 & 3;
  const unsigned rbU2 = lds0 + khf * K2_HALF + (16 * (kq >> 1) + 4 * (kq & 1) + qq) * 128 + ((((qq >> 1) & 1) | ((kq & 1) << 1)) << 5) + p4 * 8;

  bf16x8 wt0, wt1;
  const char* wtb = (const char*)(wT + mt * 128 + ksl * 32);
  f32x4 acc[2][4], keep[4];
#pragma unroll
  for (int t = 0; t < 2; ++t)
#pragma unroll
    for (int dt = 0; dt < 4; ++dt) acc[t][dt] = (f32x4){0.f, 0.f, 0.f, 0.f};

  const int tile = mt * 4 + nt;
  const __amdgpu_buffer_rsrc_t rs = __builtin_amdgcn_make_buffer_rsrc((char*)slab + (size_t)tile * (16 * 18432), 0, 16 * 18432, 0x00020000);
  const bool foreign = tq != ksl;
  auto panel_end = [&](int nd) ALWAYS_INLINE {
#pragma unroll
    for (int dt = 0; dt < 4; ++dt) if (dt < nd) {
      *(LAS f32x4*)(smem + K2_XB_OFF + ((w * 4 + dt) * 64 + lane) * 16) = khf ? acc[0][dt] : acc[1][dt];
      keep[dt] = khf ? acc[1][dt] : acc[0][dt];
      acc[0][dt] = (f32x4){0.f, 0.f, 0.f, 0.f}; acc[1][dt] = (f32x4){0.f, 0.f, 0.f, 0.f};
    }
  };
  auto publish = [&](int p, int nd) ALWAYS_INLINE {
    f32x4 sm[4];
#pragma unroll
    for (int dt = 0; dt < 4; ++dt) if (dt < nd) sm[dt] = keep[dt] + *(LAS f32x4*)(smem + K2_XB_OFF + (((w ^ 4) * 4 + dt) * 64 + lane) * 16);
    if (foreign) {
#pragma unroll
      for (int dt = 0; dt < 4; ++dt) if (dt < nd)
        __builtin_amdgcn_raw_buffer_store_b128(__builtin_bit_cast(u32x4, sm[dt]), rs, (unsigned)(((tq * 4 + ksl) * 18 + khf * 9 + p * 4 + dt) * 1024 + lane * 16), 0, 16);
    } else {
#pragma unroll
      for (int dt = 0; dt < 4; ++dt) if (dt < nd) *(LAS f32x4*)(smem + K2_OWN_OFF + ((khf * 9 + p * 4 + dt) * 64 + lane) * 16) = sm[dt];
    }
  };

  asm volatile("s_waitcnt vmcnt(%6)" : "+v"(hf[0][0]), "+v"(hf[0][1]), "+v"(hf[1][0]), "+v"(hf[1][1]), "+v"(hf[2][0]), "+v"(hf[2][1])
               : "i"(k2_younger(k2_hkey(0, 2), 0, false)) : "memory");
  WAIT_L(0);
  __builtin_amdgcn_s_barrier();
  CFENCE();
  STAMP(2, 1);

  constexpr int PD = 3;
  bf16x8 af[PD + 1];
  unsigned rb[4];
#pragma unroll
  for (int j = 0; j < 4; ++j) rb[j] = rbU2 ^ (j << 5);
#pragma unroll
  for (int g = 0; g < PD; ++g) af[g] = tr_frag((LAS char*)(uintptr_t)rb[g], 0, 1024);
  sfor<162>([&](auto gc) ALWAYS_INLINE {
    constexpr int g = gc;
    constexpr int st = k2_stage(g), f = g - k2_g0(st);
    constexpr int ks = st < 12 ? (st % 6) * 3 + f / 4 : (st - 12) * 9 + f, dt = st < 12 ? f % 4 : 0;
    if constexpr (f == K2_MID && st < 13) {
      constexpr int okey = cmax(k2_dkey(st + 1, 2), st + 1 < 6 ? k2_hkey(st + 1, 2) : -1000);
      constexpr int n0 = k2_younger(okey, 4 * g, false), n1 = k2_younger(okey, 4 * g, true);
      if constexpr (st < 8) { STAMP(0, 2 * st); }
      if constexpr (st == K2_WST) { if (w == 0) poll_flags<4>(flg + FL_W, 64, tag, lane); }
      if constexpr (n0 != n1) { if (foreign) wait_vm<n1>(); else wait_vm<n0>(); }
      else if constexpr (st == 0) { if (w == 7) wait_vm<n0 + 1>(); else wait_vm<n0>(); }
      else if constexpr (st == K2_WST + 3) {
        asm volatile("s_waitcnt vmcnt(%2)" : "+v"(wt0), "+v"(wt1) : "i"(n0) : "memory");
        *(LAS bf16x8*)(smem + K2_WT_OFF + w * 1024 + lane * 16) = wt0;
        if (w == 0) *(LAS bf16x8*)(smem + K2_WT_OFF + 8192 + lane * 16) = wt1;
      }
      else wait_vm<n0>();
      if constexpr (st + 1 < 6) {
        constexpr int k0 = 3 * (st + 1);
        asm volatile("" : "+v"(hf[k0][0]), "+v"(hf[k0][1]), "+v"(hf[k0 + 1][0]), "+v"(hf[k0 + 1][1]), "+v"(hf[k0 + 2][0]), "+v"(hf[k0 + 2][1])::"memory");
      }
      if constexpr (st == 6 || st == 12) WAIT_L(0);
      __builtin_amdgcn_s_barrier();
      CFENCE();
      if constexpr (st < 8) { STAMP(0, 2 * st + 1); }
      if constexpr (st == 1) { if (tid == 0) set_flag(flg + FL_W + bid, tag); }
      if constexpr (st == 6 || st == 12) publish(st / 6 - 1, 4);
    }
    if constexpr (st < 12 && ((f + 12 - K2_MID) % 12) % 3 == 0) __builtin_amdgcn_s_setprio(3 - ((f + 12 - K2_MID) % 12) / 3);
    sfor<36>([&](auto dc) ALWAYS_INLINE { if constexpr (k2_dkey(dc / 3 + 2, dc % 3) == 4 * g + 2) piece_load2(dc / 3 + 2, dc % 3); });
    sfor<12>([&](auto hc) ALWAYS_INLINE { if constexpr (k2_hkey(hc / 3 + 2, hc % 3) == 4 * g + 3) h_load(std::integral_constant<int, hc + 6>{}); });
    if constexpr (k2_wkey() == 4 * g + 3) {
      int l2 = lane; asm volatile("" : "+v"(l2));
      gload16_sc1<0>(wt0, wtb, (unsigned)((colA2 + w * 16 + (l2 >> 2)) * (S_ * 2) + (l2 & 3) * 16));
      gload16_sc1<0>(wt1, wtb, (unsigned)((colQ2 + (l2 >> 2)) * (S_ * 2) + (l2 & 3) * 16));
    }
    if constexpr (g + PD < 162) {
      constexpr int g1 = g + PD, st1 = k2_stage(g1), f1 = g1 - k2_g0(st1);
      if constexpr (f1 == 0) {
        if constexpr (st1 < 12) {
          unsigned b2 = rbU2; asm volatile("" : "+v"(b2));
#pragma unroll
          for (int j = 0; j < 4; ++j) { rb[j] = (b2 + (st1 % K2_NSLOT) * K2_SLOT) ^ (j << 5); asm volatile("" : "+v"(rb[j])); }
        } else {
          int l2 = lane; asm volatile("" : "+v"(l2));
          const int kq2 = l2 >> 4;
          rb[0] = lds0 + khf * K2_HALF + (16 * (kq2 >> 1) + 4 * (kq2 & 1) + ((l2 & 15) >> 2)) * 32 + (l2 & 3) * 8 + (st1 % K2_NSLOT) * K2_SLOT;
          asm volatile("" : "+v"(rb[0]));
        }
      }
      if constexpr (st1 < 12) af[g1 % (PD + 1)] = tr_frag((LAS char*)(uintptr_t)rb[f1 % 4], (f1 / 4) * 4096, 1024);
      else af[g1 % (PD + 1)] = tr_frag((LAS char*)(uintptr_t)rb[0], f1 * 1024, 256);
    }
    SCHED0();
#pragma unroll
    for (int t = 0; t < 2; ++t) acc[t][dt] = __builtin_amdgcn_mfma_f32_16x16x32_bf16(af[g % (PD + 1)], hf[ks][t], acc[t][dt], 0, 0, 0);
    SCHED0();
    if constexpr (g == 71 || g == 143) { panel_end(4); SCHED0(); }
  });
  STAMP(2, 2);
  __builtin_amdgcn_s_setprio(0);
  panel_end(1);

  u32x2 xq[3];
  float2 stt[3];
#pragma unroll
  for (int i = 0; i < 3; ++i) {
    const int fi = (w + 8 * i < 18) ? w + 8 * i : w;
    const int t = fi >= 9 ? 1 : 0, dt = fi - 9 * t;
    const int token = mt * 128 + ksl * 32 + t * 16 + (lane & 15);
    xq[i] = *(const u32x2*)(q + (size_t)token * D_ + (dt < 8 ? colA2 + dt * 16 : colQ2) + 4 * (lane >> 4));
    const u64 sv = __hip_atomic_load(stats + token, __ATOMIC_RELAXED, __HIP_MEMORY_SCOPE_AGENT);
    stt[i] = make_float2(__uint_as_float((unsigned)sv), __uint_as_float((unsigned)(sv >> 32)));
  }
  WAIT_L(0);
  __builtin_amdgcn_s_barrier();
  CFENCE();
  publish(2, 1);
  asm volatile("s_waitcnt vmcnt(0)" ::: "memory");
  __syncthreads();
  STAMP(2, 3);
  if (w == 0) {
    if (lane == 0) set_flag(flg + FL_S + tile * 4 + ksl, tag);
    poll_flags<1>(flg + FL_S + tile * 4, 4, tag, lane);
  }
  __syncthreads();
  STAMP(2, 4);
  __builtin_amdgcn_fence(__ATOMIC_ACQUIRE, "wavefront");
  {
    f32x4 pv[3][4];
#pragma unroll
    for (int i = 0; i < 3; ++i) {
      const int fi = (w + 8 * i < 18) ? w + 8 * i : w;
#pragma unroll
      for (int k = 1; k < 4; ++k)
        pv[i][k] = __builtin_bit_cast(f32x4, __builtin_amdgcn_raw_buffer_load_b128(rs, (unsigned)(((ksl * 4 + ((ksl + k) & 3)) * 18 + fi) * 1024 + lane * 16), 0, 16));
      pv[i][0] = *(LAS f32x4*)(smem + K2_OWN_OFF + (fi * 64 + lane) * 16);
    }
#pragma unroll
    for (int i = 0; i < 3; ++i) {
      if (w + 8 * i < 18) {
        const int fi = w + 8 * i;
        const int t = fi >= 9 ? 1 : 0, dt = fi - 9 * t;
        const int tl = t * 16 + (lane & 15), dl = dt * 16 + 4 * (lane >> 4);
        const f32x4 bs = ((pv[i][0] + pv[i][1]) + pv[i][2]) + pv[i][3];
        float o[4];
#pragma unroll
        for (int c = 0; c < 4; ++c) {
          const unsigned xw = (c < 2) ? xq[i].x : xq[i].y;
          const float x = (c & 1) ? bf_hi(xw) : bf_lo(xw);
          const float wy = bf2f(*(LAS u16*)(smem + K2_WT_OFF + (dl + c) * 64 + tl * 2));
          const float xn = bfr(bfr(x - stt[i].x) / stt[i].y);
          const float a = bfr(xn * wy);
          o[c] = bfr(x + a) + bfr(bs[c]);
        }
        u32x2 ov; ov.x = pk_bf16(o[0], o[1]); ov.y = pk_bf16(o[2], o[3]);
        *(u32x2*)(out + (size_t)(mt * 128 + ksl * 32 + tl) * D_ + (dt < 8 ? colA2 + dt * 16 : colQ2) + 4 * (lane >> 4)) = ov;
      }
    }
  }
  if (bid == 0 && w == 0) {
    poll_flags<4>(flg + FL_K, 64, tag, lane);
    if (lane == 0) set_flag(flg + FL_GEN, tag);
  }
  STAMP(2, 5); RSTAMP(1, 15); STAMP_FLUSH(1); STAMP_FLUSH(2); STAMP_FLUSH(0); STAMP_FLUSH(3);
  }
}

extern "C" void kernel_launch(void* const* d_in, const int* in_sizes, int n_in,
                              void* d_out, int out_size, void* d_ws, size_t ws_size,
                              hipStream_t stream) {
  const u16* q = (const u16*)d_in[0];
  const u16* k = (const u16*)d_in[1];
  const u16* v = (const u16*)d_in[2];
  const u16* shk = (const u16*)d_in[3];
  const u16* ssmk = (const u16*)d_in[4];
  const u16* ssmd = (const u16*)d_in[5];
  const u16* wg = (const u16*)d_in[7];
  const u16* wu = (const u16*)d_in[8];
  const u16* wd = (const u16*)d_in[9];
  (void)hipFuncSetAttribute((const void*)k_fused, hipFuncAttributeMaxDynamicSharedMemorySize, K1_LDS);
  k_fused<<<256, 512, K1_LDS, stream>>>(q, k, v, shk, ssmk, ssmd, wg, wu, wd, (char*)d_ws, (u16*)d_out);
}
```

```cpp
#include <hip/hip_runtime.h>
#include <hip/hip_bf16.h>
#include <stdint.h>
#include <utility>

typedef unsigned short u16;
typedef short bf16x8 __attribute__((ext_vector_type(8)));
typedef short s16x4 __attribute__((ext_vector_type(4)));
typedef float f32x4 __attribute__((ext_vector_type(4)));
typedef float f32x16 __attribute__((ext_vector_type(16)));
typedef unsigned u32x4 __attribute__((ext_vector_type(4)));
typedef unsigned u32x2 __attribute__((ext_vector_type(2)));
typedef __bf16 bf2_t __attribute__((ext_vector_type(2)));
#define LAS __attribute__((address_space(3)))
#define DI __device__ __forceinline__

constexpr int S_ = 2048, D_ = 576, F_ = 4608;

constexpr size_t WS_XFRAG = 0;
constexpr size_t WS_STATS = WS_XFRAG + (size_t)S_ * D_ * 2;
constexpr size_t WS_KVR   = WS_STATS + (size_t)S_ * 8;
constexpr size_t WS_WT    = WS_KVR + (size_t)S_ * D_ * 2;
constexpr size_t WS_H     = WS_WT + (size_t)S_ * D_ * 2;
constexpr size_t WS_SLAB  = WS_H + (size_t)S_ * F_ * 2;
constexpr size_t WS_FLG   = WS_SLAB + (size_t)4 * S_ * D_ * 4;
constexpr int FL_GEN = 0, FL_X = 32, FL_K = FL_X + 256, FL_H = FL_K + 256, FL_W = FL_H + 256, FL_S = FL_W + 256, FL_END = FL_S + 256;
constexpr size_t WS_END   = WS_FLG + FL_END * 8;
typedef unsigned long long u64;

DI float bf_lo(unsigned w) { return __uint_as_float(w << 16); }
DI float bf_hi(unsigned w) { return __uint_as_float(w & 0xffff0000u); }
DI float bf2f(u16 b) { return __uint_as_float(((unsigned)b) << 16); }
DI float bfr(float x) { return (float)(__bf16)x; }
typedef float f32x2 __attribute__((ext_vector_type(2)));
DI unsigned pk_bf16(float lo, float hi);
DI f32x2 unpk(unsigned w) { return (f32x2){__uint_as_float(w << 16), __uint_as_float(w & 0xffff0000u)}; }
DI f32x2 bfr2(f32x2 v) { return unpk(pk_bf16(v.x, v.y)); }
DI unsigned pk_bf16(float lo, float hi) { bf2_t v = {(__bf16)lo, (__bf16)hi}; return __builtin_bit_cast(unsigned, v); }

DI float wave_sum(float x) {
  x += __builtin_bit_cast(float, __builtin_amdgcn_update_dpp(0, __builtin_bit_cast(int, x), 0xB1, 0xf, 0xf, true));
  x += __builtin_bit_cast(float, __builtin_amdgcn_update_dpp(0, __builtin_bit_cast(int, x), 0x4E, 0xf, 0xf, true));
  x += __builtin_bit_cast(float, __builtin_amdgcn_update_dpp(0, __builtin_bit_cast(int, x), 0x141, 0xf, 0xf, true));
  x += __builtin_bit_cast(float, __builtin_amdgcn_update_dpp(0, __builtin_bit_cast(int, x), 0x140, 0xf, 0xf, true));
  const int xi = __builtin_bit_cast(int, x);
  return (__builtin_bit_cast(float, __builtin_amdgcn_readlane(xi, 0)) + __builtin_bit_cast(float, __builtin_amdgcn_readlane(xi, 16))) +
         (__builtin_bit_cast(float, __builtin_amdgcn_readlane(xi, 32)) + __builtin_bit_cast(float, __builtin_amdgcn_readlane(xi, 48)));
}
#define WAIT_V(n) asm volatile("s_waitcnt vmcnt(" #n ")" ::: "memory")
#define WAIT_L(n) asm volatile("s_waitcnt lgkmcnt(" #n ")" ::: "memory")
#define CFENCE() asm volatile("" ::: "memory")
#define STAMP(kid, i)
#define RSTAMP(kid, i)
#define STAMP_DECL
#define STAMP_FLUSH(kid)
#define K0STAMP(i)
#define SCHED0() __builtin_amdgcn_sched_barrier(0)
template <int... I, class Fn> DI void sfor_impl(std::integer_sequence<int, I...>, Fn&& f) { (f(std::integral_constant<int, I>{}), ...); }
template <int N, class Fn> DI void sfor(Fn&& f) { sfor_impl(std::make_integer_sequence<int, N>{}, f); }
#define ALWAYS_INLINE __attribute__((always_inline))
template <int N> DI void wait_vm() { asm volatile("s_waitcnt vmcnt(%0)" ::"i"(N) : "memory"); }

DI void glds4(const void* sbase, unsigned v0, unsigned v1, unsigned v2, unsigned v3, unsigned lds_dst) {
  unsigned keep;
  asm volatile("s_mov_b32 %0, m0\n\ts_mov_b32 m0, %6\n\ts_nop 0\n\t"
               "global_load_lds_dwordx4 %1, %5\n\t"
               "global_load_lds_dwordx4 %2, %5 offset:1024\n\t"
               "global_load_lds_dwordx4 %3, %5 offset:2048\n\t"
               "global_load_lds_dwordx4 %4, %5 offset:3072\n\t"
               "s_mov_b32 m0, %0"
               : "=&s"(keep) : "v"(v0), "v"(v1), "v"(v2), "v"(v3), "s"(sbase), "s"(lds_dst) : "memory");
}
DI void glds3(const void* sbase, unsigned v0, unsigned v1, unsigned v2, unsigned lds_dst) {
  unsigned keep;
  asm volatile("s_mov_b32 %0, m0\n\ts_mov_b32 m0, %5\n\ts_nop 0\n\t"
               "global_load_lds_dwordx4 %1, %4\n\t"
               "global_load_lds_dwordx4 %2, %4 offset:1024\n\t"
               "global_load_lds_dwordx4 %3, %4 offset:2048\n\t"
               "s_mov_b32 m0, %0"
               : "=&s"(keep) : "v"(v0), "v"(v1), "v"(v2), "s"(sbase), "s"(lds_dst) : "memory");
}
DI void glds2(const void* sbase, unsigned v0, unsigned v1, unsigned lds_dst) {
  unsigned keep;
  asm volatile("s_mov_b32 %0, m0\n\ts_mov_b32 m0, %4\n\ts_nop 0\n\t"
               "global_load_lds_dwordx4 %1, %3\n\t"
               "global_load_lds_dwordx4 %2, %3 offset:1024\n\t"
               "s_mov_b32 m0, %0"
               : "=&s"(keep) : "v"(v0), "v"(v1), "s"(sbase), "s"(lds_dst) : "memory");
}
DI void glds1(const void* sbase, unsigned v0, unsigned lds_dst) {
  unsigned keep;
  asm volatile("s_mov_b32 %0, m0\n\ts_mov_b32 m0, %3\n\ts_nop 0\n\t"
               "global_load_lds_dwordx4 %1, %2\n\t"
               "s_mov_b32 m0, %0"
               : "=&s"(keep) : "v"(v0), "s"(sbase), "s"(lds_dst) : "memory");
}

template <bool NT> DI void glds1p(const void* sbase, unsigned v0, unsigned lds_dst) {
  unsigned keep;
  if constexpr (NT)
    asm volatile("s_mov_b32 %0, m0\n\ts_mov_b32 m0, %3\n\ts_nop 0\n\tglobal_load_lds_dwordx4 %1, %2 nt\n\ts_mov_b32 m0, %0"
                 : "=&s"(keep) : "v"(v0), "s"(sbase), "s"(lds_dst) : "memory");
  else
    asm volatile("s_mov_b32 %0, m0\n\ts_mov_b32 m0, %3\n\ts_nop 0\n\tglobal_load_lds_dwordx4 %1, %2\n\ts_mov_b32 m0, %0"
                 : "=&s"(keep) : "v"(v0), "s"(sbase), "s"(lds_dst) : "memory");
}
DI void touch_lines(const void* sbase, unsigned v0, unsigned lds_dst) {
  unsigned keep;
  asm volatile("s_mov_b32 %0, m0\n\ts_mov_b32 m0, %3\n\ts_nop 0\n\tglobal_load_lds_dword %1, %2\n\ts_mov_b32 m0, %0"
               : "=&s"(keep) : "v"(v0), "s"(sbase), "s"(lds_dst) : "memory");
}
template <int OFF> DI void gload16(bf16x8& dst, const void* sbase, unsigned voff) {
  asm volatile("global_load_dwordx4 %0, %1, %2 offset:%3" : "=v"(dst) : "v"(voff), "s"(sbase), "i"(OFF) : "memory");
}
template <int OFF> DI void gload16_sc1(bf16x8& dst, const void* sbase, unsigned voff) {
  asm volatile("global_load_dwordx4 %0, %1, %2 offset:%3 sc1" : "=v"(dst) : "v"(voff), "s"(sbase), "i"(OFF) : "memory");
}
DI void gload16u(u32x4& dst, const void* sbase, unsigned voff) {
  asm volatile("global_load_dwordx4 %0, %1, %2" : "=v"(dst) : "v"(voff), "s"(sbase) : "memory");
}
DI void gstore16_sc1(void* p, const u32x4& v) { asm volatile("global_store_dwordx4 %0, %1, off sc1\n\ts_nop 1" ::"v"(p), "v"(v) : "memory"); }
DI void set_flag(u64* f, u64 tag) { __hip_atomic_store(f, tag, __ATOMIC_RELAXED, __HIP_MEMORY_SCOPE_AGENT); }
template <int PER> DI void poll_flags(u64* f, int n, u64 tag, int lane) {
  asm volatile("" : "+v"(lane));
  const int i = lane < n ? lane : 0;
  for (unsigned spins = 0; spins < (1u << 20); ++spins) {
    bool ok = true;
#pragma unroll
    for (int k = 0; k < PER; ++k) ok &= __hip_atomic_load(f + i + 64 * k, __ATOMIC_RELAXED, __HIP_MEMORY_SCOPE_AGENT) == tag;
    if (__all(ok)) break;
    __builtin_amdgcn_s_sleep(1);
  }
}

constexpr int K1_SLOT = 37120;
constexpr int K1_UOFF = 18432 + 128;
constexpr int K1_NSLOT = 4;
constexpr int K1_PF_OFF = K1_NSLOT * K1_SLOT;
constexpr int XR_OFF = K1_PF_OFF + 288, XR_STRIDE = 4608;
constexpr int HZ_OFF = XR_OFF + 3 * XR_STRIDE;
constexpr int K1_LDS = HZ_OFF + 288;
constexpr int PART_OFF = 65536;
constexpr int PARTC_OFF = PART_OFF + 65536;
constexpr int HCNT_OFF = K1_PF_OFF;
constexpr int XG_OFF = 3 * K1_SLOT;

DI bf16x8 tr_frag(LAS char* p, int off, int hi_off) {
  s16x4 lo = __builtin_amdgcn_ds_read_tr16_b64_v4i16((LAS s16x4*)(p + off));
  s16x4 hi = __builtin_amdgcn_ds_read_tr16_b64_v4i16((LAS s16x4*)(p + off + hi_off));
  return __builtin_shufflevector(lo, hi, 0, 1, 2, 3, 4, 5, 6, 7);
}

constexpr int K1_MID = 16;
constexpr int k1_xkey(int i) { return i < 9 ? -100 + i : 4 * (1 + 2 * (i - 9)) + 2; }
constexpr int k1_dstep(int d, int n) { return d == 2 ? 2 + 4 * n : d == 3 ? 24 + 4 * n : d == 4 ? 58 + 6 * n : d == 5 ? 94 + 6 * n : 36 * (d - 3) + K1_MID + 6 * n; }
constexpr int k1_dkey(int d, int n) { return d < 2 ? -400 + 10 * d + n : 4 * k1_dstep(d, n) + 1; }
constexpr int k1_cstep(int i) { return i < 7 ? 218 + 4 * i : (i < 14 ? 254 + 4 * (i - 7) : (i < 21 ? 289 + 2 * (i - 14) : 306 + 4 * (i - 21))); }
constexpr int k1_sstep(int c) { return c == 0 ? 143 : (c < 4 ? 144 + 6 * c : 182); }
constexpr int K1_TOUCH = 7 * 36 + K1_MID + 2;
constexpr int k1_younger(int okey, int wkey) {
  int n = 0;
  if (4 * K1_TOUCH + 1 > okey && 4 * K1_TOUCH + 1 < wkey) n += 1;
  if (-70 > okey && -70 < wkey) n += 2;
  for (int i = 0; i < 36; ++i) if (k1_xkey(i) > okey && k1_xkey(i) < wkey) n += 1;
  for (int d = 0; d < 9; ++d) for (int pc = 0; pc < 5; ++pc) if (k1_dkey(d, pc) > okey && k1_dkey(d, pc) < wkey) n += 1;
  for (int i = 0; i < 24; ++i) if (4 * k1_cstep(i) + 2 > okey && 4 * k1_cstep(i) + 2 < wkey) n += 2;
  for (int c = 0; c < 5; ++c) if (4 * k1_sstep(c) + 3 > okey && 4 * k1_sstep(c) + 3 < wkey) n += 1;
  return n;
}
#define HSTORE 1

constexpr int K2_HALF = 16384;
constexpr int K2_SLOT = 2 * K2_HALF;
constexpr int K2_NSLOT = 4;
constexpr int K2_OWN_OFF = K2_NSLOT * K2_SLOT;
constexpr int K2_WT_OFF = K2_OWN_OFF + 18432;
constexpr int k2_g0(int st) { return st <= 9 ? 16 * st : 153; }
constexpr int k2_stage(int g) { return g < 144 ? g / 16 : (g < 153 ? 9 : 10); }
constexpr int k2_mid(int st) { return st < 9 ? 6 : 4; }
constexpr int k2_np(int d) { return d < 9 ? 4 : 3; }
constexpr int k2_dkey(int d, int n) { return d < 2 ? -300 + 10 * d + n : (d == 2 ? 4 * (1 + n) + 2 : 4 * (k2_g0(d - 3) + k2_mid(d - 3) + 4 * n) + 2); }
constexpr int k2_hkey(int j, int m) { return j < 3 ? -250 + 10 * j + m : 4 * (k2_g0(j - 3) + k2_mid(j - 3) + 2 + 4 * m) + 3; }
constexpr int k2_skey(int p) { return 4 * (k2_g0(9 + p) + k2_mid(9 + p)) + 1; }
constexpr int K2_WST = 7;
constexpr int k2_younger(int okey, int wkey, bool stores) {
  int n = 0;
  if (-100 > okey && -100 < wkey) n += 1;
  for (int d = 0; d < 11; ++d) for (int pc = 0; pc < k2_np(d); ++pc) if (k2_dkey(d, pc) > okey && k2_dkey(d, pc) < wkey) n += 1;
  for (int j = 0; j < 9; ++j) for (int m = 0; m < 4; ++m) if (k2_hkey(j, m) > okey && k2_hkey(j, m) < wkey) n += 1;
  if (stores) for (int p = 0; p < 2; ++p) if (k2_skey(p) > okey && k2_skey(p) < wkey) n += 4;
  return n;
}
constexpr int cmax(int a, int b) { return a > b ? a : b; }

__global__ __launch_bounds__(512, 2) void k_fused(const u16* __restrict__ q, const u16* __restrict__ kk, const u16* __restrict__ vv,
                                                  const u16* __restrict__ shk, const u16* __restrict__ ssmk, const u16* __restrict__ ssmd,
                                                  const u16* __restrict__ wg, const u16* __restrict__ wu, const u16* __restrict__ wd,
                                                  char* ws, u16* __restrict__ out) {
  u16* xfrag = (u16*)(ws + WS_XFRAG);
  u64* stats = (u64*)(ws + WS_STATS);
  u16* kvr = (u16*)(ws + WS_KVR);
  u16* wT = (u16*)(ws + WS_WT);
  u16* hout = (u16*)(ws + WS_H);
  float* slab = (float*)(ws + WS_SLAB);
  u64* flg = (u64*)(ws + WS_FLG);
  extern __shared__ __attribute__((aligned(16))) char smem_g[];
  LAS char* smem = (LAS char*)smem_g;
  const unsigned lds0 = (unsigned)(uintptr_t)smem;
  const int bid = blockIdx.x, tid = threadIdx.x;
  const int w = __builtin_amdgcn_readfirstlane(tid >> 6), lane = tid & 63;
  const int xcd = bid & 7, idx = bid >> 3;
  STAMP_DECL; RSTAMP(1, 14); STAMP(1, 0);
  const u64 genv = __hip_atomic_load(flg + FL_GEN, __ATOMIC_RELAXED, __HIP_MEMORY_SCOPE_AGENT);
  u64 tag;
  auto make_tag = [&]() ALWAYS_INLINE {
    tag = (((u64)(unsigned)__builtin_amdgcn_readfirstlane((unsigned)(genv >> 32)) << 32) | (unsigned)__builtin_amdgcn_readfirstlane((unsigned)genv)) + 1ull;
  };
  const int ksl = xcd >> 1, mt = (xcd & 1) * 8 + (idx >> 2), nt = idx & 3;
  const int tq = w & 3, khf = w >> 2;
  const int kbase = ksl * 1152 + khf * 576;
  const int colA2 = 128 * nt, colQ2 = 512 + 16 * nt;
  const int mtile = (xcd & 1) * 4 + (idx & 3);
  const int cgrp = (xcd >> 1) * 2 + (idx >> 4), cq = (idx >> 2) & 3;
  const int colA = 64 * (9 * cgrp + 2 * cq), colQ = 64 * (9 * cgrp + 8) + 16 * cq;

  const int grp = bid >> 2, qd = bid & 3;
  const int chA = grp * 9 + 2 * qd, chC = grp * 9 + 8;
  float ddA, ddC;
  {
    const int c1 = chA + (w >> 2);
    const unsigned d1 = ((const unsigned*)ssmd)[c1 >> 1], d2 = ((const unsigned*)ssmd)[chC >> 1];
    ddA = (c1 & 1) ? bf_hi(d1) : bf_lo(d1);
    ddC = (chC & 1) ? bf_hi(d2) : bf_lo(d2);
  }

  const int wq = w & 3, part = w >> 2;
  const char* wbase = (const char*)(part ? wu : wg);
  unsigned voff[5];
#pragma unroll
  for (int n = 0; n < 5; ++n) {
    const int o = (n < 4) ? ((wq * 4 + n) * 1024 + lane * 16) : (16384 + wq * 512 + (lane & 31) * 16);
    const int k = o >> 7, pc = (o >> 4) & 7;
    const int j = (pc >> 1) ^ (((k >> 1) & 1) | (part << 1));
    voff[n] = (unsigned)(k * (F_ * 2) + (2 * j + (pc & 1)) * 16);
  }
  const unsigned ldst = lds0 + part * K1_UOFF + wq * 4096;
  const unsigned ldst4 = lds0 + part * K1_UOFF + 16384 + wq * 512;

  auto piece_load = [&](int st, int n) ALWAYS_INLINE {
    const int slot = st % K1_NSLOT;
    const unsigned dst = (n < 4 ? ldst + n * 1024 : ldst4) + slot * K1_SLOT;
    if (st != 4) {
      const int ks = st < 4 ? st : st - 5;
      const char* g = wbase + (size_t)ks * 144 * (F_ * 2) + (colA + (st > 4 ? 64 : 0)) * 2;
      if (n < 4 || lane < 32) glds1p<false>(g, voff[n], dst);
    } else {
      int l2 = lane; asm volatile("" : "+v"(l2));
      const int o = (n < 4) ? ((wq * 4 + n) * 1024 + l2 * 16) : (16384 + wq * 512 + (l2 & 31) * 16);
      const unsigned vq = (unsigned)((o >> 5) * (F_ * 2) + ((o >> 4) & 1) * 16);
      if (n < 4 || lane < 32) glds1p<false>(wbase + colQ * 2, vq, dst);
    }
  };

  bf16x8 xf[36];
  const char* xb = (const char*)xfrag + (size_t)(mtile * 8 + w) * 36 * 1024;
  const unsigned xv = lane * 16;
  auto x_load = [&](auto ic) ALWAYS_INLINE {
    constexpr int i = ic;
    gload16_sc1<(i & 3) * 1024>(xf[i], xb + (i >> 2) * 4096, xv);
  };
  {
    LAS u16* tile = (LAS u16*)(smem + XR_OFF);
    constexpr int KT_OFF = 2 * K1_SLOT, VT_OFF = KT_OFF + 9728, WT_OFF = VT_OFF + 9216;
    const int tt = bid >> 3, ct = bid & 7, t0 = tt * 64, c0 = ct * 72;
    const int tok0 = mtile * 256 + (cgrp * 4 + cq) * 8, token = tok0 + w;
    const bool act = lane < 48;
    u32x2 d[3];
    u32x4 kin, vin, ein;
#pragma unroll
    for (int i = 0; i < 3; ++i)
      asm volatile("global_load_dwordx2 %0, %1, %2 offset:%3" : "=v"(d[i]) : "v"((unsigned)(token * (D_ * 2) + (act ? lane : 0) * 8)), "s"(q), "i"(i * 384) : "memory");
    auto k_off = [&](int pc) ALWAYS_INLINE -> unsigned { const int r = pc / 9, cg = pc - r * 9, ts = t0 - 3 + r; return (unsigned)((ts < 0 ? 0 : ts) * (D_ * 2) + (c0 + 8 * cg) * 2); };
    auto v_off = [&](int pc) ALWAYS_INLINE -> unsigned { const int r = pc / 9, cg = pc - r * 9; return (unsigned)((t0 + r) * (D_ * 2) + (c0 + 8 * cg) * 2); };
    gload16u(kin, kk, k_off(tid));
    gload16u(vin, vv, v_off(tid));
    int edst;
    {
      const int kp = w == 0 ? 512 + lane : 576 + (lane < 27 ? lane : 0);
      const int wl = lane < 63 ? lane - 27 : 35;
      const bool isw = w == 1 && lane >= 27, isv = w >= 2;
      const char* src = isv ? (const char*)vv + v_off(512 + lane) : (isw ? (const char*)shk + c0 * 8 + wl * 16 : (const char*)kk + k_off(kp));
      edst = isv ? VT_OFF + (512 + lane) * 16 : (isw ? WT_OFF + wl * 16 : KT_OFF + kp * 16);
      asm volatile("global_load_dwordx4 %0, %1, off" : "=v"(ein) : "v"(src) : "memory");
    }
    asm volatile("s_waitcnt vmcnt(3)" : "+v"(d[0]), "+v"(d[1]), "+v"(d[2])::"memory");
    STAMP(1, 9);
    f32x2 xa[3], xb2[3];
#pragma unroll
    for (int i = 0; i < 3; ++i) { xa[i] = unpk(d[i].x); xb2[i] = unpk(d[i].y); }
    const f32x2 s2 = (xa[0] + xb2[0]) + (xa[1] + xb2[1]) + (xa[2] + xb2[2]);
    const float mean = wave_sum(act ? s2.x + s2.y : 0.f) * (1.0f / 576.0f);
    f32x2 v2 = {0.f, 0.f};
#pragma unroll
    for (int i = 0; i < 3; ++i) { const f32x2 a = xa[i] - mean, b = xb2[i] - mean; v2 += a * a + b * b; }
    const float vs = wave_sum(act ? v2.x + v2.y : 0.f);
    const float mb = bfr(mean), vb = bfr(vs * (1.0f / 576.0f));
    const float sd = bfr(sqrtf(bfr(vb + 1e-5f)));
    const float rsd = 1.0f / sd;
#pragma unroll
    for (int i = 0; i < 3; ++i) {
      const f32x2 a = bfr2(xa[i] - mb) * rsd, b = bfr2(xb2[i] - mb) * rsd;
      u32x2 o; o.x = pk_bf16(a.x, a.y); o.y = pk_bf16(b.x, b.y);
      const int p8 = lane + 48 * i;
      if (act) *(LAS u32x2*)(smem + XG_OFF + ((p8 >> 1) * 8 + w) * 16 + (p8 & 1) * 8) = o;
    }
    if (tid == 0) *(LAS unsigned*)(smem + HCNT_OFF + 4) = 0u;
    asm volatile("s_waitcnt vmcnt(0)" : "+v"(kin), "+v"(vin), "+v"(ein)::"memory");
    make_tag();
    if (t0 == 0 && tid < 27) kin = (u32x4){0u, 0u, 0u, 0u};
    *(LAS u32x4*)(smem + KT_OFF + tid * 16) = kin;
    *(LAS u32x4*)(smem + VT_OFF + tid * 16) = vin;
    if (w < 3) *(LAS u32x4*)(smem + edst) = ein;
    WAIT_L(0);
    __builtin_amdgcn_s_barrier();
    CFENCE();
    {
      char* xo = (char*)xfrag + (size_t)((tok0 >> 5) * 36 * 64 + (tok0 & 31)) * 16;
#pragma unroll
      for (int it = 0; it < 2; ++it) {
        const int id = it ? 512 + lane : tid, p16 = id >> 3;
        if (it == 0 || w == 0) {
          const u32x4 v = *(LAS u32x4*)(smem + XG_OFF + id * 16);
          gstore16_sc1(xo + ((p16 >> 1) * 64 + (p16 & 1) * 32 + (id & 7)) * 16, v);
        }
      }
      if (lane == 0) __hip_atomic_store(stats + token, (u64)__float_as_uint(mb) | ((u64)__float_as_uint(rsd) << 32), __ATOMIC_RELAXED, __HIP_MEMORY_SCOPE_AGENT);
    }
    STAMP(1, 10);
    CFENCE();
#pragma unroll
    for (int n = 0; n < 5; ++n) piece_load(0, n);
#pragma unroll
    for (int n = 0; n < 5; ++n) piece_load(1, n);
    wait_vm<10>();
    STAMP(1, 11);
    if (lane == 0) {
      const unsigned old = __hip_atomic_fetch_add((LAS unsigned*)(smem + HCNT_OFF + 4), 1u, __ATOMIC_RELAXED, __HIP_MEMORY_SCOPE_WORKGROUP);
      if (old == 7u) set_flag(flg + FL_X + (tok0 >> 3), tag);
    }
    auto kv_compute = [&](int item) ALWAYS_INLINE {
      const int tl = item / 9, cg = item - tl * 9;
      u32x4 kr[4], w4[4];
#pragma unroll
      for (int i = 0; i < 4; ++i) { kr[i] = *(LAS u32x4*)(smem + KT_OFF + (item + 9 * i) * 16); w4[i] = *(LAS u32x4*)(smem + WT_OFF + cg * 64 + i * 16); }
      const u32x4 vr = *(LAS u32x4*)(smem + VT_OFF + item * 16);
#pragma unroll
      for (int e2 = 0; e2 < 4; ++e2) {
        const f32x2 w0 = {bf_lo(w4[e2].x), bf_lo(w4[e2].z)}, w1 = {bf_hi(w4[e2].x), bf_hi(w4[e2].z)};
        const f32x2 w2 = {bf_lo(w4[e2].y), bf_lo(w4[e2].w)}, w3 = {bf_hi(w4[e2].y), bf_hi(w4[e2].w)};
        f32x2 ks = bfr2(unpk(kr[0][e2]) * w3);
        ks = bfr2(ks + bfr2(unpk(kr[1][e2]) * w2));
        ks = bfr2(ks + bfr2(unpk(kr[2][e2]) * w1));
        ks = bfr2(ks + bfr2(unpk(kr[3][e2]) * w0));
        const f32x2 kv = ks * unpk(vr[e2]);
        const unsigned pk = pk_bf16(kv.x, kv.y);
        tile[(8 * cg + 2 * e2) * 72 + (63 - tl)] = (u16)(pk & 0xffffu);
        tile[(8 * cg + 2 * e2 + 1) * 72 + (63 - tl)] = (u16)(pk >> 16);
      }
    };
    kv_compute(tid);
    STAMP(1, 7);
    poll_flags<1>(flg + FL_X + mtile * 32 + w * 4, 4, tag, lane);
    wait_vm<0>();
    STAMP(1, 8);
    sfor<9>([&](auto ic) ALWAYS_INLINE { x_load(ic); });
    if (w == 7) kv_compute(512 + lane);
    WAIT_L(0);
    __builtin_amdgcn_s_barrier();
    CFENCE();
    {
      const int ch = tid >> 3, pp = tid & 7, ch2 = 64 + (lane >> 3);
      const u32x4 o1 = *(LAS u32x4*)(tile + ch * 72 + pp * 8), o2 = *(LAS u32x4*)(tile + ch2 * 72 + pp * 8);
      gstore16_sc1(kvr + (size_t)(c0 + ch) * S_ + (1984 - t0) + pp * 8, o1);
      gstore16_sc1(kvr + (size_t)(c0 + ch2) * S_ + (1984 - t0) + pp * 8, o2);
    }
  }

  const int kh = lane >> 5, prt = (lane >> 4) & 1, qq = (lane & 15) >> 2, p4 = lane & 3;
  const unsigned rbU = lds0 + (8 * kh + qq) * 128 + prt * K1_UOFF + ((((qq >> 1) & 1) | (prt << 1)) << 5) + p4 * 8;

  f32x16 acc[4];
  const f32x16 zero16 = {0.f, 0.f, 0.f, 0.f, 0.f, 0.f, 0.f, 0.f, 0.f, 0.f, 0.f, 0.f, 0.f, 0.f, 0.f, 0.f};

  const int token = mtile * 256 + w * 32 + (lane & 31);
  char* hb = (char*)hout + ((size_t)(token >> 4) * 144 * 64 + (lane >> 5) * 16 + (token & 15)) * 16;
  auto h_word = [&](const f32x16& a, int i) ALWAYS_INLINE -> unsigned {
    return pk_bf16(bfr(a[2 * i]) * bfr(a[2 * i + 8]), bfr(a[2 * i + 1]) * bfr(a[2 * i + 9]));
  };
  auto h_store = [&](const u32x4& o, int c16) ALWAYS_INLINE {
    gstore16_sc1(hb + ((size_t)(c16 >> 1) * 64 + (c16 & 1) * 32) * 16, o);
  };
  u32x4 ho;

  const char* hpA = (const char*)(ssmk + (size_t)(chA + (w >> 2)) * S_ + 64 * (w & 3));
  const char* hpC = (const char*)(ssmk + (size_t)chC * S_ + 32 * w);
  const bool zA = (w & 3) == 0, zC = w == 0;
  const char* pf_sk = (const char*)ssmk;
  const char* pf_wd = (const char*)wd + (size_t)(xcd >> 1) * 1152 * 1152;
  const char* kvb = (const char*)kvr;
  u32x4 caf[24];
  unsigned cax[24];
  int cvo = 0, cvx = 0;

  STAMP(1, 1);

  constexpr int PD = 3, MID = K1_MID;
  bf16x8 af[PD + 1];
  unsigned rb[4];
#pragma unroll
  for (int j = 0; j < 4; ++j) rb[j] = rbU ^ (j << 5);
#pragma unroll
  for (int g = 0; g < PD; ++g) af[g] = tr_frag((LAS char*)(uintptr_t)rb[g], 0, 512);
  sfor<324>([&](auto gc) ALWAYS_INLINE {
    constexpr int g = gc;
    constexpr int st = g / 36, f = g - st * 36;
    constexpr int s = st != 4 ? f / 4 : f, c = st != 4 ? f % 4 : 0;
    constexpr int xi = st < 4 ? st * 9 + s : (st == 4 ? s : (st - 5) * 9 + s);
    constexpr int ai = st < 4 ? c : (st == 4 ? 0 : (c + 1) & 3);
    constexpr bool first = (st == 0 || st == 5) ? s == 0 : (st == 4 && f == 0);
    if constexpr (f == MID) {

      if constexpr (st < 8) wait_vm<k1_younger(k1_dkey(st + 1, 4), 4 * g)>();
      else { wait_vm<k1_younger(4 * (7 * 36 + MID + 1) + 1, 4 * g)>(); if (w <= 3) WAIT_L(0); }
      __builtin_amdgcn_s_barrier();
      CFENCE();

      if constexpr (st == 1) { if (tid == 0) set_flag(flg + FL_K + (bid & 7) * 32 + (bid >> 3), tag); }
    }
    if constexpr (((f + 36 - MID) % 36) % 9 == 0) __builtin_amdgcn_s_setprio(3 - ((f + 36 - MID) % 36) / 9);
    sfor<35>([&](auto dc) ALWAYS_INLINE { if constexpr (k1_dkey(dc / 5 + 2, dc % 5) == 4 * g + 1) piece_load(dc / 5 + 2, dc % 5); });
    sfor<27>([&](auto ic) ALWAYS_INLINE { if constexpr (k1_xkey(ic + 9) == 4 * g + 2) x_load(std::integral_constant<int, ic + 9>{}); });
    if constexpr (g == 7 * 36 + MID + 1) {
      if (w < 3) {
        const int ch = (w == 2) ? chC : chA + w;
        const unsigned lv = lane * 16;
        poll_flags<1>(flg + FL_K + (grp >> 3) * 32, 32, tag, lane);
        const __amdgpu_buffer_rsrc_t rk = __builtin_amdgcn_make_buffer_rsrc((char*)kvb + (size_t)ch * (S_ * 2), 0, S_ * 2, 0x00020000);
#pragma unroll
        for (int i = 0; i < 4; ++i)
          *(LAS u32x4*)(smem + XR_OFF + w * XR_STRIDE + i * 1024 + lv) = __builtin_amdgcn_raw_buffer_load_b128(rk, lv + i * 1024, 0, 16);
        if (lane < 4) glds1(pf_sk + (size_t)ch * (S_ * 2), lv, lds0 + HZ_OFF + w * 96 + 32);
      } else if (w == 3) {
        if (lane < 32) {
#pragma unroll
          for (int im = 0; im < 3; ++im) *(LAS u32x4*)(smem + XR_OFF + im * XR_STRIDE + 4096 + lane * 16) = (u32x4){0u, 0u, 0u, 0u};
        } else if (lane < 38) {
          const int im = (lane - 32) >> 1, hh = lane & 1;
          *(LAS u32x4*)(smem + HZ_OFF + im * 96 + hh * 16) = (u32x4){0u, 0u, 0u, 0u};
        } else if (lane == 38) *(LAS unsigned*)(smem + HCNT_OFF) = 0u;
      }
    }
    if constexpr (g == K1_TOUCH) {
      int l2 = lane; asm volatile("" : "+v"(l2));
      const unsigned line = (unsigned)(w * 64 + l2 < 324 ? idx * 324 + w * 64 + l2 : idx * 324);
      touch_lines(pf_wd, line * 128u, lds0 + K1_PF_OFF + 16);
    }
    sfor<24>([&](auto cc) ALWAYS_INLINE {
      constexpr int ci = cc;
      if constexpr (k1_cstep(ci) == g) {
        if constexpr (ci == 0) {
          int l2 = lane; asm volatile("" : "+v"(l2));
          const int e = (l2 & 15) - 15 + 8 * (l2 >> 4);
          cvo = (e & ~1) * 2; cvx = cvo + ((e & 1) ? 16 : 0);
        }
        constexpr int cb = ci < 16 ? 64 * (ci >> 3) + 512 * (ci & 7) : 512 * (ci - 16);
        const char* hp = (ci < 16 ? hpA : hpC) + cb + ((ci == 0 && zA) || (ci == 16 && zC) ? 32 : 0);
        __builtin_memcpy(&caf[ci], __builtin_assume_aligned(hp + cvo, 4), 16);
        cax[ci] = *(const unsigned*)(hp + cvx);
      }
    });
    if constexpr (g + PD < 324) {
      constexpr int g1 = g + PD, st1 = g1 / 36, f1 = g1 - st1 * 36;
      if constexpr (f1 == 0) {
        if constexpr (st1 != 4) {
          unsigned b2 = rbU; asm volatile("" : "+v"(b2));
#pragma unroll
          for (int j = 0; j < 4; ++j) { rb[j] = (b2 + (st1 % K1_NSLOT) * K1_SLOT) ^ (j << 5); asm volatile("" : "+v"(rb[j])); }
        } else {
          int l2 = lane; asm volatile("" : "+v"(l2));
          rb[0] = lds0 + (8 * (l2 >> 5) + ((l2 & 15) >> 2)) * 32 + ((l2 >> 4) & 1) * K1_UOFF + (l2 & 3) * 8 + (st1 % K1_NSLOT) * K1_SLOT;
          asm volatile("" : "+v"(rb[0]));
        }
      }
      if constexpr (st1 != 4) af[g1 % (PD + 1)] = tr_frag((LAS char*)(uintptr_t)rb[f1 % 4], (f1 / 4) * 2048, 512);
      else af[g1 % (PD + 1)] = tr_frag((LAS char*)(uintptr_t)rb[0], f1 * 512, 128);
    }
    if constexpr (st < 4 && c == 0) {
      asm volatile("s_waitcnt vmcnt(%1)" : "+v"(xf[xi]) : "i"(k1_younger(k1_xkey(xi), 4 * g + 3)) : "memory");
    }
    SCHED0();
    acc[ai] = __builtin_amdgcn_mfma_f32_32x32x16_bf16(af[g % (PD + 1)], xf[xi], first ? zero16 : acc[ai], 0, 0, 0);
    SCHED0();
    if constexpr (g == 143) {
#pragma unroll
      for (int i = 0; i < 4; ++i) ho[i] = h_word(acc[0], i);
      h_store(ho, colA >> 4);
      SCHED0();
    }
    if constexpr (g > 144 && g <= 162) {
      constexpr int cc = (g - 145) / 6 + 1, pi = (g - 145) % 6;
      if constexpr (pi < 4) ho[pi] = h_word(acc[cc], pi);
      if constexpr (pi == 5) h_store(ho, (colA >> 4) + cc);
      SCHED0();
    }
    if constexpr (g >= 180 && g <= 182) {
      if constexpr (g == 180) { ho[0] = h_word(acc[0], 0); ho[1] = h_word(acc[0], 1); }
      if constexpr (g == 181) { ho[2] = h_word(acc[0], 2); ho[3] = h_word(acc[0], 3); }
      if constexpr (g == 182) h_store(ho, colQ >> 4);
      SCHED0();
    }
  });
  const char* wbase2 = (const char*)wd + (size_t)kbase * (D_ * 2);
  unsigned voff2[4];
  int lane2 = lane; asm volatile("" : "+v"(lane2));
#pragma unroll
  for (int n = 0; n < 4; ++n) {
    const int o = (tq * 4 + n) * 1024 + lane2 * 16;
    const int k = o >> 8, pc = (o >> 4) & 15;
    const int j = (pc >> 1) ^ (k & 7);
    voff2[n] = (unsigned)(k * (D_ * 2) + (2 * j + (pc & 1)) * 16);
  }
  const unsigned ldst2 = lds0 + khf * K2_HALF;
  auto piece_load2 = [&](int st, int n) ALWAYS_INLINE {
    const int slot = st % K2_NSLOT;
    if (st < 9) {
      const char* g = wbase2 + (size_t)st * 64 * (D_ * 2) + colA2 * 2;
      glds1p<false>(g, voff2[n], ldst2 + slot * K2_SLOT + tq * 4096 + n * 1024);
    } else {
      int l2 = lane; asm volatile("" : "+v"(l2));
      const char* g = wbase2 + (size_t)(st - 9) * 288 * (D_ * 2) + colQ2 * 2;
      const int o = n < 2 ? tq * 2048 + n * 1024 + l2 * 16 : 8192 + tq * 256 + (l2 & 15) * 16;
      const unsigned v0 = (unsigned)((o >> 5) * (D_ * 2) + ((o >> 4) & 1) * 16);
      if (n < 2 || l2 < 16) glds1p<false>(g, v0, ldst2 + slot * K2_SLOT + (n < 2 ? tq * 2048 + n * 1024 : 8192 + tq * 256));
    }
  };
  const char* hbase = (const char*)hout + ((size_t)(mt * 8 + tq * 2) * 144 * 64 + (size_t)(kbase >> 5) * 64) * 16;
  const unsigned hv0 = lane2 * 16 + khf * (144 * 64 * 16), hv1 = lane2 * 16 + (1 - khf) * (144 * 64 * 16);
  bf16x8 hf[18][2];
  auto h_load = [&](auto lc) ALWAYS_INLINE {
    constexpr int k = lc / 2, t = lc % 2;
    gload16_sc1<(k & 3) * 1024>(hf[k][t], hbase + (k >> 2) * 4096, t ? hv1 : hv0);
  };
  STAMP(1, 2);
  __builtin_amdgcn_s_setprio(0);
  bf16x8 cf[24];
  {
    const unsigned sh = ((lane + 1) & 1) * 2;
#pragma unroll
    for (int i = 0; i < 24; ++i) {
      u32x4 r;
      r.x = __builtin_amdgcn_alignbyte(caf[i].y, caf[i].x, sh);
      r.y = __builtin_amdgcn_alignbyte(caf[i].z, caf[i].y, sh);
      r.z = __builtin_amdgcn_alignbyte(caf[i].w, caf[i].z, sh);
      r.w = __builtin_amdgcn_alignbyte(cax[i], caf[i].w, sh);
      cf[i] = __builtin_bit_cast(bf16x8, r);
      asm volatile("" : "+v"(cf[i]));
    }
  }

#pragma unroll
  for (int cc = 0; cc < 4; ++cc) {
    u32x4 o;
#pragma unroll
    for (int i = 0; i < 4; ++i) o[i] = h_word(acc[(cc + 1) & 3], i);
    h_store(o, (colA >> 4) + 4 + cc);
  }

  __builtin_amdgcn_s_barrier();
  CFENCE();
#pragma unroll
  for (int n = 0; n < 4; ++n) piece_load2(0, n);
#pragma unroll
  for (int n = 0; n < 4; ++n) piece_load2(1, n);

  STAMP(1, 3);
  const int ct0 = lane & 15, ckg = lane >> 4;
  {
    const int boff = (2032 - 16 * (lane & 15) + 8 * ckg) * 2;
    auto head_frag = [&](int im) ALWAYS_INLINE -> bf16x8 {
      bf16x8 r;
      LAS u16* hz = (LAS u16*)(smem + HZ_OFF + im * 96) + 16 + (ct0 - 15 + 8 * ckg);
#pragma unroll
      for (int j = 0; j < 8; ++j) r[j] = (short)hz[j];
      return r;
    };
    if (zA) cf[0] = head_frag(w >> 2);
    if (zC) cf[16] = head_frag(2);
    {
      const int im = w >> 2;
      LAS char* xr = smem + XR_OFF + im * XR_STRIDE + boff + 128 * (w & 3);
      f32x4 ac[8];
#pragma unroll
      for (int j = 0; j < 8; ++j) ac[j] = (f32x4){0.f, 0.f, 0.f, 0.f};
#pragma unroll
      for (int rr = 1; rr >= 0; --rr) {
        bf16x8 bfm[8];
#pragma unroll
        for (int b = 0; b < 8; ++b) bfm[b] = *(LAS bf16x8*)(xr - 512 * b + 64 * rr);
#pragma unroll
        for (int a = 7; a >= 0; --a)
#pragma unroll
          for (int j = a; j < 8; ++j) ac[j] = __builtin_amdgcn_mfma_f32_16x16x32_bf16(cf[rr * 8 + a], bfm[j - a], ac[j], 0, 0, 0);
      }
#pragma unroll
      for (int j = 0; j < 8; ++j)
        *(LAS f32x4*)(smem + PART_OFF + w * 8192 + (256 * j + 16 * (lane & 15) + 4 * ckg) * 4) = ac[j];
    }
    wait_vm<8>();
    if (lane == 0) {
      const unsigned old = __hip_atomic_fetch_add((LAS unsigned*)(smem + HCNT_OFF), 1u, __ATOMIC_RELAXED, __HIP_MEMORY_SCOPE_WORKGROUP);
      if (old == 7u) set_flag(flg + FL_H + (mtile * 4 + (cgrp >> 1)) * 8 + (cgrp & 1) * 4 + cq, tag);
    }
    {
      LAS char* xr = smem + XR_OFF + 2 * XR_STRIDE + boff + 64 * w;
      const int jA = 7 - qd, jB = qd;
      bf16x8 b0[8], b1[4];
#pragma unroll
      for (int a = 0; a < 8; ++a) { const int bb = jA - a; b0[a] = *(LAS bf16x8*)(xr - 512 * (bb < 0 ? 0 : bb)); }
#pragma unroll
      for (int a = 0; a < 4; ++a) { const int bb = jB - a; b1[a] = *(LAS bf16x8*)(xr - 512 * (bb < 0 ? 0 : bb)); }
      f32x4 c0 = (f32x4){0.f, 0.f, 0.f, 0.f}, c1 = c0;
#pragma unroll
      for (int a = 7; a >= 0; --a) {
        if (a <= jA) c0 = __builtin_amdgcn_mfma_f32_16x16x32_bf16(cf[16 + a], b0[a], c0, 0, 0, 0);
        if (a < 4 && a <= jB) c1 = __builtin_amdgcn_mfma_f32_16x16x32_bf16(cf[16 + a], b1[a < 4 ? a : 0], c1, 0, 0, 0);
      }
      *(LAS f32x4*)(smem + PARTC_OFF + w * 2048 + (16 * (lane & 15) + 4 * ckg) * 4) = c0;
      *(LAS f32x4*)(smem + PARTC_OFF + w * 2048 + 1024 + (16 * (lane & 15) + 4 * ckg) * 4) = c1;
    }
  }
  STAMP(1, 4);
  if (w == 0) poll_flags<1>(flg + FL_H + ((mt >> 1) * 4 + ksl) * 8, 8, tag, lane);
  WAIT_L(0);
  __builtin_amdgcn_s_barrier();
  CFENCE();
  STAMP(1, 5);
  sfor<12>([&](auto kc) ALWAYS_INLINE { h_load(kc); });
  auto finalize = [&](f32x4 s0, f32x4 s1, int im, int ch, int t8, float dd) ALWAYS_INLINE {
    const u32x4 kvv = *(LAS u32x4*)(smem + XR_OFF + im * XR_STRIDE + (2040 - t8) * 2);
    float y[8] = {s0[0], s0[1], s0[2], s0[3], s1[0], s1[1], s1[2], s1[3]};
    float o[8];
#pragma unroll
    for (int e = 0; e < 8; ++e) {
      const int ri = 7 - e;
      const float kvf = (ri & 1) ? bf_hi(kvv[ri >> 1]) : bf_lo(kvv[ri >> 1]);
      o[e] = bfr(y[e]) + bfr(kvf * dd);
    }
    u32x4 ov; ov.x = pk_bf16(o[0], o[1]); ov.y = pk_bf16(o[2], o[3]); ov.z = pk_bf16(o[4], o[5]); ov.w = pk_bf16(o[6], o[7]);
    gstore16_sc1(wT + (size_t)ch * S_ + t8, ov);
  };
  {
    const int im = tid >> 8, t8 = (tid & 255) * 8;
    f32x4 s0 = (f32x4){0.f, 0.f, 0.f, 0.f}, s1 = s0;
#pragma unroll
    for (int ww = 0; ww < 4; ++ww) {
      s0 += *(LAS f32x4*)(smem + PART_OFF + (im * 4 + ww) * 8192 + t8 * 4);
      s1 += *(LAS f32x4*)(smem + PART_OFF + (im * 4 + ww) * 8192 + t8 * 4 + 16);
    }
    finalize(s0, s1, im, chA + im, t8, ddA);
  }
  if (tid >= 448) {
    const int t2 = tid - 448;
    const int tl = t2 >> 5, l8 = (t2 & 31) * 8;
    const int jt = tl ? qd : 7 - qd, t8 = 256 * jt + l8;
    f32x4 s0 = (f32x4){0.f, 0.f, 0.f, 0.f}, s1 = s0;
#pragma unroll
    for (int ww = 0; ww < 8; ++ww) {
      s0 += *(LAS f32x4*)(smem + PARTC_OFF + ww * 2048 + tl * 1024 + l8 * 4);
      s1 += *(LAS f32x4*)(smem + PARTC_OFF + ww * 2048 + tl * 1024 + l8 * 4 + 16);
    }
    finalize(s0, s1, 2, chC, t8, ddC);
  }
  STAMP(1, 6);

  STAMP(2, 0);
  {
  const int kq = lane2 >> 4, qq = (lane2 & 15) >> 2, p4 = lane2 & 3;
  const unsigned rbU2 = lds0 + khf * K2_HALF + (16 * (kq >> 1) + 4 * (kq & 1) + qq) * 256 + ((4 * (kq & 1) + qq) << 5) + p4 * 8;

  f32x4 acc[2][8], accq[2];
  const f32x4 zero4 = {0.f, 0.f, 0.f, 0.f};

  const int tile = mt * 4 + nt;
  const __amdgpu_buffer_rsrc_t rs = __builtin_amdgcn_make_buffer_rsrc((char*)slab + (size_t)tile * (16 * 18432), 0, 16 * 18432, 0x00020000);
  const bool foreign = tq != ksl;
  auto give = [&](int xb, int i, const f32x4& a0, const f32x4& a1) ALWAYS_INLINE {
    *(LAS f32x4*)(smem + xb + ((w * 4 + i) * 64 + lane) * 16) = a1;
  };
  auto publish = [&](int xb, int i, const f32x4& a0, const f32x4& a1, int dtg) ALWAYS_INLINE {
    const f32x4 sm = a0 + *(LAS f32x4*)(smem + xb + (((w ^ 4) * 4 + i) * 64 + lane) * 16);
    if (foreign) __builtin_amdgcn_raw_buffer_store_b128(__builtin_bit_cast(u32x4, sm), rs, (unsigned)(((tq * 4 + ksl) * 18 + khf * 9 + dtg) * 1024 + lane * 16), 0, 16);
    else *(LAS f32x4*)(smem + K2_OWN_OFF + ((khf * 9 + dtg) * 64 + lane) * 16) = sm;
  };
  constexpr int XB1 = 3 * K2_SLOT, XB2 = 0, XB3 = 3 * K2_SLOT;

  asm volatile("s_waitcnt vmcnt(%4)" : "+v"(hf[0][0]), "+v"(hf[0][1]), "+v"(hf[1][0]), "+v"(hf[1][1])
               : "i"(k2_younger(k2_hkey(0, 3), 0, false)) : "memory");
  WAIT_L(0);
  __builtin_amdgcn_s_barrier();
  CFENCE();
  STAMP(2, 1);

  constexpr int PD = 2;
  bf16x8 af[PD + 1];
  unsigned sb = rbU2;
#pragma unroll
  for (int g = 0; g < PD; ++g) af[g] = tr_frag((LAS char*)(uintptr_t)(sb ^ (g << 5)), 0, 2048);
  sfor<162>([&](auto gc) ALWAYS_INLINE {
    constexpr int g = gc;
    constexpr int st = k2_stage(g), f = g - k2_g0(st);
    constexpr int ks = st < 9 ? st * 2 + f / 8 : (st - 9) * 9 + f, dt = st < 9 ? f % 8 : 0;
    if constexpr (f == k2_mid(st)) {
      if constexpr (st < 8) { STAMP(0, 2 * st); }
      if constexpr (st == K2_WST) { if (w == 0) poll_flags<4>(flg + FL_W, 64, tag, lane); }
      if constexpr (st < 10) {
        constexpr int okey = cmax(k2_dkey(st + 1, k2_np(st + 1) - 1), st + 1 < 9 ? k2_hkey(st + 1, 3) : -1000);
        constexpr int n0 = k2_younger(okey, 4 * g, false);
        if constexpr (st == 0) { if (w == 7) wait_vm<n0 + 1>(); else wait_vm<n0>(); }
        else wait_vm<n0>();
        if constexpr (st + 1 < 9) {
          constexpr int k0 = 2 * (st + 1);
          asm volatile("" : "+v"(hf[k0][0]), "+v"(hf[k0][1]), "+v"(hf[k0 + 1][0]), "+v"(hf[k0 + 1][1])::"memory");
        }
      }
      if constexpr (st >= 9) WAIT_L(0);
      __builtin_amdgcn_s_barrier();
      CFENCE();
      if constexpr (st < 8) { STAMP(0, 2 * st + 1); }
      if constexpr (st == 1) { if (tid == 0) set_flag(flg + FL_W + bid, tag); }
      if constexpr (st == 9) {
#pragma unroll
        for (int i = 0; i < 4; ++i) publish(XB1, i, acc[0][i], acc[1][i], i);
#pragma unroll
        for (int i = 0; i < 4; ++i) give(XB2, i, acc[0][4 + i], acc[1][4 + i]);
      }
      if constexpr (st == 10) {
#pragma unroll
        for (int i = 0; i < 4; ++i) publish(XB2, i, acc[0][4 + i], acc[1][4 + i], 4 + i);
      }
    }
    if constexpr (st < 9 && ((f + 16 - 6) % 16) % 4 == 0) __builtin_amdgcn_s_setprio(3 - ((f + 16 - 6) % 16) / 4);
    sfor<36>([&](auto dc) ALWAYS_INLINE { if constexpr (dc % 4 < k2_np(dc / 4 + 2) && k2_dkey(dc / 4 + 2, dc % 4) == 4 * g + 2) piece_load2(dc / 4 + 2, dc % 4); });
    sfor<24>([&](auto hc) ALWAYS_INLINE { if constexpr (k2_hkey(hc / 4 + 3, hc % 4) == 4 * g + 3) h_load(std::integral_constant<int, hc + 12>{}); });
    if constexpr (g + PD < 162) {
      constexpr int g1 = g + PD, st1 = k2_stage(g1), f1 = g1 - k2_g0(st1);
      if constexpr (f1 == 0) {
        if constexpr (st1 < 9) {
          sb += (st1 % K2_NSLOT == 0) ? -3 * K2_SLOT : K2_SLOT;
        } else {
          int l2 = lane; asm volatile("" : "+v"(l2));
          const int kq2 = l2 >> 4;
          sb = lds0 + khf * K2_HALF + (16 * (kq2 >> 1) + 4 * (kq2 & 1) + ((l2 & 15) >> 2)) * 32 + (l2 & 3) * 8 + (st1 % K2_NSLOT) * K2_SLOT;
        }
      }
      asm volatile("" : "+v"(sb));
      if constexpr (st1 < 9) af[g1 % (PD + 1)] = tr_frag((LAS char*)(uintptr_t)(sb ^ ((f1 % 8) << 5)), (f1 / 8) * 8192, 2048);
      else af[g1 % (PD + 1)] = tr_frag((LAS char*)(uintptr_t)sb, f1 * 1024, 256);
    }
    SCHED0();
#pragma unroll
    for (int t = 0; t < 2; ++t) {
      if constexpr (st < 9) acc[t][dt] = __builtin_amdgcn_mfma_f32_16x16x32_bf16(af[g % (PD + 1)], hf[ks][t], g < 8 ? zero4 : acc[t][dt], 0, 0, 0);
      else accq[t] = __builtin_amdgcn_mfma_f32_16x16x32_bf16(af[g % (PD + 1)], hf[ks][t], g == 144 ? zero4 : accq[t], 0, 0, 0);
    }
    SCHED0();
    if constexpr (g == 143) {
#pragma unroll
      for (int i = 0; i < 4; ++i) give(XB1, i, acc[0][i], acc[1][i]);
      SCHED0();
    }
  });
  STAMP(2, 2);
  __builtin_amdgcn_s_setprio(0);
  give(XB3, 0, accq[0], accq[1]);

  int lane3 = lane; asm volatile("" : "+v"(lane3));
  bf16x8 wt0, wt1;
  {
    const char* wtb = (const char*)(wT + mt * 128 + ksl * 32);
    gload16_sc1<0>(wt0, wtb, (unsigned)((colA2 + w * 16 + (lane3 >> 2)) * (S_ * 2) + (lane3 & 3) * 16));
    gload16_sc1<0>(wt1, wtb, (unsigned)((colQ2 + (lane3 >> 2)) * (S_ * 2) + (lane3 & 3) * 16));
  }
  u32x2 xq[3];
  float2 stt[3];
#pragma unroll
  for (int i = 0; i < 3; ++i) {
    const int fi = (w + 8 * i < 18) ? w + 8 * i : w;
    const int t = fi >= 9 ? 1 : 0, dt = fi - 9 * t;
    const int token = mt * 128 + ksl * 32 + t * 16 + (lane3 & 15);
    xq[i] = *(const u32x2*)(q + (size_t)token * D_ + (dt < 8 ? colA2 + dt * 16 : colQ2) + 4 * (lane3 >> 4));
    const u64 sv = __hip_atomic_load(stats + token, __ATOMIC_RELAXED, __HIP_MEMORY_SCOPE_AGENT);
    stt[i] = make_float2(__uint_as_float((unsigned)sv), __uint_as_float((unsigned)(sv >> 32)));
  }
  WAIT_L(0);
  __builtin_amdgcn_s_barrier();
  CFENCE();
  publish(XB3, 0, accq[0], accq[1], 8);
  asm volatile("s_waitcnt vmcnt(0)" : "+v"(wt0), "+v"(wt1)::"memory");
  *(LAS bf16x8*)(smem + K2_WT_OFF + w * 1024 + lane * 16) = wt0;
  if (w == 0) *(LAS bf16x8*)(smem + K2_WT_OFF + 8192 + lane * 16) = wt1;
  __syncthreads();
  STAMP(2, 3);
  if (w == 0) {
    if (lane == 0) set_flag(flg + FL_S + tile * 4 + ksl, tag);
    poll_flags<1>(flg + FL_S + tile * 4, 4, tag, lane);
  }
  __syncthreads();
  STAMP(2, 4);
  __builtin_amdgcn_fence(__ATOMIC_ACQUIRE, "wavefront");
  {
    f32x4 pv[3][4];
#pragma unroll
    for (int i = 0; i < 3; ++i) {
      const int fi = (w + 8 * i < 18) ? w + 8 * i : w;
#pragma unroll
      for (int k = 1; k < 4; ++k)
        pv[i][k] = __builtin_bit_cast(f32x4, __builtin_amdgcn_raw_buffer_load_b128(rs, (unsigned)(((ksl * 4 + ((ksl + k) & 3)) * 18 + fi) * 1024 + lane * 16), 0, 16));
      pv[i][0] = *(LAS f32x4*)(smem + K2_OWN_OFF + (fi * 64 + lane) * 16);
    }
#pragma unroll
    for (int i = 0; i < 3; ++i) {
      if (w + 8 * i < 18) {
        const int fi = w + 8 * i;
        const int t = fi >= 9 ? 1 : 0, dt = fi - 9 * t;
        const int tl = t * 16 + (lane3 & 15), dl = dt * 16 + 4 * (lane3 >> 4);
        const f32x4 bs = ((pv[i][0] + pv[i][1]) + pv[i][2]) + pv[i][3];
        float o[4];
#pragma unroll
        for (int c = 0; c < 4; ++c) {
          const unsigned xw = (c < 2) ? xq[i].x : xq[i].y;
          const float x = (c & 1) ? bf_hi(xw) : bf_lo(xw);
          const float wy = bf2f(*(LAS u16*)(smem + K2_WT_OFF + (dl + c) * 64 + tl * 2));
          const float xn = bfr(bfr(x - stt[i].x) * stt[i].y);
          const float a = bfr(xn * wy);
          o[c] = bfr(x + a) + bfr(bs[c]);
        }
        u32x2 ov; ov.x = pk_bf16(o[0], o[1]); ov.y = pk_bf16(o[2], o[3]);
        *(u32x2*)(out + (size_t)(mt * 128 + ksl * 32 + tl) * D_ + (dt < 8 ? colA2 + dt * 16 : colQ2) + 4 * (lane3 >> 4)) = ov;
      }
    }
  }
  if (bid == 0 && w == 0) {
    poll_flags<4>(flg + FL_K, 64, tag, lane);
    if (lane == 0) set_flag(flg + FL_GEN, tag);
  }
  STAMP(2, 5); RSTAMP(1, 15); STAMP_FLUSH(1); STAMP_FLUSH(2); STAMP_FLUSH(0);
  }
}

extern "C" void kernel_launch(void* const* d_in, const int* in_sizes, int n_in,
                              void* d_out, int out_size, void* d_ws, size_t ws_size,
                              hipStream_t stream) {
  const u16* q = (const u16*)d_in[0];
  const u16* k = (const u16*)d_in[1];
  const u16* v = (const u16*)d_in[2];
  const u16* shk = (const u16*)d_in[3];
  const u16* ssmk = (const u16*)d_in[4];
  const u16* ssmd = (const u16*)d_in[5];
  const u16* wg = (const u16*)d_in[7];
  const u16* wu = (const u16*)d_in[8];
  const u16* wd = (const u16*)d_in[9];
  (void)hipFuncSetAttribute((const void*)k_fused, hipFuncAttributeMaxDynamicSharedMemorySize, K1_LDS);
  k_fused<<<256, 512, K1_LDS, stream>>>(q, k, v, shk, ssmk, ssmd, wg, wu, wd, (char*)d_ws, (u16*)d_out);
}
```

```cpp
#include <hip/hip_runtime.h>
#include <hip/hip_bf16.h>
#include <stdint.h>
#include <utility>

typedef unsigned short u16;
typedef short bf16x8 __attribute__((ext_vector_type(8)));
typedef short s16x4 __attribute__((ext_vector_type(4)));
typedef float f32x4 __attribute__((ext_vector_type(4)));
typedef float f32x16 __attribute__((ext_vector_type(16)));
typedef unsigned u32x4 __attribute__((ext_vector_type(4)));
typedef unsigned u32x2 __attribute__((ext_vector_type(2)));
typedef __bf16 bf2_t __attribute__((ext_vector_type(2)));
#define LAS __attribute__((address_space(3)))
#define DI __device__ __forceinline__

constexpr int S_ = 2048, D_ = 576, F_ = 4608;

constexpr size_t WS_XFRAG = 0;
constexpr size_t WS_STATS = WS_XFRAG + (size_t)S_ * D_ * 2;
constexpr size_t WS_KVR   = WS_STATS + (size_t)S_ * 8;
constexpr size_t WS_WT    = WS_KVR + (size_t)S_ * D_ * 2;
constexpr size_t WS_H     = WS_WT + (size_t)S_ * D_ * 2;
constexpr size_t WS_SLAB  = WS_H + (size_t)S_ * F_ * 2;
constexpr size_t WS_FLG   = WS_SLAB + (size_t)4 * S_ * D_ * 4;
constexpr int FL_GEN = 0, FL_X = 32, FL_K = FL_X + 256, FL_H = FL_K + 256, FL_W = FL_H + 256, FL_S = FL_W + 256, FL_END = FL_S + 256;
constexpr size_t WS_END   = WS_FLG + FL_END * 8;
typedef unsigned long long u64;

DI float bf_lo(unsigned w) { return __uint_as_float(w << 16); }
DI float bf_hi(unsigned w) { return __uint_as_float(w & 0xffff0000u); }
DI float bf2f(u16 b) { return __uint_as_float(((unsigned)b) << 16); }
DI float bfr(float x) { return (float)(__bf16)x; }
typedef float f32x2 __attribute__((ext_vector_type(2)));
DI unsigned pk_bf16(float lo, float hi);
DI f32x2 unpk(unsigned w) { return (f32x2){__uint_as_float(w << 16), __uint_as_float(w & 0xffff0000u)}; }
DI f32x2 bfr2(f32x2 v) { return unpk(pk_bf16(v.x, v.y)); }
DI unsigned pk_bf16(float lo, float hi) { bf2_t v = {(__bf16)lo, (__bf16)hi}; return __builtin_bit_cast(unsigned, v); }

DI float wave_sum(float x) {
  x += __builtin_bit_cast(float, __builtin_amdgcn_update_dpp(0, __builtin_bit_cast(int, x), 0xB1, 0xf, 0xf, true));
  x += __builtin_bit_cast(float, __builtin_amdgcn_update_dpp(0, __builtin_bit_cast(int, x), 0x4E, 0xf, 0xf, true));
  x += __builtin_bit_cast(float, __builtin_amdgcn_update_dpp(0, __builtin_bit_cast(int, x), 0x141, 0xf, 0xf, true));
  x += __builtin_bit_cast(float, __builtin_amdgcn_update_dpp(0, __builtin_bit_cast(int, x), 0x140, 0xf, 0xf, true));
  const int xi = __builtin_bit_cast(int, x);
  return (__builtin_bit_cast(float, __builtin_amdgcn_readlane(xi, 0)) + __builtin_bit_cast(float, __builtin_amdgcn_readlane(xi, 16))) +
         (__builtin_bit_cast(float, __builtin_amdgcn_readlane(xi, 32)) + __builtin_bit_cast(float, __builtin_amdgcn_readlane(xi, 48)));
}
#define WAIT_V(n) asm volatile("s_waitcnt vmcnt(" #n ")" ::: "memory")
#define WAIT_L(n) asm volatile("s_waitcnt lgkmcnt(" #n ")" ::: "memory")
#define CFENCE() asm volatile("" ::: "memory")
#define STAMP(kid, i)
#define RSTAMP(kid, i)
#define STAMP_DECL
#define STAMP_FLUSH(kid)
#define K0STAMP(i)
#define SCHED0() __builtin_amdgcn_sched_barrier(0)
template <int... I, class Fn> DI void sfor_impl(std::integer_sequence<int, I...>, Fn&& f) { (f(std::integral_constant<int, I>{}), ...); }
template <int N, class Fn> DI void sfor(Fn&& f) { sfor_impl(std::make_integer_sequence<int, N>{}, f); }
#define ALWAYS_INLINE __attribute__((always_inline))
template <int N> DI void wait_vm() { asm volatile("s_waitcnt vmcnt(%0)" ::"i"(N) : "memory"); }

DI void glds4(const void* sbase, unsigned v0, unsigned v1, unsigned v2, unsigned v3, unsigned lds_dst) {
  unsigned keep;
  asm volatile("s_mov_b32 %0, m0\n\ts_mov_b32 m0, %6\n\ts_nop 0\n\t"
               "global_load_lds_dwordx4 %1, %5\n\t"
               "global_load_lds_dwordx4 %2, %5 offset:1024\n\t"
               "global_load_lds_dwordx4 %3, %5 offset:2048\n\t"
               "global_load_lds_dwordx4 %4, %5 offset:3072\n\t"
               "s_mov_b32 m0, %0"
               : "=&s"(keep) : "v"(v0), "v"(v1), "v"(v2), "v"(v3), "s"(sbase), "s"(lds_dst) : "memory");
}
DI void glds3(const void* sbase, unsigned v0, unsigned v1, unsigned v2, unsigned lds_dst) {
  unsigned keep;
  asm volatile("s_mov_b32 %0, m0\n\ts_mov_b32 m0, %5\n\ts_nop 0\n\t"
               "global_load_lds_dwordx4 %1, %4\n\t"
               "global_load_lds_dwordx4 %2, %4 offset:1024\n\t"
               "global_load_lds_dwordx4 %3, %4 offset:2048\n\t"
               "s_mov_b32 m0, %0"
               : "=&s"(keep) : "v"(v0), "v"(v1), "v"(v2), "s"(sbase), "s"(lds_dst) : "memory");
}
DI void glds2(const void* sbase, unsigned v0, unsigned v1, unsigned lds_dst) {
  unsigned keep;
  asm volatile("s_mov_b32 %0, m0\n\ts_mov_b32 m0, %4\n\ts_nop 0\n\t"
               "global_load_lds_dwordx4 %1, %3\n\t"
               "global_load_lds_dwordx4 %2, %3 offset:1024\n\t"
               "s_mov_b32 m0, %0"
               : "=&s"(keep) : "v"(v0), "v"(v1), "s"(sbase), "s"(lds_dst) : "memory");
}
DI void glds1(const void* sbase, unsigned v0, unsigned lds_dst) {
  unsigned keep;
  asm volatile("s_mov_b32 %0, m0\n\ts_mov_b32 m0, %3\n\ts_nop 0\n\t"
               "global_load_lds_dwordx4 %1, %2\n\t"
               "s_mov_b32 m0, %0"
               : "=&s"(keep) : "v"(v0), "s"(sbase), "s"(lds_dst) : "memory");
}

template <bool NT> DI void glds1p(const void* sbase, unsigned v0, unsigned lds_dst) {
  unsigned keep;
  if constexpr (NT)
    asm volatile("s_mov_b32 %0, m0\n\ts_mov_b32 m0, %3\n\ts_nop 0\n\tglobal_load_lds_dwordx4 %1, %2 nt\n\ts_mov_b32 m0, %0"
                 : "=&s"(keep) : "v"(v0), "s"(sbase), "s"(lds_dst) : "memory");
  else
    asm volatile("s_mov_b32 %0, m0\n\ts_mov_b32 m0, %3\n\ts_nop 0\n\tglobal_load_lds_dwordx4 %1, %2\n\ts_mov_b32 m0, %0"
                 : "=&s"(keep) : "v"(v0), "s"(sbase), "s"(lds_dst) : "memory");
}
DI void glds1_sc1(const void* sbase, unsigned v0, unsigned lds_dst) {
  unsigned keep;
  asm volatile("s_mov_b32 %0, m0\n\ts_mov_b32 m0, %3\n\ts_nop 0\n\tglobal_load_lds_dwordx4 %1, %2 sc1\n\ts_mov_b32 m0, %0"
               : "=&s"(keep) : "v"(v0), "s"(sbase), "s"(lds_dst) : "memory");
}
DI void touch_lines(const void* sbase, unsigned v0, unsigned lds_dst) {
  unsigned keep;
  asm volatile("s_mov_b32 %0, m0\n\ts_mov_b32 m0, %3\n\ts_nop 0\n\tglobal_load_lds_dword %1, %2\n\ts_mov_b32 m0, %0"
               : "=&s"(keep) : "v"(v0), "s"(sbase), "s"(lds_dst) : "memory");
}
template <int OFF> DI void gload16(bf16x8& dst, const void* sbase, unsigned voff) {
  asm volatile("global_load_dwordx4 %0, %1, %2 offset:%3" : "=v"(dst) : "v"(voff), "s"(sbase), "i"(OFF) : "memory");
}
template <int OFF> DI void gload16_sc1(bf16x8& dst, const void* sbase, unsigned voff) {
  asm volatile("global_load_dwordx4 %0, %1, %2 offset:%3 sc1" : "=v"(dst) : "v"(voff), "s"(sbase), "i"(OFF) : "memory");
}
DI void gload16u(u32x4& dst, const void* sbase, unsigned voff) {
  asm volatile("global_load_dwordx4 %0, %1, %2" : "=v"(dst) : "v"(voff), "s"(sbase) : "memory");
}
template <int OFF> DI void gstore16_sc1_s(const void* sbase, unsigned voff, const u32x4& v) {
  asm volatile("global_store_dwordx4 %0, %1, %2 offset:%3 sc1\n\ts_nop 1" ::"v"(voff), "v"(v), "s"(sbase), "i"(OFF) : "memory");
}
DI void gstore16_sc1(void* p, const u32x4& v) { asm volatile("global_store_dwordx4 %0, %1, off sc1\n\ts_nop 1" ::"v"(p), "v"(v) : "memory"); }
DI void set_flag(u64* f, u64 tag) { __hip_atomic_store(f, tag, __ATOMIC_RELAXED, __HIP_MEMORY_SCOPE_AGENT); }
template <int PER> DI void poll_flags(u64* f, int n, u64 tag, int lane) {
  asm volatile("" : "+v"(lane));
  const int i = lane < n ? lane : 0;
  for (unsigned spins = 0; spins < (1u << 20); ++spins) {
    bool ok = true;
#pragma unroll
    for (int k = 0; k < PER; ++k) ok &= __hip_atomic_load(f + i + 64 * k, __ATOMIC_RELAXED, __HIP_MEMORY_SCOPE_AGENT) == tag;
    if (__all(ok)) break;
    __builtin_amdgcn_s_sleep(1);
  }
}

constexpr int K1_SLOT = 37120;
constexpr int K1_UOFF = 18432 + 128;
constexpr int K1_NSLOT = 4;
constexpr int K1_PF_OFF = K1_NSLOT * K1_SLOT;
constexpr int XR_OFF = K1_PF_OFF + 288, XR_STRIDE = 4608;
constexpr int HZ_OFF = XR_OFF + 3 * XR_STRIDE;
constexpr int K1_LDS = HZ_OFF + 288;
constexpr int PART_OFF = 65536;
constexpr int PARTC_OFF = PART_OFF + 65536;
constexpr int HCNT_OFF = K1_PF_OFF;
constexpr int XG_OFF = 3 * K1_SLOT;

DI bf16x8 tr_frag(LAS char* p, int off, int hi_off) {
  s16x4 lo = __builtin_amdgcn_ds_read_tr16_b64_v4i16((LAS s16x4*)(p + off));
  s16x4 hi = __builtin_amdgcn_ds_read_tr16_b64_v4i16((LAS s16x4*)(p + off + hi_off));
  return __builtin_shufflevector(lo, hi, 0, 1, 2, 3, 4, 5, 6, 7);
}

constexpr int K1_MID = 16;
constexpr int k1_xkey(int i) { return i < 9 ? -100 + i : 4 * (1 + 2 * (i - 9)) + 2; }
constexpr int k1_dstep(int d, int n) { return d == 2 ? 2 + 4 * n : d == 3 ? 24 + 4 * n : d == 4 ? 58 + 6 * n : d == 5 ? 94 + 6 * n : 36 * (d - 3) + K1_MID + 6 * n; }
constexpr int k1_dkey(int d, int n) { return d < 2 ? -400 + 10 * d + n : 4 * k1_dstep(d, n) + 1; }
constexpr int k1_cstep(int i) { return i < 7 ? 218 + 4 * i : (i < 14 ? 254 + 4 * (i - 7) : (i < 21 ? 289 + 2 * (i - 14) : 306 + 4 * (i - 21))); }
constexpr int k1_sstep(int c) { return c == 0 ? 143 : (c < 4 ? 144 + 6 * c : 182); }
constexpr int k1_zstep(int i) { return 272 + 3 * i; }
constexpr int k1_astep(int i) { return i < 14 ? k1_cstep(i) + 28 : (i < 21 ? k1_cstep(i) + 20 : 1000); }
constexpr int K1_TOUCH = 7 * 36 + K1_MID + 2;
constexpr int k1_younger(int okey, int wkey) {
  int n = 0;
  if (4 * K1_TOUCH + 1 > okey && 4 * K1_TOUCH + 1 < wkey) n += 1;
  for (int i = 0; i < 9; ++i) if (4 * k1_zstep(i) + 2 > okey && 4 * k1_zstep(i) + 2 < wkey) n += 1;
  if (-70 > okey && -70 < wkey) n += 2;
  for (int i = 0; i < 36; ++i) if (k1_xkey(i) > okey && k1_xkey(i) < wkey) n += 1;
  for (int d = 0; d < 9; ++d) for (int pc = 0; pc < 5; ++pc) if (k1_dkey(d, pc) > okey && k1_dkey(d, pc) < wkey) n += 1;
  for (int i = 0; i < 24; ++i) if (4 * k1_cstep(i) + 2 > okey && 4 * k1_cstep(i) + 2 < wkey) n += 2;
  for (int c = 0; c < 5; ++c) if (4 * k1_sstep(c) + 3 > okey && 4 * k1_sstep(c) + 3 < wkey) n += 1;
  return n;
}
#define HSTORE 1

constexpr int K2_HALF = 16384;
constexpr int K2_SLOT = 2 * K2_HALF;
constexpr int K2_NSLOT = 4;
constexpr int K2_OWN_OFF = K2_NSLOT * K2_SLOT;
constexpr int K2_WT_OFF = K2_OWN_OFF + 18432;
constexpr int k2_g0(int st) { return st <= 9 ? 16 * st : 153; }
constexpr int k2_stage(int g) { return g < 144 ? g / 16 : (g < 153 ? 9 : 10); }
constexpr int k2_mid(int st) { return st < 9 ? 6 : 4; }
constexpr int k2_np(int d) { return d < 9 ? 4 : 3; }
constexpr int k2_dkey(int d, int n) { return d < 2 ? -300 + 10 * d + n : (d == 2 ? 4 * (1 + n) + 2 : 4 * (k2_g0(d - 3) + k2_mid(d - 3) + 4 * n) + 2); }
constexpr int k2_hkey(int j, int m) { return j < 3 ? -250 + 10 * j + m : 4 * (k2_g0(j - 3) + k2_mid(j - 3) + 2 + 4 * m) + 3; }
constexpr int k2_skey(int p) { return 4 * (k2_g0(9 + p) + k2_mid(9 + p)) + 1; }
constexpr int K2_WST = 7;
constexpr int k2_wkey() { return 4 * (k2_g0(K2_WST) + k2_mid(K2_WST)) + 1; }
constexpr int k2_younger(int okey, int wkey, bool stores) {
  int n = 0;
  if (k2_wkey() > okey && k2_wkey() < wkey) n += 2;
  if (-100 > okey && -100 < wkey) n += 1;
  for (int d = 0; d < 11; ++d) for (int pc = 0; pc < k2_np(d); ++pc) if (k2_dkey(d, pc) > okey && k2_dkey(d, pc) < wkey) n += 1;
  for (int j = 0; j < 9; ++j) for (int m = 0; m < 4; ++m) if (k2_hkey(j, m) > okey && k2_hkey(j, m) < wkey) n += 1;
  if (stores) for (int p = 0; p < 2; ++p) if (k2_skey(p) > okey && k2_skey(p) < wkey) n += 4;
  return n;
}
constexpr int cmax(int a, int b) { return a > b ? a : b; }

__global__ __launch_bounds__(512, 2) void k_fused(const u16* __restrict__ q, const u16* __restrict__ kk, const u16* __restrict__ vv,
                                                  const u16* __restrict__ shk, const u16* __restrict__ ssmk, const u16* __restrict__ ssmd,
                                                  const u16* __restrict__ wg, const u16* __restrict__ wu, const u16* __restrict__ wd,
                                                  char* ws, u16* __restrict__ out) {
  u16* xfrag = (u16*)(ws + WS_XFRAG);
  u64* stats = (u64*)(ws + WS_STATS);
  u16* kvr = (u16*)(ws + WS_KVR);
  u16* wT = (u16*)(ws + WS_WT);
  u16* hout = (u16*)(ws + WS_H);
  float* slab = (float*)(ws + WS_SLAB);
  u64* flg = (u64*)(ws + WS_FLG);
  extern __shared__ __attribute__((aligned(16))) char smem_g[];
  LAS char* smem = (LAS char*)smem_g;
  const unsigned lds0 = (unsigned)(uintptr_t)smem;
  const int bid = blockIdx.x, tid = threadIdx.x;
  const int w = __builtin_amdgcn_readfirstlane(tid >> 6), lane = tid & 63;
  const int xcd = bid & 7, idx = bid >> 3;
  STAMP_DECL; RSTAMP(1, 14); STAMP(1, 0);
  const u64 genv = __hip_atomic_load(flg + FL_GEN, __ATOMIC_RELAXED, __HIP_MEMORY_SCOPE_AGENT);
  u64 tag;
  auto make_tag = [&]() ALWAYS_INLINE {
    tag = (((u64)(unsigned)__builtin_amdgcn_readfirstlane((unsigned)(genv >> 32)) << 32) | (unsigned)__builtin_amdgcn_readfirstlane((unsigned)genv)) + 1ull;
  };
  const int ksl = xcd >> 1, mt = (xcd & 1) * 8 + (idx >> 2), nt = idx & 3;
  const int tq = w & 3, khf = w >> 2;
  const int kbase = ksl * 1152 + khf * 576;
  const int colA2 = 128 * nt, colQ2 = 512 + 16 * nt;
  const int mtile = (xcd & 1) * 4 + (idx & 3);
  const int cgrp = (xcd >> 1) * 2 + (idx >> 4), cq = (idx >> 2) & 3;
  const int colA = 64 * (9 * cgrp + 2 * cq), colQ = 64 * (9 * cgrp + 8) + 16 * cq;

  const int grp = bid >> 2, qd = bid & 3;
  const int chA = grp * 9 + 2 * qd, chC = grp * 9 + 8;
  float ddA, ddC;
  {
    const int c1 = chA + (w >> 2);
    const unsigned d1 = ((const unsigned*)ssmd)[c1 >> 1], d2 = ((const unsigned*)ssmd)[chC >> 1];
    ddA = (c1 & 1) ? bf_hi(d1) : bf_lo(d1);
    ddC = (chC & 1) ? bf_hi(d2) : bf_lo(d2);
  }

  const int wq = w & 3, part = w >> 2;
  const char* wbase = (const char*)(part ? wu : wg);
  unsigned voff[5];
#pragma unroll
  for (int n = 0; n < 5; ++n) {
    const int o = (n < 4) ? ((wq * 4 + n) * 1024 + lane * 16) : (16384 + wq * 512 + (lane & 31) * 16);
    const int k = o >> 7, pc = (o >> 4) & 7;
    const int j = (pc >> 1) ^ (((k >> 1) & 1) | (part << 1));
    voff[n] = (unsigned)(k * (F_ * 2) + (2 * j + (pc & 1)) * 16);
  }
  const unsigned ldst = lds0 + part * K1_UOFF + wq * 4096;
  const unsigned ldst4 = lds0 + part * K1_UOFF + 16384 + wq * 512;

  auto piece_load = [&](int st, int n) ALWAYS_INLINE {
    const int slot = st % K1_NSLOT;
    const unsigned dst = (n < 4 ? ldst + n * 1024 : ldst4) + slot * K1_SLOT;
    if (st != 4) {
      const int ks = st < 4 ? st : st - 5;
      const char* g = wbase + (size_t)ks * 144 * (F_ * 2) + (colA + (st > 4 ? 64 : 0)) * 2;
      if (n < 4 || lane < 32) glds1p<false>(g, voff[n], dst);
    } else {
      int l2 = lane; asm volatile("" : "+v"(l2));
      const int o = (n < 4) ? ((wq * 4 + n) * 1024 + l2 * 16) : (16384 + wq * 512 + (l2 & 31) * 16);
      const unsigned vq = (unsigned)((o >> 5) * (F_ * 2) + ((o >> 4) & 1) * 16);
      if (n < 4 || lane < 32) glds1p<false>(wbase + colQ * 2, vq, dst);
    }
  };

  bf16x8 xf[36];
  const char* xb = (const char*)xfrag + (size_t)(mtile * 8 + w) * 36 * 1024;
  const unsigned xv = lane * 16;
  auto x_load = [&](auto ic) ALWAYS_INLINE {
    constexpr int i = ic;
    gload16_sc1<(i & 3) * 1024>(xf[i], xb + (i >> 2) * 4096, xv);
  };
  {
    LAS u16* tile = (LAS u16*)(smem + XR_OFF);
    constexpr int KT_OFF = 2 * K1_SLOT, VT_OFF = KT_OFF + 9728, WT_OFF = VT_OFF + 9216;
    const int tt = bid >> 3, ct = bid & 7, t0 = tt * 64, c0 = ct * 72;
    const int tok0 = mtile * 256 + (cgrp * 4 + cq) * 8, token = tok0 + w;
    const bool act = lane < 48;
    u32x2 d[3];
    u32x4 kin, vin, ein;
#pragma unroll
    for (int i = 0; i < 3; ++i)
      asm volatile("global_load_dwordx2 %0, %1, %2 offset:%3" : "=v"(d[i]) : "v"((unsigned)(token * (D_ * 2) + (act ? lane : 0) * 8)), "s"(q), "i"(i * 384) : "memory");
    auto k_off = [&](int pc) ALWAYS_INLINE -> unsigned { const int r = pc / 9, cg = pc - r * 9, ts = t0 - 3 + r; return (unsigned)((ts < 0 ? 0 : ts) * (D_ * 2) + (c0 + 8 * cg) * 2); };
    auto v_off = [&](int pc) ALWAYS_INLINE -> unsigned { const int r = pc / 9, cg = pc - r * 9; return (unsigned)((t0 + r) * (D_ * 2) + (c0 + 8 * cg) * 2); };
    gload16u(kin, kk, k_off(tid));
    gload16u(vin, vv, v_off(tid));
    int edst;
    {
      const int kp = w == 0 ? 512 + lane : 576 + (lane < 27 ? lane : 0);
      const int wl = lane < 63 ? lane - 27 : 35;
      const bool isw = w == 1 && lane >= 27, isv = w >= 2;
      const char* src = isv ? (const char*)vv + v_off(512 + lane) : (isw ? (const char*)shk + c0 * 8 + wl * 16 : (const char*)kk + k_off(kp));
      edst = isv ? VT_OFF + (512 + lane) * 16 : (isw ? WT_OFF + wl * 16 : KT_OFF + kp * 16);
      asm volatile("global_load_dwordx4 %0, %1, off" : "=v"(ein) : "v"(src) : "memory");
    }
    asm volatile("s_waitcnt vmcnt(3)" : "+v"(d[0]), "+v"(d[1]), "+v"(d[2])::"memory");
    STAMP(1, 9);
    f32x2 xa[3], xb2[3];
#pragma unroll
    for (int i = 0; i < 3; ++i) { xa[i] = unpk(d[i].x); xb2[i] = unpk(d[i].y); }
    const f32x2 s2 = (xa[0] + xb2[0]) + (xa[1] + xb2[1]) + (xa[2] + xb2[2]);
    const float mean = wave_sum(act ? s2.x + s2.y : 0.f) * (1.0f / 576.0f);
    f32x2 v2 = {0.f, 0.f};
#pragma unroll
    for (int i = 0; i < 3; ++i) { const f32x2 a = xa[i] - mean, b = xb2[i] - mean; v2 += a * a + b * b; }
    const float vs = wave_sum(act ? v2.x + v2.y : 0.f);
    const float mb = bfr(mean), vb = bfr(vs * (1.0f / 576.0f));
    const float sd = bfr(sqrtf(bfr(vb + 1e-5f)));
    const float rsd = 1.0f / sd;
#pragma unroll
    for (int i = 0; i < 3; ++i) {
      const f32x2 a = bfr2(xa[i] - mb) * rsd, b = bfr2(xb2[i] - mb) * rsd;
      u32x2 o; o.x = pk_bf16(a.x, a.y); o.y = pk_bf16(b.x, b.y);
      const int p8 = lane + 48 * i;
      if (act) *(LAS u32x2*)(smem + XG_OFF + ((p8 >> 1) * 8 + w) * 16 + (p8 & 1) * 8) = o;
    }
    if (tid == 0) *(LAS unsigned*)(smem + HCNT_OFF + 4) = 0u;
    asm volatile("s_waitcnt vmcnt(0)" : "+v"(kin), "+v"(vin), "+v"(ein)::"memory");
    make_tag();
    if (t0 == 0 && tid < 27) kin = (u32x4){0u, 0u, 0u, 0u};
    *(LAS u32x4*)(smem + KT_OFF + tid * 16) = kin;
    *(LAS u32x4*)(smem + VT_OFF + tid * 16) = vin;
    if (w < 3) *(LAS u32x4*)(smem + edst) = ein;
    WAIT_L(0);
    __builtin_amdgcn_s_barrier();
    CFENCE();
    {
      char* xo = (char*)xfrag + (size_t)((tok0 >> 5) * 36 * 64 + (tok0 & 31)) * 16;
#pragma unroll
      for (int it = 0; it < 2; ++it) {
        const int id = it ? 512 + lane : tid, p16 = id >> 3;
        if (it == 0 || w == 0) {
          const u32x4 v = *(LAS u32x4*)(smem + XG_OFF + id * 16);
          gstore16_sc1(xo + ((p16 >> 1) * 64 + (p16 & 1) * 32 + (id & 7)) * 16, v);
        }
      }
      if (lane == 0) __hip_atomic_store(stats + token, (u64)__float_as_uint(mb) | ((u64)__float_as_uint(rsd) << 32), __ATOMIC_RELAXED, __HIP_MEMORY_SCOPE_AGENT);
    }
    STAMP(1, 10);
    CFENCE();
#pragma unroll
    for (int n = 0; n < 5; ++n) piece_load(0, n);
#pragma unroll
    for (int n = 0; n < 5; ++n) piece_load(1, n);
    wait_vm<10>();
    STAMP(1, 11);
    if (lane == 0) {
      const unsigned old = __hip_atomic_fetch_add((LAS unsigned*)(smem + HCNT_OFF + 4), 1u, __ATOMIC_RELAXED, __HIP_MEMORY_SCOPE_WORKGROUP);
      if (old == 7u) set_flag(flg + FL_X + (tok0 >> 3), tag);
    }
    auto kv_compute = [&](int item) ALWAYS_INLINE {
      const int tl = item / 9, cg = item - tl * 9;
      u32x4 kr[4], w4[4];
#pragma unroll
      for (int i = 0; i < 4; ++i) { kr[i] = *(LAS u32x4*)(smem + KT_OFF + (item + 9 * i) * 16); w4[i] = *(LAS u32x4*)(smem + WT_OFF + cg * 64 + i * 16); }
      const u32x4 vr = *(LAS u32x4*)(smem + VT_OFF + item * 16);
#pragma unroll
      for (int e2 = 0; e2 < 4; ++e2) {
        const f32x2 w0 = {bf_lo(w4[e2].x), bf_lo(w4[e2].z)}, w1 = {bf_hi(w4[e2].x), bf_hi(w4[e2].z)};
        const f32x2 w2 = {bf_lo(w4[e2].y), bf_lo(w4[e2].w)}, w3 = {bf_hi(w4[e2].y), bf_hi(w4[e2].w)};
        f32x2 ks = bfr2(unpk(kr[0][e2]) * w3);
        ks = bfr2(ks + bfr2(unpk(kr[1][e2]) * w2));
        ks = bfr2(ks + bfr2(unpk(kr[2][e2]) * w1));
        ks = bfr2(ks + bfr2(unpk(kr[3][e2]) * w0));
        const f32x2 kv = ks * unpk(vr[e2]);
        const unsigned pk = pk_bf16(kv.x, kv.y);
        tile[(8 * cg + 2 * e2) * 72 + (63 - tl)] = (u16)(pk & 0xffffu);
        tile[(8 * cg + 2 * e2 + 1) * 72 + (63 - tl)] = (u16)(pk >> 16);
      }
    };
    kv_compute(tid);
    STAMP(1, 7);
    poll_flags<1>(flg + FL_X + mtile * 32 + w * 4, 4, tag, lane);
    wait_vm<0>();
    STAMP(1, 8);
    sfor<9>([&](auto ic) ALWAYS_INLINE { x_load(ic); });
    if (w == 7) kv_compute(512 + lane);
    WAIT_L(0);
    __builtin_amdgcn_s_barrier();
    CFENCE();
    {
      const int ch = tid >> 3, pp = tid & 7, ch2 = 64 + (lane >> 3);
      const u32x4 o1 = *(LAS u32x4*)(tile + ch * 72 + pp * 8), o2 = *(LAS u32x4*)(tile + ch2 * 72 + pp * 8);
      gstore16_sc1(kvr + (size_t)(c0 + ch) * S_ + (1984 - t0) + pp * 8, o1);
      gstore16_sc1(kvr + (size_t)(c0 + ch2) * S_ + (1984 - t0) + pp * 8, o2);
    }
  }

  const int kh = lane >> 5, prt = (lane >> 4) & 1, qq = (lane & 15) >> 2, p4 = lane & 3;
  const unsigned rbU = lds0 + (8 * kh + qq) * 128 + prt * K1_UOFF + ((((qq >> 1) & 1) | (prt << 1)) << 5) + p4 * 8;

  f32x16 acc[4];
  const f32x16 zero16 = {0.f, 0.f, 0.f, 0.f, 0.f, 0.f, 0.f, 0.f, 0.f, 0.f, 0.f, 0.f, 0.f, 0.f, 0.f, 0.f};

  const int token = mtile * 256 + w * 32 + (lane & 31);
  char* hb = (char*)hout + ((size_t)(token >> 4) * 144 * 64 + (lane >> 5) * 16 + (token & 15)) * 16;
  auto h_word = [&](const f32x16& a, int i) ALWAYS_INLINE -> unsigned {
    return pk_bf16(bfr(a[2 * i]) * bfr(a[2 * i + 8]), bfr(a[2 * i + 1]) * bfr(a[2 * i + 9]));
  };
  auto h_store = [&](const u32x4& o, int c16) ALWAYS_INLINE {
    gstore16_sc1(hb + ((size_t)(c16 >> 1) * 64 + (c16 & 1) * 32) * 16, o);
  };
  u32x4 ho;

  const char* hpA = (const char*)(ssmk + (size_t)(chA + (w >> 2)) * S_ + 64 * (w & 3));
  const char* hpC = (const char*)(ssmk + (size_t)chC * S_ + 32 * w);
  const bool zA = (w & 3) == 0, zC = w == 0;
  const char* pf_sk = (const char*)ssmk;
  const char* pf_wd = (const char*)wd + (size_t)(xcd >> 1) * 1152 * 1152;
  const char* kvb = (const char*)kvr;
  u32x4 caf[24];
  unsigned cax[24];
  bf16x8 cf[24];
  int cvo = 0, cvx = 0;
  auto conv_align = [&](auto ic) ALWAYS_INLINE {
    constexpr int i = ic;
    const unsigned sh = ((lane + 1) & 1) * 2;
    u32x4 r;
    r.x = __builtin_amdgcn_alignbyte(caf[i].y, caf[i].x, sh);
    r.y = __builtin_amdgcn_alignbyte(caf[i].z, caf[i].y, sh);
    r.z = __builtin_amdgcn_alignbyte(caf[i].w, caf[i].z, sh);
    r.w = __builtin_amdgcn_alignbyte(cax[i], caf[i].w, sh);
    cf[i] = __builtin_bit_cast(bf16x8, r);
    asm volatile("" : "+v"(cf[i]));
  };
  const char* zbase = (const char*)slab + ((size_t)(mt * 4 + nt) * 16 + ksl * 4) * 18432 + w * 9216;

  STAMP(1, 1);

  constexpr int PD = 3, MID = K1_MID;
  bf16x8 af[PD + 1];
  unsigned rb[4];
#pragma unroll
  for (int j = 0; j < 4; ++j) rb[j] = rbU ^ (j << 5);
#pragma unroll
  for (int g = 0; g < PD; ++g) af[g] = tr_frag((LAS char*)(uintptr_t)rb[g], 0, 512);
  sfor<324>([&](auto gc) ALWAYS_INLINE {
    constexpr int g = gc;
    constexpr int st = g / 36, f = g - st * 36;
    constexpr int s = st != 4 ? f / 4 : f, c = st != 4 ? f % 4 : 0;
    constexpr int xi = st < 4 ? st * 9 + s : (st == 4 ? s : (st - 5) * 9 + s);
    constexpr int ai = st < 4 ? c : (st == 4 ? 0 : (c + 1) & 3);
    constexpr bool first = (st == 0 || st == 5) ? s == 0 : (st == 4 && f == 0);
    if constexpr (f == MID) {

      if constexpr (st < 8) wait_vm<k1_younger(k1_dkey(st + 1, 4), 4 * g)>();
      else { wait_vm<k1_younger(4 * (7 * 36 + MID + 1) + 1, 4 * g)>(); if (w <= 3) WAIT_L(0); }
      __builtin_amdgcn_s_barrier();
      CFENCE();

      if constexpr (st == 1) { if (tid == 0) set_flag(flg + FL_K + (bid & 7) * 32 + (bid >> 3), tag); }
    }
    if constexpr (((f + 36 - MID) % 36) % 9 == 0) __builtin_amdgcn_s_setprio(3 - ((f + 36 - MID) % 36) / 9);
    sfor<35>([&](auto dc) ALWAYS_INLINE { if constexpr (k1_dkey(dc / 5 + 2, dc % 5) == 4 * g + 1) piece_load(dc / 5 + 2, dc % 5); });
    sfor<27>([&](auto ic) ALWAYS_INLINE { if constexpr (k1_xkey(ic + 9) == 4 * g + 2) x_load(std::integral_constant<int, ic + 9>{}); });
    if constexpr (g == 7 * 36 + MID + 1) {
      if (w < 3) {
        const int ch = (w == 2) ? chC : chA + w;
        const unsigned lv = lane * 16;
        poll_flags<1>(flg + FL_K + (grp >> 3) * 32, 32, tag, lane);
        const __amdgpu_buffer_rsrc_t rk = __builtin_amdgcn_make_buffer_rsrc((char*)kvb + (size_t)ch * (S_ * 2), 0, S_ * 2, 0x00020000);
#pragma unroll
        for (int i = 0; i < 4; ++i)
          *(LAS u32x4*)(smem + XR_OFF + w * XR_STRIDE + i * 1024 + lv) = __builtin_amdgcn_raw_buffer_load_b128(rk, lv + i * 1024, 0, 16);
        if (lane < 4) glds1(pf_sk + (size_t)ch * (S_ * 2), lv, lds0 + HZ_OFF + w * 96 + 32);
      } else if (w == 3) {
        if (lane < 32) {
#pragma unroll
          for (int im = 0; im < 3; ++im) *(LAS u32x4*)(smem + XR_OFF + im * XR_STRIDE + 4096 + lane * 16) = (u32x4){0u, 0u, 0u, 0u};
        } else if (lane < 38) {
          const int im = (lane - 32) >> 1, hh = lane & 1;
          *(LAS u32x4*)(smem + HZ_OFF + im * 96 + hh * 16) = (u32x4){0u, 0u, 0u, 0u};
        } else if (lane == 38) *(LAS unsigned*)(smem + HCNT_OFF) = 0u;
      }
    }
    sfor<9>([&](auto zc) ALWAYS_INLINE {
      if constexpr (k1_zstep(zc) == g) {
        if constexpr (zc == 0) { ho = (u32x4){~0u, ~0u, ~0u, ~0u}; asm volatile("" : "+v"(ho)); }
        gstore16_sc1_s<(zc & 3) * 1024>(zbase + (zc >> 2) * 4096, xv, ho);
      }
    });
    sfor<21>([&](auto ac) ALWAYS_INLINE { if constexpr (k1_astep(ac) == g) conv_align(ac); });
    if constexpr (g == K1_TOUCH) {
      int l2 = lane; asm volatile("" : "+v"(l2));
      const unsigned line = (unsigned)(w * 64 + l2 < 324 ? idx * 324 + w * 64 + l2 : idx * 324);
      touch_lines(pf_wd, line * 128u, lds0 + K1_PF_OFF + 16);
    }
    sfor<24>([&](auto cc) ALWAYS_INLINE {
      constexpr int ci = cc;
      if constexpr (k1_cstep(ci) == g) {
        if constexpr (ci == 0) {
          int l2 = lane; asm volatile("" : "+v"(l2));
          const int e = (l2 & 15) - 15 + 8 * (l2 >> 4);
          cvo = (e & ~1) * 2; cvx = cvo + ((e & 1) ? 16 : 0);
        }
        constexpr int cb = ci < 16 ? 64 * (ci >> 3) + 512 * (ci & 7) : 512 * (ci - 16);
        const char* hp = (ci < 16 ? hpA : hpC) + cb + ((ci == 0 && zA) || (ci == 16 && zC) ? 32 : 0);
        __builtin_memcpy(&caf[ci], __builtin_assume_aligned(hp + cvo, 4), 16);
        cax[ci] = *(const unsigned*)(hp + cvx);
      }
    });
    if constexpr (g + PD < 324) {
      constexpr int g1 = g + PD, st1 = g1 / 36, f1 = g1 - st1 * 36;
      if constexpr (f1 == 0) {
        if constexpr (st1 != 4) {
          unsigned b2 = rbU; asm volatile("" : "+v"(b2));
#pragma unroll
          for (int j = 0; j < 4; ++j) { rb[j] = (b2 + (st1 % K1_NSLOT) * K1_SLOT) ^ (j << 5); asm volatile("" : "+v"(rb[j])); }
        } else {
          int l2 = lane; asm volatile("" : "+v"(l2));
          rb[0] = lds0 + (8 * (l2 >> 5) + ((l2 & 15) >> 2)) * 32 + ((l2 >> 4) & 1) * K1_UOFF + (l2 & 3) * 8 + (st1 % K1_NSLOT) * K1_SLOT;
          asm volatile("" : "+v"(rb[0]));
        }
      }
      if constexpr (st1 != 4) af[g1 % (PD + 1)] = tr_frag((LAS char*)(uintptr_t)rb[f1 % 4], (f1 / 4) * 2048, 512);
      else af[g1 % (PD + 1)] = tr_frag((LAS char*)(uintptr_t)rb[0], f1 * 512, 128);
    }
    if constexpr (st < 4 && c == 0) {
      asm volatile("s_waitcnt vmcnt(%1)" : "+v"(xf[xi]) : "i"(k1_younger(k1_xkey(xi), 4 * g + 3)) : "memory");
    }
    SCHED0();
    acc[ai] = __builtin_amdgcn_mfma_f32_32x32x16_bf16(af[g % (PD + 1)], xf[xi], first ? zero16 : acc[ai], 0, 0, 0);
    SCHED0();
    if constexpr (g == 143) {
#pragma unroll
      for (int i = 0; i < 4; ++i) ho[i] = h_word(acc[0], i);
      h_store(ho, colA >> 4);
      SCHED0();
    }
    if constexpr (g > 144 && g <= 162) {
      constexpr int cc = (g - 145) / 6 + 1, pi = (g - 145) % 6;
      if constexpr (pi < 4) ho[pi] = h_word(acc[cc], pi);
      if constexpr (pi == 5) h_store(ho, (colA >> 4) + cc);
      SCHED0();
    }
    if constexpr (g >= 180 && g <= 182) {
      if constexpr (g == 180) { ho[0] = h_word(acc[0], 0); ho[1] = h_word(acc[0], 1); }
      if constexpr (g == 181) { ho[2] = h_word(acc[0], 2); ho[3] = h_word(acc[0], 3); }
      if constexpr (g == 182) h_store(ho, colQ >> 4);
      SCHED0();
    }
  });
  const char* wbase2 = (const char*)wd + (size_t)kbase * (D_ * 2);
  unsigned voff2[4];
  int lane2 = lane; asm volatile("" : "+v"(lane2));
#pragma unroll
  for (int n = 0; n < 4; ++n) {
    const int o = (tq * 4 + n) * 1024 + lane2 * 16;
    const int k = o >> 8, pc = (o >> 4) & 15;
    const int j = (pc >> 1) ^ (k & 7);
    voff2[n] = (unsigned)(k * (D_ * 2) + (2 * j + (pc & 1)) * 16);
  }
  const unsigned ldst2 = lds0 + khf * K2_HALF;
  auto piece_load2 = [&](int st, int n) ALWAYS_INLINE {
    const int slot = st % K2_NSLOT;
    if (st < 9) {
      const char* g = wbase2 + (size_t)st * 64 * (D_ * 2) + colA2 * 2;
      glds1p<false>(g, voff2[n], ldst2 + slot * K2_SLOT + tq * 4096 + n * 1024);
    } else {
      int l2 = lane; asm volatile("" : "+v"(l2));
      const char* g = wbase2 + (size_t)(st - 9) * 288 * (D_ * 2) + colQ2 * 2;
      const int o = n < 2 ? tq * 2048 + n * 1024 + l2 * 16 : 8192 + tq * 256 + (l2 & 15) * 16;
      const unsigned v0 = (unsigned)((o >> 5) * (D_ * 2) + ((o >> 4) & 1) * 16);
      if (n < 2 || l2 < 16) glds1p<false>(g, v0, ldst2 + slot * K2_SLOT + (n < 2 ? tq * 2048 + n * 1024 : 8192 + tq * 256));
    }
  };
  const char* hbase = (const char*)hout + ((size_t)(mt * 8 + tq * 2) * 144 * 64 + (size_t)(kbase >> 5) * 64) * 16;
  const unsigned hv0 = lane2 * 16 + khf * (144 * 64 * 16), hv1 = lane2 * 16 + (1 - khf) * (144 * 64 * 16);
  bf16x8 hf[18][2];
  auto h_load = [&](auto lc) ALWAYS_INLINE {
    constexpr int k = lc / 2, t = lc % 2;
    gload16_sc1<(k & 3) * 1024>(hf[k][t], hbase + (k >> 2) * 4096, t ? hv1 : hv0);
  };
  STAMP(1, 2);
  __builtin_amdgcn_s_setprio(0);
  sfor<3>([&](auto ac) ALWAYS_INLINE { conv_align(std::integral_constant<int, 21 + ac>{}); });
#pragma unroll
  for (int cc = 0; cc < 4; ++cc) {
    u32x4 o;
#pragma unroll
    for (int i = 0; i < 4; ++i) o[i] = h_word(acc[(cc + 1) & 3], i);
    h_store(o, (colA >> 4) + 4 + cc);
  }

  __builtin_amdgcn_s_barrier();
  CFENCE();
#pragma unroll
  for (int n = 0; n < 4; ++n) piece_load2(0, n);
#pragma unroll
  for (int n = 0; n < 4; ++n) piece_load2(1, n);

  STAMP(1, 3);
  const int ct0 = lane & 15, ckg = lane >> 4;
  {
    const int boff = (2032 - 16 * (lane & 15) + 8 * ckg) * 2;
    auto head_frag = [&](int im) ALWAYS_INLINE -> bf16x8 {
      bf16x8 r;
      LAS u16* hz = (LAS u16*)(smem + HZ_OFF + im * 96) + 16 + (ct0 - 15 + 8 * ckg);
#pragma unroll
      for (int j = 0; j < 8; ++j) r[j] = (short)hz[j];
      return r;
    };
    if (zA) cf[0] = head_frag(w >> 2);
    if (zC) cf[16] = head_frag(2);
    {
      const int im = w >> 2;
      LAS char* xr = smem + XR_OFF + im * XR_STRIDE + boff + 128 * (w & 3);
      f32x4 ac[8];
#pragma unroll
      for (int j = 0; j < 8; ++j) ac[j] = (f32x4){0.f, 0.f, 0.f, 0.f};
#pragma unroll
      for (int rr = 1; rr >= 0; --rr) {
        bf16x8 bfm[8];
#pragma unroll
        for (int b = 0; b < 8; ++b) bfm[b] = *(LAS bf16x8*)(xr - 512 * b + 64 * rr);
#pragma unroll
        for (int a = 7; a >= 0; --a)
#pragma unroll
          for (int j = a; j < 8; ++j) ac[j] = __builtin_amdgcn_mfma_f32_16x16x32_bf16(cf[rr * 8 + a], bfm[j - a], ac[j], 0, 0, 0);
        if (rr == 1) {
          SCHED0();
          wait_vm<8>();
          if (lane == 0) {
            const unsigned old = __hip_atomic_fetch_add((LAS unsigned*)(smem + HCNT_OFF), 1u, __ATOMIC_RELAXED, __HIP_MEMORY_SCOPE_WORKGROUP);
            if (old == 7u) set_flag(flg + FL_H + (mtile * 4 + (cgrp >> 1)) * 8 + (cgrp & 1) * 4 + cq, tag);
          }
          SCHED0();
        }
      }
#pragma unroll
      for (int j = 0; j < 8; ++j)
        *(LAS f32x4*)(smem + PART_OFF + w * 8192 + (256 * j + 16 * (lane & 15) + 4 * ckg) * 4) = ac[j];
    }
    poll_flags<1>(flg + FL_H + ((mt >> 1) * 4 + ksl) * 8, 8, tag, lane);
    sfor<12>([&](auto kc) ALWAYS_INLINE { h_load(kc); });
    {
      LAS char* xr = smem + XR_OFF + 2 * XR_STRIDE + boff + 64 * w;
      const int jA = 7 - qd, jB = qd;
      bf16x8 b0[8], b1[4];
#pragma unroll
      for (int a = 0; a < 8; ++a) { const int bb = jA - a; b0[a] = *(LAS bf16x8*)(xr - 512 * (bb < 0 ? 0 : bb)); }
#pragma unroll
      for (int a = 0; a < 4; ++a) { const int bb = jB - a; b1[a] = *(LAS bf16x8*)(xr - 512 * (bb < 0 ? 0 : bb)); }
      f32x4 c0 = (f32x4){0.f, 0.f, 0.f, 0.f}, c1 = c0;
#pragma unroll
      for (int a = 7; a >= 0; --a) {
        if (a <= jA) c0 = __builtin_amdgcn_mfma_f32_16x16x32_bf16(cf[16 + a], b0[a], c0, 0, 0, 0);
        if (a < 4 && a <= jB) c1 = __builtin_amdgcn_mfma_f32_16x16x32_bf16(cf[16 + a], b1[a < 4 ? a : 0], c1, 0, 0, 0);
      }
      *(LAS f32x4*)(smem + PARTC_OFF + w * 2048 + (16 * (lane & 15) + 4 * ckg) * 4) = c0;
      *(LAS f32x4*)(smem + PARTC_OFF + w * 2048 + 1024 + (16 * (lane & 15) + 4 * ckg) * 4) = c1;
    }
  }
  STAMP(1, 4);
  WAIT_L(0);
  __builtin_amdgcn_s_barrier();
  CFENCE();
  STAMP(1, 5);
  auto finalize = [&](f32x4 s0, f32x4 s1, int im, int ch, int t8, float dd) ALWAYS_INLINE {
    const u32x4 kvv = *(LAS u32x4*)(smem + XR_OFF + im * XR_STRIDE + (2040 - t8) * 2);
    float y[8] = {s0[0], s0[1], s0[2], s0[3], s1[0], s1[1], s1[2], s1[3]};
    float o[8];
#pragma unroll
    for (int e = 0; e < 8; ++e) {
      const int ri = 7 - e;
      const float kvf = (ri & 1) ? bf_hi(kvv[ri >> 1]) : bf_lo(kvv[ri >> 1]);
      o[e] = bfr(y[e]) + bfr(kvf * dd);
    }
    u32x4 ov; ov.x = pk_bf16(o[0], o[1]); ov.y = pk_bf16(o[2], o[3]); ov.z = pk_bf16(o[4], o[5]); ov.w = pk_bf16(o[6], o[7]);
    gstore16_sc1(wT + (size_t)ch * S_ + t8, ov);
  };
  {
    const int im = tid >> 8, t8 = (tid & 255) * 8;
    f32x4 s0 = (f32x4){0.f, 0.f, 0.f, 0.f}, s1 = s0;
#pragma unroll
    for (int ww = 0; ww < 4; ++ww) {
      s0 += *(LAS f32x4*)(smem + PART_OFF + (im * 4 + ww) * 8192 + t8 * 4);
      s1 += *(LAS f32x4*)(smem + PART_OFF + (im * 4 + ww) * 8192 + t8 * 4 + 16);
    }
    finalize(s0, s1, im, chA + im, t8, ddA);
  }
  if (tid >= 448) {
    const int t2 = tid - 448;
    const int tl = t2 >> 5, l8 = (t2 & 31) * 8;
    const int jt = tl ? qd : 7 - qd, t8 = 256 * jt + l8;
    f32x4 s0 = (f32x4){0.f, 0.f, 0.f, 0.f}, s1 = s0;
#pragma unroll
    for (int ww = 0; ww < 8; ++ww) {
      s0 += *(LAS f32x4*)(smem + PARTC_OFF + ww * 2048 + tl * 1024 + l8 * 4);
      s1 += *(LAS f32x4*)(smem + PARTC_OFF + ww * 2048 + tl * 1024 + l8 * 4 + 16);
    }
    finalize(s0, s1, 2, chC, t8, ddC);
  }
  STAMP(1, 6);

  STAMP(2, 0);
  {
  const int kq = lane2 >> 4, qq = (lane2 & 15) >> 2, p4 = lane2 & 3;
  const unsigned rbU2 = lds0 + khf * K2_HALF + (16 * (kq >> 1) + 4 * (kq & 1) + qq) * 256 + ((4 * (kq & 1) + qq) << 5) + p4 * 8;

  const char* wtb = (const char*)(wT + mt * 128 + ksl * 32);
  f32x4 acc[2][8], accq[2];
  const f32x4 zero4 = {0.f, 0.f, 0.f, 0.f};

  const int tile = mt * 4 + nt;
  const __amdgpu_buffer_rsrc_t rs = __builtin_amdgcn_make_buffer_rsrc((char*)slab + (size_t)tile * (16 * 18432), 0, 16 * 18432, 0x00020000);
  const bool foreign = tq != ksl;
  auto give = [&](int xb, int i, const f32x4& a0, const f32x4& a1) ALWAYS_INLINE {
    *(LAS f32x4*)(smem + xb + ((w * 4 + i) * 64 + lane) * 16) = a1;
  };
  auto publish = [&](int xb, int i, const f32x4& a0, const f32x4& a1, int dtg) ALWAYS_INLINE {
    const f32x4 sm = a0 + *(LAS f32x4*)(smem + xb + (((w ^ 4) * 4 + i) * 64 + lane) * 16);
    u32x4 su = __builtin_bit_cast(u32x4, sm);
#pragma unroll
    for (int e = 0; e < 4; ++e) su[e] = su[e] == ~0u ? 0x7fc00000u : su[e];
    if (foreign) __builtin_amdgcn_raw_buffer_store_b128(su, rs, (unsigned)(((tq * 4 + ksl) * 18 + khf * 9 + dtg) * 1024 + lane * 16), 0, 16);
    else *(LAS f32x4*)(smem + K2_OWN_OFF + ((khf * 9 + dtg) * 64 + lane) * 16) = sm;
  };
  constexpr int XB1 = 3 * K2_SLOT, XB2 = 0, XB3 = 3 * K2_SLOT;

  asm volatile("s_waitcnt vmcnt(%4)" : "+v"(hf[0][0]), "+v"(hf[0][1]), "+v"(hf[1][0]), "+v"(hf[1][1])
               : "i"(k2_younger(k2_hkey(0, 3), 0, false)) : "memory");
  WAIT_L(0);
  __builtin_amdgcn_s_barrier();
  CFENCE();
  STAMP(2, 1);

  constexpr int PD = 2;
  bf16x8 af[PD + 1];
  unsigned sb = rbU2;
#pragma unroll
  for (int g = 0; g < PD; ++g) af[g] = tr_frag((LAS char*)(uintptr_t)(sb ^ (g << 5)), 0, 2048);
  sfor<162>([&](auto gc) ALWAYS_INLINE {
    constexpr int g = gc;
    constexpr int st = k2_stage(g), f = g - k2_g0(st);
    constexpr int ks = st < 9 ? st * 2 + f / 8 : (st - 9) * 9 + f, dt = st < 9 ? f % 8 : 0;
    if constexpr (f == k2_mid(st)) {
      if constexpr (st < 8) { STAMP(0, 2 * st); }
      if constexpr (st == K2_WST) { if (w == 0) poll_flags<4>(flg + FL_W, 64, tag, lane); }
      if constexpr (st < 10) {
        constexpr int okey = cmax(k2_dkey(st + 1, k2_np(st + 1) - 1), st + 1 < 9 ? k2_hkey(st + 1, 3) : -1000);
        constexpr int n0 = k2_younger(okey, 4 * g, false);
        if constexpr (st == 0) { if (w == 7) wait_vm<n0 + 1>(); else wait_vm<n0>(); }
        else wait_vm<n0>();
        if constexpr (st + 1 < 9) {
          constexpr int k0 = 2 * (st + 1);
          asm volatile("" : "+v"(hf[k0][0]), "+v"(hf[k0][1]), "+v"(hf[k0 + 1][0]), "+v"(hf[k0 + 1][1])::"memory");
        }
      }
      if constexpr (st >= 9) WAIT_L(0);
      __builtin_amdgcn_s_barrier();
      CFENCE();
      if constexpr (st < 8) { STAMP(0, 2 * st + 1); }
      if constexpr (st == 1) { if (tid == 0) set_flag(flg + FL_W + bid, tag); }
      if constexpr (st == K2_WST) {
        int l2 = lane; asm volatile("" : "+v"(l2));
        glds1_sc1(wtb, (unsigned)((colA2 + w * 16 + (l2 >> 2)) * (S_ * 2) + (l2 & 3) * 16), lds0 + K2_WT_OFF + w * 1024);
        glds1_sc1(wtb, (unsigned)((colQ2 + (l2 >> 2)) * (S_ * 2) + (l2 & 3) * 16), lds0 + K2_WT_OFF + 8192);
      }
      if constexpr (st == 9) {
#pragma unroll
        for (int i = 0; i < 4; ++i) publish(XB1, i, acc[0][i], acc[1][i], i);
#pragma unroll
        for (int i = 0; i < 4; ++i) give(XB2, i, acc[0][4 + i], acc[1][4 + i]);
      }
      if constexpr (st == 10) {
#pragma unroll
        for (int i = 0; i < 4; ++i) publish(XB2, i, acc[0][4 + i], acc[1][4 + i], 4 + i);
      }
    }
    if constexpr (st < 9 && ((f + 16 - 6) % 16) % 4 == 0) __builtin_amdgcn_s_setprio(3 - ((f + 16 - 6) % 16) / 4);
    sfor<36>([&](auto dc) ALWAYS_INLINE { if constexpr (dc % 4 < k2_np(dc / 4 + 2) && k2_dkey(dc / 4 + 2, dc % 4) == 4 * g + 2) piece_load2(dc / 4 + 2, dc % 4); });
    sfor<24>([&](auto hc) ALWAYS_INLINE { if constexpr (k2_hkey(hc / 4 + 3, hc % 4) == 4 * g + 3) h_load(std::integral_constant<int, hc + 12>{}); });
    if constexpr (g + PD < 162) {
      constexpr int g1 = g + PD, st1 = k2_stage(g1), f1 = g1 - k2_g0(st1);
      if constexpr (f1 == 0) {
        if constexpr (st1 < 9) {
          sb += (st1 % K2_NSLOT == 0) ? -3 * K2_SLOT : K2_SLOT;
        } else {
          int l2 = lane; asm volatile("" : "+v"(l2));
          const int kq2 = l2 >> 4;
          sb = lds0 + khf * K2_HALF + (16 * (kq2 >> 1) + 4 * (kq2 & 1) + ((l2 & 15) >> 2)) * 32 + (l2 & 3) * 8 + (st1 % K2_NSLOT) * K2_SLOT;
        }
      }
      asm volatile("" : "+v"(sb));
      if constexpr (st1 < 9) af[g1 % (PD + 1)] = tr_frag((LAS char*)(uintptr_t)(sb ^ ((f1 % 8) << 5)), (f1 / 8) * 8192, 2048);
      else af[g1 % (PD + 1)] = tr_frag((LAS char*)(uintptr_t)sb, f1 * 1024, 256);
    }
    SCHED0();
#pragma unroll
    for (int t = 0; t < 2; ++t) {
      if constexpr (st < 9) acc[t][dt] = __builtin_amdgcn_mfma_f32_16x16x32_bf16(af[g % (PD + 1)], hf[ks][t], g < 8 ? zero4 : acc[t][dt], 0, 0, 0);
      else accq[t] = __builtin_amdgcn_mfma_f32_16x16x32_bf16(af[g % (PD + 1)], hf[ks][t], g == 144 ? zero4 : accq[t], 0, 0, 0);
    }
    SCHED0();
    if constexpr (g == 143) {
#pragma unroll
      for (int i = 0; i < 4; ++i) give(XB1, i, acc[0][i], acc[1][i]);
      SCHED0();
    }
  });
  STAMP(2, 2);
  __builtin_amdgcn_s_setprio(0);
  give(XB3, 0, accq[0], accq[1]);

  int lane3 = lane; asm volatile("" : "+v"(lane3));
  u32x2 xq[3];
  float2 stt[2];
#pragma unroll
  for (int i = 0; i < 3; ++i) {
    const int t = i < 2 ? i : (w & 1);
    const int token = mt * 128 + ksl * 32 + t * 16 + (lane3 & 15);
    xq[i] = *(const u32x2*)(q + (size_t)token * D_ + (i < 2 ? colA2 + w * 16 : colQ2) + 4 * (lane3 >> 4));
    if (i < 2) {
      const u64 sv = __hip_atomic_load(stats + token, __ATOMIC_RELAXED, __HIP_MEMORY_SCOPE_AGENT);
      stt[i] = make_float2(__uint_as_float((unsigned)sv), __uint_as_float((unsigned)(sv >> 32)));
    }
  }
  WAIT_L(0);
  __builtin_amdgcn_s_barrier();
  CFENCE();
  publish(XB3, 0, accq[0], accq[1], 8);
  WAIT_L(0);
  __builtin_amdgcn_s_barrier();
  CFENCE();
  STAMP(2, 3);
  auto fetch = [&](f32x4 (&pv)[3], int fi) ALWAYS_INLINE -> bool {
    bool ok = true;
#pragma unroll
    for (int k = 1; k < 4; ++k) {
      const u32x4 u = __builtin_amdgcn_raw_buffer_load_b128(rs, (unsigned)(((ksl * 4 + ((ksl + k) & 3)) * 18 + fi) * 1024 + lane3 * 16), 0, 16);
      ok &= (u.x != ~0u) & (u.y != ~0u) & (u.z != ~0u) & (u.w != ~0u);
      pv[k - 1] = __builtin_bit_cast(f32x4, u);
    }
    return ok;
  };
  auto combine = [&](const f32x4 (&pv)[3], int t, int dt, const u32x2& xw2, const float2& st2) ALWAYS_INLINE {
    const int tl = t * 16 + (lane3 & 15), dl = dt * 16 + 4 * (lane3 >> 4);
    const f32x4 own = *(LAS f32x4*)(smem + K2_OWN_OFF + ((t * 9 + dt) * 64 + lane3) * 16);
    const f32x4 bs = ((own + pv[0]) + pv[1]) + pv[2];
    float o[4];
#pragma unroll
    for (int c = 0; c < 4; ++c) {
      const unsigned xw = (c < 2) ? xw2.x : xw2.y;
      const float x = (c & 1) ? bf_hi(xw) : bf_lo(xw);
      const float wy = bf2f(*(LAS u16*)(smem + K2_WT_OFF + (dl + c) * 64 + tl * 2));
      const float xn = bfr(bfr(x - st2.x) * st2.y);
      const float a = bfr(xn * wy);
      o[c] = bfr(x + a) + bfr(bs[c]);
    }
    u32x2 ov; ov.x = pk_bf16(o[0], o[1]); ov.y = pk_bf16(o[2], o[3]);
    *(u32x2*)(out + (size_t)(mt * 128 + ksl * 32 + tl) * D_ + (dt < 8 ? colA2 + dt * 16 : colQ2) + 4 * (lane3 >> 4)) = ov;
  };
  {
    f32x4 p0[3], p1[3];
    for (unsigned spins = 0; spins < (1u << 20); ++spins) {
      const bool ok0 = fetch(p0, w), ok1 = fetch(p1, 9 + w);
      if (__all(ok0 & ok1)) break;
      __builtin_amdgcn_s_sleep(1);
    }
    STAMP(2, 4);
    combine(p0, 0, w, xq[0], stt[0]);
    combine(p1, 1, w, xq[1], stt[1]);
    if (w < 2) {
      for (unsigned spins = 0; spins < (1u << 20); ++spins) {
        if (__all(fetch(p0, 9 * w + 8))) break;
        __builtin_amdgcn_s_sleep(1);
      }
      combine(p0, w, 8, xq[2], w ? stt[1] : stt[0]);
    }
  }
  if (bid == 0 && w == 0) {
    poll_flags<4>(flg + FL_K, 64, tag, lane);
    if (lane == 0) set_flag(flg + FL_GEN, tag);
  }
  STAMP(2, 5); RSTAMP(1, 15); STAMP_FLUSH(1); STAMP_FLUSH(2); STAMP_FLUSH(0);
  }
}

extern "C" void kernel_launch(void* const* d_in, const int* in_sizes, int n_in,
                              void* d_out, int out_size, void* d_ws, size_t ws_size,
                              hipStream_t stream) {
  const u16* q = (const u16*)d_in[0];
  const u16* k = (const u16*)d_in[1];
  const u16* v = (const u16*)d_in[2];
  const u16* shk = (const u16*)d_in[3];
  const u16* ssmk = (const u16*)d_in[4];
  const u16* ssmd = (const u16*)d_in[5];
  const u16* wg = (const u16*)d_in[7];
  const u16* wu = (const u16*)d_in[8];
  const u16* wd = (const u16*)d_in[9];
  (void)hipFuncSetAttribute((const void*)k_fused, hipFuncAttributeMaxDynamicSharedMemorySize, K1_LDS);
  k_fused<<<256, 512, K1_LDS, stream>>>(q, k, v, shk, ssmk, ssmd, wg, wu, wd, (char*)d_ws, (u16*)d_out);
}
```

```cpp
#include <hip/hip_runtime.h>
#include <hip/hip_bf16.h>
#include <stdint.h>
#include <utility>

typedef unsigned short u16;
typedef short bf16x8 __attribute__((ext_vector_type(8)));
typedef short s16x4 __attribute__((ext_vector_type(4)));
typedef float f32x4 __attribute__((ext_vector_type(4)));
typedef float f32x16 __attribute__((ext_vector_type(16)));
typedef unsigned u32x4 __attribute__((ext_vector_type(4)));
typedef unsigned u32x2 __attribute__((ext_vector_type(2)));
typedef __bf16 bf2_t __attribute__((ext_vector_type(2)));
#define LAS __attribute__((address_space(3)))
#define DI __device__ __forceinline__

constexpr int S_ = 2048, D_ = 576, F_ = 4608;

constexpr size_t WS_XFRAG = 0;
constexpr size_t WS_STATS = WS_XFRAG + (size_t)S_ * D_ * 2;
constexpr size_t WS_KVR   = WS_STATS + (size_t)S_ * 8;
constexpr size_t WS_WT    = WS_KVR + (size_t)S_ * D_ * 2;
constexpr size_t WS_H     = WS_WT + (size_t)S_ * D_ * 2;
constexpr size_t WS_SLAB  = WS_H + (size_t)S_ * F_ * 2;
constexpr size_t WS_FLG   = WS_SLAB + (size_t)4 * S_ * D_ * 4;
constexpr int FL_GEN = 0, FL_X = 32, FL_K = FL_X + 256, FL_H = FL_K + 256, FL_HE = FL_H + 256, FL_W = FL_HE + 256, FL_S = FL_W + 256, FL_END = FL_S + 256;
constexpr size_t WS_END   = WS_FLG + FL_END * 8;
typedef unsigned long long u64;

DI float bf_lo(unsigned w) { return __uint_as_float(w << 16); }
DI float bf_hi(unsigned w) { return __uint_as_float(w & 0xffff0000u); }
DI float bf2f(u16 b) { return __uint_as_float(((unsigned)b) << 16); }
DI float bfr(float x) { return (float)(__bf16)x; }
typedef float f32x2 __attribute__((ext_vector_type(2)));
DI unsigned pk_bf16(float lo, float hi);
DI f32x2 unpk(unsigned w) { return (f32x2){__uint_as_float(w << 16), __uint_as_float(w & 0xffff0000u)}; }
DI f32x2 bfr2(f32x2 v) { return unpk(pk_bf16(v.x, v.y)); }
DI unsigned pk_bf16(float lo, float hi) { bf2_t v = {(__bf16)lo, (__bf16)hi}; return __builtin_bit_cast(unsigned, v); }

DI float wave_sum(float x) {
  x += __builtin_bit_cast(float, __builtin_amdgcn_update_dpp(0, __builtin_bit_cast(int, x), 0xB1, 0xf, 0xf, true));
  x += __builtin_bit_cast(float, __builtin_amdgcn_update_dpp(0, __builtin_bit_cast(int, x), 0x4E, 0xf, 0xf, true));
  x += __builtin_bit_cast(float, __builtin_amdgcn_update_dpp(0, __builtin_bit_cast(int, x), 0x141, 0xf, 0xf, true));
  x += __builtin_bit_cast(float, __builtin_amdgcn_update_dpp(0, __builtin_bit_cast(int, x), 0x140, 0xf, 0xf, true));
  const int xi = __builtin_bit_cast(int, x);
  return (__builtin_bit_cast(float, __builtin_amdgcn_readlane(xi, 0)) + __builtin_bit_cast(float, __builtin_amdgcn_readlane(xi, 16))) +
         (__builtin_bit_cast(float, __builtin_amdgcn_readlane(xi, 32)) + __builtin_bit_cast(float, __builtin_amdgcn_readlane(xi, 48)));
}
#define WAIT_V(n) asm volatile("s_waitcnt vmcnt(" #n ")" ::: "memory")
#define WAIT_L(n) asm volatile("s_waitcnt lgkmcnt(" #n ")" ::: "memory")
#define CFENCE() asm volatile("" ::: "memory")
#define STAMP(kid, i)
#define RSTAMP(kid, i)
#define STAMP_DECL
#define STAMP_FLUSH(kid)
#define K0STAMP(i)
#define SCHED0() __builtin_amdgcn_sched_barrier(0)
template <int... I, class Fn> DI void sfor_impl(std::integer_sequence<int, I...>, Fn&& f) { (f(std::integral_constant<int, I>{}), ...); }
template <int N, class Fn> DI void sfor(Fn&& f) { sfor_impl(std::make_integer_sequence<int, N>{}, f); }
#define ALWAYS_INLINE __attribute__((always_inline))
template <int N> DI void wait_vm() { asm volatile("s_waitcnt vmcnt(%0)" ::"i"(N) : "memory"); }

DI void glds4(const void* sbase, unsigned v0, unsigned v1, unsigned v2, unsigned v3, unsigned lds_dst) {
  unsigned keep;
  asm volatile("s_mov_b32 %0, m0\n\ts_mov_b32 m0, %6\n\ts_nop 0\n\t"
               "global_load_lds_dwordx4 %1, %5\n\t"
               "global_load_lds_dwordx4 %2, %5 offset:1024\n\t"
               "global_load_lds_dwordx4 %3, %5 offset:2048\n\t"
               "global_load_lds_dwordx4 %4, %5 offset:3072\n\t"
               "s_mov_b32 m0, %0"
               : "=&s"(keep) : "v"(v0), "v"(v1), "v"(v2), "v"(v3), "s"(sbase), "s"(lds_dst) : "memory");
}
DI void glds3(const void* sbase, unsigned v0, unsigned v1, unsigned v2, unsigned lds_dst) {
  unsigned keep;
  asm volatile("s_mov_b32 %0, m0\n\ts_mov_b32 m0, %5\n\ts_nop 0\n\t"
               "global_load_lds_dwordx4 %1, %4\n\t"
               "global_load_lds_dwordx4 %2, %4 offset:1024\n\t"
               "global_load_lds_dwordx4 %3, %4 offset:2048\n\t"
               "s_mov_b32 m0, %0"
               : "=&s"(keep) : "v"(v0), "v"(v1), "v"(v2), "s"(sbase), "s"(lds_dst) : "memory");
}
DI void glds2(const void* sbase, unsigned v0, unsigned v1, unsigned lds_dst) {
  unsigned keep;
  asm volatile("s_mov_b32 %0, m0\n\ts_mov_b32 m0, %4\n\ts_nop 0\n\t"
               "global_load_lds_dwordx4 %1, %3\n\t"
               "global_load_lds_dwordx4 %2, %3 offset:1024\n\t"
               "s_mov_b32 m0, %0"
               : "=&s"(keep) : "v"(v0), "v"(v1), "s"(sbase), "s"(lds_dst) : "memory");
}
DI void glds1(const void* sbase, unsigned v0, unsigned lds_dst) {
  unsigned keep;
  asm volatile("s_mov_b32 %0, m0\n\ts_mov_b32 m0, %3\n\ts_nop 0\n\t"
               "global_load_lds_dwordx4 %1, %2\n\t"
               "s_mov_b32 m0, %0"
               : "=&s"(keep) : "v"(v0), "s"(sbase), "s"(lds_dst) : "memory");
}

template <bool NT> DI void glds1p(const void* sbase, unsigned v0, unsigned lds_dst) {
  unsigned keep;
  if constexpr (NT)
    asm volatile("s_mov_b32 %0, m0\n\ts_mov_b32 m0, %3\n\ts_nop 0\n\tglobal_load_lds_dwordx4 %1, %2 nt\n\ts_mov_b32 m0, %0"
                 : "=&s"(keep) : "v"(v0), "s"(sbase), "s"(lds_dst) : "memory");
  else
    asm volatile("s_mov_b32 %0, m0\n\ts_mov_b32 m0, %3\n\ts_nop 0\n\tglobal_load_lds_dwordx4 %1, %2\n\ts_mov_b32 m0, %0"
                 : "=&s"(keep) : "v"(v0), "s"(sbase), "s"(lds_dst) : "memory");
}
DI void glds1_sc1(const void* sbase, unsigned v0, unsigned lds_dst) {
  unsigned keep;
  asm volatile("s_mov_b32 %0, m0\n\ts_mov_b32 m0, %3\n\ts_nop 0\n\tglobal_load_lds_dwordx4 %1, %2 sc1\n\ts_mov_b32 m0, %0"
               : "=&s"(keep) : "v"(v0), "s"(sbase), "s"(lds_dst) : "memory");
}
DI void touch_lines(const void* sbase, unsigned v0, unsigned lds_dst) {
  unsigned keep;
  asm volatile("s_mov_b32 %0, m0\n\ts_mov_b32 m0, %3\n\ts_nop 0\n\tglobal_load_lds_dword %1, %2\n\ts_mov_b32 m0, %0"
               : "=&s"(keep) : "v"(v0), "s"(sbase), "s"(lds_dst) : "memory");
}
template <int OFF> DI void gload16(bf16x8& dst, const void* sbase, unsigned voff) {
  asm volatile("global_load_dwordx4 %0, %1, %2 offset:%3" : "=v"(dst) : "v"(voff), "s"(sbase), "i"(OFF) : "memory");
}
template <int OFF> DI void gload16_sc1(bf16x8& dst, const void* sbase, unsigned voff) {
  asm volatile("global_load_dwordx4 %0, %1, %2 offset:%3 sc1" : "=v"(dst) : "v"(voff), "s"(sbase), "i"(OFF) : "memory");
}
DI void gload16u(u32x4& dst, const void* sbase, unsigned voff) {
  asm volatile("global_load_dwordx4 %0, %1, %2" : "=v"(dst) : "v"(voff), "s"(sbase) : "memory");
}
template <int OFF> DI void gstore16_sc1_s(const void* sbase, unsigned voff, const u32x4& v) {
  asm volatile("global_store_dwordx4 %0, %1, %2 offset:%3 sc1\n\ts_nop 1" ::"v"(voff), "v"(v), "s"(sbase), "i"(OFF) : "memory");
}
DI void gstore16_sc1(void* p, const u32x4& v) { asm volatile("global_store_dwordx4 %0, %1, off sc1\n\ts_nop 1" ::"v"(p), "v"(v) : "memory"); }
DI void set_flag(u64* f, u64 tag) { __hip_atomic_store(f, tag, __ATOMIC_RELAXED, __HIP_MEMORY_SCOPE_AGENT); }
template <int PER> DI void poll_flags(u64* f, int n, u64 tag, int lane) {
  asm volatile("" : "+v"(lane));
  const int i = lane < n ? lane : 0;
  for (unsigned spins = 0; spins < (1u << 20); ++spins) {
    bool ok = true;
#pragma unroll
    for (int k = 0; k < PER; ++k) ok &= __hip_atomic_load(f + i + 64 * k, __ATOMIC_RELAXED, __HIP_MEMORY_SCOPE_AGENT) == tag;
    if (__all(ok)) break;
    __builtin_amdgcn_s_sleep(1);
  }
}

constexpr int K1_SLOT = 37120;
constexpr int K1_UOFF = 18432 + 128;
constexpr int K1_NSLOT = 4;
constexpr int K1_PF_OFF = K1_NSLOT * K1_SLOT;
constexpr int XR_OFF = K1_PF_OFF + 288, XR_STRIDE = 4608;
constexpr int HZ_OFF = XR_OFF + 3 * XR_STRIDE;
constexpr int K1_LDS = HZ_OFF + 288;
constexpr int PART_OFF = 65536;
constexpr int PARTC_OFF = PART_OFF + 65536;
constexpr int HCNT_OFF = K1_PF_OFF;
constexpr int XG_OFF = 3 * K1_SLOT;

DI bf16x8 tr_frag(LAS char* p, int off, int hi_off) {
  s16x4 lo = __builtin_amdgcn_ds_read_tr16_b64_v4i16((LAS s16x4*)(p + off));
  s16x4 hi = __builtin_amdgcn_ds_read_tr16_b64_v4i16((LAS s16x4*)(p + off + hi_off));
  return __builtin_shufflevector(lo, hi, 0, 1, 2, 3, 4, 5, 6, 7);
}

constexpr int K1_MID = 16;
constexpr int k1_xkey(int i) { return i < 9 ? -100 + i : 4 * (1 + 2 * (i - 9)) + 2; }
constexpr int k1_dstep(int d, int n) { return d == 2 ? 2 + 4 * n : d == 3 ? 24 + 4 * n : d == 4 ? 58 + 6 * n : d == 5 ? 94 + 6 * n : 36 * (d - 3) + K1_MID + 6 * n; }
constexpr int k1_dkey(int d, int n) { return d < 2 ? -400 + 10 * d + n : 4 * k1_dstep(d, n) + 1; }
constexpr int k1_cstep(int i) { return i < 7 ? 218 + 4 * i : (i < 14 ? 254 + 4 * (i - 7) : (i < 21 ? 289 + (i - 14) : 304 + 2 * (i - 21))); }
constexpr int k1_sstep(int c) { return c == 0 ? 143 : (c < 4 ? 144 + 6 * c : (c == 4 ? 182 : 302 + 9 * (c - 5))); }
constexpr int k1_astep(int i) { return i < 14 ? k1_cstep(i) + 28 : (i < 21 ? k1_cstep(i) + 20 : 321 + (i - 21)); }
constexpr int K1_TOUCH = 7 * 36 + K1_MID + 2;
constexpr int k1_younger(int okey, int wkey) {
  int n = 0;
  if (4 * K1_TOUCH + 1 > okey && 4 * K1_TOUCH + 1 < wkey) n += 1;
  if (-70 > okey && -70 < wkey) n += 2;
  for (int i = 0; i < 36; ++i) if (k1_xkey(i) > okey && k1_xkey(i) < wkey) n += 1;
  for (int d = 0; d < 9; ++d) for (int pc = 0; pc < 5; ++pc) if (k1_dkey(d, pc) > okey && k1_dkey(d, pc) < wkey) n += 1;
  for (int i = 0; i < 24; ++i) if (4 * k1_cstep(i) + 2 > okey && 4 * k1_cstep(i) + 2 < wkey) n += 2;
  for (int c = 0; c < 8; ++c) if (4 * k1_sstep(c) + 3 > okey && 4 * k1_sstep(c) + 3 < wkey) n += 1;
  return n;
}
#define HSTORE 1

constexpr int K2_HALF = 16384;
constexpr int K2_SLOT = 2 * K2_HALF;
constexpr int K2_NSLOT = 4;
constexpr int K2_OWN_OFF = K2_NSLOT * K2_SLOT;
constexpr int K2_WT_OFF = K2_OWN_OFF + 18432;
constexpr int k2_g0(int st) { return st <= 9 ? 16 * st : 153; }
constexpr int k2_stage(int g) { return g < 144 ? g / 16 : (g < 153 ? 9 : 10); }
constexpr int k2_mid(int st) { return st < 9 ? 6 : 4; }
constexpr int k2_unit(int st) { return st < 5 ? 2 * st : 2 * (st - 5) + 1; }
constexpr int k2_np(int d) { return d < 9 ? 4 : 3; }
constexpr int k2_dkey(int d, int n) { return d < 2 ? -300 + 10 * d + n : (d == 2 ? 4 * (1 + n) + 2 : 4 * (k2_g0(d - 3) + k2_mid(d - 3) + 4 * n) + 2); }
constexpr int k2_hkey(int j, int m) { return j < 3 ? -250 + 10 * j + m : 4 * (k2_g0(j - 3) + k2_mid(j - 3) + 2 + 4 * m) + 3; }
constexpr int k2_skey(int p) { return 4 * (k2_g0(9 + p) + k2_mid(9 + p)) + 1; }
constexpr int K2_WST = 7;
constexpr int k2_wkey() { return 4 * (k2_g0(K2_WST) + k2_mid(K2_WST)) + 1; }
constexpr int k2_younger(int okey, int wkey, bool stores) {
  int n = 0;
  if (k2_wkey() > okey && k2_wkey() < wkey) n += 2;
  if (-100 > okey && -100 < wkey) n += 1;
  for (int d = 0; d < 11; ++d) for (int pc = 0; pc < k2_np(d); ++pc) if (k2_dkey(d, pc) > okey && k2_dkey(d, pc) < wkey) n += 1;
  for (int j = 0; j < 9; ++j) for (int m = 0; m < 4; ++m) if (k2_hkey(j, m) > okey && k2_hkey(j, m) < wkey) n += 1;
  if (stores) for (int p = 0; p < 2; ++p) if (k2_skey(p) > okey && k2_skey(p) < wkey) n += 4;
  return n;
}
constexpr int cmax(int a, int b) { return a > b ? a : b; }

__global__ __launch_bounds__(512, 2) void k_fused(const u16* __restrict__ q, const u16* __restrict__ kk, const u16* __restrict__ vv,
                                                  const u16* __restrict__ shk, const u16* __restrict__ ssmk, const u16* __restrict__ ssmd,
                                                  const u16* __restrict__ wg, const u16* __restrict__ wu, const u16* __restrict__ wd,
                                                  char* ws, u16* __restrict__ out) {
  u16* xfrag = (u16*)(ws + WS_XFRAG);
  u64* stats = (u64*)(ws + WS_STATS);
  u16* kvr = (u16*)(ws + WS_KVR);
  u16* wT = (u16*)(ws + WS_WT);
  u16* hout = (u16*)(ws + WS_H);
  float* slab = (float*)(ws + WS_SLAB);
  u64* flg = (u64*)(ws + WS_FLG);
  extern __shared__ __attribute__((aligned(16))) char smem_g[];
  LAS char* smem = (LAS char*)smem_g;
  const unsigned lds0 = (unsigned)(uintptr_t)smem;
  const int bid = blockIdx.x, tid = threadIdx.x;
  const int w = __builtin_amdgcn_readfirstlane(tid >> 6), lane = tid & 63;
  const int xcd = bid & 7, idx = bid >> 3;
  STAMP_DECL; RSTAMP(1, 14); STAMP(1, 0);
  const u64 genv = __hip_atomic_load(flg + FL_GEN, __ATOMIC_RELAXED, __HIP_MEMORY_SCOPE_AGENT);
  u64 tag;
  auto make_tag = [&]() ALWAYS_INLINE {
    tag = (((u64)(unsigned)__builtin_amdgcn_readfirstlane((unsigned)(genv >> 32)) << 32) | (unsigned)__builtin_amdgcn_readfirstlane((unsigned)genv)) + 1ull;
  };
  const int ksl = xcd >> 1, mt = (xcd & 1) * 8 + (idx >> 2), nt = idx & 3;
  const int tq = w & 3, khf = w >> 2;
  const int kbase = ksl * 1152 + khf * 576;
  const int colA2 = 128 * nt, colQ2 = 512 + 16 * nt;
  const int mtile = (xcd & 1) * 4 + (idx & 3);
  const int cgrp = (xcd >> 1) * 2 + (idx >> 4), cq = (idx >> 2) & 3;
  const int colA = 64 * (9 * cgrp + 2 * cq), colQ = 64 * (9 * cgrp + 8) + 16 * cq;

  const int grp = bid >> 2, qd = bid & 3;
  const int chA = grp * 9 + 2 * qd, chC = grp * 9 + 8;
  float ddA, ddC;
  {
    const int c1 = chA + (w >> 2);
    const unsigned d1 = ((const unsigned*)ssmd)[c1 >> 1], d2 = ((const unsigned*)ssmd)[chC >> 1];
    ddA = (c1 & 1) ? bf_hi(d1) : bf_lo(d1);
    ddC = (chC & 1) ? bf_hi(d2) : bf_lo(d2);
  }

  const int wq = w & 3, part = w >> 2;
  const char* wbase = (const char*)(part ? wu : wg);
  unsigned voff[5];
#pragma unroll
  for (int n = 0; n < 5; ++n) {
    const int o = (n < 4) ? ((wq * 4 + n) * 1024 + lane * 16) : (16384 + wq * 512 + (lane & 31) * 16);
    const int k = o >> 7, pc = (o >> 4) & 7;
    const int j = (pc >> 1) ^ (((k >> 1) & 1) | (part << 1));
    voff[n] = (unsigned)(k * (F_ * 2) + (2 * j + (pc & 1)) * 16);
  }
  const unsigned ldst = lds0 + part * K1_UOFF + wq * 4096;
  const unsigned ldst4 = lds0 + part * K1_UOFF + 16384 + wq * 512;

  auto piece_load = [&](int st, int n) ALWAYS_INLINE {
    const int slot = st % K1_NSLOT;
    const unsigned dst = (n < 4 ? ldst + n * 1024 : ldst4) + slot * K1_SLOT;
    if (st != 4) {
      const int ks = st < 4 ? st : st - 5;
      const char* g = wbase + (size_t)ks * 144 * (F_ * 2) + (colA + (st > 4 ? 64 : 0)) * 2;
      if (n < 4 || lane < 32) glds1p<false>(g, voff[n], dst);
    } else {
      int l2 = lane; asm volatile("" : "+v"(l2));
      const int o = (n < 4) ? ((wq * 4 + n) * 1024 + l2 * 16) : (16384 + wq * 512 + (l2 & 31) * 16);
      const unsigned vq = (unsigned)((o >> 5) * (F_ * 2) + ((o >> 4) & 1) * 16);
      if (n < 4 || lane < 32) glds1p<false>(wbase + colQ * 2, vq, dst);
    }
  };

  bf16x8 xf[36];
  const char* xb = (const char*)xfrag + (size_t)(mtile * 8 + w) * 36 * 1024;
  const unsigned xv = lane * 16;
  auto x_load = [&](auto ic) ALWAYS_INLINE {
    constexpr int i = ic;
    gload16_sc1<(i & 3) * 1024>(xf[i], xb + (i >> 2) * 4096, xv);
  };
  {
    LAS u16* tile = (LAS u16*)(smem + XR_OFF);
    constexpr int KT_OFF = 2 * K1_SLOT, VT_OFF = KT_OFF + 9728, WT_OFF = VT_OFF + 9216;
    const int tt = bid >> 3, ct = bid & 7, t0 = tt * 64, c0 = ct * 72;
    const int tok0 = mtile * 256 + (cgrp * 4 + cq) * 8, token = tok0 + w;
    const bool act = lane < 48;
    u32x2 d[3];
    u32x4 kin, vin, ein;
#pragma unroll
    for (int i = 0; i < 3; ++i)
      asm volatile("global_load_dwordx2 %0, %1, %2 offset:%3" : "=v"(d[i]) : "v"((unsigned)(token * (D_ * 2) + (act ? lane : 0) * 8)), "s"(q), "i"(i * 384) : "memory");
    auto k_off = [&](int pc) ALWAYS_INLINE -> unsigned { const int r = pc / 9, cg = pc - r * 9, ts = t0 - 3 + r; return (unsigned)((ts < 0 ? 0 : ts) * (D_ * 2) + (c0 + 8 * cg) * 2); };
    auto v_off = [&](int pc) ALWAYS_INLINE -> unsigned { const int r = pc / 9, cg = pc - r * 9; return (unsigned)((t0 + r) * (D_ * 2) + (c0 + 8 * cg) * 2); };
    gload16u(kin, kk, k_off(tid));
    gload16u(vin, vv, v_off(tid));
    int edst;
    {
      const int kp = w == 0 ? 512 + lane : 576 + (lane < 27 ? lane : 0);
      const int wl = lane < 63 ? lane - 27 : 35;
      const bool isw = w == 1 && lane >= 27, isv = w >= 2;
      const char* src = isv ? (const char*)vv + v_off(512 + lane) : (isw ? (const char*)shk + c0 * 8 + wl * 16 : (const char*)kk + k_off(kp));
      edst = isv ? VT_OFF + (512 + lane) * 16 : (isw ? WT_OFF + wl * 16 : KT_OFF + kp * 16);
      asm volatile("global_load_dwordx4 %0, %1, off" : "=v"(ein) : "v"(src) : "memory");
    }
    asm volatile("s_waitcnt vmcnt(3)" : "+v"(d[0]), "+v"(d[1]), "+v"(d[2])::"memory");
    STAMP(1, 9);
    f32x2 xa[3], xb2[3];
#pragma unroll
    for (int i = 0; i < 3; ++i) { xa[i] = unpk(d[i].x); xb2[i] = unpk(d[i].y); }
    const f32x2 s2 = (xa[0] + xb2[0]) + (xa[1] + xb2[1]) + (xa[2] + xb2[2]);
    const float mean = wave_sum(act ? s2.x + s2.y : 0.f) * (1.0f / 576.0f);
    f32x2 v2 = {0.f, 0.f};
#pragma unroll
    for (int i = 0; i < 3; ++i) { const f32x2 a = xa[i] - mean, b = xb2[i] - mean; v2 += a * a + b * b; }
    const float vs = wave_sum(act ? v2.x + v2.y : 0.f);
    const float mb = bfr(mean), vb = bfr(vs * (1.0f / 576.0f));
    const float sd = bfr(sqrtf(bfr(vb + 1e-5f)));
    const float rsd = 1.0f / sd;
#pragma unroll
    for (int i = 0; i < 3; ++i) {
      const f32x2 a = bfr2(xa[i] - mb) * rsd, b = bfr2(xb2[i] - mb) * rsd;
      u32x2 o; o.x = pk_bf16(a.x, a.y); o.y = pk_bf16(b.x, b.y);
      const int p8 = lane + 48 * i;
      if (act) *(LAS u32x2*)(smem + XG_OFF + ((p8 >> 1) * 8 + w) * 16 + (p8 & 1) * 8) = o;
    }
    if (tid == 0) *(LAS unsigned*)(smem + HCNT_OFF + 4) = 0u;
    asm volatile("s_waitcnt vmcnt(0)" : "+v"(kin), "+v"(vin), "+v"(ein)::"memory");
    make_tag();
    if (t0 == 0 && tid < 27) kin = (u32x4){0u, 0u, 0u, 0u};
    *(LAS u32x4*)(smem + KT_OFF + tid * 16) = kin;
    *(LAS u32x4*)(smem + VT_OFF + tid * 16) = vin;
    if (w < 3) *(LAS u32x4*)(smem + edst) = ein;
    WAIT_L(0);
    __builtin_amdgcn_s_barrier();
    CFENCE();
    {
      char* xo = (char*)xfrag + (size_t)((tok0 >> 5) * 36 * 64 + (tok0 & 31)) * 16;
#pragma unroll
      for (int it = 0; it < 2; ++it) {
        const int id = it ? 512 + lane : tid, p16 = id >> 3;
        if (it == 0 || w == 0) {
          const u32x4 v = *(LAS u32x4*)(smem + XG_OFF + id * 16);
          gstore16_sc1(xo + ((p16 >> 1) * 64 + (p16 & 1) * 32 + (id & 7)) * 16, v);
        }
      }
      if (lane == 0) __hip_atomic_store(stats + token, (u64)__float_as_uint(mb) | ((u64)__float_as_uint(rsd) << 32), __ATOMIC_RELAXED, __HIP_MEMORY_SCOPE_AGENT);
    }
    STAMP(1, 10);
    CFENCE();
#pragma unroll
    for (int n = 0; n < 5; ++n) piece_load(0, n);
#pragma unroll
    for (int n = 0; n < 5; ++n) piece_load(1, n);
    wait_vm<10>();
    STAMP(1, 11);
    if (lane == 0) {
      const unsigned old = __hip_atomic_fetch_add((LAS unsigned*)(smem + HCNT_OFF + 4), 1u, __ATOMIC_RELAXED, __HIP_MEMORY_SCOPE_WORKGROUP);
      if (old == 7u) set_flag(flg + FL_X + (tok0 >> 3), tag);
    }
    auto kv_compute = [&](int item) ALWAYS_INLINE {
      const int tl = item / 9, cg = item - tl * 9;
      u32x4 kr[4], w4[4];
#pragma unroll
      for (int i = 0; i < 4; ++i) { kr[i] = *(LAS u32x4*)(smem + KT_OFF + (item + 9 * i) * 16); w4[i] = *(LAS u32x4*)(smem + WT_OFF + cg * 64 + i * 16); }
      const u32x4 vr = *(LAS u32x4*)(smem + VT_OFF + item * 16);
#pragma unroll
      for (int e2 = 0; e2 < 4; ++e2) {
        const f32x2 w0 = {bf_lo(w4[e2].x), bf_lo(w4[e2].z)}, w1 = {bf_hi(w4[e2].x), bf_hi(w4[e2].z)};
        const f32x2 w2 = {bf_lo(w4[e2].y), bf_lo(w4[e2].w)}, w3 = {bf_hi(w4[e2].y), bf_hi(w4[e2].w)};
        f32x2 ks = bfr2(unpk(kr[0][e2]) * w3);
        ks = bfr2(ks + bfr2(unpk(kr[1][e2]) * w2));
        ks = bfr2(ks + bfr2(unpk(kr[2][e2]) * w1));
        ks = bfr2(ks + bfr2(unpk(kr[3][e2]) * w0));
        const f32x2 kv = ks * unpk(vr[e2]);
        const unsigned pk = pk_bf16(kv.x, kv.y);
        tile[(8 * cg + 2 * e2) * 72 + (63 - tl)] = (u16)(pk & 0xffffu);
        tile[(8 * cg + 2 * e2 + 1) * 72 + (63 - tl)] = (u16)(pk >> 16);
      }
    };
    kv_compute(tid);
    STAMP(1, 7);
    poll_flags<1>(flg + FL_X + mtile * 32 + w * 4, 4, tag, lane);
    wait_vm<0>();
    STAMP(1, 8);
    sfor<9>([&](auto ic) ALWAYS_INLINE { x_load(ic); });
    if (w == 7) kv_compute(512 + lane);
    WAIT_L(0);
    __builtin_amdgcn_s_barrier();
    CFENCE();
    {
      const int ch = tid >> 3, pp = tid & 7, ch2 = 64 + (lane >> 3);
      const u32x4 o1 = *(LAS u32x4*)(tile + ch * 72 + pp * 8), o2 = *(LAS u32x4*)(tile + ch2 * 72 + pp * 8);
      gstore16_sc1(kvr + (size_t)(c0 + ch) * S_ + (1984 - t0) + pp * 8, o1);
      gstore16_sc1(kvr + (size_t)(c0 + ch2) * S_ + (1984 - t0) + pp * 8, o2);
    }
  }

  const int kh = lane >> 5, prt = (lane >> 4) & 1, qq = (lane & 15) >> 2, p4 = lane & 3;
  const unsigned rbU = lds0 + (8 * kh + qq) * 128 + prt * K1_UOFF + ((((qq >> 1) & 1) | (prt << 1)) << 5) + p4 * 8;

  f32x16 acc[4];
  const f32x16 zero16 = {0.f, 0.f, 0.f, 0.f, 0.f, 0.f, 0.f, 0.f, 0.f, 0.f, 0.f, 0.f, 0.f, 0.f, 0.f, 0.f};

  const int token = mtile * 256 + w * 32 + (lane & 31);
  char* hb = (char*)hout + ((size_t)(token >> 4) * 144 * 64 + (lane >> 5) * 16 + (token & 15)) * 16;
  auto h_word = [&](const f32x16& a, int i) ALWAYS_INLINE -> unsigned {
    return pk_bf16(bfr(a[2 * i]) * bfr(a[2 * i + 8]), bfr(a[2 * i + 1]) * bfr(a[2 * i + 9]));
  };
  auto h_store = [&](const u32x4& o, int c16) ALWAYS_INLINE {
    gstore16_sc1(hb + ((size_t)(c16 >> 1) * 64 + (c16 & 1) * 32) * 16, o);
  };
  u32x4 ho;

  const char* hpA = (const char*)(ssmk + (size_t)(chA + (w >> 2)) * S_ + 64 * (w & 3));
  const char* hpC = (const char*)(ssmk + (size_t)chC * S_ + 32 * w);
  const bool zA = (w & 3) == 0, zC = w == 0;
  const char* pf_sk = (const char*)ssmk;
  const char* pf_wd = (const char*)wd + (size_t)(xcd >> 1) * 1152 * 1152;
  const char* kvb = (const char*)kvr;
  u32x4 caf[24];
  unsigned cax[24];
  bf16x8 cf[24];
  int cvo = 0, cvx = 0;
  auto conv_align = [&](auto ic) ALWAYS_INLINE {
    constexpr int i = ic;
    const unsigned sh = ((lane + 1) & 1) * 2;
    u32x4 r;
    r.x = __builtin_amdgcn_alignbyte(caf[i].y, caf[i].x, sh);
    r.y = __builtin_amdgcn_alignbyte(caf[i].z, caf[i].y, sh);
    r.z = __builtin_amdgcn_alignbyte(caf[i].w, caf[i].z, sh);
    r.w = __builtin_amdgcn_alignbyte(cax[i], caf[i].w, sh);
    cf[i] = __builtin_bit_cast(bf16x8, r);
    asm volatile("" : "+v"(cf[i]));
  };

  STAMP(1, 1);

  constexpr int PD = 3, MID = K1_MID;
  bf16x8 af[PD + 1];
  unsigned rb[4];
#pragma unroll
  for (int j = 0; j < 4; ++j) rb[j] = rbU ^ (j << 5);
#pragma unroll
  for (int g = 0; g < PD; ++g) af[g] = tr_frag((LAS char*)(uintptr_t)rb[g], 0, 512);
  sfor<324>([&](auto gc) ALWAYS_INLINE {
    constexpr int g = gc;
    constexpr int st = g / 36, f = g - st * 36;
    constexpr int s = st == 4 ? f : (st == 8 ? f % 9 : f / 4), c = st == 4 ? 0 : (st == 8 ? f / 9 : f % 4);
    constexpr int xi = st < 4 ? st * 9 + s : (st == 4 ? s : (st - 5) * 9 + s);
    constexpr int ai = st < 4 ? c : (st == 4 ? 0 : (c + 1) & 3);
    constexpr bool first = (st == 0 || st == 5) ? s == 0 : (st == 4 && f == 0);
    if constexpr (f == MID) {

      if constexpr (st < 8) wait_vm<k1_younger(k1_dkey(st + 1, 4), 4 * g)>();
      else { wait_vm<k1_younger(4 * (7 * 36 + MID + 1) + 1, 4 * g)>(); if (w <= 3) WAIT_L(0); }
      __builtin_amdgcn_s_barrier();
      CFENCE();

      if constexpr (st == 1) { if (tid == 0) set_flag(flg + FL_K + (bid & 7) * 32 + (bid >> 3), tag); }
      if constexpr (st == 6) {
        static_assert(k1_dkey(7, 4) > 4 * k1_sstep(4) + 3, "the stage 7 wait must cover the h stores of panels A and Q");
        if (tid == 0) set_flag(flg + FL_HE + (mtile * 4 + (cgrp >> 1)) * 8 + (cgrp & 1) * 4 + cq, tag);
      }
    }
    if constexpr (((f + 36 - MID) % 36) % 9 == 0) __builtin_amdgcn_s_setprio(3 - ((f + 36 - MID) % 36) / 9);
    sfor<35>([&](auto dc) ALWAYS_INLINE { if constexpr (k1_dkey(dc / 5 + 2, dc % 5) == 4 * g + 1) piece_load(dc / 5 + 2, dc % 5); });
    sfor<27>([&](auto ic) ALWAYS_INLINE { if constexpr (k1_xkey(ic + 9) == 4 * g + 2) x_load(std::integral_constant<int, ic + 9>{}); });
    if constexpr (g == 7 * 36 + MID + 1) {
      if (w < 3) {
        const int ch = (w == 2) ? chC : chA + w;
        const unsigned lv = lane * 16;
        poll_flags<1>(flg + FL_K + (grp >> 3) * 32, 32, tag, lane);
        const __amdgpu_buffer_rsrc_t rk = __builtin_amdgcn_make_buffer_rsrc((char*)kvb + (size_t)ch * (S_ * 2), 0, S_ * 2, 0x00020000);
#pragma unroll
        for (int i = 0; i < 4; ++i)
          *(LAS u32x4*)(smem + XR_OFF + w * XR_STRIDE + i * 1024 + lv) = __builtin_amdgcn_raw_buffer_load_b128(rk, lv + i * 1024, 0, 16);
        if (lane < 4) glds1(pf_sk + (size_t)ch * (S_ * 2), lv, lds0 + HZ_OFF + w * 96 + 32);
      } else if (w == 3) {
        if (lane < 32) {
#pragma unroll
          for (int im = 0; im < 3; ++im) *(LAS u32x4*)(smem + XR_OFF + im * XR_STRIDE + 4096 + lane * 16) = (u32x4){0u, 0u, 0u, 0u};
        } else if (lane < 38) {
          const int im = (lane - 32) >> 1, hh = lane & 1;
          *(LAS u32x4*)(smem + HZ_OFF + im * 96 + hh * 16) = (u32x4){0u, 0u, 0u, 0u};
        } else if (lane == 38) *(LAS unsigned*)(smem + HCNT_OFF) = 0u;
      }
    }
    sfor<24>([&](auto ac) ALWAYS_INLINE { if constexpr (k1_astep(ac) == g) conv_align(ac); });
    if constexpr (g == K1_TOUCH) {
      int l2 = lane; asm volatile("" : "+v"(l2));
      const unsigned line = (unsigned)(w * 64 + l2 < 324 ? idx * 324 + w * 64 + l2 : idx * 324);
      touch_lines(pf_wd, line * 128u, lds0 + K1_PF_OFF + 16);
    }
    sfor<24>([&](auto cc) ALWAYS_INLINE {
      constexpr int ci = cc;
      if constexpr (k1_cstep(ci) == g) {
        if constexpr (ci == 0) {
          int l2 = lane; asm volatile("" : "+v"(l2));
          const int e = (l2 & 15) - 15 + 8 * (l2 >> 4);
          cvo = (e & ~1) * 2; cvx = cvo + ((e & 1) ? 16 : 0);
        }
        constexpr int cb = ci < 16 ? 64 * (ci >> 3) + 512 * (ci & 7) : 512 * (ci - 16);
        const char* hp = (ci < 16 ? hpA : hpC) + cb + ((ci == 0 && zA) || (ci == 16 && zC) ? 32 : 0);
        __builtin_memcpy(&caf[ci], __builtin_assume_aligned(hp + cvo, 4), 16);
        cax[ci] = *(const unsigned*)(hp + cvx);
      }
    });
    if constexpr (g + PD < 324) {
      constexpr int g1 = g + PD, st1 = g1 / 36, f1 = g1 - st1 * 36;
      if constexpr (f1 == 0) {
        if constexpr (st1 != 4) {
          unsigned b2 = rbU; asm volatile("" : "+v"(b2));
#pragma unroll
          for (int j = 0; j < 4; ++j) { rb[j] = (b2 + (st1 % K1_NSLOT) * K1_SLOT) ^ (j << 5); asm volatile("" : "+v"(rb[j])); }
        } else {
          int l2 = lane; asm volatile("" : "+v"(l2));
          rb[0] = lds0 + (8 * (l2 >> 5) + ((l2 & 15) >> 2)) * 32 + ((l2 >> 4) & 1) * K1_UOFF + (l2 & 3) * 8 + (st1 % K1_NSLOT) * K1_SLOT;
          asm volatile("" : "+v"(rb[0]));
        }
      }
      if constexpr (st1 == 8) af[g1 % (PD + 1)] = tr_frag((LAS char*)(uintptr_t)rb[f1 / 9], (f1 % 9) * 2048, 512);
      else if constexpr (st1 != 4) af[g1 % (PD + 1)] = tr_frag((LAS char*)(uintptr_t)rb[f1 % 4], (f1 / 4) * 2048, 512);
      else af[g1 % (PD + 1)] = tr_frag((LAS char*)(uintptr_t)rb[0], f1 * 512, 128);
    }
    if constexpr (st < 4 && c == 0) {
      asm volatile("s_waitcnt vmcnt(%1)" : "+v"(xf[xi]) : "i"(k1_younger(k1_xkey(xi), 4 * g + 3)) : "memory");
    }
    SCHED0();
    acc[ai] = __builtin_amdgcn_mfma_f32_32x32x16_bf16(af[g % (PD + 1)], xf[xi], first ? zero16 : acc[ai], 0, 0, 0);
    SCHED0();
    if constexpr (g == 143) {
#pragma unroll
      for (int i = 0; i < 4; ++i) ho[i] = h_word(acc[0], i);
      h_store(ho, colA >> 4);
      SCHED0();
    }
    if constexpr (g > 144 && g <= 162) {
      constexpr int cc = (g - 145) / 6 + 1, pi = (g - 145) % 6;
      if constexpr (pi < 4) ho[pi] = h_word(acc[cc], pi);
      if constexpr (pi == 5) h_store(ho, (colA >> 4) + cc);
      SCHED0();
    }
    if constexpr (g >= 298 && (g - 298) % 9 < 5 && (g - 298) / 9 < 3) {
      constexpr int cc = (g - 298) / 9, pi = (g - 298) % 9;
      if constexpr (pi < 4) ho[pi] = h_word(acc[(cc + 1) & 3], pi);
      if constexpr (pi == 4) h_store(ho, (colA >> 4) + 4 + cc);
      SCHED0();
    }
    if constexpr (g >= 180 && g <= 182) {
      if constexpr (g == 180) { ho[0] = h_word(acc[0], 0); ho[1] = h_word(acc[0], 1); }
      if constexpr (g == 181) { ho[2] = h_word(acc[0], 2); ho[3] = h_word(acc[0], 3); }
      if constexpr (g == 182) h_store(ho, colQ >> 4);
      SCHED0();
    }
  });
  const char* wbase2 = (const char*)wd + (size_t)kbase * (D_ * 2);
  unsigned voff2[4];
  int lane2 = lane; asm volatile("" : "+v"(lane2));
#pragma unroll
  for (int n = 0; n < 4; ++n) {
    const int o = (tq * 4 + n) * 1024 + lane2 * 16;
    const int k = o >> 8, pc = (o >> 4) & 15;
    const int j = (pc >> 1) ^ (k & 7);
    voff2[n] = (unsigned)(k * (D_ * 2) + (2 * j + (pc & 1)) * 16);
  }
  const unsigned ldst2 = lds0 + khf * K2_HALF;
  auto piece_load2 = [&](int st, int n) ALWAYS_INLINE {
    const int slot = st % K2_NSLOT;
    if (st < 9) {
      const char* g = wbase2 + (size_t)k2_unit(st) * 64 * (D_ * 2) + colA2 * 2;
      glds1p<false>(g, voff2[n], ldst2 + slot * K2_SLOT + tq * 4096 + n * 1024);
    } else {
      int l2 = lane; asm volatile("" : "+v"(l2));
      const char* g = wbase2 + (size_t)(st - 9) * 288 * (D_ * 2) + colQ2 * 2;
      const int o = n < 2 ? tq * 2048 + n * 1024 + l2 * 16 : 8192 + tq * 256 + (l2 & 15) * 16;
      const unsigned v0 = (unsigned)((o >> 5) * (D_ * 2) + ((o >> 4) & 1) * 16);
      if (n < 2 || l2 < 16) glds1p<false>(g, v0, ldst2 + slot * K2_SLOT + (n < 2 ? tq * 2048 + n * 1024 : 8192 + tq * 256));
    }
  };
  const char* hbase = (const char*)hout + ((size_t)(mt * 8 + tq * 2) * 144 * 64 + (size_t)(kbase >> 5) * 64) * 16;
  const unsigned hv0 = lane2 * 16 + khf * (144 * 64 * 16), hv1 = lane2 * 16 + (1 - khf) * (144 * 64 * 16);
  bf16x8 hf[18][2];
  auto h_load = [&](auto lc) ALWAYS_INLINE {
    constexpr int k = 2 * k2_unit(lc / 4) + (lc / 2) % 2, t = lc % 2;
    gload16_sc1<(k & 3) * 1024>(hf[k][t], hbase + (k >> 2) * 4096, t ? hv1 : hv0);
  };
  STAMP(1, 2);
  __builtin_amdgcn_s_setprio(0);
  {
    u32x4 o;
#pragma unroll
    for (int i = 0; i < 4; ++i) o[i] = h_word(acc[0], i);
    h_store(o, (colA >> 4) + 7);
  }

  __builtin_amdgcn_s_barrier();
  CFENCE();
#pragma unroll
  for (int n = 0; n < 4; ++n) piece_load2(0, n);
#pragma unroll
  for (int n = 0; n < 4; ++n) piece_load2(1, n);

  STAMP(1, 3);
  const int ct0 = lane & 15, ckg = lane >> 4;
  {
    const int boff = (2032 - 16 * (lane & 15) + 8 * ckg) * 2;
    auto head_frag = [&](int im) ALWAYS_INLINE -> bf16x8 {
      bf16x8 r;
      LAS u16* hz = (LAS u16*)(smem + HZ_OFF + im * 96) + 16 + (ct0 - 15 + 8 * ckg);
#pragma unroll
      for (int j = 0; j < 8; ++j) r[j] = (short)hz[j];
      return r;
    };
    if (zA) cf[0] = head_frag(w >> 2);
    if (zC) cf[16] = head_frag(2);
    {
      const int im = w >> 2;
      LAS char* xr = smem + XR_OFF + im * XR_STRIDE + boff + 128 * (w & 3);
      f32x4 ac[8];
#pragma unroll
      for (int j = 0; j < 8; ++j) ac[j] = (f32x4){0.f, 0.f, 0.f, 0.f};
#pragma unroll
      for (int rr = 1; rr >= 0; --rr) {
        bf16x8 bfm[8];
#pragma unroll
        for (int b = 0; b < 8; ++b) bfm[b] = *(LAS bf16x8*)(xr - 512 * b + 64 * rr);
#pragma unroll
        for (int a = 7; a >= 0; --a)
#pragma unroll
          for (int j = a; j < 8; ++j) ac[j] = __builtin_amdgcn_mfma_f32_16x16x32_bf16(cf[rr * 8 + a], bfm[j - a], ac[j], 0, 0, 0);
        if (rr == 1) {
          SCHED0();
          wait_vm<8>();
          if (lane == 0) {
            const unsigned old = __hip_atomic_fetch_add((LAS unsigned*)(smem + HCNT_OFF), 1u, __ATOMIC_RELAXED, __HIP_MEMORY_SCOPE_WORKGROUP);
            if (old == 7u) set_flag(flg + FL_H + (mtile * 4 + (cgrp >> 1)) * 8 + (cgrp & 1) * 4 + cq, tag);
          }
          SCHED0();
        }
      }
#pragma unroll
      for (int j = 0; j < 8; ++j)
        *(LAS f32x4*)(smem + PART_OFF + w * 8192 + (j * 64 + lane) * 16) = ac[j];
    }
    poll_flags<1>(flg + FL_HE + ((mt >> 1) * 4 + ksl) * 8, 8, tag, lane);
    sfor<12>([&](auto kc) ALWAYS_INLINE { h_load(kc); });
    {
      LAS char* xr = smem + XR_OFF + 2 * XR_STRIDE + boff + 64 * w;
      const int jA = 7 - qd, jB = qd;
      bf16x8 b0[8], b1[4];
#pragma unroll
      for (int a = 0; a < 8; ++a) { const int bb = jA - a; b0[a] = *(LAS bf16x8*)(xr - 512 * (bb < 0 ? 0 : bb)); }
#pragma unroll
      for (int a = 0; a < 4; ++a) { const int bb = jB - a; b1[a] = *(LAS bf16x8*)(xr - 512 * (bb < 0 ? 0 : bb)); }
      f32x4 c0 = (f32x4){0.f, 0.f, 0.f, 0.f}, c1 = c0;
#pragma unroll
      for (int a = 7; a >= 0; --a) {
        if (a <= jA) c0 = __builtin_amdgcn_mfma_f32_16x16x32_bf16(cf[16 + a], b0[a], c0, 0, 0, 0);
        if (a < 4 && a <= jB) c1 = __builtin_amdgcn_mfma_f32_16x16x32_bf16(cf[16 + a], b1[a < 4 ? a : 0], c1, 0, 0, 0);
      }
      *(LAS f32x4*)(smem + PARTC_OFF + w * 2048 + lane * 16) = c0;
      *(LAS f32x4*)(smem + PARTC_OFF + w * 2048 + 1024 + lane * 16) = c1;
    }
  }
  STAMP(1, 4);
  WAIT_L(0);
  __builtin_amdgcn_s_barrier();
  CFENCE();
  STAMP(1, 5);
  auto finalize = [&](f32x4 s0, f32x4 s1, int im, int ch, int t8, float dd) ALWAYS_INLINE {
    const u32x4 kvv = *(LAS u32x4*)(smem + XR_OFF + im * XR_STRIDE + (2040 - t8) * 2);
    float y[8] = {s0[0], s0[1], s0[2], s0[3], s1[0], s1[1], s1[2], s1[3]};
    float o[8];
#pragma unroll
    for (int e = 0; e < 8; ++e) {
      const int ri = 7 - e;
      const float kvf = (ri & 1) ? bf_hi(kvv[ri >> 1]) : bf_lo(kvv[ri >> 1]);
      o[e] = bfr(y[e]) + bfr(kvf * dd);
    }
    u32x4 ov; ov.x = pk_bf16(o[0], o[1]); ov.y = pk_bf16(o[2], o[3]); ov.z = pk_bf16(o[4], o[5]); ov.w = pk_bf16(o[6], o[7]);
    gstore16_sc1(wT + (size_t)ch * S_ + t8, ov);
  };
  {
    const int im = tid >> 8, t8 = (tid & 255) * 8;
    const int po = ((t8 >> 8) * 64 + ((t8 >> 2) & 3) * 16 + ((t8 >> 4) & 15)) * 16;
    f32x4 s0 = (f32x4){0.f, 0.f, 0.f, 0.f}, s1 = s0;
#pragma unroll
    for (int ww = 0; ww < 4; ++ww) {
      s0 += *(LAS f32x4*)(smem + PART_OFF + (im * 4 + ww) * 8192 + po);
      s1 += *(LAS f32x4*)(smem + PART_OFF + (im * 4 + ww) * 8192 + po + 256);
    }
    finalize(s0, s1, im, chA + im, t8, ddA);
  }
  if (tid >= 448) {
    const int t2 = tid - 448;
    const int tl = t2 >> 5, l8 = (t2 & 31) * 8;
    const int jt = tl ? qd : 7 - qd, t8 = 256 * jt + l8;
    const int po = (((l8 >> 2) & 3) * 16 + (l8 >> 4)) * 16;
    f32x4 s0 = (f32x4){0.f, 0.f, 0.f, 0.f}, s1 = s0;
#pragma unroll
    for (int ww = 0; ww < 8; ++ww) {
      s0 += *(LAS f32x4*)(smem + PARTC_OFF + ww * 2048 + tl * 1024 + po);
      s1 += *(LAS f32x4*)(smem + PARTC_OFF + ww * 2048 + tl * 1024 + po + 256);
    }
    finalize(s0, s1, 2, chC, t8, ddC);
  }
  STAMP(1, 6);

  STAMP(2, 0);
  {
  const int kq = lane2 >> 4, qq = (lane2 & 15) >> 2, p4 = lane2 & 3;
  const unsigned rbU2 = lds0 + khf * K2_HALF + (16 * (kq >> 1) + 4 * (kq & 1) + qq) * 256 + ((4 * (kq & 1) + qq) << 5) + p4 * 8;

  const char* wtb = (const char*)(wT + mt * 128 + ksl * 32);
  f32x4 acc[2][8], accq[2];
  const f32x4 zero4 = {0.f, 0.f, 0.f, 0.f};

  const int tile = mt * 4 + nt;
  const __amdgpu_buffer_rsrc_t rs = __builtin_amdgcn_make_buffer_rsrc((char*)slab + (size_t)tile * (16 * 18432), 0, 16 * 18432, 0x00020000);
  const bool foreign = tq != ksl;
  auto give = [&](int xb, int i, const f32x4& a0, const f32x4& a1) ALWAYS_INLINE {
    *(LAS f32x4*)(smem + xb + ((w * 4 + i) * 64 + lane) * 16) = a1;
  };
  auto publish = [&](int xb, int i, const f32x4& a0, const f32x4& a1, int dtg) ALWAYS_INLINE {
    const f32x4 sm = a0 + *(LAS f32x4*)(smem + xb + (((w ^ 4) * 4 + i) * 64 + lane) * 16);
    if (foreign) __builtin_amdgcn_raw_buffer_store_b128(__builtin_bit_cast(u32x4, sm), rs, (unsigned)(((tq * 4 + ksl) * 18 + khf * 9 + dtg) * 1024 + lane * 16), 0, 16);
    else *(LAS f32x4*)(smem + K2_OWN_OFF + ((khf * 9 + dtg) * 64 + lane) * 16) = sm;
  };
  constexpr int XB1 = 3 * K2_SLOT, XB2 = 0, XB3 = 3 * K2_SLOT;

  asm volatile("s_waitcnt vmcnt(%4)" : "+v"(hf[0][0]), "+v"(hf[0][1]), "+v"(hf[1][0]), "+v"(hf[1][1])
               : "i"(k2_younger(k2_hkey(0, 3), 0, false)) : "memory");
  WAIT_L(0);
  __builtin_amdgcn_s_barrier();
  CFENCE();
  STAMP(2, 1);

  constexpr int PD = 2;
  bf16x8 af[PD + 1];
  unsigned sb = rbU2;
#pragma unroll
  for (int g = 0; g < PD; ++g) af[g] = tr_frag((LAS char*)(uintptr_t)(sb ^ (g << 5)), 0, 2048);
  sfor<162>([&](auto gc) ALWAYS_INLINE {
    constexpr int g = gc;
    constexpr int st = k2_stage(g), f = g - k2_g0(st);
    constexpr int ks = st < 9 ? k2_unit(st) * 2 + f / 8 : (st - 9) * 9 + f, dt = st < 9 ? f % 8 : 0;
    if constexpr (f == k2_mid(st)) {
      if constexpr (st < 8) { STAMP(0, 2 * st); }
      if constexpr (st == 2) { if (w == 0) poll_flags<1>(flg + FL_H + ((mt >> 1) * 4 + ksl) * 8, 8, tag, lane); }
      if constexpr (st == K2_WST) { if (w == 0) poll_flags<4>(flg + FL_W, 64, tag, lane); }
      if constexpr (st < 10) {
        constexpr int okey = cmax(k2_dkey(st + 1, k2_np(st + 1) - 1), st + 1 < 9 ? k2_hkey(st + 1, 3) : -1000);
        constexpr int n0 = k2_younger(okey, 4 * g, false);
        if constexpr (st == 0) { if (w == 7) wait_vm<n0 + 1>(); else wait_vm<n0>(); }
        else wait_vm<n0>();
        if constexpr (st + 1 < 9) {
          constexpr int k0 = 2 * k2_unit(st + 1);
          asm volatile("" : "+v"(hf[k0][0]), "+v"(hf[k0][1]), "+v"(hf[k0 + 1][0]), "+v"(hf[k0 + 1][1])::"memory");
        }
      }
      if constexpr (st >= 9) WAIT_L(0);
      __builtin_amdgcn_s_barrier();
      CFENCE();
      if constexpr (st < 8) { STAMP(0, 2 * st + 1); }
      if constexpr (st == 1) { if (tid == 0) set_flag(flg + FL_W + bid, tag); }
      if constexpr (st == K2_WST) {
        int l2 = lane; asm volatile("" : "+v"(l2));
        glds1_sc1(wtb, (unsigned)((colA2 + w * 16 + (l2 >> 2)) * (S_ * 2) + (l2 & 3) * 16), lds0 + K2_WT_OFF + w * 1024);
        glds1_sc1(wtb, (unsigned)((colQ2 + (l2 >> 2)) * (S_ * 2) + (l2 & 3) * 16), lds0 + K2_WT_OFF + 8192);
      }
      if constexpr (st == 9) {
#pragma unroll
        for (int i = 0; i < 4; ++i) publish(XB1, i, acc[0][i], acc[1][i], i);
#pragma unroll
        for (int i = 0; i < 4; ++i) give(XB2, i, acc[0][4 + i], acc[1][4 + i]);
      }
      if constexpr (st == 10) {
#pragma unroll
        for (int i = 0; i < 4; ++i) publish(XB2, i, acc[0][4 + i], acc[1][4 + i], 4 + i);
      }
    }
    if constexpr (st < 9 && ((f + 16 - 6) % 16) % 4 == 0) __builtin_amdgcn_s_setprio(3 - ((f + 16 - 6) % 16) / 4);
    sfor<36>([&](auto dc) ALWAYS_INLINE { if constexpr (dc % 4 < k2_np(dc / 4 + 2) && k2_dkey(dc / 4 + 2, dc % 4) == 4 * g + 2) piece_load2(dc / 4 + 2, dc % 4); });
    sfor<24>([&](auto hc) ALWAYS_INLINE { if constexpr (k2_hkey(hc / 4 + 3, hc % 4) == 4 * g + 3) h_load(std::integral_constant<int, hc + 12>{}); });
    if constexpr (g + PD < 162) {
      constexpr int g1 = g + PD, st1 = k2_stage(g1), f1 = g1 - k2_g0(st1);
      if constexpr (f1 == 0) {
        if constexpr (st1 < 9) {
          sb += (st1 % K2_NSLOT == 0) ? -3 * K2_SLOT : K2_SLOT;
        } else {
          int l2 = lane; asm volatile("" : "+v"(l2));
          const int kq2 = l2 >> 4;
          sb = lds0 + khf * K2_HALF + (16 * (kq2 >> 1) + 4 * (kq2 & 1) + ((l2 & 15) >> 2)) * 32 + (l2 & 3) * 8 + (st1 % K2_NSLOT) * K2_SLOT;
        }
      }
      asm volatile("" : "+v"(sb));
      if constexpr (st1 < 9) af[g1 % (PD + 1)] = tr_frag((LAS char*)(uintptr_t)(sb ^ ((f1 % 8) << 5)), (f1 / 8) * 8192, 2048);
      else af[g1 % (PD + 1)] = tr_frag((LAS char*)(uintptr_t)sb, f1 * 1024, 256);
    }
    SCHED0();
#pragma unroll
    for (int t = 0; t < 2; ++t) {
      if constexpr (st < 9) acc[t][dt] = __builtin_amdgcn_mfma_f32_16x16x32_bf16(af[g % (PD + 1)], hf[ks][t], g < 8 ? zero4 : acc[t][dt], 0, 0, 0);
      else accq[t] = __builtin_amdgcn_mfma_f32_16x16x32_bf16(af[g % (PD + 1)], hf[ks][t], g == 144 ? zero4 : accq[t], 0, 0, 0);
    }
    SCHED0();
    if constexpr (g == 143) {
#pragma unroll
      for (int i = 0; i < 4; ++i) give(XB1, i, acc[0][i], acc[1][i]);
      SCHED0();
    }
  });
  STAMP(2, 2);
  __builtin_amdgcn_s_setprio(0);
  give(XB3, 0, accq[0], accq[1]);

  int lane3 = lane; asm volatile("" : "+v"(lane3));
  u32x2 xq[3];
  float2 stt[2];
#pragma unroll
  for (int i = 0; i < 3; ++i) {
    const int t = i < 2 ? i : (w & 1);
    const int token = mt * 128 + ksl * 32 + t * 16 + (lane3 & 15);
    xq[i] = *(const u32x2*)(q + (size_t)token * D_ + (i < 2 ? colA2 + w * 16 : colQ2) + 4 * (lane3 >> 4));
    if (i < 2) {
      const u64 sv = __hip_atomic_load(stats + token, __ATOMIC_RELAXED, __HIP_MEMORY_SCOPE_AGENT);
      stt[i] = make_float2(__uint_as_float((unsigned)sv), __uint_as_float((unsigned)(sv >> 32)));
    }
  }
  WAIT_L(0);
  __builtin_amdgcn_s_barrier();
  CFENCE();
  publish(XB3, 0, accq[0], accq[1], 8);
  asm volatile("s_waitcnt vmcnt(0) lgkmcnt(0)" ::: "memory");
  __builtin_amdgcn_s_barrier();
  CFENCE();
  STAMP(2, 3);
  if (tid == 0) set_flag(flg + FL_S + tile * 4 + ksl, tag);
  poll_flags<1>(flg + FL_S + tile * 4, 4, tag, lane3);
  STAMP(2, 4);
  auto fetch = [&](f32x4 (&pv)[3], int fi) ALWAYS_INLINE {
#pragma unroll
    for (int k = 1; k < 4; ++k)
      pv[k - 1] = __builtin_bit_cast(f32x4, __builtin_amdgcn_raw_buffer_load_b128(rs, (unsigned)(((ksl * 4 + ((ksl + k) & 3)) * 18 + fi) * 1024 + lane3 * 16), 0, 16));
  };
  auto combine = [&](const f32x4 (&pv)[3], int t, int dt, const u32x2& xw2, const float2& st2) ALWAYS_INLINE {
    const int tl = t * 16 + (lane3 & 15), dl = dt * 16 + 4 * (lane3 >> 4);
    const f32x4 own = *(LAS f32x4*)(smem + K2_OWN_OFF + ((t * 9 + dt) * 64 + lane3) * 16);
    const f32x4 bs = ((own + pv[0]) + pv[1]) + pv[2];
    float o[4];
#pragma unroll
    for (int c = 0; c < 4; ++c) {
      const unsigned xw = (c < 2) ? xw2.x : xw2.y;
      const float x = (c & 1) ? bf_hi(xw) : bf_lo(xw);
      const float wy = bf2f(*(LAS u16*)(smem + K2_WT_OFF + (dl + c) * 64 + tl * 2));
      const float xn = bfr(bfr(x - st2.x) * st2.y);
      const float a = bfr(xn * wy);
      o[c] = bfr(x + a) + bfr(bs[c]);
    }
    u32x2 ov; ov.x = pk_bf16(o[0], o[1]); ov.y = pk_bf16(o[2], o[3]);
    *(u32x2*)(out + (size_t)(mt * 128 + ksl * 32 + tl) * D_ + (dt < 8 ? colA2 + dt * 16 : colQ2) + 4 * (lane3 >> 4)) = ov;
  };
  {
    f32x4 p0[3], p1[3], p2[3];
    fetch(p0, w); fetch(p1, 9 + w); fetch(p2, 9 * (w & 1) + 8);
    combine(p0, 0, w, xq[0], stt[0]);
    combine(p1, 1, w, xq[1], stt[1]);
    if (w < 2) combine(p2, w, 8, xq[2], w ? stt[1] : stt[0]);
  }
  if (bid == 0 && w == 0) {
    poll_flags<4>(flg + FL_K, 64, tag, lane);
    if (lane == 0) set_flag(flg + FL_GEN, tag);
  }
  STAMP(2, 5); RSTAMP(1, 15); STAMP_FLUSH(1); STAMP_FLUSH(2); STAMP_FLUSH(0);
  }
}

extern "C" void kernel_launch(void* const* d_in, const int* in_sizes, int n_in,
                              void* d_out, int out_size, void* d_ws, size_t ws_size,
                              hipStream_t stream) {
  const u16* q = (const u16*)d_in[0];
  const u16* k = (const u16*)d_in[1];
  const u16* v = (const u16*)d_in[2];
  const u16* shk = (const u16*)d_in[3];
  const u16* ssmk = (const u16*)d_in[4];
  const u16* ssmd = (const u16*)d_in[5];
  const u16* wg = (const u16*)d_in[7];
  const u16* wu = (const u16*)d_in[8];
  const u16* wd = (const u16*)d_in[9];
  (void)hipFuncSetAttribute((const void*)k_fused, hipFuncAttributeMaxDynamicSharedMemorySize, K1_LDS);
  k_fused<<<256, 512, K1_LDS, stream>>>(q, k, v, shk, ssmk, ssmd, wg, wu, wd, (char*)d_ws, (u16*)d_out);
}
```

```cpp
#include <hip/hip_runtime.h>
#include <hip/hip_bf16.h>
#include <stdint.h>
#include <utility>

typedef unsigned short u16;
typedef short bf16x8 __attribute__((ext_vector_type(8)));
typedef short s16x4 __attribute__((ext_vector_type(4)));
typedef float f32x4 __attribute__((ext_vector_type(4)));
typedef float f32x16 __attribute__((ext_vector_type(16)));
typedef unsigned u32x4 __attribute__((ext_vector_type(4)));
typedef unsigned u32x2 __attribute__((ext_vector_type(2)));
typedef __bf16 bf2_t __attribute__((ext_vector_type(2)));
#define LAS __attribute__((address_space(3)))
#define DI __device__ __forceinline__

constexpr int S_ = 2048, D_ = 576, F_ = 4608;

constexpr size_t WS_XFRAG = 0;
constexpr size_t WS_STATS = WS_XFRAG + (size_t)S_ * D_ * 2;
constexpr size_t WS_KVR   = WS_STATS + (size_t)S_ * 8;
constexpr size_t WS_WT    = WS_KVR + (size_t)S_ * D_ * 2;
constexpr size_t WS_H     = WS_WT + (size_t)S_ * D_ * 2;
constexpr size_t WS_SLAB  = WS_H + (size_t)S_ * F_ * 2;
constexpr size_t WS_FLG   = WS_SLAB + (size_t)4 * S_ * D_ * 4;
constexpr int FL_GEN = 0, FL_X = 32, FL_K = FL_X + 256, FL_H = FL_K + 256, FL_HE = FL_H + 256, FL_W = FL_HE + 256, FL_S = FL_W + 256, FL_END = FL_S + 256;
constexpr size_t WS_END   = WS_FLG + FL_END * 8;
typedef unsigned long long u64;

DI float bf_lo(unsigned w) { return __uint_as_float(w << 16); }
DI float bf_hi(unsigned w) { return __uint_as_float(w & 0xffff0000u); }
DI float bf2f(u16 b) { return __uint_as_float(((unsigned)b) << 16); }
DI float bfr(float x) { return (float)(__bf16)x; }
typedef float f32x2 __attribute__((ext_vector_type(2)));
DI unsigned pk_bf16(float lo, float hi);
DI f32x2 unpk(unsigned w) { return (f32x2){__uint_as_float(w << 16), __uint_as_float(w & 0xffff0000u)}; }
DI f32x2 bfr2(f32x2 v) { return unpk(pk_bf16(v.x, v.y)); }
DI unsigned pk_bf16(float lo, float hi) { bf2_t v = {(__bf16)lo, (__bf16)hi}; return __builtin_bit_cast(unsigned, v); }

DI float wave_sum(float x) {
  x += __builtin_bit_cast(float, __builtin_amdgcn_update_dpp(0, __builtin_bit_cast(int, x), 0xB1, 0xf, 0xf, true));
  x += __builtin_bit_cast(float, __builtin_amdgcn_update_dpp(0, __builtin_bit_cast(int, x), 0x4E, 0xf, 0xf, true));
  x += __builtin_bit_cast(float, __builtin_amdgcn_update_dpp(0, __builtin_bit_cast(int, x), 0x141, 0xf, 0xf, true));
  x += __builtin_bit_cast(float, __builtin_amdgcn_update_dpp(0, __builtin_bit_cast(int, x), 0x140, 0xf, 0xf, true));
  const int xi = __builtin_bit_cast(int, x);
  return (__builtin_bit_cast(float, __builtin_amdgcn_readlane(xi, 0)) + __builtin_bit_cast(float, __builtin_amdgcn_readlane(xi, 16))) +
         (__builtin_bit_cast(float, __builtin_amdgcn_readlane(xi, 32)) + __builtin_bit_cast(float, __builtin_amdgcn_readlane(xi, 48)));
}
#define WAIT_V(n) asm volatile("s_waitcnt vmcnt(" #n ")" ::: "memory")
#define WAIT_L(n) asm volatile("s_waitcnt lgkmcnt(" #n ")" ::: "memory")
#define CFENCE() asm volatile("" ::: "memory")
#define STAMP(kid, i)
#define RSTAMP(kid, i)
#define STAMP_DECL
#define STAMP_FLUSH(kid)
#define K0STAMP(i)
#define SCHED0() __builtin_amdgcn_sched_barrier(0)
template <int... I, class Fn> DI void sfor_impl(std::integer_sequence<int, I...>, Fn&& f) { (f(std::integral_constant<int, I>{}), ...); }
template <int N, class Fn> DI void sfor(Fn&& f) { sfor_impl(std::make_integer_sequence<int, N>{}, f); }
#define ALWAYS_INLINE __attribute__((always_inline))
template <int N> DI void wait_vm() { asm volatile("s_waitcnt vmcnt(%0)" ::"i"(N) : "memory"); }

DI void glds4(const void* sbase, unsigned v0, unsigned v1, unsigned v2, unsigned v3, unsigned lds_dst) {
  unsigned keep;
  asm volatile("s_mov_b32 %0, m0\n\ts_mov_b32 m0, %6\n\ts_nop 0\n\t"
               "global_load_lds_dwordx4 %1, %5\n\t"
               "global_load_lds_dwordx4 %2, %5 offset:1024\n\t"
               "global_load_lds_dwordx4 %3, %5 offset:2048\n\t"
               "global_load_lds_dwordx4 %4, %5 offset:3072\n\t"
               "s_mov_b32 m0, %0"
               : "=&s"(keep) : "v"(v0), "v"(v1), "v"(v2), "v"(v3), "s"(sbase), "s"(lds_dst) : "memory");
}
DI void glds3(const void* sbase, unsigned v0, unsigned v1, unsigned v2, unsigned lds_dst) {
  unsigned keep;
  asm volatile("s_mov_b32 %0, m0\n\ts_mov_b32 m0, %5\n\ts_nop 0\n\t"
               "global_load_lds_dwordx4 %1, %4\n\t"
               "global_load_lds_dwordx4 %2, %4 offset:1024\n\t"
               "global_load_lds_dwordx4 %3, %4 offset:2048\n\t"
               "s_mov_b32 m0, %0"
               : "=&s"(keep) : "v"(v0), "v"(v1), "v"(v2), "s"(sbase), "s"(lds_dst) : "memory");
}
DI void glds2(const void* sbase, unsigned v0, unsigned v1, unsigned lds_dst) {
  unsigned keep;
  asm volatile("s_mov_b32 %0, m0\n\ts_mov_b32 m0, %4\n\ts_nop 0\n\t"
               "global_load_lds_dwordx4 %1, %3\n\t"
               "global_load_lds_dwordx4 %2, %3 offset:1024\n\t"
               "s_mov_b32 m0, %0"
               : "=&s"(keep) : "v"(v0), "v"(v1), "s"(sbase), "s"(lds_dst) : "memory");
}
DI void glds1(const void* sbase, unsigned v0, unsigned lds_dst) {
  unsigned keep;
  asm volatile("s_mov_b32 %0, m0\n\ts_mov_b32 m0, %3\n\ts_nop 0\n\t"
               "global_load_lds_dwordx4 %1, %2\n\t"
               "s_mov_b32 m0, %0"
               : "=&s"(keep) : "v"(v0), "s"(sbase), "s"(lds_dst) : "memory");
}

template <bool NT> DI void glds1p(const void* sbase, unsigned v0, unsigned lds_dst) {
  unsigned keep;
  if constexpr (NT)
    asm volatile("s_mov_b32 %0, m0\n\ts_mov_b32 m0, %3\n\ts_nop 0\n\tglobal_load_lds_dwordx4 %1, %2 nt\n\ts_mov_b32 m0, %0"
                 : "=&s"(keep) : "v"(v0), "s"(sbase), "s"(lds_dst) : "memory");
  else
    asm volatile("s_mov_b32 %0, m0\n\ts_mov_b32 m0, %3\n\ts_nop 0\n\tglobal_load_lds_dwordx4 %1, %2\n\ts_mov_b32 m0, %0"
                 : "=&s"(keep) : "v"(v0), "s"(sbase), "s"(lds_dst) : "memory");
}
DI void glds1_sc1(const void* sbase, unsigned v0, unsigned lds_dst) {
  unsigned keep;
  asm volatile("s_mov_b32 %0, m0\n\ts_mov_b32 m0, %3\n\ts_nop 0\n\tglobal_load_lds_dwordx4 %1, %2 sc1\n\ts_mov_b32 m0, %0"
               : "=&s"(keep) : "v"(v0), "s"(sbase), "s"(lds_dst) : "memory");
}
DI void touch_lines(const void* sbase, unsigned v0, unsigned lds_dst) {
  unsigned keep;
  asm volatile("s_mov_b32 %0, m0\n\ts_mov_b32 m0, %3\n\ts_nop 0\n\tglobal_load_lds_dword %1, %2\n\ts_mov_b32 m0, %0"
               : "=&s"(keep) : "v"(v0), "s"(sbase), "s"(lds_dst) : "memory");
}
template <int OFF> DI void gload16(bf16x8& dst, const void* sbase, unsigned voff) {
  asm volatile("global_load_dwordx4 %0, %1, %2 offset:%3" : "=v"(dst) : "v"(voff), "s"(sbase), "i"(OFF) : "memory");
}
template <int OFF> DI void gload16_sc1(bf16x8& dst, const void* sbase, unsigned voff) {
  asm volatile("global_load_dwordx4 %0, %1, %2 offset:%3 sc1" : "=v"(dst) : "v"(voff), "s"(sbase), "i"(OFF) : "memory");
}
DI void gload16u(u32x4& dst, const void* sbase, unsigned voff) {
  asm volatile("global_load_dwordx4 %0, %1, %2" : "=v"(dst) : "v"(voff), "s"(sbase) : "memory");
}
template <int OFF> DI void gstore16_sc1_s(const void* sbase, unsigned voff, const u32x4& v) {
  asm volatile("global_store_dwordx4 %0, %1, %2 offset:%3 sc1\n\ts_nop 1" ::"v"(voff), "v"(v), "s"(sbase), "i"(OFF) : "memory");
}
DI void gload4u(unsigned& dst, const void* sbase, unsigned voff) {
  asm volatile("global_load_dword %0, %1, %2" : "=v"(dst) : "v"(voff), "s"(sbase) : "memory");
}
DI void gstore16_sc1(void* p, const u32x4& v) { asm volatile("global_store_dwordx4 %0, %1, off sc1\n\ts_nop 1" ::"v"(p), "v"(v) : "memory"); }
DI void set_flag(u64* f, u64 tag) { __hip_atomic_store(f, tag, __ATOMIC_RELAXED, __HIP_MEMORY_SCOPE_AGENT); }
template <int PER> DI void poll_flags(u64* f, int n, u64 tag, int lane) {
  asm volatile("" : "+v"(lane));
  const int i = lane < n ? lane : 0;
  for (unsigned spins = 0; spins < (1u << 20); ++spins) {
    bool ok = true;
#pragma unroll
    for (int k = 0; k < PER; ++k) ok &= __hip_atomic_load(f + i + 64 * k, __ATOMIC_RELAXED, __HIP_MEMORY_SCOPE_AGENT) == tag;
    if (__all(ok)) break;
    __builtin_amdgcn_s_sleep(1);
  }
}

constexpr int K1_SLOT = 37120;
constexpr int K1_UOFF = 18432 + 128;
constexpr int K1_NSLOT = 4;
constexpr int K1_PF_OFF = K1_NSLOT * K1_SLOT;
constexpr int XR_OFF = K1_PF_OFF + 288, XR_STRIDE = 4608;
constexpr int HZ_OFF = XR_OFF + 3 * XR_STRIDE;
constexpr int K1_LDS = HZ_OFF + 288;
constexpr int PART_OFF = 65536;
constexpr int PARTC_OFF = PART_OFF + 65536;
constexpr int HCNT_OFF = K1_PF_OFF;
constexpr int XG_OFF = 3 * K1_SLOT;

DI bf16x8 tr_frag(LAS char* p, int off, int hi_off) {
  s16x4 lo = __builtin_amdgcn_ds_read_tr16_b64_v4i16((LAS s16x4*)(p + off));
  s16x4 hi = __builtin_amdgcn_ds_read_tr16_b64_v4i16((LAS s16x4*)(p + off + hi_off));
  return __builtin_shufflevector(lo, hi, 0, 1, 2, 3, 4, 5, 6, 7);
}

constexpr int K1_MID = 16;
constexpr int k1_xkey(int i) { return i < 9 ? -100 + i : 4 * (1 + 2 * (i - 9)) + 2; }
constexpr int k1_dstep(int d, int n) { return d == 2 ? 2 + 4 * n : d == 3 ? 24 + 4 * n : d == 4 ? 58 + 6 * n : d == 5 ? 94 + 6 * n : 36 * (d - 3) + K1_MID + 6 * n; }
constexpr int k1_dkey(int d, int n) { return d < 2 ? -400 + 10 * d + n : 4 * k1_dstep(d, n) + 1; }
constexpr int k1_cstep(int i) { return i < 7 ? 218 + 4 * i : (i < 14 ? 254 + 4 * (i - 7) : (i < 21 ? 289 + (i - 14) : 304 + 2 * (i - 21))); }
constexpr int k1_sstep(int c) { return c == 0 ? 143 : (c < 4 ? 144 + 6 * c : (c == 4 ? 182 : 302 + 9 * (c - 5))); }
constexpr int k1_astep(int i) { return i < 14 ? k1_cstep(i) + 28 : (i < 21 ? k1_cstep(i) + 20 : 321 + (i - 21)); }
constexpr int K1_TOUCH = 5 * 36 + K1_MID + 1;
constexpr int k1_younger(int okey, int wkey) {
  int n = 0;
  if (4 * K1_TOUCH + 1 > okey && 4 * K1_TOUCH + 1 < wkey) n += 1;
  if (-70 > okey && -70 < wkey) n += 2;
  for (int i = 0; i < 36; ++i) if (k1_xkey(i) > okey && k1_xkey(i) < wkey) n += 1;
  for (int d = 0; d < 9; ++d) for (int pc = 0; pc < 5; ++pc) if (k1_dkey(d, pc) > okey && k1_dkey(d, pc) < wkey) n += 1;
  for (int i = 0; i < 24; ++i) if (4 * k1_cstep(i) + 2 > okey && 4 * k1_cstep(i) + 2 < wkey) n += 2;
  for (int c = 0; c < 8; ++c) if (4 * k1_sstep(c) + 3 > okey && 4 * k1_sstep(c) + 3 < wkey) n += 1;
  return n;
}
#define HSTORE 1

constexpr int K2_HALF = 16384;
constexpr int K2_SLOT = 2 * K2_HALF;
constexpr int K2_NSLOT = 4;
constexpr int K2_OWN_OFF = K2_NSLOT * K2_SLOT;
constexpr int K2_WT_OFF = K2_OWN_OFF + 18432;
constexpr int k2_g0(int st) { return st <= 9 ? 16 * st : 153; }
constexpr int k2_stage(int g) { return g < 144 ? g / 16 : (g < 153 ? 9 : 10); }
constexpr int k2_mid(int st) { return st < 9 ? 6 : 4; }
constexpr int k2_unit(int st) { return st < 5 ? 2 * st : 2 * (st - 5) + 1; }
constexpr int k2_np(int d) { return d < 9 ? 4 : 3; }
constexpr int k2_dkey(int d, int n) { return d < 2 ? -300 + 10 * d + n : (d == 2 ? 4 * (1 + n) + 2 : 4 * (k2_g0(d - 3) + k2_mid(d - 3) + 4 * n) + 2); }
constexpr int k2_hkey(int j, int m) { return j < 3 ? -250 + 10 * j + m : 4 * (k2_g0(j - 3) + k2_mid(j - 3) + 2 + 4 * m) + 3; }
constexpr int k2_skey(int p) { return 4 * (k2_g0(9 + p) + k2_mid(9 + p)) + 1; }
constexpr int K2_WST = 7;
constexpr int k2_wkey() { return 4 * (k2_g0(K2_WST) + k2_mid(K2_WST)) + 1; }
constexpr int k2_younger(int okey, int wkey, bool stores) {
  int n = 0;
  if (k2_wkey() > okey && k2_wkey() < wkey) n += 2;
  if (-100 > okey && -100 < wkey) n += 1;
  for (int d = 0; d < 11; ++d) for (int pc = 0; pc < k2_np(d); ++pc) if (k2_dkey(d, pc) > okey && k2_dkey(d, pc) < wkey) n += 1;
  for (int j = 0; j < 9; ++j) for (int m = 0; m < 4; ++m) if (k2_hkey(j, m) > okey && k2_hkey(j, m) < wkey) n += 1;
  if (stores) for (int p = 0; p < 2; ++p) if (k2_skey(p) > okey && k2_skey(p) < wkey) n += 4;
  return n;
}
constexpr int cmax(int a, int b) { return a > b ? a : b; }

__global__ __launch_bounds__(512, 2) void k_fused(const u16* __restrict__ q, const u16* __restrict__ kk, const u16* __restrict__ vv,
                                                  const u16* __restrict__ shk, const u16* __restrict__ ssmk, const u16* __restrict__ ssmd,
                                                  const u16* __restrict__ wg, const u16* __restrict__ wu, const u16* __restrict__ wd,
                                                  char* ws, u16* __restrict__ out) {
  u16* xfrag = (u16*)(ws + WS_XFRAG);
  u64* stats = (u64*)(ws + WS_STATS);
  u16* kvr = (u16*)(ws + WS_KVR);
  u16* wT = (u16*)(ws + WS_WT);
  u16* hout = (u16*)(ws + WS_H);
  float* slab = (float*)(ws + WS_SLAB);
  u64* flg = (u64*)(ws + WS_FLG);
  extern __shared__ __attribute__((aligned(16))) char smem_g[];
  LAS char* smem = (LAS char*)smem_g;
  const unsigned lds0 = (unsigned)(uintptr_t)smem;
  const int bid = blockIdx.x, tid = threadIdx.x;
  const int w = __builtin_amdgcn_readfirstlane(tid >> 6), lane = tid & 63;
  const int xcd = bid & 7, idx = bid >> 3;
  STAMP_DECL; RSTAMP(1, 14); STAMP(1, 0);
  const u64 genv = __hip_atomic_load(flg + FL_GEN, __ATOMIC_RELAXED, __HIP_MEMORY_SCOPE_AGENT);
  u64 tag;
  auto make_tag = [&]() ALWAYS_INLINE {
    tag = (((u64)(unsigned)__builtin_amdgcn_readfirstlane((unsigned)(genv >> 32)) << 32) | (unsigned)__builtin_amdgcn_readfirstlane((unsigned)genv)) + 1ull;
  };
  const int ksl = xcd >> 1, mt = (xcd & 1) * 8 + (idx >> 2), nt = idx & 3;
  const int tq = w & 3, khf = w >> 2;
  const int kbase = ksl * 1152 + khf * 576;
  const int colA2 = 128 * nt, colQ2 = 512 + 16 * nt;
  const int mtile = (xcd & 1) * 4 + (idx & 3);
  const int cgrp = (xcd >> 1) * 2 + (idx >> 4), cq = (idx >> 2) & 3;
  const int colA = 64 * (9 * cgrp + 2 * cq), colQ = 64 * (9 * cgrp + 8) + 16 * cq;

  const int grp = bid >> 2, qd = bid & 3;
  const int chA = grp * 9 + 2 * qd, chC = grp * 9 + 8;
  float ddA, ddC;
  {
    const int c1 = chA + (w >> 2);
    const unsigned d1 = ((const unsigned*)ssmd)[c1 >> 1], d2 = ((const unsigned*)ssmd)[chC >> 1];
    ddA = (c1 & 1) ? bf_hi(d1) : bf_lo(d1);
    ddC = (chC & 1) ? bf_hi(d2) : bf_lo(d2);
  }

  const int wq = w & 3, part = w >> 2;
  const char* wbase = (const char*)(part ? wu : wg);
  unsigned voff[5];
#pragma unroll
  for (int n = 0; n < 5; ++n) {
    const int o = (n < 4) ? ((wq * 4 + n) * 1024 + lane * 16) : (16384 + wq * 512 + (lane & 31) * 16);
    const int k = o >> 7, pc = (o >> 4) & 7;
    const int j = (pc >> 1) ^ (((k >> 1) & 1) | (part << 1));
    voff[n] = (unsigned)(k * (F_ * 2) + (2 * j + (pc & 1)) * 16);
  }
  const unsigned ldst = lds0 + part * K1_UOFF + wq * 4096;
  const unsigned ldst4 = lds0 + part * K1_UOFF + 16384 + wq * 512;

  auto piece_load = [&](int st, int n) ALWAYS_INLINE {
    const int slot = st % K1_NSLOT;
    const unsigned dst = (n < 4 ? ldst + n * 1024 : ldst4) + slot * K1_SLOT;
    if (st != 4) {
      const int ks = st < 4 ? st : st - 5;
      const char* g = wbase + (size_t)ks * 144 * (F_ * 2) + (colA + (st > 4 ? 64 : 0)) * 2;
      if (n < 4 || lane < 32) glds1p<false>(g, voff[n], dst);
    } else {
      int l2 = lane; asm volatile("" : "+v"(l2));
      const int o = (n < 4) ? ((wq * 4 + n) * 1024 + l2 * 16) : (16384 + wq * 512 + (l2 & 31) * 16);
      const unsigned vq = (unsigned)((o >> 5) * (F_ * 2) + ((o >> 4) & 1) * 16);
      if (n < 4 || lane < 32) glds1p<false>(wbase + colQ * 2, vq, dst);
    }
  };

  bf16x8 xf[36];
  const char* xb = (const char*)xfrag + (size_t)(mtile * 8 + w) * 36 * 1024;
  const unsigned xv = lane * 16;
  auto x_load = [&](auto ic) ALWAYS_INLINE {
    constexpr int i = ic;
    gload16_sc1<(i & 3) * 1024>(xf[i], xb + (i >> 2) * 4096, xv);
  };
  {
    LAS u16* tile = (LAS u16*)(smem + XR_OFF);
    constexpr int KT_OFF = 2 * K1_SLOT, VT_OFF = KT_OFF + 9728, WT_OFF = VT_OFF + 9216;
    const int tt = bid >> 3, ct = bid & 7, t0 = tt * 64, c0 = ct * 72;
    const int tok0 = mtile * 256 + (cgrp * 4 + cq) * 8, token = tok0 + w;
    const bool act = lane < 48;
    u32x2 d[3];
    u32x4 kin, vin, ein;
#pragma unroll
    for (int i = 0; i < 3; ++i)
      asm volatile("global_load_dwordx2 %0, %1, %2 offset:%3" : "=v"(d[i]) : "v"((unsigned)(token * (D_ * 2) + (act ? lane : 0) * 8)), "s"(q), "i"(i * 384) : "memory");
    auto k_off = [&](int pc) ALWAYS_INLINE -> unsigned { const int r = pc / 9, cg = pc - r * 9, ts = t0 - 3 + r; return (unsigned)((ts < 0 ? 0 : ts) * (D_ * 2) + (c0 + 8 * cg) * 2); };
    auto v_off = [&](int pc) ALWAYS_INLINE -> unsigned { const int r = pc / 9, cg = pc - r * 9; return (unsigned)((t0 + r) * (D_ * 2) + (c0 + 8 * cg) * 2); };
    gload16u(kin, kk, k_off(tid));
    gload16u(vin, vv, v_off(tid));
    int edst;
    {
      const int kp = w == 0 ? 512 + lane : 576 + (lane < 27 ? lane : 0);
      const int wl = lane < 63 ? lane - 27 : 35;
      const bool isw = w == 1 && lane >= 27, isv = w >= 2;
      const char* src = isv ? (const char*)vv + v_off(512 + lane) : (isw ? (const char*)shk + c0 * 8 + wl * 16 : (const char*)kk + k_off(kp));
      edst = isv ? VT_OFF + (512 + lane) * 16 : (isw ? WT_OFF + wl * 16 : KT_OFF + kp * 16);
      asm volatile("global_load_dwordx4 %0, %1, off" : "=v"(ein) : "v"(src) : "memory");
    }
    {
      const int li = (w == 3 || w == 4) ? (w & 1) * 64 + lane : 0;
      const int ch = li < 32 ? chA : (li < 64 ? chA + 1 : chC);
      touch_lines(ssmk, (unsigned)(ch * (S_ * 2) + (li & 31) * 128), lds0 + K1_PF_OFF + 16);
    }
    asm volatile("s_waitcnt vmcnt(4)" : "+v"(d[0]), "+v"(d[1]), "+v"(d[2])::"memory");
    STAMP(1, 9);
    f32x2 xa[3], xb2[3];
#pragma unroll
    for (int i = 0; i < 3; ++i) { xa[i] = unpk(d[i].x); xb2[i] = unpk(d[i].y); }
    const f32x2 s2 = (xa[0] + xb2[0]) + (xa[1] + xb2[1]) + (xa[2] + xb2[2]);
    const float mean = wave_sum(act ? s2.x + s2.y : 0.f) * (1.0f / 576.0f);
    f32x2 v2 = {0.f, 0.f};
#pragma unroll
    for (int i = 0; i < 3; ++i) { const f32x2 a = xa[i] - mean, b = xb2[i] - mean; v2 += a * a + b * b; }
    const float vs = wave_sum(act ? v2.x + v2.y : 0.f);
    const float mb = bfr(mean), vb = bfr(vs * (1.0f / 576.0f));
    const float sd = bfr(sqrtf(bfr(vb + 1e-5f)));
    const float rsd = 1.0f / sd;
#pragma unroll
    for (int i = 0; i < 3; ++i) {
      const f32x2 a = bfr2(xa[i] - mb) * rsd, b = bfr2(xb2[i] - mb) * rsd;
      u32x2 o; o.x = pk_bf16(a.x, a.y); o.y = pk_bf16(b.x, b.y);
      const int p8 = lane + 48 * i;
      if (act) *(LAS u32x2*)(smem + XG_OFF + ((p8 >> 1) * 8 + w) * 16 + (p8 & 1) * 8) = o;
    }
    if (tid == 0) *(LAS unsigned*)(smem + HCNT_OFF + 4) = 0u;
    asm volatile("s_waitcnt vmcnt(0)" : "+v"(kin), "+v"(vin), "+v"(ein)::"memory");
    make_tag();
    if (t0 == 0 && tid < 27) kin = (u32x4){0u, 0u, 0u, 0u};
    *(LAS u32x4*)(smem + KT_OFF + tid * 16) = kin;
    *(LAS u32x4*)(smem + VT_OFF + tid * 16) = vin;
    if (w < 3) *(LAS u32x4*)(smem + edst) = ein;
    WAIT_L(0);
    __builtin_amdgcn_s_barrier();
    CFENCE();
    {
      char* xo = (char*)xfrag + (size_t)((tok0 >> 5) * 36 * 64 + (tok0 & 31)) * 16;
#pragma unroll
      for (int it = 0; it < 2; ++it) {
        const int id = it ? 512 + lane : tid, p16 = id >> 3;
        if (it == 0 || w == 0) {
          const u32x4 v = *(LAS u32x4*)(smem + XG_OFF + id * 16);
          gstore16_sc1(xo + ((p16 >> 1) * 64 + (p16 & 1) * 32 + (id & 7)) * 16, v);
        }
      }
      if (lane == 0) __hip_atomic_store(stats + token, (u64)__float_as_uint(mb) | ((u64)__float_as_uint(rsd) << 32), __ATOMIC_RELAXED, __HIP_MEMORY_SCOPE_AGENT);
    }
    STAMP(1, 10);
    CFENCE();
#pragma unroll
    for (int n = 0; n < 5; ++n) piece_load(0, n);
#pragma unroll
    for (int n = 0; n < 5; ++n) piece_load(1, n);
    wait_vm<10>();
    STAMP(1, 11);
    if (lane == 0) {
      const unsigned old = __hip_atomic_fetch_add((LAS unsigned*)(smem + HCNT_OFF + 4), 1u, __ATOMIC_RELAXED, __HIP_MEMORY_SCOPE_WORKGROUP);
      if (old == 7u) set_flag(flg + FL_X + (tok0 >> 3), tag);
    }
    auto kv_compute = [&](int item) ALWAYS_INLINE {
      const int tl = item / 9, cg = item - tl * 9;
      u32x4 kr[4], w4[4];
#pragma unroll
      for (int i = 0; i < 4; ++i) { kr[i] = *(LAS u32x4*)(smem + KT_OFF + (item + 9 * i) * 16); w4[i] = *(LAS u32x4*)(smem + WT_OFF + cg * 64 + i * 16); }
      const u32x4 vr = *(LAS u32x4*)(smem + VT_OFF + item * 16);
#pragma unroll
      for (int e2 = 0; e2 < 4; ++e2) {
        const f32x2 w0 = {bf_lo(w4[e2].x), bf_lo(w4[e2].z)}, w1 = {bf_hi(w4[e2].x), bf_hi(w4[e2].z)};
        const f32x2 w2 = {bf_lo(w4[e2].y), bf_lo(w4[e2].w)}, w3 = {bf_hi(w4[e2].y), bf_hi(w4[e2].w)};
        f32x2 ks = bfr2(unpk(kr[0][e2]) * w3);
        ks = bfr2(ks + bfr2(unpk(kr[1][e2]) * w2));
        ks = bfr2(ks + bfr2(unpk(kr[2][e2]) * w1));
        ks = bfr2(ks + bfr2(unpk(kr[3][e2]) * w0));
        const f32x2 kv = ks * unpk(vr[e2]);
        const unsigned pk = pk_bf16(kv.x, kv.y);
        tile[(8 * cg + 2 * e2) * 72 + (63 - tl)] = (u16)(pk & 0xffffu);
        tile[(8 * cg + 2 * e2 + 1) * 72 + (63 - tl)] = (u16)(pk >> 16);
      }
    };
    kv_compute(tid);
    STAMP(1, 7);
    poll_flags<1>(flg + FL_X + mtile * 32 + w * 4, 4, tag, lane);
    wait_vm<0>();
    STAMP(1, 8);
    sfor<9>([&](auto ic) ALWAYS_INLINE { x_load(ic); });
    if (w == 7) kv_compute(512 + lane);
    WAIT_L(0);
    __builtin_amdgcn_s_barrier();
    CFENCE();
    {
      const int ch = tid >> 3, pp = tid & 7, ch2 = 64 + (lane >> 3);
      const u32x4 o1 = *(LAS u32x4*)(tile + ch * 72 + pp * 8), o2 = *(LAS u32x4*)(tile + ch2 * 72 + pp * 8);
      gstore16_sc1(kvr + (size_t)(c0 + ch) * S_ + (1984 - t0) + pp * 8, o1);
      gstore16_sc1(kvr + (size_t)(c0 + ch2) * S_ + (1984 - t0) + pp * 8, o2);
    }
  }

  const int kh = lane >> 5, prt = (lane >> 4) & 1, qq = (lane & 15) >> 2, p4 = lane & 3;
  const unsigned rbU = lds0 + (8 * kh + qq) * 128 + prt * K1_UOFF + ((((qq >> 1) & 1) | (prt << 1)) << 5) + p4 * 8;

  f32x16 acc[4];
  const f32x16 zero16 = {0.f, 0.f, 0.f, 0.f, 0.f, 0.f, 0.f, 0.f, 0.f, 0.f, 0.f, 0.f, 0.f, 0.f, 0.f, 0.f};

  const int token = mtile * 256 + w * 32 + (lane & 31);
  char* hb = (char*)hout + ((size_t)(token >> 4) * 144 * 64 + (lane >> 5) * 16 + (token & 15)) * 16;
  auto h_word = [&](const f32x16& a, int i) ALWAYS_INLINE -> unsigned {
    return pk_bf16(bfr(a[2 * i]) * bfr(a[2 * i + 8]), bfr(a[2 * i + 1]) * bfr(a[2 * i + 9]));
  };
  auto h_store = [&](const u32x4& o, int c16) ALWAYS_INLINE {
    gstore16_sc1(hb + ((size_t)(c16 >> 1) * 64 + (c16 & 1) * 32) * 16, o);
  };
  u32x4 ho;

  const char* hpA = (const char*)(ssmk + (size_t)(chA + (w >> 2)) * S_ + 64 * (w & 3));
  const char* hpC = (const char*)(ssmk + (size_t)chC * S_ + 32 * w);
  const bool zA = (w & 3) == 0, zC = w == 0;
  const char* pf_sk = (const char*)ssmk;
  const char* pf_wd = (const char*)wd + (size_t)(xcd >> 1) * 1152 * 1152;
  const char* kvb = (const char*)kvr;
  u32x4 caf[24];
  unsigned cax[24];
  bf16x8 cf[24];
  int cvo = 0, cvx = 0;
  auto conv_align = [&](auto ic) ALWAYS_INLINE {
    constexpr int i = ic;
    const unsigned sh = ((lane + 1) & 1) * 2;
    u32x4 r;
    r.x = __builtin_amdgcn_alignbyte(caf[i].y, caf[i].x, sh);
    r.y = __builtin_amdgcn_alignbyte(caf[i].z, caf[i].y, sh);
    r.z = __builtin_amdgcn_alignbyte(caf[i].w, caf[i].z, sh);
    r.w = __builtin_amdgcn_alignbyte(cax[i], caf[i].w, sh);
    cf[i] = __builtin_bit_cast(bf16x8, r);
    asm volatile("" : "+v"(cf[i]));
  };

  STAMP(1, 1);

  constexpr int PD = 3, MID = K1_MID;
  bf16x8 af[PD + 1];
  unsigned rb[4];
#pragma unroll
  for (int j = 0; j < 4; ++j) rb[j] = rbU ^ (j << 5);
#pragma unroll
  for (int g = 0; g < PD; ++g) af[g] = tr_frag((LAS char*)(uintptr_t)rb[g], 0, 512);
  sfor<324>([&](auto gc) ALWAYS_INLINE {
    constexpr int g = gc;
    constexpr int st = g / 36, f = g - st * 36;
    constexpr int s = st == 4 ? f : (st == 8 ? f % 9 : f / 4), c = st == 4 ? 0 : (st == 8 ? f / 9 : f % 4);
    constexpr int xi = st < 4 ? st * 9 + s : (st == 4 ? s : (st - 5) * 9 + s);
    constexpr int ai = st < 4 ? c : (st == 4 ? 0 : (c + 1) & 3);
    constexpr bool first = (st == 0 || st == 5) ? s == 0 : (st == 4 && f == 0);
    if constexpr (f == MID) {

      if constexpr (st < 8) wait_vm<k1_younger(k1_dkey(st + 1, 4), 4 * g)>();
      else { wait_vm<k1_younger(4 * (7 * 36 + MID + 1) + 1, 4 * g)>(); if (w <= 3) WAIT_L(0); }
      __builtin_amdgcn_s_barrier();
      CFENCE();

      if constexpr (st == 1) { if (tid == 0) set_flag(flg + FL_K + (bid & 7) * 32 + (bid >> 3), tag); }
      if constexpr (st == 6) {
        static_assert(k1_dkey(7, 4) > 4 * k1_sstep(4) + 3, "the stage 7 wait must cover the h stores of panels A and Q");
        if (tid == 0) set_flag(flg + FL_HE + (mtile * 4 + (cgrp >> 1)) * 8 + (cgrp & 1) * 4 + cq, tag);
      }
    }
    if constexpr (((f + 36 - MID) % 36) % 9 == 0) __builtin_amdgcn_s_setprio(3 - ((f + 36 - MID) % 36) / 9);
    sfor<35>([&](auto dc) ALWAYS_INLINE { if constexpr (k1_dkey(dc / 5 + 2, dc % 5) == 4 * g + 1) piece_load(dc / 5 + 2, dc % 5); });
    sfor<27>([&](auto ic) ALWAYS_INLINE { if constexpr (k1_xkey(ic + 9) == 4 * g + 2) x_load(std::integral_constant<int, ic + 9>{}); });
    if constexpr (g == 7 * 36 + MID + 1) {
      if (w < 3) {
        const int ch = (w == 2) ? chC : chA + w;
        const unsigned lv = lane * 16;
        poll_flags<1>(flg + FL_K + (grp >> 3) * 32, 32, tag, lane);
#pragma unroll
        for (int i = 0; i < 4; ++i) glds1_sc1(kvb + (size_t)ch * (S_ * 2) + i * 1024, lv, lds0 + XR_OFF + w * XR_STRIDE + i * 1024);
        if (lane < 4) glds1(pf_sk + (size_t)ch * (S_ * 2), lv, lds0 + HZ_OFF + w * 96 + 32);
      } else if (w == 3) {
        if (lane < 32) {
#pragma unroll
          for (int im = 0; im < 3; ++im) *(LAS u32x4*)(smem + XR_OFF + im * XR_STRIDE + 4096 + lane * 16) = (u32x4){0u, 0u, 0u, 0u};
        } else if (lane < 38) {
          const int im = (lane - 32) >> 1, hh = lane & 1;
          *(LAS u32x4*)(smem + HZ_OFF + im * 96 + hh * 16) = (u32x4){0u, 0u, 0u, 0u};
        } else if (lane == 38) *(LAS unsigned*)(smem + HCNT_OFF) = 0u;
      }
    }
    sfor<24>([&](auto ac) ALWAYS_INLINE {
      if constexpr (k1_astep(ac) == g) {
        asm volatile("s_waitcnt vmcnt(%2)" : "+v"(caf[ac]), "+v"(cax[ac]) : "i"(k1_younger(4 * k1_cstep(ac) + 2, 4 * g + 2)) : "memory");
        conv_align(ac);
      }
    });
    if constexpr (g == K1_TOUCH) {
      int l2 = lane; asm volatile("" : "+v"(l2));
      const unsigned line = (unsigned)(w * 64 + l2 < 324 ? idx * 324 + w * 64 + l2 : idx * 324);
      touch_lines(pf_wd, line * 128u, lds0 + K1_PF_OFF + 16);
    }
    sfor<24>([&](auto cc) ALWAYS_INLINE {
      constexpr int ci = cc;
      if constexpr (k1_cstep(ci) == g) {
        if constexpr (ci == 0) {
          int l2 = lane; asm volatile("" : "+v"(l2));
          const int e = (l2 & 15) - 15 + 8 * (l2 >> 4);
          cvo = (e & ~1) * 2; cvx = cvo + ((e & 1) ? 16 : 0);
        }
        constexpr int cb = ci < 16 ? 64 * (ci >> 3) + 512 * (ci & 7) : 512 * (ci - 16);
        const char* hp = (ci < 16 ? hpA : hpC) + cb + ((ci == 0 && zA) || (ci == 16 && zC) ? 32 : 0);
        gload16u(caf[ci], hp - 32, (unsigned)(cvo + 32));
        gload4u(cax[ci], hp - 32, (unsigned)(cvx + 32));
      }
    });
    if constexpr (g + PD < 324) {
      constexpr int g1 = g + PD, st1 = g1 / 36, f1 = g1 - st1 * 36;
      if constexpr (f1 == 0) {
        if constexpr (st1 != 4) {
          unsigned b2 = rbU; asm volatile("" : "+v"(b2));
#pragma unroll
          for (int j = 0; j < 4; ++j) { rb[j] = (b2 + (st1 % K1_NSLOT) * K1_SLOT) ^ (j << 5); asm volatile("" : "+v"(rb[j])); }
        } else {
          int l2 = lane; asm volatile("" : "+v"(l2));
          rb[0] = lds0 + (8 * (l2 >> 5) + ((l2 & 15) >> 2)) * 32 + ((l2 >> 4) & 1) * K1_UOFF + (l2 & 3) * 8 + (st1 % K1_NSLOT) * K1_SLOT;
          asm volatile("" : "+v"(rb[0]));
        }
      }
      if constexpr (st1 == 8) af[g1 % (PD + 1)] = tr_frag((LAS char*)(uintptr_t)rb[f1 / 9], (f1 % 9) * 2048, 512);
      else if constexpr (st1 != 4) af[g1 % (PD + 1)] = tr_frag((LAS char*)(uintptr_t)rb[f1 % 4], (f1 / 4) * 2048, 512);
      else af[g1 % (PD + 1)] = tr_frag((LAS char*)(uintptr_t)rb[0], f1 * 512, 128);
    }
    if constexpr (st < 4 && c == 0) {
      asm volatile("s_waitcnt vmcnt(%1)" : "+v"(xf[xi]) : "i"(k1_younger(k1_xkey(xi), 4 * g + 3)) : "memory");
    }
    SCHED0();
    acc[ai] = __builtin_amdgcn_mfma_f32_32x32x16_bf16(af[g % (PD + 1)], xf[xi], first ? zero16 : acc[ai], 0, 0, 0);
    SCHED0();
    if constexpr (g == 143) {
#pragma unroll
      for (int i = 0; i < 4; ++i) ho[i] = h_word(acc[0], i);
      h_store(ho, colA >> 4);
      SCHED0();
    }
    if constexpr (g > 144 && g <= 162) {
      constexpr int cc = (g - 145) / 6 + 1, pi = (g - 145) % 6;
      if constexpr (pi < 4) ho[pi] = h_word(acc[cc], pi);
      if constexpr (pi == 5) h_store(ho, (colA >> 4) + cc);
      SCHED0();
    }
    if constexpr (g >= 298 && (g - 298) % 9 < 5 && (g - 298) / 9 < 3) {
      constexpr int cc = (g - 298) / 9, pi = (g - 298) % 9;
      if constexpr (pi < 4) ho[pi] = h_word(acc[(cc + 1) & 3], pi);
      if constexpr (pi == 4) h_store(ho, (colA >> 4) + 4 + cc);
      SCHED0();
    }
    if constexpr (g >= 180 && g <= 182) {
      if constexpr (g == 180) { ho[0] = h_word(acc[0], 0); ho[1] = h_word(acc[0], 1); }
      if constexpr (g == 181) { ho[2] = h_word(acc[0], 2); ho[3] = h_word(acc[0], 3); }
      if constexpr (g == 182) h_store(ho, colQ >> 4);
      SCHED0();
    }
  });
  const char* wbase2 = (const char*)wd + (size_t)kbase * (D_ * 2);
  unsigned voff2[4];
  int lane2 = lane; asm volatile("" : "+v"(lane2));
#pragma unroll
  for (int n = 0; n < 4; ++n) {
    const int o = (tq * 4 + n) * 1024 + lane2 * 16;
    const int k = o >> 8, pc = (o >> 4) & 15;
    const int j = (pc >> 1) ^ (k & 7);
    voff2[n] = (unsigned)(k * (D_ * 2) + (2 * j + (pc & 1)) * 16);
  }
  const unsigned ldst2 = lds0 + khf * K2_HALF;
  auto piece_load2 = [&](int st, int n) ALWAYS_INLINE {
    const int slot = st % K2_NSLOT;
    if (st < 9) {
      const char* g = wbase2 + (size_t)k2_unit(st) * 64 * (D_ * 2) + colA2 * 2;
      glds1p<false>(g, voff2[n], ldst2 + slot * K2_SLOT + tq * 4096 + n * 1024);
    } else {
      int l2 = lane; asm volatile("" : "+v"(l2));
      const char* g = wbase2 + (size_t)(st - 9) * 288 * (D_ * 2) + colQ2 * 2;
      const int o = n < 2 ? tq * 2048 + n * 1024 + l2 * 16 : 8192 + tq * 256 + (l2 & 15) * 16;
      const unsigned v0 = (unsigned)((o >> 5) * (D_ * 2) + ((o >> 4) & 1) * 16);
      if (n < 2 || l2 < 16) glds1p<false>(g, v0, ldst2 + slot * K2_SLOT + (n < 2 ? tq * 2048 + n * 1024 : 8192 + tq * 256));
    }
  };
  const char* hbase = (const char*)hout + ((size_t)(mt * 8 + tq * 2) * 144 * 64 + (size_t)(kbase >> 5) * 64) * 16;
  const unsigned hv0 = lane2 * 16 + khf * (144 * 64 * 16), hv1 = lane2 * 16 + (1 - khf) * (144 * 64 * 16);
  bf16x8 hf[18][2];
  auto h_load = [&](auto lc) ALWAYS_INLINE {
    constexpr int k = 2 * k2_unit(lc / 4) + (lc / 2) % 2, t = lc % 2;
    gload16_sc1<(k & 3) * 1024>(hf[k][t], hbase + (k >> 2) * 4096, t ? hv1 : hv0);
  };
  STAMP(1, 2);
  __builtin_amdgcn_s_setprio(0);
  {
    u32x4 o;
#pragma unroll
    for (int i = 0; i < 4; ++i) o[i] = h_word(acc[0], i);
    h_store(o, (colA >> 4) + 7);
  }

  __builtin_amdgcn_s_barrier();
  CFENCE();
#pragma unroll
  for (int n = 0; n < 4; ++n) piece_load2(0, n);
#pragma unroll
  for (int n = 0; n < 4; ++n) piece_load2(1, n);

  STAMP(1, 3);
  const int ct0 = lane & 15, ckg = lane >> 4;
  {
    const int boff = (2032 - 16 * (lane & 15) + 8 * ckg) * 2;
    auto head_frag = [&](int im) ALWAYS_INLINE -> bf16x8 {
      bf16x8 r;
      LAS u16* hz = (LAS u16*)(smem + HZ_OFF + im * 96) + 16 + (ct0 - 15 + 8 * ckg);
#pragma unroll
      for (int j = 0; j < 8; ++j) r[j] = (short)hz[j];
      return r;
    };
    if (zA) cf[0] = head_frag(w >> 2);
    if (zC) cf[16] = head_frag(2);
    {
      const int im = w >> 2;
      LAS char* xr = smem + XR_OFF + im * XR_STRIDE + boff + 128 * (w & 3);
      f32x4 ac[8];
#pragma unroll
      for (int j = 0; j < 8; ++j) ac[j] = (f32x4){0.f, 0.f, 0.f, 0.f};
#pragma unroll
      for (int rr = 1; rr >= 0; --rr) {
        bf16x8 bfm[8];
#pragma unroll
        for (int b = 0; b < 8; ++b) bfm[b] = *(LAS bf16x8*)(xr - 512 * b + 64 * rr);
#pragma unroll
        for (int a = 7; a >= 0; --a)
#pragma unroll
          for (int j = a; j < 8; ++j) ac[j] = __builtin_amdgcn_mfma_f32_16x16x32_bf16(cf[rr * 8 + a], bfm[j - a], ac[j], 0, 0, 0);
        if (rr == 1) {
          SCHED0();
          wait_vm<8>();
          if (lane == 0) {
            const unsigned old = __hip_atomic_fetch_add((LAS unsigned*)(smem + HCNT_OFF), 1u, __ATOMIC_RELAXED, __HIP_MEMORY_SCOPE_WORKGROUP);
            if (old == 7u) set_flag(flg + FL_H + (mtile * 4 + (cgrp >> 1)) * 8 + (cgrp & 1) * 4 + cq, tag);
          }
          SCHED0();
        }
      }
#pragma unroll
      for (int j = 0; j < 8; ++j)
        *(LAS f32x4*)(smem + PART_OFF + w * 8192 + (j * 64 + lane) * 16) = ac[j];
    }
    poll_flags<1>(flg + FL_HE + ((mt >> 1) * 4 + ksl) * 8, 8, tag, lane);
    sfor<12>([&](auto kc) ALWAYS_INLINE { h_load(kc); });
    {
      LAS char* xr = smem + XR_OFF + 2 * XR_STRIDE + boff + 64 * w;
      const int jA = 7 - qd, jB = qd;
      bf16x8 b0[8], b1[4];
#pragma unroll
      for (int a = 0; a < 8; ++a) { const int bb = jA - a; b0[a] = *(LAS bf16x8*)(xr - 512 * (bb < 0 ? 0 : bb)); }
#pragma unroll
      for (int a = 0; a < 4; ++a) { const int bb = jB - a; b1[a] = *(LAS bf16x8*)(xr - 512 * (bb < 0 ? 0 : bb)); }
      f32x4 c0 = (f32x4){0.f, 0.f, 0.f, 0.f}, c1 = c0;
#pragma unroll
      for (int a = 7; a >= 0; --a) {
        if (a <= jA) c0 = __builtin_amdgcn_mfma_f32_16x16x32_bf16(cf[16 + a], b0[a], c0, 0, 0, 0);
        if (a < 4 && a <= jB) c1 = __builtin_amdgcn_mfma_f32_16x16x32_bf16(cf[16 + a], b1[a < 4 ? a : 0], c1, 0, 0, 0);
      }
      *(LAS f32x4*)(smem + PARTC_OFF + w * 2048 + lane * 16) = c0;
      *(LAS f32x4*)(smem + PARTC_OFF + w * 2048 + 1024 + lane * 16) = c1;
    }
  }
  STAMP(1, 4);
  WAIT_L(0);
  __builtin_amdgcn_s_barrier();
  CFENCE();
  STAMP(1, 5);
  auto finalize = [&](f32x4 s0, f32x4 s1, int im, int ch, int t8, float dd) ALWAYS_INLINE {
    const u32x4 kvv = *(LAS u32x4*)(smem + XR_OFF + im * XR_STRIDE + (2040 - t8) * 2);
    float y[8] = {s0[0], s0[1], s0[2], s0[3], s1[0], s1[1], s1[2], s1[3]};
    float o[8];
#pragma unroll
    for (int e = 0; e < 8; ++e) {
      const int ri = 7 - e;
      const float kvf = (ri & 1) ? bf_hi(kvv[ri >> 1]) : bf_lo(kvv[ri >> 1]);
      o[e] = bfr(y[e]) + bfr(kvf * dd);
    }
    u32x4 ov; ov.x = pk_bf16(o[0], o[1]); ov.y = pk_bf16(o[2], o[3]); ov.z = pk_bf16(o[4], o[5]); ov.w = pk_bf16(o[6], o[7]);
    gstore16_sc1(wT + (size_t)ch * S_ + t8, ov);
  };
  {
    const int im = tid >> 8, t8 = (tid & 255) * 8;
    const int po = ((t8 >> 8) * 64 + ((t8 >> 2) & 3) * 16 + ((t8 >> 4) & 15)) * 16;
    f32x4 s0 = (f32x4){0.f, 0.f, 0.f, 0.f}, s1 = s0;
#pragma unroll
    for (int ww = 0; ww < 4; ++ww) {
      s0 += *(LAS f32x4*)(smem + PART_OFF + (im * 4 + ww) * 8192 + po);
      s1 += *(LAS f32x4*)(smem + PART_OFF + (im * 4 + ww) * 8192 + po + 256);
    }
    finalize(s0, s1, im, chA + im, t8, ddA);
  }
  if (tid >= 448) {
    const int t2 = tid - 448;
    const int tl = t2 >> 5, l8 = (t2 & 31) * 8;
    const int jt = tl ? qd : 7 - qd, t8 = 256 * jt + l8;
    const int po = (((l8 >> 2) & 3) * 16 + (l8 >> 4)) * 16;
    f32x4 s0 = (f32x4){0.f, 0.f, 0.f, 0.f}, s1 = s0;
#pragma unroll
    for (int ww = 0; ww < 8; ++ww) {
      s0 += *(LAS f32x4*)(smem + PARTC_OFF + ww * 2048 + tl * 1024 + po);
      s1 += *(LAS f32x4*)(smem + PARTC_OFF + ww * 2048 + tl * 1024 + po + 256);
    }
    finalize(s0, s1, 2, chC, t8, ddC);
  }
  STAMP(1, 6);

  STAMP(2, 0);
  {
  const int kq = lane2 >> 4, qq = (lane2 & 15) >> 2, p4 = lane2 & 3;
  const unsigned rbU2 = lds0 + khf * K2_HALF + (16 * (kq >> 1) + 4 * (kq & 1) + qq) * 256 + ((4 * (kq & 1) + qq) << 5) + p4 * 8;

  const char* wtb = (const char*)(wT + mt * 128 + ksl * 32);
  f32x4 acc[2][8], accq[2];
  const f32x4 zero4 = {0.f, 0.f, 0.f, 0.f};

  const int tile = mt * 4 + nt;
  const __amdgpu_buffer_rsrc_t rs = __builtin_amdgcn_make_buffer_rsrc((char*)slab + (size_t)tile * (16 * 18432), 0, 16 * 18432, 0x00020000);
  const bool foreign = tq != ksl;
  auto give = [&](int xb, int i, const f32x4& a0, const f32x4& a1) ALWAYS_INLINE {
    *(LAS f32x4*)(smem + xb + ((w * 4 + i) * 64 + lane) * 16) = a1;
  };
  auto publish = [&](int xb, int i, const f32x4& a0, const f32x4& a1, int dtg) ALWAYS_INLINE {
    const f32x4 sm = a0 + *(LAS f32x4*)(smem + xb + (((w ^ 4) * 4 + i) * 64 + lane) * 16);
    if (foreign) __builtin_amdgcn_raw_buffer_store_b128(__builtin_bit_cast(u32x4, sm), rs, (unsigned)(((tq * 4 + ksl) * 18 + khf * 9 + dtg) * 1024 + lane * 16), 0, 16);
    else *(LAS f32x4*)(smem + K2_OWN_OFF + ((khf * 9 + dtg) * 64 + lane) * 16) = sm;
  };
  constexpr int XB1 = 3 * K2_SLOT, XB2 = 0, XB3 = 3 * K2_SLOT;

  asm volatile("s_waitcnt vmcnt(%4)" : "+v"(hf[0][0]), "+v"(hf[0][1]), "+v"(hf[1][0]), "+v"(hf[1][1])
               : "i"(k2_younger(k2_hkey(0, 3), 0, false)) : "memory");
  WAIT_L(0);
  __builtin_amdgcn_s_barrier();
  CFENCE();
  STAMP(2, 1);

  constexpr int PD = 2;
  bf16x8 af[PD + 1];
  unsigned sb = rbU2;
#pragma unroll
  for (int g = 0; g < PD; ++g) af[g] = tr_frag((LAS char*)(uintptr_t)(sb ^ (g << 5)), 0, 2048);
  sfor<162>([&](auto gc) ALWAYS_INLINE {
    constexpr int g = gc;
    constexpr int st = k2_stage(g), f = g - k2_g0(st);
    constexpr int ks = st < 9 ? k2_unit(st) * 2 + f / 8 : (st - 9) * 9 + f, dt = st < 9 ? f % 8 : 0;
    if constexpr (f == k2_mid(st)) {
      if constexpr (st < 8) { STAMP(0, 2 * st); }
      if constexpr (st == 2) { if (w == 0) poll_flags<1>(flg + FL_H + ((mt >> 1) * 4 + ksl) * 8, 8, tag, lane); }
      if constexpr (st == K2_WST) { if (w == 0) poll_flags<4>(flg + FL_W, 64, tag, lane); }
      if constexpr (st < 10) {
        constexpr int okey = cmax(k2_dkey(st + 1, k2_np(st + 1) - 1), st + 1 < 9 ? k2_hkey(st + 1, 3) : -1000);
        constexpr int n0 = k2_younger(okey, 4 * g, false);
        if constexpr (st == 0) { if (w == 7) wait_vm<n0 + 1>(); else wait_vm<n0>(); }
        else wait_vm<n0>();
        if constexpr (st + 1 < 9) {
          constexpr int k0 = 2 * k2_unit(st + 1);
          asm volatile("" : "+v"(hf[k0][0]), "+v"(hf[k0][1]), "+v"(hf[k0 + 1][0]), "+v"(hf[k0 + 1][1])::"memory");
        }
      }
      if constexpr (st >= 9) WAIT_L(0);
      __builtin_amdgcn_s_barrier();
      CFENCE();
      if constexpr (st < 8) { STAMP(0, 2 * st + 1); }
      if constexpr (st == 1) { if (tid == 0) set_flag(flg + FL_W + bid, tag); }
      if constexpr (st == K2_WST) {
        int l2 = lane; asm volatile("" : "+v"(l2));
        glds1_sc1(wtb, (unsigned)((colA2 + w * 16 + (l2 >> 2)) * (S_ * 2) + (l2 & 3) * 16), lds0 + K2_WT_OFF + w * 1024);
        glds1_sc1(wtb, (unsigned)((colQ2 + (l2 >> 2)) * (S_ * 2) + (l2 & 3) * 16), lds0 + K2_WT_OFF + 8192);
      }
      if constexpr (st == 9) {
#pragma unroll
        for (int i = 0; i < 4; ++i) publish(XB1, i, acc[0][i], acc[1][i], i);
#pragma unroll
        for (int i = 0; i < 4; ++i) give(XB2, i, acc[0][4 + i], acc[1][4 + i]);
      }
      if constexpr (st == 10) {
#pragma unroll
        for (int i = 0; i < 4; ++i) publish(XB2, i, acc[0][4 + i], acc[1][4 + i], 4 + i);
      }
    }
    if constexpr (st < 9 && ((f + 16 - 6) % 16) % 4 == 0) __builtin_amdgcn_s_setprio(3 - ((f + 16 - 6) % 16) / 4);
    sfor<36>([&](auto dc) ALWAYS_INLINE { if constexpr (dc % 4 < k2_np(dc / 4 + 2) && k2_dkey(dc / 4 + 2, dc % 4) == 4 * g + 2) piece_load2(dc / 4 + 2, dc % 4); });
    sfor<24>([&](auto hc) ALWAYS_INLINE { if constexpr (k2_hkey(hc / 4 + 3, hc % 4) == 4 * g + 3) h_load(std::integral_constant<int, hc + 12>{}); });
    if constexpr (g + PD < 162) {
      constexpr int g1 = g + PD, st1 = k2_stage(g1), f1 = g1 - k2_g0(st1);
      if constexpr (f1 == 0) {
        if constexpr (st1 < 9) {
          sb += (st1 % K2_NSLOT == 0) ? -3 * K2_SLOT : K2_SLOT;
        } else {
          int l2 = lane; asm volatile("" : "+v"(l2));
          const int kq2 = l2 >> 4;
          sb = lds0 + khf * K2_HALF + (16 * (kq2 >> 1) + 4 * (kq2 & 1) + ((l2 & 15) >> 2)) * 32 + (l2 & 3) * 8 + (st1 % K2_NSLOT) * K2_SLOT;
        }
      }
      asm volatile("" : "+v"(sb));
      if constexpr (st1 < 9) af[g1 % (PD + 1)] = tr_frag((LAS char*)(uintptr_t)(sb ^ ((f1 % 8) << 5)), (f1 / 8) * 8192, 2048);
      else af[g1 % (PD + 1)] = tr_frag((LAS char*)(uintptr_t)sb, f1 * 1024, 256);
    }
    SCHED0();
#pragma unroll
    for (int t = 0; t < 2; ++t) {
      if constexpr (st < 9) acc[t][dt] = __builtin_amdgcn_mfma_f32_16x16x32_bf16(af[g % (PD + 1)], hf[ks][t], g < 8 ? zero4 : acc[t][dt], 0, 0, 0);
      else accq[t] = __builtin_amdgcn_mfma_f32_16x16x32_bf16(af[g % (PD + 1)], hf[ks][t], g == 144 ? zero4 : accq[t], 0, 0, 0);
    }
    SCHED0();
    if constexpr (g == 143) {
#pragma unroll
      for (int i = 0; i < 4; ++i) give(XB1, i, acc[0][i], acc[1][i]);
      SCHED0();
    }
  });
  STAMP(2, 2);
  __builtin_amdgcn_s_setprio(0);
  give(XB3, 0, accq[0], accq[1]);

  int lane3 = lane; asm volatile("" : "+v"(lane3));
  u32x2 xq[3];
  float2 stt[2];
#pragma unroll
  for (int i = 0; i < 3; ++i) {
    const int t = i < 2 ? i : (w & 1);
    const int token = mt * 128 + ksl * 32 + t * 16 + (lane3 & 15);
    xq[i] = *(const u32x2*)(q + (size_t)token * D_ + (i < 2 ? colA2 + w * 16 : colQ2) + 4 * (lane3 >> 4));
    if (i < 2) {
      const u64 sv = __hip_atomic_load(stats + token, __ATOMIC_RELAXED, __HIP_MEMORY_SCOPE_AGENT);
      stt[i] = make_float2(__uint_as_float((unsigned)sv), __uint_as_float((unsigned)(sv >> 32)));
    }
  }
  WAIT_L(0);
  __builtin_amdgcn_s_barrier();
  CFENCE();
  publish(XB3, 0, accq[0], accq[1], 8);
  asm volatile("s_waitcnt vmcnt(0) lgkmcnt(0)" ::: "memory");
  __builtin_amdgcn_s_barrier();
  CFENCE();
  STAMP(2, 3);
  if (tid == 0) set_flag(flg + FL_S + tile * 4 + ksl, tag);
  poll_flags<1>(flg + FL_S + tile * 4, 4, tag, lane3);
  STAMP(2, 4);
  auto fetch = [&](f32x4 (&pv)[3], int fi) ALWAYS_INLINE {
#pragma unroll
    for (int k = 1; k < 4; ++k)
      pv[k - 1] = __builtin_bit_cast(f32x4, __builtin_amdgcn_raw_buffer_load_b128(rs, (unsigned)(((ksl * 4 + ((ksl + k) & 3)) * 18 + fi) * 1024 + lane3 * 16), 0, 16));
  };
  auto combine = [&](const f32x4 (&pv)[3], int t, int dt, const u32x2& xw2, const float2& st2) ALWAYS_INLINE {
    const int tl = t * 16 + (lane3 & 15), dl = dt * 16 + 4 * (lane3 >> 4);
    const f32x4 own = *(LAS f32x4*)(smem + K2_OWN_OFF + ((t * 9 + dt) * 64 + lane3) * 16);
    const f32x4 bs = ((own + pv[0]) + pv[1]) + pv[2];
    float o[4];
#pragma unroll
    for (int c = 0; c < 4; ++c) {
      const unsigned xw = (c < 2) ? xw2.x : xw2.y;
      const float x = (c & 1) ? bf_hi(xw) : bf_lo(xw);
      const float wy = bf2f(*(LAS u16*)(smem + K2_WT_OFF + (dl + c) * 64 + tl * 2));
      const float xn = bfr(bfr(x - st2.x) * st2.y);
      const float a = bfr(xn * wy);
      o[c] = bfr(x + a) + bfr(bs[c]);
    }
    u32x2 ov; ov.x = pk_bf16(o[0], o[1]); ov.y = pk_bf16(o[2], o[3]);
    *(u32x2*)(out + (size_t)(mt * 128 + ksl * 32 + tl) * D_ + (dt < 8 ? colA2 + dt * 16 : colQ2) + 4 * (lane3 >> 4)) = ov;
  };
  {
    f32x4 p0[3], p1[3], p2[3];
    fetch(p0, w); fetch(p1, 9 + w); fetch(p2, 9 * (w & 1) + 8);
    combine(p0, 0, w, xq[0], stt[0]);
    combine(p1, 1, w, xq[1], stt[1]);
    if (w < 2) combine(p2, w, 8, xq[2], w ? stt[1] : stt[0]);
  }
  if (bid == 0 && w == 0) {
    poll_flags<4>(flg + FL_K, 64, tag, lane);
    if (lane == 0) set_flag(flg + FL_GEN, tag);
  }
  STAMP(2, 5); RSTAMP(1, 15); STAMP_FLUSH(1); STAMP_FLUSH(2); STAMP_FLUSH(0);
  }
}

extern "C" void kernel_launch(void* const* d_in, const int* in_sizes, int n_in,
                              void* d_out, int out_size, void* d_ws, size_t ws_size,
                              hipStream_t stream) {
  const u16* q = (const u16*)d_in[0];
  const u16* k = (const u16*)d_in[1];
  const u16* v = (const u16*)d_in[2];
  const u16* shk = (const u16*)d_in[3];
  const u16* ssmk = (const u16*)d_in[4];
  const u16* ssmd = (const u16*)d_in[5];
  const u16* wg = (const u16*)d_in[7];
  const u16* wu = (const u16*)d_in[8];
  const u16* wd = (const u16*)d_in[9];
  (void)hipFuncSetAttribute((const void*)k_fused, hipFuncAttributeMaxDynamicSharedMemorySize, K1_LDS);
  k_fused<<<256, 512, K1_LDS, stream>>>(q, k, v, shk, ssmk, ssmd, wg, wu, wd, (char*)d_ws, (u16*)d_out);
}
```

```cpp
#include <hip/hip_runtime.h>
#include <hip/hip_bf16.h>
#include <stdint.h>
#include <utility>

typedef unsigned short u16;
typedef short bf16x8 __attribute__((ext_vector_type(8)));
typedef short s16x4 __attribute__((ext_vector_type(4)));
typedef float f32x4 __attribute__((ext_vector_type(4)));
typedef float f32x16 __attribute__((ext_vector_type(16)));
typedef unsigned u32x4 __attribute__((ext_vector_type(4)));
typedef unsigned u32x2 __attribute__((ext_vector_type(2)));
typedef __bf16 bf2_t __attribute__((ext_vector_type(2)));
#define LAS __attribute__((address_space(3)))
#define DI __device__ __forceinline__

constexpr int S_ = 2048, D_ = 576, F_ = 4608;

constexpr size_t WS_XFRAG = 0;
constexpr size_t WS_STATS = WS_XFRAG + (size_t)S_ * D_ * 2;
constexpr size_t WS_KVR   = WS_STATS + (size_t)S_ * 8;
constexpr size_t WS_WT    = WS_KVR + (size_t)S_ * D_ * 2;
constexpr size_t WS_H     = WS_WT + (size_t)S_ * D_ * 2;
constexpr size_t WS_SLAB  = WS_H + (size_t)S_ * F_ * 2;
constexpr size_t WS_FLG   = WS_SLAB + (size_t)4 * S_ * D_ * 4;
constexpr int FL_GEN = 0, FL_X = 32, FL_K = FL_X + 256, FL_H = FL_K + 256, FL_HE = FL_H + 256, FL_W = FL_HE + 256, FL_S = FL_W + 256, FL_END = FL_S + 256;
constexpr size_t WS_END   = WS_FLG + FL_END * 8;
typedef unsigned long long u64;

DI float bf_lo(unsigned w) { return __uint_as_float(w << 16); }
DI float bf_hi(unsigned w) { return __uint_as_float(w & 0xffff0000u); }
DI float bf2f(u16 b) { return __uint_as_float(((unsigned)b) << 16); }
DI float bfr(float x) { return (float)(__bf16)x; }
typedef float f32x2 __attribute__((ext_vector_type(2)));
DI unsigned pk_bf16(float lo, float hi);
DI f32x2 unpk(unsigned w) { return (f32x2){__uint_as_float(w << 16), __uint_as_float(w & 0xffff0000u)}; }
DI f32x2 bfr2(f32x2 v) { return unpk(pk_bf16(v.x, v.y)); }
DI unsigned pk_bf16(float lo, float hi) { bf2_t v = {(__bf16)lo, (__bf16)hi}; return __builtin_bit_cast(unsigned, v); }

DI float wave_sum(float x) {
  x += __builtin_bit_cast(float, __builtin_amdgcn_update_dpp(0, __builtin_bit_cast(int, x), 0xB1, 0xf, 0xf, true));
  x += __builtin_bit_cast(float, __builtin_amdgcn_update_dpp(0, __builtin_bit_cast(int, x), 0x4E, 0xf, 0xf, true));
  x += __builtin_bit_cast(float, __builtin_amdgcn_update_dpp(0, __builtin_bit_cast(int, x), 0x141, 0xf, 0xf, true));
  x += __builtin_bit_cast(float, __builtin_amdgcn_update_dpp(0, __builtin_bit_cast(int, x), 0x140, 0xf, 0xf, true));
  const int xi = __builtin_bit_cast(int, x);
  return (__builtin_bit_cast(float, __builtin_amdgcn_readlane(xi, 0)) + __builtin_bit_cast(float, __builtin_amdgcn_readlane(xi, 16))) +
         (__builtin_bit_cast(float, __builtin_amdgcn_readlane(xi, 32)) + __builtin_bit_cast(float, __builtin_amdgcn_readlane(xi, 48)));
}
#define WAIT_V(n) asm volatile("s_waitcnt vmcnt(" #n ")" ::: "memory")
#define WAIT_L(n) asm volatile("s_waitcnt lgkmcnt(" #n ")" ::: "memory")
#define CFENCE() asm volatile("" ::: "memory")
#define STAMP(kid, i)
#define RSTAMP(kid, i)
#define STAMP_DECL
#define STAMP_FLUSH(kid)
#define K0STAMP(i)
#define SCHED0() __builtin_amdgcn_sched_barrier(0)
template <int... I, class Fn> DI void sfor_impl(std::integer_sequence<int, I...>, Fn&& f) { (f(std::integral_constant<int, I>{}), ...); }
template <int N, class Fn> DI void sfor(Fn&& f) { sfor_impl(std::make_integer_sequence<int, N>{}, f); }
#define ALWAYS_INLINE __attribute__((always_inline))
template <int N> DI void wait_vm() { asm volatile("s_waitcnt vmcnt(%0)" ::"i"(N) : "memory"); }

DI void glds4(const void* sbase, unsigned v0, unsigned v1, unsigned v2, unsigned v3, unsigned lds_dst) {
  unsigned keep;
  asm volatile("s_mov_b32 %0, m0\n\ts_mov_b32 m0, %6\n\ts_nop 0\n\t"
               "global_load_lds_dwordx4 %1, %5\n\t"
               "global_load_lds_dwordx4 %2, %5 offset:1024\n\t"
               "global_load_lds_dwordx4 %3, %5 offset:2048\n\t"
               "global_load_lds_dwordx4 %4, %5 offset:3072\n\t"
               "s_mov_b32 m0, %0"
               : "=&s"(keep) : "v"(v0), "v"(v1), "v"(v2), "v"(v3), "s"(sbase), "s"(lds_dst) : "memory");
}
DI void glds3(const void* sbase, unsigned v0, unsigned v1, unsigned v2, unsigned lds_dst) {
  unsigned keep;
  asm volatile("s_mov_b32 %0, m0\n\ts_mov_b32 m0, %5\n\ts_nop 0\n\t"
               "global_load_lds_dwordx4 %1, %4\n\t"
               "global_load_lds_dwordx4 %2, %4 offset:1024\n\t"
               "global_load_lds_dwordx4 %3, %4 offset:2048\n\t"
               "s_mov_b32 m0, %0"
               : "=&s"(keep) : "v"(v0), "v"(v1), "v"(v2), "s"(sbase), "s"(lds_dst) : "memory");
}
DI void glds2(const void* sbase, unsigned v0, unsigned v1, unsigned lds_dst) {
  unsigned keep;
  asm volatile("s_mov_b32 %0, m0\n\ts_mov_b32 m0, %4\n\ts_nop 0\n\t"
               "global_load_lds_dwordx4 %1, %3\n\t"
               "global_load_lds_dwordx4 %2, %3 offset:1024\n\t"
               "s_mov_b32 m0, %0"
               : "=&s"(keep) : "v"(v0), "v"(v1), "s"(sbase), "s"(lds_dst) : "memory");
}
DI void glds1(const void* sbase, unsigned v0, unsigned lds_dst) {
  unsigned keep;
  asm volatile("s_mov_b32 %0, m0\n\ts_mov_b32 m0, %3\n\ts_nop 0\n\t"
               "global_load_lds_dwordx4 %1, %2\n\t"
               "s_mov_b32 m0, %0"
               : "=&s"(keep) : "v"(v0), "s"(sbase), "s"(lds_dst) : "memory");
}

template <bool NT> DI void glds1p(const void* sbase, unsigned v0, unsigned lds_dst) {
  unsigned keep;
  if constexpr (NT)
    asm volatile("s_mov_b32 %0, m0\n\ts_mov_b32 m0, %3\n\ts_nop 0\n\tglobal_load_lds_dwordx4 %1, %2 nt\n\ts_mov_b32 m0, %0"
                 : "=&s"(keep) : "v"(v0), "s"(sbase), "s"(lds_dst) : "memory");
  else
    asm volatile("s_mov_b32 %0, m0\n\ts_mov_b32 m0, %3\n\ts_nop 0\n\tglobal_load_lds_dwordx4 %1, %2\n\ts_mov_b32 m0, %0"
                 : "=&s"(keep) : "v"(v0), "s"(sbase), "s"(lds_dst) : "memory");
}
DI void glds1_sc1(const void* sbase, unsigned v0, unsigned lds_dst) {
  unsigned keep;
  asm volatile("s_mov_b32 %0, m0\n\ts_mov_b32 m0, %3\n\ts_nop 0\n\tglobal_load_lds_dwordx4 %1, %2 sc1\n\ts_mov_b32 m0, %0"
               : "=&s"(keep) : "v"(v0), "s"(sbase), "s"(lds_dst) : "memory");
}
DI void touch_lines(const void* sbase, unsigned v0, unsigned lds_dst) {
  unsigned keep;
  asm volatile("s_mov_b32 %0, m0\n\ts_mov_b32 m0, %3\n\ts_nop 0\n\tglobal_load_lds_dword %1, %2\n\ts_mov_b32 m0, %0"
               : "=&s"(keep) : "v"(v0), "s"(sbase), "s"(lds_dst) : "memory");
}
template <int OFF> DI void gload16(bf16x8& dst, const void* sbase, unsigned voff) {
  asm volatile("global_load_dwordx4 %0, %1, %2 offset:%3" : "=v"(dst) : "v"(voff), "s"(sbase), "i"(OFF) : "memory");
}
template <int OFF> DI void gload16_sc1(bf16x8& dst, const void* sbase, unsigned voff) {
  asm volatile("global_load_dwordx4 %0, %1, %2 offset:%3 sc1" : "=v"(dst) : "v"(voff), "s"(sbase), "i"(OFF) : "memory");
}
DI void gload16u(u32x4& dst, const void* sbase, unsigned voff) {
  asm volatile("global_load_dwordx4 %0, %1, %2" : "=v"(dst) : "v"(voff), "s"(sbase) : "memory");
}
template <int OFF> DI void gstore16_sc1_s(const void* sbase, unsigned voff, const u32x4& v) {
  asm volatile("global_store_dwordx4 %0, %1, %2 offset:%3 sc1\n\ts_nop 1" ::"v"(voff), "v"(v), "s"(sbase), "i"(OFF) : "memory");
}
DI void gload4u(unsigned& dst, const void* sbase, unsigned voff) {
  asm volatile("global_load_dword %0, %1, %2" : "=v"(dst) : "v"(voff), "s"(sbase) : "memory");
}
DI void gstore16_sc1(void* p, const u32x4& v) { asm volatile("global_store_dwordx4 %0, %1, off sc1\n\ts_nop 1" ::"v"(p), "v"(v) : "memory"); }
DI void set_flag(u64* f, u64 tag) { __hip_atomic_store(f, tag, __ATOMIC_RELAXED, __HIP_MEMORY_SCOPE_AGENT); }
template <int PER> DI void poll_flags(u64* f, int n, u64 tag, int lane) {
  asm volatile("" : "+v"(lane));
  const int i = lane < n ? lane : 0;
  for (unsigned spins = 0; spins < (1u << 20); ++spins) {
    bool ok = true;
#pragma unroll
    for (int k = 0; k < PER; ++k) ok &= __hip_atomic_load(f + i + 64 * k, __ATOMIC_RELAXED, __HIP_MEMORY_SCOPE_AGENT) == tag;
    if (__all(ok)) break;
    __builtin_amdgcn_s_sleep(1);
  }
}

constexpr int K1_SLOT = 37120;
constexpr int K1_UOFF = 18432 + 128;
constexpr int K1_NSLOT = 4;
constexpr int K1_PF_OFF = K1_NSLOT * K1_SLOT;
constexpr int XR_OFF = K1_PF_OFF + 288, XR_STRIDE = 4608;
constexpr int HZ_OFF = XR_OFF + 3 * XR_STRIDE;
constexpr int K1_LDS = HZ_OFF + 288;
constexpr int PART_OFF = 65536;
constexpr int PARTC_OFF = PART_OFF + 65536;
constexpr int HCNT_OFF = K1_PF_OFF;
constexpr int XG_OFF = 3 * K1_SLOT;

DI bf16x8 tr_frag(LAS char* p, int off, int hi_off) {
  s16x4 lo = __builtin_amdgcn_ds_read_tr16_b64_v4i16((LAS s16x4*)(p + off));
  s16x4 hi = __builtin_amdgcn_ds_read_tr16_b64_v4i16((LAS s16x4*)(p + off + hi_off));
  return __builtin_shufflevector(lo, hi, 0, 1, 2, 3, 4, 5, 6, 7);
}

constexpr int K1_MID = 16;
constexpr int k1_xkey(int i) { return i < 9 ? -100 + i : 4 * (1 + 2 * (i - 9)) + 2; }
constexpr int k1_dstep(int d, int n) { return d == 2 ? 2 + 4 * n : d == 3 ? 24 + 4 * n : d == 4 ? 58 + 6 * n : d == 5 ? 94 + 6 * n : 36 * (d - 3) + K1_MID + 6 * n; }
constexpr int k1_dkey(int d, int n) { return d < 2 ? -400 + 10 * d + n : 4 * k1_dstep(d, n) + 1; }
constexpr int k1_cstep(int i) { return i < 7 ? 218 + 4 * i : (i < 14 ? 254 + 4 * (i - 7) : (i < 21 ? 289 + (i - 14) : 304 + 2 * (i - 21))); }
constexpr int k1_sstep(int c) { return c == 0 ? 143 : (c < 4 ? 144 + 6 * c : (c == 4 ? 182 : 302 + 9 * (c - 5))); }
constexpr int k1_astep(int i) { return i < 14 ? k1_cstep(i) + 28 : (i < 21 ? k1_cstep(i) + 20 : 321 + (i - 21)); }
constexpr int K1_TOUCH = 5 * 36 + K1_MID + 1;
constexpr int k1_younger(int okey, int wkey) {
  int n = 0;
  if (4 * K1_TOUCH + 1 > okey && 4 * K1_TOUCH + 1 < wkey) n += 1;
  if (-70 > okey && -70 < wkey) n += 2;
  for (int i = 0; i < 36; ++i) if (k1_xkey(i) > okey && k1_xkey(i) < wkey) n += 1;
  for (int d = 0; d < 9; ++d) for (int pc = 0; pc < 5; ++pc) if (k1_dkey(d, pc) > okey && k1_dkey(d, pc) < wkey) n += 1;
  for (int i = 0; i < 24; ++i) if (4 * k1_cstep(i) + 2 > okey && 4 * k1_cstep(i) + 2 < wkey) n += 2;
  for (int c = 0; c < 8; ++c) if (4 * k1_sstep(c) + 3 > okey && 4 * k1_sstep(c) + 3 < wkey) n += 1;
  return n;
}
#define HSTORE 1

constexpr int K2_HALF = 16384;
constexpr int K2_SLOT = 2 * K2_HALF;
constexpr int K2_NSLOT = 4;
constexpr int K2_OWN_OFF = K2_NSLOT * K2_SLOT;
constexpr int K2_WT_OFF = K2_OWN_OFF + 18432;
constexpr int k2_g0(int st) { return st <= 9 ? 16 * st : 153; }
constexpr int k2_stage(int g) { return g < 144 ? g / 16 : (g < 153 ? 9 : 10); }
constexpr int k2_mid(int st) { return st < 9 ? 6 : 4; }
constexpr int k2_unit(int st) { return st < 5 ? 2 * st : 2 * (st - 5) + 1; }
constexpr int k2_np(int d) { return d < 9 ? 4 : 3; }
constexpr int k2_dkey(int d, int n) { return d < 2 ? -300 + 10 * d + n : (d == 2 ? 4 * (1 + n) + 2 : 4 * (k2_g0(d - 3) + k2_mid(d - 3) + 4 * n) + 2); }
constexpr int k2_hkey(int j, int m) { return j < 3 ? -250 + 10 * j + m : 4 * (k2_g0(j - 3) + k2_mid(j - 3) + 2 + 4 * m) + 3; }
constexpr int k2_skey(int p) { return 4 * (k2_g0(9 + p) + k2_mid(9 + p)) + 1; }
constexpr int K2_WST = 7;
constexpr int k2_wkey() { return 4 * (k2_g0(K2_WST) + k2_mid(K2_WST)) + 1; }
constexpr int k2_younger(int okey, int wkey, bool stores) {
  int n = 0;
  if (k2_wkey() > okey && k2_wkey() < wkey) n += 2;
  if (-100 > okey && -100 < wkey) n += 1;
  for (int d = 0; d < 11; ++d) for (int pc = 0; pc < k2_np(d); ++pc) if (k2_dkey(d, pc) > okey && k2_dkey(d, pc) < wkey) n += 1;
  for (int j = 0; j < 9; ++j) for (int m = 0; m < 4; ++m) if (k2_hkey(j, m) > okey && k2_hkey(j, m) < wkey) n += 1;
  if (stores) for (int p = 0; p < 2; ++p) if (k2_skey(p) > okey && k2_skey(p) < wkey) n += 4;
  return n;
}
constexpr unsigned CODE_BYTES = 42 * 1024;
constexpr int cmax(int a, int b) { return a > b ? a : b; }

__global__ __launch_bounds__(512, 2) void k_fused(const u16* __restrict__ q, const u16* __restrict__ kk, const u16* __restrict__ vv,
                                                  const u16* __restrict__ shk, const u16* __restrict__ ssmk, const u16* __restrict__ ssmd,
                                                  const u16* __restrict__ wg, const u16* __restrict__ wu, const u16* __restrict__ wd,
                                                  char* ws, u16* __restrict__ out) {
  u16* xfrag = (u16*)(ws + WS_XFRAG);
  u64* stats = (u64*)(ws + WS_STATS);
  u16* kvr = (u16*)(ws + WS_KVR);
  u16* wT = (u16*)(ws + WS_WT);
  u16* hout = (u16*)(ws + WS_H);
  float* slab = (float*)(ws + WS_SLAB);
  u64* flg = (u64*)(ws + WS_FLG);
  extern __shared__ __attribute__((aligned(16))) char smem_g[];
  LAS char* smem = (LAS char*)smem_g;
  const unsigned lds0 = (unsigned)(uintptr_t)smem;
  const int bid = blockIdx.x, tid = threadIdx.x;
  const int w = __builtin_amdgcn_readfirstlane(tid >> 6), lane = tid & 63;
  const int xcd = bid & 7, idx = bid >> 3;
  STAMP_DECL; RSTAMP(1, 14); STAMP(1, 0);
  const u64 genv = __hip_atomic_load(flg + FL_GEN, __ATOMIC_RELAXED, __HIP_MEMORY_SCOPE_AGENT);
  u64 tag;
  auto make_tag = [&]() ALWAYS_INLINE {
    tag = (((u64)(unsigned)__builtin_amdgcn_readfirstlane((unsigned)(genv >> 32)) << 32) | (unsigned)__builtin_amdgcn_readfirstlane((unsigned)genv)) + 1ull;
  };
  const int ksl = xcd >> 1, mt = (xcd & 1) * 8 + (idx >> 2), nt = idx & 3;
  const int tq = w & 3, khf = w >> 2;
  const int kbase = ksl * 1152 + khf * 576;
  const int colA2 = 128 * nt, colQ2 = 512 + 16 * nt;
  const int mtile = (xcd & 1) * 4 + (idx & 3);
  const int cgrp = (xcd >> 1) * 2 + (idx >> 4), cq = (idx >> 2) & 3;
  const int colA = 64 * (9 * cgrp + 2 * cq), colQ = 64 * (9 * cgrp + 8) + 16 * cq;

  const int grp = bid >> 2, qd = bid & 3;
  const int chA = grp * 9 + 2 * qd, chC = grp * 9 + 8;
  float ddA, ddC;
  {
    const int c1 = chA + (w >> 2);
    const unsigned d1 = ((const unsigned*)ssmd)[c1 >> 1], d2 = ((const unsigned*)ssmd)[chC >> 1];
    ddA = (c1 & 1) ? bf_hi(d1) : bf_lo(d1);
    ddC = (chC & 1) ? bf_hi(d2) : bf_lo(d2);
  }

  const int wq = w & 3, part = w >> 2;
  const char* wbase = (const char*)(part ? wu : wg);
  unsigned voff[5];
#pragma unroll
  for (int n = 0; n < 5; ++n) {
    const int o = (n < 4) ? ((wq * 4 + n) * 1024 + lane * 16) : (16384 + wq * 512 + (lane & 31) * 16);
    const int k = o >> 7, pc = (o >> 4) & 7;
    const int j = (pc >> 1) ^ (((k >> 1) & 1) | (part << 1));
    voff[n] = (unsigned)(k * (F_ * 2) + (2 * j + (pc & 1)) * 16);
  }
  const unsigned ldst = lds0 + part * K1_UOFF + wq * 4096;
  const unsigned ldst4 = lds0 + part * K1_UOFF + 16384 + wq * 512;

  auto piece_load = [&](int st, int n) ALWAYS_INLINE {
    const int slot = st % K1_NSLOT;
    const unsigned dst = (n < 4 ? ldst + n * 1024 : ldst4) + slot * K1_SLOT;
    if (st != 4) {
      const int ks = st < 4 ? st : st - 5;
      const char* g = wbase + (size_t)ks * 144 * (F_ * 2) + (colA + (st > 4 ? 64 : 0)) * 2;
      if (n < 4 || lane < 32) glds1p<false>(g, voff[n], dst);
    } else {
      int l2 = lane; asm volatile("" : "+v"(l2));
      const int o = (n < 4) ? ((wq * 4 + n) * 1024 + l2 * 16) : (16384 + wq * 512 + (l2 & 31) * 16);
      const unsigned vq = (unsigned)((o >> 5) * (F_ * 2) + ((o >> 4) & 1) * 16);
      if (n < 4 || lane < 32) glds1p<false>(wbase + colQ * 2, vq, dst);
    }
  };

  bf16x8 xf[36];
  const char* xb = (const char*)xfrag + (size_t)(mtile * 8 + w) * 36 * 1024;
  const unsigned xv = lane * 16;
  auto x_load = [&](auto ic) ALWAYS_INLINE {
    constexpr int i = ic;
    gload16_sc1<(i & 3) * 1024>(xf[i], xb + (i >> 2) * 4096, xv);
  };
  {
    LAS u16* tile = (LAS u16*)(smem + XR_OFF);
    constexpr int KT_OFF = 2 * K1_SLOT, VT_OFF = KT_OFF + 9728, WT_OFF = VT_OFF + 9216;
    const int tt = bid >> 3, ct = bid & 7, t0 = tt * 64, c0 = ct * 72;
    const int tok0 = mtile * 256 + (cgrp * 4 + cq) * 8, token = tok0 + w;
    const bool act = lane < 48;
    u32x2 d[3];
    u32x4 kin, vin, ein;
#pragma unroll
    for (int i = 0; i < 3; ++i)
      asm volatile("global_load_dwordx2 %0, %1, %2 offset:%3" : "=v"(d[i]) : "v"((unsigned)(token * (D_ * 2) + (act ? lane : 0) * 8)), "s"(q), "i"(i * 384) : "memory");
    auto k_off = [&](int pc) ALWAYS_INLINE -> unsigned { const int r = pc / 9, cg = pc - r * 9, ts = t0 - 3 + r; return (unsigned)((ts < 0 ? 0 : ts) * (D_ * 2) + (c0 + 8 * cg) * 2); };
    auto v_off = [&](int pc) ALWAYS_INLINE -> unsigned { const int r = pc / 9, cg = pc - r * 9; return (unsigned)((t0 + r) * (D_ * 2) + (c0 + 8 * cg) * 2); };
    gload16u(kin, kk, k_off(tid));
    gload16u(vin, vv, v_off(tid));
    int edst;
    {
      const int kp = w == 0 ? 512 + lane : 576 + (lane < 27 ? lane : 0);
      const int wl = lane < 63 ? lane - 27 : 35;
      const bool isw = w == 1 && lane >= 27, isv = w >= 2;
      const char* src = isv ? (const char*)vv + v_off(512 + lane) : (isw ? (const char*)shk + c0 * 8 + wl * 16 : (const char*)kk + k_off(kp));
      edst = isv ? VT_OFF + (512 + lane) * 16 : (isw ? WT_OFF + wl * 16 : KT_OFF + kp * 16);
      asm volatile("global_load_dwordx4 %0, %1, off" : "=v"(ein) : "v"(src) : "memory");
    }
    {
      const int li = (w == 3 || w == 4) ? (w & 1) * 64 + lane : 0;
      const int ch = li < 32 ? chA : (li < 64 ? chA + 1 : chC);
      u64 pc; asm volatile("s_getpc_b64 %0" : "=s"(pc));
      const unsigned cl = (unsigned)((idx & 7) * 64 + lane) * 128u;
      const char* tb = w == 5 ? (const char*)(pc & ~127ull) : (const char*)ssmk;
      touch_lines(tb, w == 5 ? (cl < CODE_BYTES ? cl : 0u) : (unsigned)(ch * (S_ * 2) + (li & 31) * 128), lds0 + K1_PF_OFF + 16);
    }
    asm volatile("s_waitcnt vmcnt(4)" : "+v"(d[0]), "+v"(d[1]), "+v"(d[2])::"memory");
    STAMP(1, 9);
    f32x2 xa[3], xb2[3];
#pragma unroll
    for (int i = 0; i < 3; ++i) { xa[i] = unpk(d[i].x); xb2[i] = unpk(d[i].y); }
    const f32x2 s2 = (xa[0] + xb2[0]) + (xa[1] + xb2[1]) + (xa[2] + xb2[2]);
    const float mean = wave_sum(act ? s2.x + s2.y : 0.f) * (1.0f / 576.0f);
    f32x2 v2 = {0.f, 0.f};
#pragma unroll
    for (int i = 0; i < 3; ++i) { const f32x2 a = xa[i] - mean, b = xb2[i] - mean; v2 += a * a + b * b; }
    const float vs = wave_sum(act ? v2.x + v2.y : 0.f);
    const float mb = bfr(mean), vb = bfr(vs * (1.0f / 576.0f));
    const float sd = bfr(sqrtf(bfr(vb + 1e-5f)));
    const float rsd = 1.0f / sd;
#pragma unroll
    for (int i = 0; i < 3; ++i) {
      const f32x2 a = bfr2(xa[i] - mb) * rsd, b = bfr2(xb2[i] - mb) * rsd;
      u32x2 o; o.x = pk_bf16(a.x, a.y); o.y = pk_bf16(b.x, b.y);
      const int p8 = lane + 48 * i;
      if (act) *(LAS u32x2*)(smem + XG_OFF + ((p8 >> 1) * 8 + w) * 16 + (p8 & 1) * 8) = o;
    }
    if (tid == 0) *(LAS unsigned*)(smem + HCNT_OFF + 4) = 0u;
    asm volatile("s_waitcnt vmcnt(0)" : "+v"(kin), "+v"(vin), "+v"(ein)::"memory");
    make_tag();
    if (t0 == 0 && tid < 27) kin = (u32x4){0u, 0u, 0u, 0u};
    *(LAS u32x4*)(smem + KT_OFF + tid * 16) = kin;
    *(LAS u32x4*)(smem + VT_OFF + tid * 16) = vin;
    if (w < 3) *(LAS u32x4*)(smem + edst) = ein;
    WAIT_L(0);
    __builtin_amdgcn_s_barrier();
    CFENCE();
    {
      char* xo = (char*)xfrag + (size_t)((tok0 >> 5) * 36 * 64 + (tok0 & 31)) * 16;
#pragma unroll
      for (int it = 0; it < 2; ++it) {
        const int id = it ? 512 + lane : tid, p16 = id >> 3;
        if (it == 0 || w == 0) {
          const u32x4 v = *(LAS u32x4*)(smem + XG_OFF + id * 16);
          gstore16_sc1(xo + ((p16 >> 1) * 64 + (p16 & 1) * 32 + (id & 7)) * 16, v);
        }
      }
      if (lane == 0) __hip_atomic_store(stats + token, (u64)__float_as_uint(mb) | ((u64)__float_as_uint(rsd) << 32), __ATOMIC_RELAXED, __HIP_MEMORY_SCOPE_AGENT);
    }
    STAMP(1, 10);
    CFENCE();
#pragma unroll
    for (int n = 0; n < 5; ++n) piece_load(0, n);
#pragma unroll
    for (int n = 0; n < 5; ++n) piece_load(1, n);
    wait_vm<10>();
    STAMP(1, 11);
    if (lane == 0) {
      const unsigned old = __hip_atomic_fetch_add((LAS unsigned*)(smem + HCNT_OFF + 4), 1u, __ATOMIC_RELAXED, __HIP_MEMORY_SCOPE_WORKGROUP);
      if (old == 7u) set_flag(flg + FL_X + (tok0 >> 3), tag);
    }
    auto kv_compute = [&](int item) ALWAYS_INLINE {
      const int tl = item / 9, cg = item - tl * 9;
      u32x4 kr[4], w4[4];
#pragma unroll
      for (int i = 0; i < 4; ++i) { kr[i] = *(LAS u32x4*)(smem + KT_OFF + (item + 9 * i) * 16); w4[i] = *(LAS u32x4*)(smem + WT_OFF + cg * 64 + i * 16); }
      const u32x4 vr = *(LAS u32x4*)(smem + VT_OFF + item * 16);
#pragma unroll
      for (int e2 = 0; e2 < 4; ++e2) {
        const f32x2 w0 = {bf_lo(w4[e2].x), bf_lo(w4[e2].z)}, w1 = {bf_hi(w4[e2].x), bf_hi(w4[e2].z)};
        const f32x2 w2 = {bf_lo(w4[e2].y), bf_lo(w4[e2].w)}, w3 = {bf_hi(w4[e2].y), bf_hi(w4[e2].w)};
        f32x2 ks = bfr2(unpk(kr[0][e2]) * w3);
        ks = bfr2(ks + bfr2(unpk(kr[1][e2]) * w2));
        ks = bfr2(ks + bfr2(unpk(kr[2][e2]) * w1));
        ks = bfr2(ks + bfr2(unpk(kr[3][e2]) * w0));
        const f32x2 kv = ks * unpk(vr[e2]);
        const unsigned pk = pk_bf16(kv.x, kv.y);
        tile[(8 * cg + 2 * e2) * 72 + (63 - tl)] = (u16)(pk & 0xffffu);
        tile[(8 * cg + 2 * e2 + 1) * 72 + (63 - tl)] = (u16)(pk >> 16);
      }
    };
    kv_compute(tid);
    STAMP(1, 7);
    poll_flags<1>(flg + FL_X + mtile * 32 + w * 4, 4, tag, lane);
    wait_vm<0>();
    STAMP(1, 8);
    sfor<9>([&](auto ic) ALWAYS_INLINE { x_load(ic); });
    if (w == 7) kv_compute(512 + lane);
    WAIT_L(0);
    __builtin_amdgcn_s_barrier();
    CFENCE();
    {
      const int ch = tid >> 3, pp = tid & 7, ch2 = 64 + (lane >> 3);
      const u32x4 o1 = *(LAS u32x4*)(tile + ch * 72 + pp * 8), o2 = *(LAS u32x4*)(tile + ch2 * 72 + pp * 8);
      gstore16_sc1(kvr + (size_t)(c0 + ch) * S_ + (1984 - t0) + pp * 8, o1);
      gstore16_sc1(kvr + (size_t)(c0 + ch2) * S_ + (1984 - t0) + pp * 8, o2);
    }
  }

  const int kh = lane >> 5, prt = (lane >> 4) & 1, qq = (lane & 15) >> 2, p4 = lane & 3;
  const unsigned rbU = lds0 + (8 * kh + qq) * 128 + prt * K1_UOFF + ((((qq >> 1) & 1) | (prt << 1)) << 5) + p4 * 8;

  f32x16 acc[4];
  const f32x16 zero16 = {0.f, 0.f, 0.f, 0.f, 0.f, 0.f, 0.f, 0.f, 0.f, 0.f, 0.f, 0.f, 0.f, 0.f, 0.f, 0.f};

  const int token = mtile * 256 + w * 32 + (lane & 31);
  char* hb = (char*)hout + ((size_t)(token >> 4) * 144 * 64 + (lane >> 5) * 16 + (token & 15)) * 16;
  auto h_word = [&](const f32x16& a, int i) ALWAYS_INLINE -> unsigned {
    return pk_bf16(bfr(a[2 * i]) * bfr(a[2 * i + 8]), bfr(a[2 * i + 1]) * bfr(a[2 * i + 9]));
  };
  auto h_store = [&](const u32x4& o, int c16) ALWAYS_INLINE {
    gstore16_sc1(hb + ((size_t)(c16 >> 1) * 64 + (c16 & 1) * 32) * 16, o);
  };
  u32x4 ho;

  const char* hpA = (const char*)(ssmk + (size_t)(chA + (w >> 2)) * S_ + 64 * (w & 3));
  const char* hpC = (const char*)(ssmk + (size_t)chC * S_ + 32 * w);
  const bool zA = (w & 3) == 0, zC = w == 0;
  const char* pf_sk = (const char*)ssmk;
  const char* pf_wd = (const char*)wd + (size_t)(xcd >> 1) * 1152 * 1152;
  const char* kvb = (const char*)kvr;
  u32x4 caf[24];
  unsigned cax[24];
  bf16x8 cf[24];
  int cvo = 0, cvx = 0;
  auto conv_align = [&](auto ic) ALWAYS_INLINE {
    constexpr int i = ic;
    const unsigned sh = ((lane + 1) & 1) * 2;
    u32x4 r;
    r.x = __builtin_amdgcn_alignbyte(caf[i].y, caf[i].x, sh);
    r.y = __builtin_amdgcn_alignbyte(caf[i].z, caf[i].y, sh);
    r.z = __builtin_amdgcn_alignbyte(caf[i].w, caf[i].z, sh);
    r.w = __builtin_amdgcn_alignbyte(cax[i], caf[i].w, sh);
    cf[i] = __builtin_bit_cast(bf16x8, r);
    asm volatile("" : "+v"(cf[i]));
  };

  STAMP(1, 1);

  constexpr int PD = 3, MID = K1_MID;
  bf16x8 af[PD + 1];
  unsigned rb[4];
#pragma unroll
  for (int j = 0; j < 4; ++j) rb[j] = rbU ^ (j << 5);
#pragma unroll
  for (int g = 0; g < PD; ++g) af[g] = tr_frag((LAS char*)(uintptr_t)rb[g], 0, 512);
  sfor<324>([&](auto gc) ALWAYS_INLINE {
    constexpr int g = gc;
    constexpr int st = g / 36, f = g - st * 36;
    constexpr int s = st == 4 ? f : (st == 8 ? f % 9 : f / 4), c = st == 4 ? 0 : (st == 8 ? f / 9 : f % 4);
    constexpr int xi = st < 4 ? st * 9 + s : (st == 4 ? s : (st - 5) * 9 + s);
    constexpr int ai = st < 4 ? c : (st == 4 ? 0 : (c + 1) & 3);
    constexpr bool first = (st == 0 || st == 5) ? s == 0 : (st == 4 && f == 0);
    if constexpr (f == MID) {

      if constexpr (st < 8) wait_vm<k1_younger(k1_dkey(st + 1, 4), 4 * g)>();
      else { wait_vm<k1_younger(4 * (7 * 36 + MID + 1) + 1, 4 * g)>(); if (w <= 3) WAIT_L(0); }
      __builtin_amdgcn_s_barrier();
      CFENCE();

      if constexpr (st == 1) { if (tid == 0) set_flag(flg + FL_K + (bid & 7) * 32 + (bid >> 3), tag); }
      if constexpr (st == 6) {
        static_assert(k1_dkey(7, 4) > 4 * k1_sstep(4) + 3, "the stage 7 wait must cover the h stores of panels A and Q");
        if (tid == 0) set_flag(flg + FL_HE + (mtile * 4 + (cgrp >> 1)) * 8 + (cgrp & 1) * 4 + cq, tag);
      }
    }
    if constexpr (((f + 36 - MID) % 36) % 9 == 0) __builtin_amdgcn_s_setprio(3 - ((f + 36 - MID) % 36) / 9);
    sfor<35>([&](auto dc) ALWAYS_INLINE { if constexpr (k1_dkey(dc / 5 + 2, dc % 5) == 4 * g + 1) piece_load(dc / 5 + 2, dc % 5); });
    sfor<27>([&](auto ic) ALWAYS_INLINE { if constexpr (k1_xkey(ic + 9) == 4 * g + 2) x_load(std::integral_constant<int, ic + 9>{}); });
    if constexpr (g == 7 * 36 + MID + 1) {
      if (w < 3) {
        const int ch = (w == 2) ? chC : chA + w;
        const unsigned lv = lane * 16;
        poll_flags<1>(flg + FL_K + (grp >> 3) * 32, 32, tag, lane);
#pragma unroll
        for (int i = 0; i < 4; ++i) glds1_sc1(kvb + (size_t)ch * (S_ * 2) + i * 1024, lv, lds0 + XR_OFF + w * XR_STRIDE + i * 1024);
        if (lane < 4) glds1(pf_sk + (size_t)ch * (S_ * 2), lv, lds0 + HZ_OFF + w * 96 + 32);
      } else if (w == 3) {
        if (lane < 32) {
#pragma unroll
          for (int im = 0; im < 3; ++im) *(LAS u32x4*)(smem + XR_OFF + im * XR_STRIDE + 4096 + lane * 16) = (u32x4){0u, 0u, 0u, 0u};
        } else if (lane < 38) {
          const int im = (lane - 32) >> 1, hh = lane & 1;
          *(LAS u32x4*)(smem + HZ_OFF + im * 96 + hh * 16) = (u32x4){0u, 0u, 0u, 0u};
        } else if (lane == 38) *(LAS unsigned*)(smem + HCNT_OFF) = 0u;
      }
    }
    sfor<24>([&](auto ac) ALWAYS_INLINE {
      if constexpr (k1_astep(ac) == g) {
        asm volatile("s_waitcnt vmcnt(%2)" : "+v"(caf[ac]), "+v"(cax[ac]) : "i"(k1_younger(4 * k1_cstep(ac) + 2, 4 * g + 2)) : "memory");
        conv_align(ac);
      }
    });
    if constexpr (g == K1_TOUCH) {
      int l2 = lane; asm volatile("" : "+v"(l2));
      const unsigned line = (unsigned)(w * 64 + l2 < 324 ? idx * 324 + w * 64 + l2 : idx * 324);
      touch_lines(pf_wd, line * 128u, lds0 + K1_PF_OFF + 16);
    }
    sfor<24>([&](auto cc) ALWAYS_INLINE {
      constexpr int ci = cc;
      if constexpr (k1_cstep(ci) == g) {
        if constexpr (ci == 0) {
          int l2 = lane; asm volatile("" : "+v"(l2));
          const int e = (l2 & 15) - 15 + 8 * (l2 >> 4);
          cvo = (e & ~1) * 2; cvx = cvo + ((e & 1) ? 16 : 0);
        }
        constexpr int cb = ci < 16 ? 64 * (ci >> 3) + 512 * (ci & 7) : 512 * (ci - 16);
        const char* hp = (ci < 16 ? hpA : hpC) + cb + ((ci == 0 && zA) || (ci == 16 && zC) ? 32 : 0);
        gload16u(caf[ci], hp - 32, (unsigned)(cvo + 32));
        gload4u(cax[ci], hp - 32, (unsigned)(cvx + 32));
      }
    });
    if constexpr (g + PD < 324) {
      constexpr int g1 = g + PD, st1 = g1 / 36, f1 = g1 - st1 * 36;
      if constexpr (f1 == 0) {
        if constexpr (st1 != 4) {
          unsigned b2 = rbU; asm volatile("" : "+v"(b2));
#pragma unroll
          for (int j = 0; j < 4; ++j) { rb[j] = (b2 + (st1 % K1_NSLOT) * K1_SLOT) ^ (j << 5); asm volatile("" : "+v"(rb[j])); }
        } else {
          int l2 = lane; asm volatile("" : "+v"(l2));
          rb[0] = lds0 + (8 * (l2 >> 5) + ((l2 & 15) >> 2)) * 32 + ((l2 >> 4) & 1) * K1_UOFF + (l2 & 3) * 8 + (st1 % K1_NSLOT) * K1_SLOT;
          asm volatile("" : "+v"(rb[0]));
        }
      }
      if constexpr (st1 == 8) af[g1 % (PD + 1)] = tr_frag((LAS char*)(uintptr_t)rb[f1 / 9], (f1 % 9) * 2048, 512);
      else if constexpr (st1 != 4) af[g1 % (PD + 1)] = tr_frag((LAS char*)(uintptr_t)rb[f1 % 4], (f1 / 4) * 2048, 512);
      else af[g1 % (PD + 1)] = tr_frag((LAS char*)(uintptr_t)rb[0], f1 * 512, 128);
    }
    if constexpr (st < 4 && c == 0) {
      asm volatile("s_waitcnt vmcnt(%1)" : "+v"(xf[xi]) : "i"(k1_younger(k1_xkey(xi), 4 * g + 3)) : "memory");
    }
    SCHED0();
    acc[ai] = __builtin_amdgcn_mfma_f32_32x32x16_bf16(af[g % (PD + 1)], xf[xi], first ? zero16 : acc[ai], 0, 0, 0);
    SCHED0();
    if constexpr (g == 143) {
#pragma unroll
      for (int i = 0; i < 4; ++i) ho[i] = h_word(acc[0], i);
      h_store(ho, colA >> 4);
      SCHED0();
    }
    if constexpr (g > 144 && g <= 162) {
      constexpr int cc = (g - 145) / 6 + 1, pi = (g - 145) % 6;
      if constexpr (pi < 4) ho[pi] = h_word(acc[cc], pi);
      if constexpr (pi == 5) h_store(ho, (colA >> 4) + cc);
      SCHED0();
    }
    if constexpr (g >= 298 && (g - 298) % 9 < 5 && (g - 298) / 9 < 3) {
      constexpr int cc = (g - 298) / 9, pi = (g - 298) % 9;
      if constexpr (pi < 4) ho[pi] = h_word(acc[(cc + 1) & 3], pi);
      if constexpr (pi == 4) h_store(ho, (colA >> 4) + 4 + cc);
      SCHED0();
    }
    if constexpr (g >= 180 && g <= 182) {
      if constexpr (g == 180) { ho[0] = h_word(acc[0], 0); ho[1] = h_word(acc[0], 1); }
      if constexpr (g == 181) { ho[2] = h_word(acc[0], 2); ho[3] = h_word(acc[0], 3); }
      if constexpr (g == 182) h_store(ho, colQ >> 4);
      SCHED0();
    }
  });
  const char* wbase2 = (const char*)wd + (size_t)kbase * (D_ * 2);
  unsigned voff2[4];
  int lane2 = lane; asm volatile("" : "+v"(lane2));
#pragma unroll
  for (int n = 0; n < 4; ++n) {
    const int o = (tq * 4 + n) * 1024 + lane2 * 16;
    const int k = o >> 8, pc = (o >> 4) & 15;
    const int j = (pc >> 1) ^ (k & 7);
    voff2[n] = (unsigned)(k * (D_ * 2) + (2 * j + (pc & 1)) * 16);
  }
  const unsigned ldst2 = lds0 + khf * K2_HALF;
  auto piece_load2 = [&](int st, int n) ALWAYS_INLINE {
    const int slot = st % K2_NSLOT;
    if (st < 9) {
      const char* g = wbase2 + (size_t)k2_unit(st) * 64 * (D_ * 2) + colA2 * 2;
      glds1p<false>(g, voff2[n], ldst2 + slot * K2_SLOT + tq * 4096 + n * 1024);
    } else {
      int l2 = lane; asm volatile("" : "+v"(l2));
      const char* g = wbase2 + (size_t)(st - 9) * 288 * (D_ * 2) + colQ2 * 2;
      const int o = n < 2 ? tq * 2048 + n * 1024 + l2 * 16 : 8192 + tq * 256 + (l2 & 15) * 16;
      const unsigned v0 = (unsigned)((o >> 5) * (D_ * 2) + ((o >> 4) & 1) * 16);
      if (n < 2 || l2 < 16) glds1p<false>(g, v0, ldst2 + slot * K2_SLOT + (n < 2 ? tq * 2048 + n * 1024 : 8192 + tq * 256));
    }
  };
  const char* hbase = (const char*)hout + ((size_t)(mt * 8 + tq * 2) * 144 * 64 + (size_t)(kbase >> 5) * 64) * 16;
  const unsigned hv0 = lane2 * 16 + khf * (144 * 64 * 16), hv1 = lane2 * 16 + (1 - khf) * (144 * 64 * 16);
  bf16x8 hf[18][2];
  auto h_load = [&](auto lc) ALWAYS_INLINE {
    constexpr int k = 2 * k2_unit(lc / 4) + (lc / 2) % 2, t = lc % 2;
    gload16_sc1<(k & 3) * 1024>(hf[k][t], hbase + (k >> 2) * 4096, t ? hv1 : hv0);
  };
  STAMP(1, 2);
  __builtin_amdgcn_s_setprio(0);
  {
    u32x4 o;
#pragma unroll
    for (int i = 0; i < 4; ++i) o[i] = h_word(acc[0], i);
    h_store(o, (colA >> 4) + 7);
  }

  __builtin_amdgcn_s_barrier();
  CFENCE();
#pragma unroll
  for (int n = 0; n < 4; ++n) piece_load2(0, n);
#pragma unroll
  for (int n = 0; n < 4; ++n) piece_load2(1, n);

  STAMP(1, 3);
  const int ct0 = lane & 15, ckg = lane >> 4;
  {
    const int boff = (2032 - 16 * (lane & 15) + 8 * ckg) * 2;
    auto head_frag = [&](int im) ALWAYS_INLINE -> bf16x8 {
      bf16x8 r;
      LAS u16* hz = (LAS u16*)(smem + HZ_OFF + im * 96) + 16 + (ct0 - 15 + 8 * ckg);
#pragma unroll
      for (int j = 0; j < 8; ++j) r[j] = (short)hz[j];
      return r;
    };
    if (zA) cf[0] = head_frag(w >> 2);
    if (zC) cf[16] = head_frag(2);
    {
      const int im = w >> 2;
      LAS char* xr = smem + XR_OFF + im * XR_STRIDE + boff + 128 * (w & 3);
      f32x4 ac[8];
#pragma unroll
      for (int j = 0; j < 8; ++j) ac[j] = (f32x4){0.f, 0.f, 0.f, 0.f};
#pragma unroll
      for (int rr = 1; rr >= 0; --rr) {
        bf16x8 bfm[8];
#pragma unroll
        for (int b = 0; b < 8; ++b) bfm[b] = *(LAS bf16x8*)(xr - 512 * b + 64 * rr);
#pragma unroll
        for (int a = 7; a >= 0; --a)
#pragma unroll
          for (int j = a; j < 8; ++j) ac[j] = __builtin_amdgcn_mfma_f32_16x16x32_bf16(cf[rr * 8 + a], bfm[j - a], ac[j], 0, 0, 0);
        if (rr == 1) {
          SCHED0();
          wait_vm<8>();
          if (lane == 0) {
            const unsigned old = __hip_atomic_fetch_add((LAS unsigned*)(smem + HCNT_OFF), 1u, __ATOMIC_RELAXED, __HIP_MEMORY_SCOPE_WORKGROUP);
            if (old == 7u) set_flag(flg + FL_H + (mtile * 4 + (cgrp >> 1)) * 8 + (cgrp & 1) * 4 + cq, tag);
          }
          SCHED0();
        }
      }
#pragma unroll
      for (int j = 0; j < 8; ++j)
        *(LAS f32x4*)(smem + PART_OFF + w * 8192 + (j * 64 + lane) * 16) = ac[j];
    }
    poll_flags<1>(flg + FL_HE + ((mt >> 1) * 4 + ksl) * 8, 8, tag, lane);
    sfor<12>([&](auto kc) ALWAYS_INLINE { h_load(kc); });
    {
      LAS char* xr = smem + XR_OFF + 2 * XR_STRIDE + boff + 64 * w;
      const int jA = 7 - qd, jB = qd;
      bf16x8 b0[8], b1[4];
#pragma unroll
      for (int a = 0; a < 8; ++a) { const int bb = jA - a; b0[a] = *(LAS bf16x8*)(xr - 512 * (bb < 0 ? 0 : bb)); }
#pragma unroll
      for (int a = 0; a < 4; ++a) { const int bb = jB - a; b1[a] = *(LAS bf16x8*)(xr - 512 * (bb < 0 ? 0 : bb)); }
      f32x4 c0 = (f32x4){0.f, 0.f, 0.f, 0.f}, c1 = c0;
#pragma unroll
      for (int a = 7; a >= 0; --a) {
        if (a <= jA) c0 = __builtin_amdgcn_mfma_f32_16x16x32_bf16(cf[16 + a], b0[a], c0, 0, 0, 0);
        if (a < 4 && a <= jB) c1 = __builtin_amdgcn_mfma_f32_16x16x32_bf16(cf[16 + a], b1[a < 4 ? a : 0], c1, 0, 0, 0);
      }
      *(LAS f32x4*)(smem + PARTC_OFF + w * 2048 + lane * 16) = c0;
      *(LAS f32x4*)(smem + PARTC_OFF + w * 2048 + 1024 + lane * 16) = c1;
    }
  }
  STAMP(1, 4);
  WAIT_L(0);
  __builtin_amdgcn_s_barrier();
  CFENCE();
  STAMP(1, 5);
  auto finalize = [&](f32x4 s0, f32x4 s1, int im, int ch, int t8, float dd) ALWAYS_INLINE {
    const u32x4 kvv = *(LAS u32x4*)(smem + XR_OFF + im * XR_STRIDE + (2040 - t8) * 2);
    float y[8] = {s0[0], s0[1], s0[2], s0[3], s1[0], s1[1], s1[2], s1[3]};
    float o[8];
#pragma unroll
    for (int e = 0; e < 8; ++e) {
      const int ri = 7 - e;
      const float kvf = (ri & 1) ? bf_hi(kvv[ri >> 1]) : bf_lo(kvv[ri >> 1]);
      o[e] = bfr(y[e]) + bfr(kvf * dd);
    }
    u32x4 ov; ov.x = pk_bf16(o[0], o[1]); ov.y = pk_bf16(o[2], o[3]); ov.z = pk_bf16(o[4], o[5]); ov.w = pk_bf16(o[6], o[7]);
    gstore16_sc1(wT + (size_t)ch * S_ + t8, ov);
  };
  {
    const int im = tid >> 8, t8 = (tid & 255) * 8;
    const int po = ((t8 >> 8) * 64 + ((t8 >> 2) & 3) * 16 + ((t8 >> 4) & 15)) * 16;
    f32x4 s0 = (f32x4){0.f, 0.f, 0.f, 0.f}, s1 = s0;
#pragma unroll
    for (int ww = 0; ww < 4; ++ww) {
      s0 += *(LAS f32x4*)(smem + PART_OFF + (im * 4 + ww) * 8192 + po);
      s1 += *(LAS f32x4*)(smem + PART_OFF + (im * 4 + ww) * 8192 + po + 256);
    }
    finalize(s0, s1, im, chA + im, t8, ddA);
  }
  if (tid >= 448) {
    const int t2 = tid - 448;
    const int tl = t2 >> 5, l8 = (t2 & 31) * 8;
    const int jt = tl ? qd : 7 - qd, t8 = 256 * jt + l8;
    const int po = (((l8 >> 2) & 3) * 16 + (l8 >> 4)) * 16;
    f32x4 s0 = (f32x4){0.f, 0.f, 0.f, 0.f}, s1 = s0;
#pragma unroll
    for (int ww = 0; ww < 8; ++ww) {
      s0 += *(LAS f32x4*)(smem + PARTC_OFF + ww * 2048 + tl * 1024 + po);
      s1 += *(LAS f32x4*)(smem + PARTC_OFF + ww * 2048 + tl * 1024 + po + 256);
    }
    finalize(s0, s1, 2, chC, t8, ddC);
  }
  STAMP(1, 6);

  STAMP(2, 0);
  {
  const int kq = lane2 >> 4, qq = (lane2 & 15) >> 2, p4 = lane2 & 3;
  const unsigned rbU2 = lds0 + khf * K2_HALF + (16 * (kq >> 1) + 4 * (kq & 1) + qq) * 256 + ((4 * (kq & 1) + qq) << 5) + p4 * 8;

  const char* wtb = (const char*)(wT + mt * 128 + ksl * 32);
  f32x4 acc[2][8], accq[2];
  const f32x4 zero4 = {0.f, 0.f, 0.f, 0.f};

  const int tile = mt * 4 + nt;
  const __amdgpu_buffer_rsrc_t rs = __builtin_amdgcn_make_buffer_rsrc((char*)slab + (size_t)tile * (16 * 18432), 0, 16 * 18432, 0x00020000);
  const bool foreign = tq != ksl;
  auto give = [&](int xb, int i, const f32x4& a0, const f32x4& a1) ALWAYS_INLINE {
    *(LAS f32x4*)(smem + xb + ((w * 4 + i) * 64 + lane) * 16) = a1;
  };
  auto publish = [&](int xb, int i, const f32x4& a0, const f32x4& a1, int dtg) ALWAYS_INLINE {
    const f32x4 sm = a0 + *(LAS f32x4*)(smem + xb + (((w ^ 4) * 4 + i) * 64 + lane) * 16);
    if (foreign) __builtin_amdgcn_raw_buffer_store_b128(__builtin_bit_cast(u32x4, sm), rs, (unsigned)(((tq * 4 + ksl) * 18 + khf * 9 + dtg) * 1024 + lane * 16), 0, 16);
    else *(LAS f32x4*)(smem + K2_OWN_OFF + ((khf * 9 + dtg) * 64 + lane) * 16) = sm;
  };
  constexpr int XB1 = 3 * K2_SLOT, XB2 = 0, XB3 = 3 * K2_SLOT;

  asm volatile("s_waitcnt vmcnt(%4)" : "+v"(hf[0][0]), "+v"(hf[0][1]), "+v"(hf[1][0]), "+v"(hf[1][1])
               : "i"(k2_younger(k2_hkey(0, 3), 0, false)) : "memory");
  WAIT_L(0);
  __builtin_amdgcn_s_barrier();
  CFENCE();
  STAMP(2, 1);

  constexpr int PD = 2;
  bf16x8 af[PD + 1];
  unsigned sb = rbU2;
#pragma unroll
  for (int g = 0; g < PD; ++g) af[g] = tr_frag((LAS char*)(uintptr_t)(sb ^ (g << 5)), 0, 2048);
  sfor<162>([&](auto gc) ALWAYS_INLINE {
    constexpr int g = gc;
    constexpr int st = k2_stage(g), f = g - k2_g0(st);
    constexpr int ks = st < 9 ? k2_unit(st) * 2 + f / 8 : (st - 9) * 9 + f, dt = st < 9 ? f % 8 : 0;
    if constexpr (f == k2_mid(st)) {
      if constexpr (st < 8) { STAMP(0, 2 * st); }
      if constexpr (st == 2) { if (w == 0) poll_flags<1>(flg + FL_H + ((mt >> 1) * 4 + ksl) * 8, 8, tag, lane); }
      if constexpr (st == K2_WST) { if (w == 0) poll_flags<4>(flg + FL_W, 64, tag, lane); }
      if constexpr (st < 10) {
        constexpr int okey = cmax(k2_dkey(st + 1, k2_np(st + 1) - 1), st + 1 < 9 ? k2_hkey(st + 1, 3) : -1000);
        constexpr int n0 = k2_younger(okey, 4 * g, false);
        if constexpr (st == 0) { if (w == 7) wait_vm<n0 + 1>(); else wait_vm<n0>(); }
        else wait_vm<n0>();
        if constexpr (st + 1 < 9) {
          constexpr int k0 = 2 * k2_unit(st + 1);
          asm volatile("" : "+v"(hf[k0][0]), "+v"(hf[k0][1]), "+v"(hf[k0 + 1][0]), "+v"(hf[k0 + 1][1])::"memory");
        }
      }
      if constexpr (st >= 9) WAIT_L(0);
      __builtin_amdgcn_s_barrier();
      CFENCE();
      if constexpr (st < 8) { STAMP(0, 2 * st + 1); }
      if constexpr (st == 1) { if (tid == 0) set_flag(flg + FL_W + bid, tag); }
      if constexpr (st == K2_WST) {
        int l2 = lane; asm volatile("" : "+v"(l2));
        glds1_sc1(wtb, (unsigned)((colA2 + w * 16 + (l2 >> 2)) * (S_ * 2) + (l2 & 3) * 16), lds0 + K2_WT_OFF + w * 1024);
        glds1_sc1(wtb, (unsigned)((colQ2 + (l2 >> 2)) * (S_ * 2) + (l2 & 3) * 16), lds0 + K2_WT_OFF + 8192);
      }
      if constexpr (st == 9) {
#pragma unroll
        for (int i = 0; i < 4; ++i) publish(XB1, i, acc[0][i], acc[1][i], i);
#pragma unroll
        for (int i = 0; i < 4; ++i) give(XB2, i, acc[0][4 + i], acc[1][4 + i]);
      }
      if constexpr (st == 10) {
#pragma unroll
        for (int i = 0; i < 4; ++i) publish(XB2, i, acc[0][4 + i], acc[1][4 + i], 4 + i);
      }
    }
    if constexpr (st < 9 && ((f + 16 - 6) % 16) % 4 == 0) __builtin_amdgcn_s_setprio(3 - ((f + 16 - 6) % 16) / 4);
    sfor<36>([&](auto dc) ALWAYS_INLINE { if constexpr (dc % 4 < k2_np(dc / 4 + 2) && k2_dkey(dc / 4 + 2, dc % 4) == 4 * g + 2) piece_load2(dc / 4 + 2, dc % 4); });
    sfor<24>([&](auto hc) ALWAYS_INLINE { if constexpr (k2_hkey(hc / 4 + 3, hc % 4) == 4 * g + 3) h_load(std::integral_constant<int, hc + 12>{}); });
    if constexpr (g + PD < 162) {
      constexpr int g1 = g + PD, st1 = k2_stage(g1), f1 = g1 - k2_g0(st1);
      if constexpr (f1 == 0) {
        if constexpr (st1 < 9) {
          sb += (st1 % K2_NSLOT == 0) ? -3 * K2_SLOT : K2_SLOT;
        } else {
          int l2 = lane; asm volatile("" : "+v"(l2));
          const int kq2 = l2 >> 4;
          sb = lds0 + khf * K2_HALF + (16 * (kq2 >> 1) + 4 * (kq2 & 1) + ((l2 & 15) >> 2)) * 32 + (l2 & 3) * 8 + (st1 % K2_NSLOT) * K2_SLOT;
        }
      }
      asm volatile("" : "+v"(sb));
      if constexpr (st1 < 9) af[g1 % (PD + 1)] = tr_frag((LAS char*)(uintptr_t)(sb ^ ((f1 % 8) << 5)), (f1 / 8) * 8192, 2048);
      else af[g1 % (PD + 1)] = tr_frag((LAS char*)(uintptr_t)sb, f1 * 1024, 256);
    }
    SCHED0();
#pragma unroll
    for (int t = 0; t < 2; ++t) {
      if constexpr (st < 9) acc[t][dt] = __builtin_amdgcn_mfma_f32_16x16x32_bf16(af[g % (PD + 1)], hf[ks][t], g < 8 ? zero4 : acc[t][dt], 0, 0, 0);
      else accq[t] = __builtin_amdgcn_mfma_f32_16x16x32_bf16(af[g % (PD + 1)], hf[ks][t], g == 144 ? zero4 : accq[t], 0, 0, 0);
    }
    SCHED0();
    if constexpr (g == 143) {
#pragma unroll
      for (int i = 0; i < 4; ++i) give(XB1, i, acc[0][i], acc[1][i]);
      SCHED0();
    }
  });
  STAMP(2, 2);
  __builtin_amdgcn_s_setprio(0);
  give(XB3, 0, accq[0], accq[1]);

  int lane3 = lane; asm volatile("" : "+v"(lane3));
  u32x2 xq[3];
  float2 stt[2];
#pragma unroll
  for (int i = 0; i < 3; ++i) {
    const int t = i < 2 ? i : (w & 1);
    const int token = mt * 128 + ksl * 32 + t * 16 + (lane3 & 15);
    xq[i] = *(const u32x2*)(q + (size_t)token * D_ + (i < 2 ? colA2 + w * 16 : colQ2) + 4 * (lane3 >> 4));
    if (i < 2) {
      const u64 sv = __hip_atomic_load(stats + token, __ATOMIC_RELAXED, __HIP_MEMORY_SCOPE_AGENT);
      stt[i] = make_float2(__uint_as_float((unsigned)sv), __uint_as_float((unsigned)(sv >> 32)));
    }
  }
  WAIT_L(0);
  __builtin_amdgcn_s_barrier();
  CFENCE();
  publish(XB3, 0, accq[0], accq[1], 8);
  asm volatile("s_waitcnt vmcnt(0) lgkmcnt(0)" ::: "memory");
  __builtin_amdgcn_s_barrier();
  CFENCE();
  STAMP(2, 3);
  if (tid == 0) set_flag(flg + FL_S + tile * 4 + ksl, tag);
  poll_flags<1>(flg + FL_S + tile * 4, 4, tag, lane3);
  STAMP(2, 4);
  auto fetch = [&](f32x4 (&pv)[3], int fi) ALWAYS_INLINE {
#pragma unroll
    for (int k = 1; k < 4; ++k)
      pv[k - 1] = __builtin_bit_cast(f32x4, __builtin_amdgcn_raw_buffer_load_b128(rs, (unsigned)(((ksl * 4 + ((ksl + k) & 3)) * 18 + fi) * 1024 + lane3 * 16), 0, 16));
  };
  auto combine = [&](const f32x4 (&pv)[3], int t, int dt, const u32x2& xw2, const float2& st2) ALWAYS_INLINE {
    const int tl = t * 16 + (lane3 & 15), dl = dt * 16 + 4 * (lane3 >> 4);
    const f32x4 own = *(LAS f32x4*)(smem + K2_OWN_OFF + ((t * 9 + dt) * 64 + lane3) * 16);
    const f32x4 bs = ((own + pv[0]) + pv[1]) + pv[2];
    float o[4];
#pragma unroll
    for (int c = 0; c < 4; ++c) {
      const unsigned xw = (c < 2) ? xw2.x : xw2.y;
      const float x = (c & 1) ? bf_hi(xw) : bf_lo(xw);
      const float wy = bf2f(*(LAS u16*)(smem + K2_WT_OFF + (dl + c) * 64 + tl * 2));
      const float xn = bfr(bfr(x - st2.x) * st2.y);
      const float a = bfr(xn * wy);
      o[c] = bfr(x + a) + bfr(bs[c]);
    }
    u32x2 ov; ov.x = pk_bf16(o[0], o[1]); ov.y = pk_bf16(o[2], o[3]);
    *(u32x2*)(out + (size_t)(mt * 128 + ksl * 32 + tl) * D_ + (dt < 8 ? colA2 + dt * 16 : colQ2) + 4 * (lane3 >> 4)) = ov;
  };
  {
    f32x4 p0[3], p1[3], p2[3];
    fetch(p0, w); fetch(p1, 9 + w); fetch(p2, 9 * (w & 1) + 8);
    combine(p0, 0, w, xq[0], stt[0]);
    combine(p1, 1, w, xq[1], stt[1]);
    if (w < 2) combine(p2, w, 8, xq[2], w ? stt[1] : stt[0]);
  }
  if (bid == 0 && w == 0) {
    poll_flags<4>(flg + FL_K, 64, tag, lane);
    if (lane == 0) set_flag(flg + FL_GEN, tag);
  }
  STAMP(2, 5); RSTAMP(1, 15); STAMP_FLUSH(1); STAMP_FLUSH(2); STAMP_FLUSH(0);
  }
}

extern "C" void kernel_launch(void* const* d_in, const int* in_sizes, int n_in,
                              void* d_out, int out_size, void* d_ws, size_t ws_size,
                              hipStream_t stream) {
  const u16* q = (const u16*)d_in[0];
  const u16* k = (const u16*)d_in[1];
  const u16* v = (const u16*)d_in[2];
  const u16* shk = (const u16*)d_in[3];
  const u16* ssmk = (const u16*)d_in[4];
  const u16* ssmd = (const u16*)d_in[5];
  const u16* wg = (const u16*)d_in[7];
  const u16* wu = (const u16*)d_in[8];
  const u16* wd = (const u16*)d_in[9];
  (void)hipFuncSetAttribute((const void*)k_fused, hipFuncAttributeMaxDynamicSharedMemorySize, K1_LDS);
  k_fused<<<256, 512, K1_LDS, stream>>>(q, k, v, shk, ssmk, ssmd, wg, wu, wd, (char*)d_ws, (u16*)d_out);
}
```

```cpp
#include <hip/hip_runtime.h>
#include <hip/hip_bf16.h>
#include <stdint.h>
#include <utility>

typedef unsigned short u16;
typedef short bf16x8 __attribute__((ext_vector_type(8)));
typedef short s16x4 __attribute__((ext_vector_type(4)));
typedef float f32x4 __attribute__((ext_vector_type(4)));
typedef float f32x16 __attribute__((ext_vector_type(16)));
typedef unsigned u32x4 __attribute__((ext_vector_type(4)));
typedef unsigned u32x2 __attribute__((ext_vector_type(2)));
typedef __bf16 bf2_t __attribute__((ext_vector_type(2)));
#define LAS __attribute__((address_space(3)))
#define DI __device__ __forceinline__

constexpr int S_ = 2048, D_ = 576, F_ = 4608;

constexpr size_t WS_XFRAG = 0;
constexpr size_t WS_STATS = WS_XFRAG + (size_t)S_ * D_ * 2;
constexpr size_t WS_KVR   = WS_STATS + (size_t)S_ * 8;
constexpr size_t WS_WT    = WS_KVR + (size_t)S_ * D_ * 2;
constexpr size_t WS_H     = WS_WT + (size_t)S_ * D_ * 2;
constexpr size_t WS_SLAB  = WS_H + (size_t)S_ * F_ * 2;
constexpr size_t WS_FLG   = WS_SLAB + (size_t)4 * S_ * D_ * 4;
constexpr int FL_GEN = 0, FL_X = 32, FL_K = FL_X + 256, FL_H = FL_K + 256, FL_HE = FL_H + 256, FL_W = FL_HE + 256, FL_S = FL_W + 256, FL_END = FL_S + 256;
constexpr size_t WS_END   = WS_FLG + FL_END * 8;
typedef unsigned long long u64;

DI float bf_lo(unsigned w) { return __uint_as_float(w << 16); }
DI float bf_hi(unsigned w) { return __uint_as_float(w & 0xffff0000u); }
DI float bf2f(u16 b) { return __uint_as_float(((unsigned)b) << 16); }
DI float bfr(float x) { return (float)(__bf16)x; }
typedef float f32x2 __attribute__((ext_vector_type(2)));
DI unsigned pk_bf16(float lo, float hi);
DI f32x2 unpk(unsigned w) { return (f32x2){__uint_as_float(w << 16), __uint_as_float(w & 0xffff0000u)}; }
DI f32x2 bfr2(f32x2 v) { return unpk(pk_bf16(v.x, v.y)); }
DI unsigned pk_bf16(float lo, float hi) { bf2_t v = {(__bf16)lo, (__bf16)hi}; return __builtin_bit_cast(unsigned, v); }

DI float wave_sum(float x) {
  x += __builtin_bit_cast(float, __builtin_amdgcn_update_dpp(0, __builtin_bit_cast(int, x), 0xB1, 0xf, 0xf, true));
  x += __builtin_bit_cast(float, __builtin_amdgcn_update_dpp(0, __builtin_bit_cast(int, x), 0x4E, 0xf, 0xf, true));
  x += __builtin_bit_cast(float, __builtin_amdgcn_update_dpp(0, __builtin_bit_cast(int, x), 0x141, 0xf, 0xf, true));
  x += __builtin_bit_cast(float, __builtin_amdgcn_update_dpp(0, __builtin_bit_cast(int, x), 0x140, 0xf, 0xf, true));
  const int xi = __builtin_bit_cast(int, x);
  return (__builtin_bit_cast(float, __builtin_amdgcn_readlane(xi, 0)) + __builtin_bit_cast(float, __builtin_amdgcn_readlane(xi, 16))) +
         (__builtin_bit_cast(float, __builtin_amdgcn_readlane(xi, 32)) + __builtin_bit_cast(float, __builtin_amdgcn_readlane(xi, 48)));
}
#define WAIT_V(n) asm volatile("s_waitcnt vmcnt(" #n ")" ::: "memory")
#define WAIT_L(n) asm volatile("s_waitcnt lgkmcnt(" #n ")" ::: "memory")
#define CFENCE() asm volatile("" ::: "memory")
#define STAMP(kid, i)
#define RSTAMP(kid, i)
#define STAMP_DECL
#define STAMP_FLUSH(kid)
#define K0STAMP(i)
#define SCHED0() __builtin_amdgcn_sched_barrier(0)
template <int... I, class Fn> DI void sfor_impl(std::integer_sequence<int, I...>, Fn&& f) { (f(std::integral_constant<int, I>{}), ...); }
template <int N, class Fn> DI void sfor(Fn&& f) { sfor_impl(std::make_integer_sequence<int, N>{}, f); }
#define ALWAYS_INLINE __attribute__((always_inline))
template <int N> DI void wait_vm() { asm volatile("s_waitcnt vmcnt(%0)" ::"i"(N) : "memory"); }

DI void glds4(const void* sbase, unsigned v0, unsigned v1, unsigned v2, unsigned v3, unsigned lds_dst) {
  unsigned keep;
  asm volatile("s_mov_b32 %0, m0\n\ts_mov_b32 m0, %6\n\ts_nop 0\n\t"
               "global_load_lds_dwordx4 %1, %5\n\t"
               "global_load_lds_dwordx4 %2, %5 offset:1024\n\t"
               "global_load_lds_dwordx4 %3, %5 offset:2048\n\t"
               "global_load_lds_dwordx4 %4, %5 offset:3072\n\t"
               "s_mov_b32 m0, %0"
               : "=&s"(keep) : "v"(v0), "v"(v1), "v"(v2), "v"(v3), "s"(sbase), "s"(lds_dst) : "memory");
}
DI void glds3(const void* sbase, unsigned v0, unsigned v1, unsigned v2, unsigned lds_dst) {
  unsigned keep;
  asm volatile("s_mov_b32 %0, m0\n\ts_mov_b32 m0, %5\n\ts_nop 0\n\t"
               "global_load_lds_dwordx4 %1, %4\n\t"
               "global_load_lds_dwordx4 %2, %4 offset:1024\n\t"
               "global_load_lds_dwordx4 %3, %4 offset:2048\n\t"
               "s_mov_b32 m0, %0"
               : "=&s"(keep) : "v"(v0), "v"(v1), "v"(v2), "s"(sbase), "s"(lds_dst) : "memory");
}
DI void glds2(const void* sbase, unsigned v0, unsigned v1, unsigned lds_dst) {
  unsigned keep;
  asm volatile("s_mov_b32 %0, m0\n\ts_mov_b32 m0, %4\n\ts_nop 0\n\t"
               "global_load_lds_dwordx4 %1, %3\n\t"
               "global_load_lds_dwordx4 %2, %3 offset:1024\n\t"
               "s_mov_b32 m0, %0"
               : "=&s"(keep) : "v"(v0), "v"(v1), "s"(sbase), "s"(lds_dst) : "memory");
}
DI void glds1(const void* sbase, unsigned v0, unsigned lds_dst) {
  unsigned keep;
  asm volatile("s_mov_b32 %0, m0\n\ts_mov_b32 m0, %3\n\ts_nop 0\n\t"
               "global_load_lds_dwordx4 %1, %2\n\t"
               "s_mov_b32 m0, %0"
               : "=&s"(keep) : "v"(v0), "s"(sbase), "s"(lds_dst) : "memory");
}

template <bool NT> DI void glds1p(const void* sbase, unsigned v0, unsigned lds_dst) {
  unsigned keep;
  if constexpr (NT)
    asm volatile("s_mov_b32 %0, m0\n\ts_mov_b32 m0, %3\n\ts_nop 0\n\tglobal_load_lds_dwordx4 %1, %2 nt\n\ts_mov_b32 m0, %0"
                 : "=&s"(keep) : "v"(v0), "s"(sbase), "s"(lds_dst) : "memory");
  else
    asm volatile("s_mov_b32 %0, m0\n\ts_mov_b32 m0, %3\n\ts_nop 0\n\tglobal_load_lds_dwordx4 %1, %2\n\ts_mov_b32 m0, %0"
                 : "=&s"(keep) : "v"(v0), "s"(sbase), "s"(lds_dst) : "memory");
}
DI void glds1_sc1(const void* sbase, unsigned v0, unsigned lds_dst) {
  unsigned keep;
  asm volatile("s_mov_b32 %0, m0\n\ts_mov_b32 m0, %3\n\ts_nop 0\n\tglobal_load_lds_dwordx4 %1, %2 sc1\n\ts_mov_b32 m0, %0"
               : "=&s"(keep) : "v"(v0), "s"(sbase), "s"(lds_dst) : "memory");
}
DI void touch_lines(const void* sbase, unsigned v0, unsigned lds_dst) {
  unsigned keep;
  asm volatile("s_mov_b32 %0, m0\n\ts_mov_b32 m0, %3\n\ts_nop 0\n\tglobal_load_lds_dword %1, %2\n\ts_mov_b32 m0, %0"
               : "=&s"(keep) : "v"(v0), "s"(sbase), "s"(lds_dst) : "memory");
}
template <int OFF> DI void gload16(bf16x8& dst, const void* sbase, unsigned voff) {
  asm volatile("global_load_dwordx4 %0, %1, %2 offset:%3" : "=v"(dst) : "v"(voff), "s"(sbase), "i"(OFF) : "memory");
}
template <int OFF> DI void gload16_sc1(bf16x8& dst, const void* sbase, unsigned voff) {
  asm volatile("global_load_dwordx4 %0, %1, %2 offset:%3 sc1" : "=v"(dst) : "v"(voff), "s"(sbase), "i"(OFF) : "memory");
}
DI void gload16u(u32x4& dst, const void* sbase, unsigned voff) {
  asm volatile("global_load_dwordx4 %0, %1, %2" : "=v"(dst) : "v"(voff), "s"(sbase) : "memory");
}
template <int OFF> DI void gstore16_sc1_s(const void* sbase, unsigned voff, const u32x4& v) {
  asm volatile("global_store_dwordx4 %0, %1, %2 offset:%3 sc1\n\ts_nop 1" ::"v"(voff), "v"(v), "s"(sbase), "i"(OFF) : "memory");
}
DI void gload4u(unsigned& dst, const void* sbase, unsigned voff) {
  asm volatile("global_load_dword %0, %1, %2" : "=v"(dst) : "v"(voff), "s"(sbase) : "memory");
}
DI void gstore16_sc1(void* p, const u32x4& v) { asm volatile("global_store_dwordx4 %0, %1, off sc1\n\ts_nop 1" ::"v"(p), "v"(v) : "memory"); }
DI void set_flag(u64* f, u64 tag) { __hip_atomic_store(f, tag, __ATOMIC_RELAXED, __HIP_MEMORY_SCOPE_AGENT); }
template <int PER> DI void poll_flags(u64* f, int n, u64 tag, int lane) {
  asm volatile("" : "+v"(lane));
  const int i = lane < n ? lane : 0;
  for (unsigned spins = 0; spins < (1u << 20); ++spins) {
    bool ok = true;
#pragma unroll
    for (int k = 0; k < PER; ++k) ok &= __hip_atomic_load(f + i + 64 * k, __ATOMIC_RELAXED, __HIP_MEMORY_SCOPE_AGENT) == tag;
    if (__all(ok)) break;
    __builtin_amdgcn_s_sleep(1);
  }
}

constexpr int K1_SLOT = 37120;
constexpr int K1_UOFF = 18432 + 128;
constexpr int K1_NSLOT = 4;
constexpr int K1_PF_OFF = K1_NSLOT * K1_SLOT;
constexpr int XR_OFF = K1_PF_OFF + 288, XR_STRIDE = 4608;
constexpr int HZ_OFF = XR_OFF + 3 * XR_STRIDE;
constexpr int K1_LDS = HZ_OFF + 288;
constexpr int PART_OFF = 65536;
constexpr int PARTC_OFF = PART_OFF + 65536;
constexpr int HCNT_OFF = K1_PF_OFF;
constexpr int XG_OFF = 3 * K1_SLOT;

DI bf16x8 tr_frag(LAS char* p, int off, int hi_off) {
  s16x4 lo = __builtin_amdgcn_ds_read_tr16_b64_v4i16((LAS s16x4*)(p + off));
  s16x4 hi = __builtin_amdgcn_ds_read_tr16_b64_v4i16((LAS s16x4*)(p + off + hi_off));
  return __builtin_shufflevector(lo, hi, 0, 1, 2, 3, 4, 5, 6, 7);
}

constexpr int K1_MID = 16;
constexpr int k1_xkey(int i) { return i < 9 ? -100 + i : 4 * (1 + 2 * (i - 9)) + 2; }
constexpr int k1_dstep(int d, int n) { return d == 2 ? 2 + 4 * n : d == 3 ? 24 + 4 * n : d == 4 ? 58 + 6 * n : d == 5 ? 94 + 6 * n : 36 * (d - 3) + K1_MID + 6 * n; }
constexpr int k1_dkey(int d, int n) { return d < 2 ? -400 + 10 * d + n : 4 * k1_dstep(d, n) + 1; }
constexpr int k1_cstep(int i) { return i < 7 ? 218 + 4 * i : (i < 14 ? 254 + 4 * (i - 7) : (i < 21 ? 289 + (i - 14) : 304 + 2 * (i - 21))); }
constexpr int k1_sstep(int c) { return c == 0 ? 143 : (c < 4 ? 144 + 6 * c : (c == 4 ? 182 : 302 + 9 * (c - 5))); }
constexpr int k1_astep(int i) { return i < 14 ? k1_cstep(i) + 28 : (i < 21 ? k1_cstep(i) + 20 : 321 + (i - 21)); }
constexpr int k1_tstep(int i) { return 36 * (2 + i) + K1_MID + 1; }
constexpr int k1_younger(int okey, int wkey) {
  int n = 0;
  for (int i = 0; i < 6; ++i) if (4 * k1_tstep(i) + 2 > okey && 4 * k1_tstep(i) + 2 < wkey) n += 1;
  if (-70 > okey && -70 < wkey) n += 2;
  for (int i = 0; i < 36; ++i) if (k1_xkey(i) > okey && k1_xkey(i) < wkey) n += 1;
  for (int d = 0; d < 9; ++d) for (int pc = 0; pc < 5; ++pc) if (k1_dkey(d, pc) > okey && k1_dkey(d, pc) < wkey) n += 1;
  for (int i = 0; i < 24; ++i) if (4 * k1_cstep(i) + 2 > okey && 4 * k1_cstep(i) + 2 < wkey) n += 2;
  for (int c = 0; c < 8; ++c) if (4 * k1_sstep(c) + 3 > okey && 4 * k1_sstep(c) + 3 < wkey) n += 1;
  return n;
}
#define HSTORE 1

constexpr int K2_HALF = 16384;
constexpr int K2_SLOT = 2 * K2_HALF;
constexpr int K2_NSLOT = 4;
constexpr int K2_OWN_OFF = K2_NSLOT * K2_SLOT;
constexpr int K2_WT_OFF = K2_OWN_OFF + 18432;
constexpr int k2_g0(int st) { return st <= 9 ? 16 * st : 153; }
constexpr int k2_stage(int g) { return g < 144 ? g / 16 : (g < 153 ? 9 : 10); }
constexpr int k2_mid(int st) { return st < 9 ? 6 : 4; }
constexpr int k2_unit(int st) { return st < 5 ? 2 * st : 2 * (st - 5) + 1; }
constexpr int k2_np(int d) { return d < 9 ? 4 : 3; }
constexpr int k2_dkey(int d, int n) { return d < 2 ? -300 + 10 * d + n : (d == 2 ? 4 * (1 + n) + 2 : 4 * (k2_g0(d - 3) + k2_mid(d - 3) + 4 * n) + 2); }
constexpr int k2_hkey(int j, int m) { return j < 3 ? -250 + 10 * j + m : 4 * (k2_g0(j - 3) + k2_mid(j - 3) + 2 + 4 * m) + 3; }
constexpr int k2_skey(int p) { return 4 * (k2_g0(9 + p) + k2_mid(9 + p)) + 1; }
constexpr int K2_WST = 7;
constexpr int k2_wkey() { return 4 * (k2_g0(K2_WST) + k2_mid(K2_WST)) + 1; }
constexpr int k2_younger(int okey, int wkey, bool stores) {
  int n = 0;
  if (k2_wkey() > okey && k2_wkey() < wkey) n += 2;
  if (-100 > okey && -100 < wkey) n += 1;
  for (int d = 0; d < 11; ++d) for (int pc = 0; pc < k2_np(d); ++pc) if (k2_dkey(d, pc) > okey && k2_dkey(d, pc) < wkey) n += 1;
  for (int j = 0; j < 9; ++j) for (int m = 0; m < 4; ++m) if (k2_hkey(j, m) > okey && k2_hkey(j, m) < wkey) n += 1;
  if (stores) for (int p = 0; p < 2; ++p) if (k2_skey(p) > okey && k2_skey(p) < wkey) n += 4;
  return n;
}
constexpr unsigned CODE_BYTES = 42 * 1024;
constexpr int cmax(int a, int b) { return a > b ? a : b; }

__global__ __launch_bounds__(512, 2) void k_fused(const u16* __restrict__ q, const u16* __restrict__ kk, const u16* __restrict__ vv,
                                                  const u16* __restrict__ shk, const u16* __restrict__ ssmk, const u16* __restrict__ ssmd,
                                                  const u16* __restrict__ wg, const u16* __restrict__ wu, const u16* __restrict__ wd,
                                                  char* ws, u16* __restrict__ out) {
  u16* xfrag = (u16*)(ws + WS_XFRAG);
  u64* stats = (u64*)(ws + WS_STATS);
  u16* kvr = (u16*)(ws + WS_KVR);
  u16* wT = (u16*)(ws + WS_WT);
  u16* hout = (u16*)(ws + WS_H);
  float* slab = (float*)(ws + WS_SLAB);
  u64* flg = (u64*)(ws + WS_FLG);
  extern __shared__ __attribute__((aligned(16))) char smem_g[];
  LAS char* smem = (LAS char*)smem_g;
  const unsigned lds0 = (unsigned)(uintptr_t)smem;
  const int bid = blockIdx.x, tid = threadIdx.x;
  const int w = __builtin_amdgcn_readfirstlane(tid >> 6), lane = tid & 63;
  const int xcd = bid & 7, idx = bid >> 3;
  STAMP_DECL; RSTAMP(1, 14); STAMP(1, 0);
  const u64 genv = __hip_atomic_load(flg + FL_GEN, __ATOMIC_RELAXED, __HIP_MEMORY_SCOPE_AGENT);
  u64 tag;
  auto make_tag = [&]() ALWAYS_INLINE {
    tag = (((u64)(unsigned)__builtin_amdgcn_readfirstlane((unsigned)(genv >> 32)) << 32) | (unsigned)__builtin_amdgcn_readfirstlane((unsigned)genv)) + 1ull;
  };
  const int ksl = xcd >> 1, mt = (xcd & 1) * 8 + (idx >> 2), nt = idx & 3;
  const int tq = w & 3, khf = w >> 2;
  const int kbase = ksl * 1152 + khf * 576;
  const int colA2 = 128 * nt, colQ2 = 512 + 16 * nt;
  const int mtile = (xcd & 1) * 4 + (idx & 3);
  const int cgrp = (xcd >> 1) * 2 + (idx >> 4), cq = (idx >> 2) & 3;
  const int colA = 64 * (9 * cgrp + 2 * cq), colQ = 64 * (9 * cgrp + 8) + 16 * cq;

  const int grp = bid >> 2, qd = bid & 3;
  const int chA = grp * 9 + 2 * qd, chC = grp * 9 + 8;
  float ddA, ddC;
  {
    const int c1 = chA + (w >> 2);
    const unsigned d1 = ((const unsigned*)ssmd)[c1 >> 1], d2 = ((const unsigned*)ssmd)[chC >> 1];
    ddA = (c1 & 1) ? bf_hi(d1) : bf_lo(d1);
    ddC = (chC & 1) ? bf_hi(d2) : bf_lo(d2);
  }

  const int wq = w & 3, part = w >> 2;
  const char* wbase = (const char*)(part ? wu : wg);
  unsigned voff[5];
#pragma unroll
  for (int n = 0; n < 5; ++n) {
    const int o = (n < 4) ? ((wq * 4 + n) * 1024 + lane * 16) : (16384 + wq * 512 + (lane & 31) * 16);
    const int k = o >> 7, pc = (o >> 4) & 7;
    const int j = (pc >> 1) ^ (((k >> 1) & 1) | (part << 1));
    voff[n] = (unsigned)(k * (F_ * 2) + (2 * j + (pc & 1)) * 16);
  }
  const unsigned ldst = lds0 + part * K1_UOFF + wq * 4096;
  const unsigned ldst4 = lds0 + part * K1_UOFF + 16384 + wq * 512;

  auto piece_load = [&](int st, int n) ALWAYS_INLINE {
    const int slot = st % K1_NSLOT;
    const unsigned dst = (n < 4 ? ldst + n * 1024 : ldst4) + slot * K1_SLOT;
    if (st != 4) {
      const int ks = st < 4 ? st : st - 5;
      const char* g = wbase + (size_t)ks * 144 * (F_ * 2) + (colA + (st > 4 ? 64 : 0)) * 2;
      if (n < 4 || lane < 32) glds1p<false>(g, voff[n], dst);
    } else {
      int l2 = lane; asm volatile("" : "+v"(l2));
      const int o = (n < 4) ? ((wq * 4 + n) * 1024 + l2 * 16) : (16384 + wq * 512 + (l2 & 31) * 16);
      const unsigned vq = (unsigned)((o >> 5) * (F_ * 2) + ((o >> 4) & 1) * 16);
      if (n < 4 || lane < 32) glds1p<false>(wbase + colQ * 2, vq, dst);
    }
  };

  bf16x8 xf[36];
  const char* xb = (const char*)xfrag + (size_t)(mtile * 8 + w) * 36 * 1024;
  const unsigned xv = lane * 16;
  auto x_load = [&](auto ic) ALWAYS_INLINE {
    constexpr int i = ic;
    gload16_sc1<(i & 3) * 1024>(xf[i], xb + (i >> 2) * 4096, xv);
  };
  {
    LAS u16* tile = (LAS u16*)(smem + XR_OFF);
    constexpr int KT_OFF = 2 * K1_SLOT, VT_OFF = KT_OFF + 9728, WT_OFF = VT_OFF + 9216;
    const int tt = bid >> 3, ct = bid & 7, t0 = tt * 64, c0 = ct * 72;
    const int tok0 = mtile * 256 + (cgrp * 4 + cq) * 8, token = tok0 + w;
    const bool act = lane < 48;
    u32x2 d[3];
    u32x4 kin, vin, ein;
#pragma unroll
    for (int i = 0; i < 3; ++i)
      asm volatile("global_load_dwordx2 %0, %1, %2 offset:%3" : "=v"(d[i]) : "v"((unsigned)(token * (D_ * 2) + (act ? lane : 0) * 8)), "s"(q), "i"(i * 384) : "memory");
    auto k_off = [&](int pc) ALWAYS_INLINE -> unsigned { const int r = pc / 9, cg = pc - r * 9, ts = t0 - 3 + r; return (unsigned)((ts < 0 ? 0 : ts) * (D_ * 2) + (c0 + 8 * cg) * 2); };
    auto v_off = [&](int pc) ALWAYS_INLINE -> unsigned { const int r = pc / 9, cg = pc - r * 9; return (unsigned)((t0 + r) * (D_ * 2) + (c0 + 8 * cg) * 2); };
    gload16u(kin, kk, k_off(tid));
    gload16u(vin, vv, v_off(tid));
    int edst;
    {
      const int kp = w == 0 ? 512 + lane : 576 + (lane < 27 ? lane : 0);
      const int wl = lane < 63 ? lane - 27 : 35;
      const bool isw = w == 1 && lane >= 27, isv = w >= 2;
      const char* src = isv ? (const char*)vv + v_off(512 + lane) : (isw ? (const char*)shk + c0 * 8 + wl * 16 : (const char*)kk + k_off(kp));
      edst = isv ? VT_OFF + (512 + lane) * 16 : (isw ? WT_OFF + wl * 16 : KT_OFF + kp * 16);
      asm volatile("global_load_dwordx4 %0, %1, off" : "=v"(ein) : "v"(src) : "memory");
    }
    {
      const int li = (w == 3 || w == 4) ? (w & 1) * 64 + lane : 0;
      const int ch = li < 32 ? chA : (li < 64 ? chA + 1 : chC);
      u64 pc; asm volatile("s_getpc_b64 %0" : "=s"(pc));
      const unsigned cl = (unsigned)((idx & 7) * 64 + lane) * 128u;
      const char* tb = w == 5 ? (const char*)(pc & ~127ull) : (const char*)ssmk;
      touch_lines(tb, w == 5 ? (cl < CODE_BYTES ? cl : 0u) : (unsigned)(ch * (S_ * 2) + (li & 31) * 128), lds0 + K1_PF_OFF + 16);
    }
    asm volatile("s_waitcnt vmcnt(4)" : "+v"(d[0]), "+v"(d[1]), "+v"(d[2])::"memory");
    STAMP(1, 9);
    f32x2 xa[3], xb2[3];
#pragma unroll
    for (int i = 0; i < 3; ++i) { xa[i] = unpk(d[i].x); xb2[i] = unpk(d[i].y); }
    const f32x2 s2 = (xa[0] + xb2[0]) + (xa[1] + xb2[1]) + (xa[2] + xb2[2]);
    const float mean = wave_sum(act ? s2.x + s2.y : 0.f) * (1.0f / 576.0f);
    f32x2 v2 = {0.f, 0.f};
#pragma unroll
    for (int i = 0; i < 3; ++i) { const f32x2 a = xa[i] - mean, b = xb2[i] - mean; v2 += a * a + b * b; }
    const float vs = wave_sum(act ? v2.x + v2.y : 0.f);
    const float mb = bfr(mean), vb = bfr(vs * (1.0f / 576.0f));
    const float sd = bfr(sqrtf(bfr(vb + 1e-5f)));
    const float rsd = 1.0f / sd;
#pragma unroll
    for (int i = 0; i < 3; ++i) {
      const f32x2 a = bfr2(xa[i] - mb) * rsd, b = bfr2(xb2[i] - mb) * rsd;
      u32x2 o; o.x = pk_bf16(a.x, a.y); o.y = pk_bf16(b.x, b.y);
      const int p8 = lane + 48 * i;
      if (act) *(LAS u32x2*)(smem + XG_OFF + ((p8 >> 1) * 8 + w) * 16 + (p8 & 1) * 8) = o;
    }
    if (tid == 0) *(LAS unsigned*)(smem + HCNT_OFF + 4) = 0u;
    asm volatile("s_waitcnt vmcnt(0)" : "+v"(kin), "+v"(vin), "+v"(ein)::"memory");
    make_tag();
    if (t0 == 0 && tid < 27) kin = (u32x4){0u, 0u, 0u, 0u};
    *(LAS u32x4*)(smem + KT_OFF + tid * 16) = kin;
    *(LAS u32x4*)(smem + VT_OFF + tid * 16) = vin;
    if (w < 3) *(LAS u32x4*)(smem + edst) = ein;
    WAIT_L(0);
    __builtin_amdgcn_s_barrier();
    CFENCE();
    {
      char* xo = (char*)xfrag + (size_t)((tok0 >> 5) * 36 * 64 + (tok0 & 31)) * 16;
#pragma unroll
      for (int it = 0; it < 2; ++it) {
        const int id = it ? 512 + lane : tid, p16 = id >> 3;
        if (it == 0 || w == 0) {
          const u32x4 v = *(LAS u32x4*)(smem + XG_OFF + id * 16);
          gstore16_sc1(xo + ((p16 >> 1) * 64 + (p16 & 1) * 32 + (id & 7)) * 16, v);
        }
      }
      if (lane == 0) __hip_atomic_store(stats + token, (u64)__float_as_uint(mb) | ((u64)__float_as_uint(rsd) << 32), __ATOMIC_RELAXED, __HIP_MEMORY_SCOPE_AGENT);
    }
    STAMP(1, 10);
    CFENCE();
#pragma unroll
    for (int n = 0; n < 5; ++n) piece_load(0, n);
#pragma unroll
    for (int n = 0; n < 5; ++n) piece_load(1, n);
    wait_vm<10>();
    STAMP(1, 11);
    if (lane == 0) {
      const unsigned old = __hip_atomic_fetch_add((LAS unsigned*)(smem + HCNT_OFF + 4), 1u, __ATOMIC_RELAXED, __HIP_MEMORY_SCOPE_WORKGROUP);
      if (old == 7u) set_flag(flg + FL_X + (tok0 >> 3), tag);
    }
    auto kv_compute = [&](int item) ALWAYS_INLINE {
      const int tl = item / 9, cg = item - tl * 9;
      u32x4 kr[4], w4[4];
#pragma unroll
      for (int i = 0; i < 4; ++i) { kr[i] = *(LAS u32x4*)(smem + KT_OFF + (item + 9 * i) * 16); w4[i] = *(LAS u32x4*)(smem + WT_OFF + cg * 64 + i * 16); }
      const u32x4 vr = *(LAS u32x4*)(smem + VT_OFF + item * 16);
#pragma unroll
      for (int e2 = 0; e2 < 4; ++e2) {
        const f32x2 w0 = {bf_lo(w4[e2].x), bf_lo(w4[e2].z)}, w1 = {bf_hi(w4[e2].x), bf_hi(w4[e2].z)};
        const f32x2 w2 = {bf_lo(w4[e2].y), bf_lo(w4[e2].w)}, w3 = {bf_hi(w4[e2].y), bf_hi(w4[e2].w)};
        f32x2 ks = bfr2(unpk(kr[0][e2]) * w3);
        ks = bfr2(ks + bfr2(unpk(kr[1][e2]) * w2));
        ks = bfr2(ks + bfr2(unpk(kr[2][e2]) * w1));
        ks = bfr2(ks + bfr2(unpk(kr[3][e2]) * w0));
        const f32x2 kv = ks * unpk(vr[e2]);
        const unsigned pk = pk_bf16(kv.x, kv.y);
        tile[(8 * cg + 2 * e2) * 72 + (63 - tl)] = (u16)(pk & 0xffffu);
        tile[(8 * cg + 2 * e2 + 1) * 72 + (63 - tl)] = (u16)(pk >> 16);
      }
    };
    kv_compute(tid);
    STAMP(1, 7);
    poll_flags<1>(flg + FL_X + mtile * 32 + w * 4, 4, tag, lane);
    wait_vm<0>();
    STAMP(1, 8);
    sfor<9>([&](auto ic) ALWAYS_INLINE { x_load(ic); });
    if (w == 7) kv_compute(512 + lane);
    WAIT_L(0);
    __builtin_amdgcn_s_barrier();
    CFENCE();
    {
      const int ch = tid >> 3, pp = tid & 7, ch2 = 64 + (lane >> 3);
      const u32x4 o1 = *(LAS u32x4*)(tile + ch * 72 + pp * 8), o2 = *(LAS u32x4*)(tile + ch2 * 72 + pp * 8);
      gstore16_sc1(kvr + (size_t)(c0 + ch) * S_ + (1984 - t0) + pp * 8, o1);
      gstore16_sc1(kvr + (size_t)(c0 + ch2) * S_ + (1984 - t0) + pp * 8, o2);
    }
  }

  const int kh = lane >> 5, prt = (lane >> 4) & 1, qq = (lane & 15) >> 2, p4 = lane & 3;
  const unsigned rbU = lds0 + (8 * kh + qq) * 128 + prt * K1_UOFF + ((((qq >> 1) & 1) | (prt << 1)) << 5) + p4 * 8;

  f32x16 acc[4];
  const f32x16 zero16 = {0.f, 0.f, 0.f, 0.f, 0.f, 0.f, 0.f, 0.f, 0.f, 0.f, 0.f, 0.f, 0.f, 0.f, 0.f, 0.f};

  const int token = mtile * 256 + w * 32 + (lane & 31);
  char* hb = (char*)hout + ((size_t)(token >> 4) * 144 * 64 + (lane >> 5) * 16 + (token & 15)) * 16;
  auto h_word = [&](const f32x16& a, int i) ALWAYS_INLINE -> unsigned {
    return pk_bf16(bfr(a[2 * i]) * bfr(a[2 * i + 8]), bfr(a[2 * i + 1]) * bfr(a[2 * i + 9]));
  };
  auto h_store = [&](const u32x4& o, int c16) ALWAYS_INLINE {
    gstore16_sc1(hb + ((size_t)(c16 >> 1) * 64 + (c16 & 1) * 32) * 16, o);
  };
  u32x4 ho;

  const char* hpA = (const char*)(ssmk + (size_t)(chA + (w >> 2)) * S_ + 64 * (w & 3));
  const char* hpC = (const char*)(ssmk + (size_t)chC * S_ + 32 * w);
  const bool zA = (w & 3) == 0, zC = w == 0;
  const char* pf_sk = (const char*)ssmk;
  const char* pf_wd = (const char*)wd + (size_t)(xcd >> 1) * 1152 * 1152;
  const char* kvb = (const char*)kvr;
  u32x4 caf[24];
  unsigned cax[24];
  bf16x8 cf[24];
  int cvo = 0, cvx = 0;
  auto conv_align = [&](auto ic) ALWAYS_INLINE {
    constexpr int i = ic;
    const unsigned sh = ((lane + 1) & 1) * 2;
    u32x4 r;
    r.x = __builtin_amdgcn_alignbyte(caf[i].y, caf[i].x, sh);
    r.y = __builtin_amdgcn_alignbyte(caf[i].z, caf[i].y, sh);
    r.z = __builtin_amdgcn_alignbyte(caf[i].w, caf[i].z, sh);
    r.w = __builtin_amdgcn_alignbyte(cax[i], caf[i].w, sh);
    cf[i] = __builtin_bit_cast(bf16x8, r);
    asm volatile("" : "+v"(cf[i]));
  };

  STAMP(1, 1);

  constexpr int PD = 3, MID = K1_MID;
  bf16x8 af[PD + 1];
  unsigned rb[4];
#pragma unroll
  for (int j = 0; j < 4; ++j) rb[j] = rbU ^ (j << 5);
#pragma unroll
  for (int g = 0; g < PD; ++g) af[g] = tr_frag((LAS char*)(uintptr_t)rb[g], 0, 512);
  sfor<324>([&](auto gc) ALWAYS_INLINE {
    constexpr int g = gc;
    constexpr int st = g / 36, f = g - st * 36;
    constexpr int s = st == 4 ? f : (st == 8 ? f % 9 : f / 4), c = st == 4 ? 0 : (st == 8 ? f / 9 : f % 4);
    constexpr int xi = st < 4 ? st * 9 + s : (st == 4 ? s : (st - 5) * 9 + s);
    constexpr int ai = st < 4 ? c : (st == 4 ? 0 : (c + 1) & 3);
    constexpr bool first = (st == 0 || st == 5) ? s == 0 : (st == 4 && f == 0);
    if constexpr (f == MID) {

      if constexpr (st < 8) wait_vm<k1_younger(k1_dkey(st + 1, 4), 4 * g)>();
      else { wait_vm<k1_younger(4 * (7 * 36 + MID + 1) + 1, 4 * g)>(); if (w <= 3) WAIT_L(0); }
      __builtin_amdgcn_s_barrier();
      CFENCE();

      if constexpr (st == 1) { if (tid == 0) set_flag(flg + FL_K + (bid & 7) * 32 + (bid >> 3), tag); }
      if constexpr (st == 6) {
        static_assert(k1_dkey(7, 4) > 4 * k1_sstep(4) + 3, "the stage 7 wait must cover the h stores of panels A and Q");
        if (tid == 0) set_flag(flg + FL_HE + (mtile * 4 + (cgrp >> 1)) * 8 + (cgrp & 1) * 4 + cq, tag);
      }
    }
    if constexpr (((f + 36 - MID) % 36) % 9 == 0) __builtin_amdgcn_s_setprio(3 - ((f + 36 - MID) % 36) / 9);
    sfor<35>([&](auto dc) ALWAYS_INLINE { if constexpr (k1_dkey(dc / 5 + 2, dc % 5) == 4 * g + 1) piece_load(dc / 5 + 2, dc % 5); });
    sfor<27>([&](auto ic) ALWAYS_INLINE { if constexpr (k1_xkey(ic + 9) == 4 * g + 2) x_load(std::integral_constant<int, ic + 9>{}); });
    if constexpr (g == 7 * 36 + MID + 1) {
      if (w < 3) {
        const int ch = (w == 2) ? chC : chA + w;
        const unsigned lv = lane * 16;
        poll_flags<1>(flg + FL_K + (grp >> 3) * 32, 32, tag, lane);
#pragma unroll
        for (int i = 0; i < 4; ++i) glds1_sc1(kvb + (size_t)ch * (S_ * 2) + i * 1024, lv, lds0 + XR_OFF + w * XR_STRIDE + i * 1024);
        if (lane < 4) glds1(pf_sk + (size_t)ch * (S_ * 2), lv, lds0 + HZ_OFF + w * 96 + 32);
      } else if (w == 3) {
        if (lane < 32) {
#pragma unroll
          for (int im = 0; im < 3; ++im) *(LAS u32x4*)(smem + XR_OFF + im * XR_STRIDE + 4096 + lane * 16) = (u32x4){0u, 0u, 0u, 0u};
        } else if (lane < 38) {
          const int im = (lane - 32) >> 1, hh = lane & 1;
          *(LAS u32x4*)(smem + HZ_OFF + im * 96 + hh * 16) = (u32x4){0u, 0u, 0u, 0u};
        } else if (lane == 38) *(LAS unsigned*)(smem + HCNT_OFF) = 0u;
      }
    }
    sfor<24>([&](auto ac) ALWAYS_INLINE {
      if constexpr (k1_astep(ac) == g) {
        asm volatile("s_waitcnt vmcnt(%2)" : "+v"(caf[ac]), "+v"(cax[ac]) : "i"(k1_younger(4 * k1_cstep(ac) + 2, 4 * g + 2)) : "memory");
        conv_align(ac);
      }
    });
    sfor<6>([&](auto tc) ALWAYS_INLINE {
      if constexpr (k1_tstep(tc) == g) {
        int l2 = lane; asm volatile("" : "+v"(l2));
        const int li = tc * 54 + w * 7 + l2;
        if (l2 < 7) touch_lines(pf_wd, (unsigned)(idx * 324 + (w * 7 + l2 < 54 ? li : tc * 54)) * 128u, lds0 + K1_PF_OFF + 16);
      }
    });
    sfor<24>([&](auto cc) ALWAYS_INLINE {
      constexpr int ci = cc;
      if constexpr (k1_cstep(ci) == g) {
        if constexpr (ci == 0) {
          int l2 = lane; asm volatile("" : "+v"(l2));
          const int e = (l2 & 15) - 15 + 8 * (l2 >> 4);
          cvo = (e & ~1) * 2; cvx = cvo + ((e & 1) ? 16 : 0);
        }
        constexpr int cb = ci < 16 ? 64 * (ci >> 3) + 512 * (ci & 7) : 512 * (ci - 16);
        const char* hp = (ci < 16 ? hpA : hpC) + cb + ((ci == 0 && zA) || (ci == 16 && zC) ? 32 : 0);
        gload16u(caf[ci], hp - 32, (unsigned)(cvo + 32));
        gload4u(cax[ci], hp - 32, (unsigned)(cvx + 32));
      }
    });
    if constexpr (g + PD < 324) {
      constexpr int g1 = g + PD, st1 = g1 / 36, f1 = g1 - st1 * 36;
      if constexpr (f1 == 0) {
        if constexpr (st1 != 4) {
          unsigned b2 = rbU; asm volatile("" : "+v"(b2));
#pragma unroll
          for (int j = 0; j < 4; ++j) { rb[j] = (b2 + (st1 % K1_NSLOT) * K1_SLOT) ^ (j << 5); asm volatile("" : "+v"(rb[j])); }
        } else {
          int l2 = lane; asm volatile("" : "+v"(l2));
          rb[0] = lds0 + (8 * (l2 >> 5) + ((l2 & 15) >> 2)) * 32 + ((l2 >> 4) & 1) * K1_UOFF + (l2 & 3) * 8 + (st1 % K1_NSLOT) * K1_SLOT;
          asm volatile("" : "+v"(rb[0]));
        }
      }
      if constexpr (st1 == 8) af[g1 % (PD + 1)] = tr_frag((LAS char*)(uintptr_t)rb[f1 / 9], (f1 % 9) * 2048, 512);
      else if constexpr (st1 != 4) af[g1 % (PD + 1)] = tr_frag((LAS char*)(uintptr_t)rb[f1 % 4], (f1 / 4) * 2048, 512);
      else af[g1 % (PD + 1)] = tr_frag((LAS char*)(uintptr_t)rb[0], f1 * 512, 128);
    }
    if constexpr (st < 4 && c == 0) {
      asm volatile("s_waitcnt vmcnt(%1)" : "+v"(xf[xi]) : "i"(k1_younger(k1_xkey(xi), 4 * g + 3)) : "memory");
    }
    SCHED0();
    acc[ai] = __builtin_amdgcn_mfma_f32_32x32x16_bf16(af[g % (PD + 1)], xf[xi], first ? zero16 : acc[ai], 0, 0, 0);
    SCHED0();
    if constexpr (g == 143) {
#pragma unroll
      for (int i = 0; i < 4; ++i) ho[i] = h_word(acc[0], i);
      h_store(ho, colA >> 4);
      SCHED0();
    }
    if constexpr (g > 144 && g <= 162) {
      constexpr int cc = (g - 145) / 6 + 1, pi = (g - 145) % 6;
      if constexpr (pi < 4) ho[pi] = h_word(acc[cc], pi);
      if constexpr (pi == 5) h_store(ho, (colA >> 4) + cc);
      SCHED0();
    }
    if constexpr (g >= 298 && (g - 298) % 9 < 5 && (g - 298) / 9 < 3) {
      constexpr int cc = (g - 298) / 9, pi = (g - 298) % 9;
      if constexpr (pi < 4) ho[pi] = h_word(acc[(cc + 1) & 3], pi);
      if constexpr (pi == 4) h_store(ho, (colA >> 4) + 4 + cc);
      SCHED0();
    }
    if constexpr (g >= 180 && g <= 182) {
      if constexpr (g == 180) { ho[0] = h_word(acc[0], 0); ho[1] = h_word(acc[0], 1); }
      if constexpr (g == 181) { ho[2] = h_word(acc[0], 2); ho[3] = h_word(acc[0], 3); }
      if constexpr (g == 182) h_store(ho, colQ >> 4);
      SCHED0();
    }
  });
  const char* wbase2 = (const char*)wd + (size_t)kbase * (D_ * 2);
  unsigned voff2[4];
  int lane2 = lane; asm volatile("" : "+v"(lane2));
#pragma unroll
  for (int n = 0; n < 4; ++n) {
    const int o = (tq * 4 + n) * 1024 + lane2 * 16;
    const int k = o >> 8, pc = (o >> 4) & 15;
    const int j = (pc >> 1) ^ (k & 7);
    voff2[n] = (unsigned)(k * (D_ * 2) + (2 * j + (pc & 1)) * 16);
  }
  const unsigned ldst2 = lds0 + khf * K2_HALF;
  auto piece_load2 = [&](int st, int n) ALWAYS_INLINE {
    const int slot = st % K2_NSLOT;
    if (st < 9) {
      const char* g = wbase2 + (size_t)k2_unit(st) * 64 * (D_ * 2) + colA2 * 2;
      glds1p<false>(g, voff2[n], ldst2 + slot * K2_SLOT + tq * 4096 + n * 1024);
    } else {
      int l2 = lane; asm volatile("" : "+v"(l2));
      const char* g = wbase2 + (size_t)(st - 9) * 288 * (D_ * 2) + colQ2 * 2;
      const int o = n < 2 ? tq * 2048 + n * 1024 + l2 * 16 : 8192 + tq * 256 + (l2 & 15) * 16;
      const unsigned v0 = (unsigned)((o >> 5) * (D_ * 2) + ((o >> 4) & 1) * 16);
      if (n < 2 || l2 < 16) glds1p<false>(g, v0, ldst2 + slot * K2_SLOT + (n < 2 ? tq * 2048 + n * 1024 : 8192 + tq * 256));
    }
  };
  const char* hbase = (const char*)hout + ((size_t)(mt * 8 + tq * 2) * 144 * 64 + (size_t)(kbase >> 5) * 64) * 16;
  const unsigned hv0 = lane2 * 16 + khf * (144 * 64 * 16), hv1 = lane2 * 16 + (1 - khf) * (144 * 64 * 16);
  bf16x8 hf[18][2];
  auto h_load = [&](auto lc) ALWAYS_INLINE {
    constexpr int k = 2 * k2_unit(lc / 4) + (lc / 2) % 2, t = lc % 2;
    gload16_sc1<(k & 3) * 1024>(hf[k][t], hbase + (k >> 2) * 4096, t ? hv1 : hv0);
  };
  STAMP(1, 2);
  __builtin_amdgcn_s_setprio(0);
  {
    u32x4 o;
#pragma unroll
    for (int i = 0; i < 4; ++i) o[i] = h_word(acc[0], i);
    h_store(o, (colA >> 4) + 7);
  }

  __builtin_amdgcn_s_barrier();
  CFENCE();
#pragma unroll
  for (int n = 0; n < 4; ++n) piece_load2(0, n);
#pragma unroll
  for (int n = 0; n < 4; ++n) piece_load2(1, n);

  STAMP(1, 3);
  const int ct0 = lane & 15, ckg = lane >> 4;
  {
    const int boff = (2032 - 16 * (lane & 15) + 8 * ckg) * 2;
    auto head_frag = [&](int im) ALWAYS_INLINE -> bf16x8 {
      bf16x8 r;
      LAS u16* hz = (LAS u16*)(smem + HZ_OFF + im * 96) + 16 + (ct0 - 15 + 8 * ckg);
#pragma unroll
      for (int j = 0; j < 8; ++j) r[j] = (short)hz[j];
      return r;
    };
    if (zA) cf[0] = head_frag(w >> 2);
    if (zC) cf[16] = head_frag(2);
    {
      const int im = w >> 2;
      LAS char* xr = smem + XR_OFF + im * XR_STRIDE + boff + 128 * (w & 3);
      f32x4 ac[8];
#pragma unroll
      for (int j = 0; j < 8; ++j) ac[j] = (f32x4){0.f, 0.f, 0.f, 0.f};
#pragma unroll
      for (int rr = 1; rr >= 0; --rr) {
        bf16x8 bfm[8];
#pragma unroll
        for (int b = 0; b < 8; ++b) bfm[b] = *(LAS bf16x8*)(xr - 512 * b + 64 * rr);
#pragma unroll
        for (int a = 7; a >= 0; --a)
#pragma unroll
          for (int j = a; j < 8; ++j) ac[j] = __builtin_amdgcn_mfma_f32_16x16x32_bf16(cf[rr * 8 + a], bfm[j - a], ac[j], 0, 0, 0);
        if (rr == 1) {
          SCHED0();
          wait_vm<8>();
          if (lane == 0) {
            const unsigned old = __hip_atomic_fetch_add((LAS unsigned*)(smem + HCNT_OFF), 1u, __ATOMIC_RELAXED, __HIP_MEMORY_SCOPE_WORKGROUP);
            if (old == 7u) set_flag(flg + FL_H + (mtile * 4 + (cgrp >> 1)) * 8 + (cgrp & 1) * 4 + cq, tag);
          }
          SCHED0();
        }
      }
#pragma unroll
      for (int j = 0; j < 8; ++j)
        *(LAS f32x4*)(smem + PART_OFF + w * 8192 + (j * 64 + lane) * 16) = ac[j];
    }
    poll_flags<1>(flg + FL_HE + ((mt >> 1) * 4 + ksl) * 8, 8, tag, lane);
    sfor<12>([&](auto kc) ALWAYS_INLINE { h_load(kc); });
    {
      LAS char* xr = smem + XR_OFF + 2 * XR_STRIDE + boff + 64 * w;
      const int jA = 7 - qd, jB = qd;
      bf16x8 b0[8], b1[4];
#pragma unroll
      for (int a = 0; a < 8; ++a) { const int bb = jA - a; b0[a] = *(LAS bf16x8*)(xr - 512 * (bb < 0 ? 0 : bb)); }
#pragma unroll
      for (int a = 0; a < 4; ++a) { const int bb = jB - a; b1[a] = *(LAS bf16x8*)(xr - 512 * (bb < 0 ? 0 : bb)); }
      f32x4 c0 = (f32x4){0.f, 0.f, 0.f, 0.f}, c1 = c0;
#pragma unroll
      for (int a = 7; a >= 0; --a) {
        if (a <= jA) c0 = __builtin_amdgcn_mfma_f32_16x16x32_bf16(cf[16 + a], b0[a], c0, 0, 0, 0);
        if (a < 4 && a <= jB) c1 = __builtin_amdgcn_mfma_f32_16x16x32_bf16(cf[16 + a], b1[a < 4 ? a : 0], c1, 0, 0, 0);
      }
      *(LAS f32x4*)(smem + PARTC_OFF + w * 2048 + lane * 16) = c0;
      *(LAS f32x4*)(smem + PARTC_OFF + w * 2048 + 1024 + lane * 16) = c1;
    }
  }
  STAMP(1, 4);
  WAIT_L(0);
  __builtin_amdgcn_s_barrier();
  CFENCE();
  STAMP(1, 5);
  auto finalize = [&](f32x4 s0, f32x4 s1, int im, int ch, int t8, float dd) ALWAYS_INLINE {
    const u32x4 kvv = *(LAS u32x4*)(smem + XR_OFF + im * XR_STRIDE + (2040 - t8) * 2);
    float y[8] = {s0[0], s0[1], s0[2], s0[3], s1[0], s1[1], s1[2], s1[3]};
    float o[8];
#pragma unroll
    for (int e = 0; e < 8; ++e) {
      const int ri = 7 - e;
      const float kvf = (ri & 1) ? bf_hi(kvv[ri >> 1]) : bf_lo(kvv[ri >> 1]);
      o[e] = bfr(y[e]) + bfr(kvf * dd);
    }
    u32x4 ov; ov.x = pk_bf16(o[0], o[1]); ov.y = pk_bf16(o[2], o[3]); ov.z = pk_bf16(o[4], o[5]); ov.w = pk_bf16(o[6], o[7]);
    gstore16_sc1(wT + (size_t)ch * S_ + t8, ov);
  };
  {
    const int im = tid >> 8, t8 = (tid & 255) * 8;
    const int po = ((t8 >> 8) * 64 + ((t8 >> 2) & 3) * 16 + ((t8 >> 4) & 15)) * 16;
    f32x4 s0 = (f32x4){0.f, 0.f, 0.f, 0.f}, s1 = s0;
#pragma unroll
    for (int ww = 0; ww < 4; ++ww) {
      s0 += *(LAS f32x4*)(smem + PART_OFF + (im * 4 + ww) * 8192 + po);
      s1 += *(LAS f32x4*)(smem + PART_OFF + (im * 4 + ww) * 8192 + po + 256);
    }
    finalize(s0, s1, im, chA + im, t8, ddA);
  }
  if (tid >= 448) {
    const int t2 = tid - 448;
    const int tl = t2 >> 5, l8 = (t2 & 31) * 8;
    const int jt = tl ? qd : 7 - qd, t8 = 256 * jt + l8;
    const int po = (((l8 >> 2) & 3) * 16 + (l8 >> 4)) * 16;
    f32x4 s0 = (f32x4){0.f, 0.f, 0.f, 0.f}, s1 = s0;
#pragma unroll
    for (int ww = 0; ww < 8; ++ww) {
      s0 += *(LAS f32x4*)(smem + PARTC_OFF + ww * 2048 + tl * 1024 + po);
      s1 += *(LAS f32x4*)(smem + PARTC_OFF + ww * 2048 + tl * 1024 + po + 256);
    }
    finalize(s0, s1, 2, chC, t8, ddC);
  }
  STAMP(1, 6);

  STAMP(2, 0);
  {
  const int kq = lane2 >> 4, qq = (lane2 & 15) >> 2, p4 = lane2 & 3;
  const unsigned rbU2 = lds0 + khf * K2_HALF + (16 * (kq >> 1) + 4 * (kq & 1) + qq) * 256 + ((4 * (kq & 1) + qq) << 5) + p4 * 8;

  const char* wtb = (const char*)(wT + mt * 128 + ksl * 32);
  f32x4 acc[2][8], accq[2];
  const f32x4 zero4 = {0.f, 0.f, 0.f, 0.f};

  const int tile = mt * 4 + nt;
  const __amdgpu_buffer_rsrc_t rs = __builtin_amdgcn_make_buffer_rsrc((char*)slab + (size_t)tile * (16 * 18432), 0, 16 * 18432, 0x00020000);
  const bool foreign = tq != ksl;
  auto give = [&](int xb, int i, const f32x4& a0, const f32x4& a1) ALWAYS_INLINE {
    *(LAS f32x4*)(smem + xb + ((w * 4 + i) * 64 + lane) * 16) = a1;
  };
  auto publish = [&](int xb, int i, const f32x4& a0, const f32x4& a1, int dtg) ALWAYS_INLINE {
    const f32x4 sm = a0 + *(LAS f32x4*)(smem + xb + (((w ^ 4) * 4 + i) * 64 + lane) * 16);
    if (foreign) __builtin_amdgcn_raw_buffer_store_b128(__builtin_bit_cast(u32x4, sm), rs, (unsigned)(((tq * 4 + ksl) * 18 + khf * 9 + dtg) * 1024 + lane * 16), 0, 16);
    else *(LAS f32x4*)(smem + K2_OWN_OFF + ((khf * 9 + dtg) * 64 + lane) * 16) = sm;
  };
  constexpr int XB1 = 3 * K2_SLOT, XB2 = 0, XB3 = 3 * K2_SLOT;

  asm volatile("s_waitcnt vmcnt(%4)" : "+v"(hf[0][0]), "+v"(hf[0][1]), "+v"(hf[1][0]), "+v"(hf[1][1])
               : "i"(k2_younger(k2_hkey(0, 3), 0, false)) : "memory");
  WAIT_L(0);
  __builtin_amdgcn_s_barrier();
  CFENCE();
  STAMP(2, 1);

  constexpr int PD = 2;
  bf16x8 af[PD + 1];
  unsigned sb = rbU2;
#pragma unroll
  for (int g = 0; g < PD; ++g) af[g] = tr_frag((LAS char*)(uintptr_t)(sb ^ (g << 5)), 0, 2048);
  sfor<162>([&](auto gc) ALWAYS_INLINE {
    constexpr int g = gc;
    constexpr int st = k2_stage(g), f = g - k2_g0(st);
    constexpr int ks = st < 9 ? k2_unit(st) * 2 + f / 8 : (st - 9) * 9 + f, dt = st < 9 ? f % 8 : 0;
    if constexpr (f == k2_mid(st)) {
      if constexpr (st < 8) { STAMP(0, 2 * st); }
      if constexpr (st == 2) { if (w == 0) poll_flags<1>(flg + FL_H + ((mt >> 1) * 4 + ksl) * 8, 8, tag, lane); }
      if constexpr (st == K2_WST) { if (w == 0) poll_flags<4>(flg + FL_W, 64, tag, lane); }
      if constexpr (st < 10) {
        constexpr int okey = cmax(k2_dkey(st + 1, k2_np(st + 1) - 1), st + 1 < 9 ? k2_hkey(st + 1, 3) : -1000);
        constexpr int n0 = k2_younger(okey, 4 * g, false);
        if constexpr (st == 0) { if (w == 7) wait_vm<n0 + 1>(); else wait_vm<n0>(); }
        else wait_vm<n0>();
        if constexpr (st + 1 < 9) {
          constexpr int k0 = 2 * k2_unit(st + 1);
          asm volatile("" : "+v"(hf[k0][0]), "+v"(hf[k0][1]), "+v"(hf[k0 + 1][0]), "+v"(hf[k0 + 1][1])::"memory");
        }
      }
      if constexpr (st >= 9) WAIT_L(0);
      __builtin_amdgcn_s_barrier();
      CFENCE();
      if constexpr (st < 8) { STAMP(0, 2 * st + 1); }
      if constexpr (st == 1) { if (tid == 0) set_flag(flg + FL_W + bid, tag); }
      if constexpr (st == K2_WST) {
        int l2 = lane; asm volatile("" : "+v"(l2));
        glds1_sc1(wtb, (unsigned)((colA2 + w * 16 + (l2 >> 2)) * (S_ * 2) + (l2 & 3) * 16), lds0 + K2_WT_OFF + w * 1024);
        glds1_sc1(wtb, (unsigned)((colQ2 + (l2 >> 2)) * (S_ * 2) + (l2 & 3) * 16), lds0 + K2_WT_OFF + 8192);
      }
      if constexpr (st == 9) {
#pragma unroll
        for (int i = 0; i < 4; ++i) publish(XB1, i, acc[0][i], acc[1][i], i);
#pragma unroll
        for (int i = 0; i < 4; ++i) give(XB2, i, acc[0][4 + i], acc[1][4 + i]);
      }
      if constexpr (st == 10) {
#pragma unroll
        for (int i = 0; i < 4; ++i) publish(XB2, i, acc[0][4 + i], acc[1][4 + i], 4 + i);
      }
    }
    if constexpr (st < 9 && ((f + 16 - 6) % 16) % 4 == 0) __builtin_amdgcn_s_setprio(3 - ((f + 16 - 6) % 16) / 4);
    sfor<36>([&](auto dc) ALWAYS_INLINE { if constexpr (dc % 4 < k2_np(dc / 4 + 2) && k2_dkey(dc / 4 + 2, dc % 4) == 4 * g + 2) piece_load2(dc / 4 + 2, dc % 4); });
    sfor<24>([&](auto hc) ALWAYS_INLINE { if constexpr (k2_hkey(hc / 4 + 3, hc % 4) == 4 * g + 3) h_load(std::integral_constant<int, hc + 12>{}); });
    if constexpr (g + PD < 162) {
      constexpr int g1 = g + PD, st1 = k2_stage(g1), f1 = g1 - k2_g0(st1);
      if constexpr (f1 == 0) {
        if constexpr (st1 < 9) {
          sb += (st1 % K2_NSLOT == 0) ? -3 * K2_SLOT : K2_SLOT;
        } else {
          int l2 = lane; asm volatile("" : "+v"(l2));
          const int kq2 = l2 >> 4;
          sb = lds0 + khf * K2_HALF + (16 * (kq2 >> 1) + 4 * (kq2 & 1) + ((l2 & 15) >> 2)) * 32 + (l2 & 3) * 8 + (st1 % K2_NSLOT) * K2_SLOT;
        }
      }
      asm volatile("" : "+v"(sb));
      if constexpr (st1 < 9) af[g1 % (PD + 1)] = tr_frag((LAS char*)(uintptr_t)(sb ^ ((f1 % 8) << 5)), (f1 / 8) * 8192, 2048);
      else af[g1 % (PD + 1)] = tr_frag((LAS char*)(uintptr_t)sb, f1 * 1024, 256);
    }
    SCHED0();
#pragma unroll
    for (int t = 0; t < 2; ++t) {
      if constexpr (st < 9) acc[t][dt] = __builtin_amdgcn_mfma_f32_16x16x32_bf16(af[g % (PD + 1)], hf[ks][t], g < 8 ? zero4 : acc[t][dt], 0, 0, 0);
      else accq[t] = __builtin_amdgcn_mfma_f32_16x16x32_bf16(af[g % (PD + 1)], hf[ks][t], g == 144 ? zero4 : accq[t], 0, 0, 0);
    }
    SCHED0();
    if constexpr (g == 143) {
#pragma unroll
      for (int i = 0; i < 4; ++i) give(XB1, i, acc[0][i], acc[1][i]);
      SCHED0();
    }
  });
  STAMP(2, 2);
  __builtin_amdgcn_s_setprio(0);
  give(XB3, 0, accq[0], accq[1]);

  int lane3 = lane; asm volatile("" : "+v"(lane3));
  u32x2 xq[3];
  float2 stt[2];
#pragma unroll
  for (int i = 0; i < 3; ++i) {
    const int t = i < 2 ? i : (w & 1);
    const int token = mt * 128 + ksl * 32 + t * 16 + (lane3 & 15);
    xq[i] = *(const u32x2*)(q + (size_t)token * D_ + (i < 2 ? colA2 + w * 16 : colQ2) + 4 * (lane3 >> 4));
    if (i < 2) {
      const u64 sv = __hip_atomic_load(stats + token, __ATOMIC_RELAXED, __HIP_MEMORY_SCOPE_AGENT);
      stt[i] = make_float2(__uint_as_float((unsigned)sv), __uint_as_float((unsigned)(sv >> 32)));
    }
  }
  WAIT_L(0);
  __builtin_amdgcn_s_barrier();
  CFENCE();
  publish(XB3, 0, accq[0], accq[1], 8);
  asm volatile("s_waitcnt vmcnt(0) lgkmcnt(0)" ::: "memory");
  __builtin_amdgcn_s_barrier();
  CFENCE();
  STAMP(2, 3);
  if (tid == 0) set_flag(flg + FL_S + tile * 4 + ksl, tag);
  poll_flags<1>(flg + FL_S + tile * 4, 4, tag, lane3);
  STAMP(2, 4);
  auto fetch = [&](f32x4 (&pv)[3], int fi) ALWAYS_INLINE {
#pragma unroll
    for (int k = 1; k < 4; ++k)
      pv[k - 1] = __builtin_bit_cast(f32x4, __builtin_amdgcn_raw_buffer_load_b128(rs, (unsigned)(((ksl * 4 + ((ksl + k) & 3)) * 18 + fi) * 1024 + lane3 * 16), 0, 16));
  };
  auto combine = [&](const f32x4 (&pv)[3], int t, int dt, const u32x2& xw2, const float2& st2) ALWAYS_INLINE {
    const int tl = t * 16 + (lane3 & 15), dl = dt * 16 + 4 * (lane3 >> 4);
    const f32x4 own = *(LAS f32x4*)(smem + K2_OWN_OFF + ((t * 9 + dt) * 64 + lane3) * 16);
    const f32x4 bs = ((own + pv[0]) + pv[1]) + pv[2];
    float o[4];
#pragma unroll
    for (int c = 0; c < 4; ++c) {
      const unsigned xw = (c < 2) ? xw2.x : xw2.y;
      const float x = (c & 1) ? bf_hi(xw) : bf_lo(xw);
      const float wy = bf2f(*(LAS u16*)(smem + K2_WT_OFF + (dl + c) * 64 + tl * 2));
      const float xn = bfr(bfr(x - st2.x) * st2.y);
      const float a = bfr(xn * wy);
      o[c] = bfr(x + a) + bfr(bs[c]);
    }
    u32x2 ov; ov.x = pk_bf16(o[0], o[1]); ov.y = pk_bf16(o[2], o[3]);
    *(u32x2*)(out + (size_t)(mt * 128 + ksl * 32 + tl) * D_ + (dt < 8 ? colA2 + dt * 16 : colQ2) + 4 * (lane3 >> 4)) = ov;
  };
  {
    f32x4 p0[3], p1[3], p2[3];
    fetch(p0, w); fetch(p1, 9 + w); fetch(p2, 9 * (w & 1) + 8);
    combine(p0, 0, w, xq[0], stt[0]);
    combine(p1, 1, w, xq[1], stt[1]);
    if (w < 2) combine(p2, w, 8, xq[2], w ? stt[1] : stt[0]);
  }
  if (bid == 0 && w == 0) {
    poll_flags<4>(flg + FL_K, 64, tag, lane);
    if (lane == 0) set_flag(flg + FL_GEN, tag);
  }
  STAMP(2, 5); RSTAMP(1, 15); STAMP_FLUSH(1); STAMP_FLUSH(2); STAMP_FLUSH(0);
  }
}

extern "C" void kernel_launch(void* const* d_in, const int* in_sizes, int n_in,
                              void* d_out, int out_size, void* d_ws, size_t ws_size,
                              hipStream_t stream) {
  const u16* q = (const u16*)d_in[0];
  const u16* k = (const u16*)d_in[1];
  const u16* v = (const u16*)d_in[2];
  const u16* shk = (const u16*)d_in[3];
  const u16* ssmk = (const u16*)d_in[4];
  const u16* ssmd = (const u16*)d_in[5];
  const u16* wg = (const u16*)d_in[7];
  const u16* wu = (const u16*)d_in[8];
  const u16* wd = (const u16*)d_in[9];
  (void)hipFuncSetAttribute((const void*)k_fused, hipFuncAttributeMaxDynamicSharedMemorySize, K1_LDS);
  k_fused<<<256, 512, K1_LDS, stream>>>(q, k, v, shk, ssmk, ssmd, wg, wu, wd, (char*)d_ws, (u16*)d_out);
}
```

```cpp
#include <hip/hip_runtime.h>
#include <hip/hip_bf16.h>
#include <stdint.h>
#include <utility>

typedef unsigned short u16;
typedef short bf16x8 __attribute__((ext_vector_type(8)));
typedef short s16x4 __attribute__((ext_vector_type(4)));
typedef float f32x4 __attribute__((ext_vector_type(4)));
typedef float f32x16 __attribute__((ext_vector_type(16)));
typedef unsigned u32x4 __attribute__((ext_vector_type(4)));
typedef unsigned u32x2 __attribute__((ext_vector_type(2)));
typedef __bf16 bf2_t __attribute__((ext_vector_type(2)));
#define LAS __attribute__((address_space(3)))
#define DI __device__ __forceinline__

constexpr int S_ = 2048, D_ = 576, F_ = 4608;

constexpr size_t WS_XFRAG = 0;
constexpr size_t WS_STATS = WS_XFRAG + (size_t)S_ * D_ * 2;
constexpr size_t WS_KVR   = WS_STATS + (size_t)S_ * 8;
constexpr size_t WS_WT    = WS_KVR + (size_t)S_ * D_ * 2;
constexpr size_t WS_H     = WS_WT + (size_t)S_ * D_ * 2;
constexpr size_t WS_SLAB  = WS_H + (size_t)S_ * F_ * 2;
constexpr size_t WS_FLG   = WS_SLAB + (size_t)4 * S_ * D_ * 4;
constexpr int FL_GEN = 0, FL_X = 32, FL_K = FL_X + 256, FL_H = FL_K + 256, FL_HE = FL_H + 256, FL_W = FL_HE + 256, FL_S = FL_W + 256, FL_END = FL_S + 256;
constexpr size_t WS_END   = WS_FLG + FL_END * 8;
typedef unsigned long long u64;

DI float bf_lo(unsigned w) { return __uint_as_float(w << 16); }
DI float bf_hi(unsigned w) { return __uint_as_float(w & 0xffff0000u); }
DI float bf2f(u16 b) { return __uint_as_float(((unsigned)b) << 16); }
DI float bfr(float x) { return (float)(__bf16)x; }
typedef float f32x2 __attribute__((ext_vector_type(2)));
DI unsigned pk_bf16(float lo, float hi);
DI f32x2 unpk(unsigned w) { return (f32x2){__uint_as_float(w << 16), __uint_as_float(w & 0xffff0000u)}; }
DI f32x2 bfr2(f32x2 v) { return unpk(pk_bf16(v.x, v.y)); }
DI unsigned pk_bf16(float lo, float hi) { bf2_t v = {(__bf16)lo, (__bf16)hi}; return __builtin_bit_cast(unsigned, v); }

DI float wave_sum(float x) {
  x += __builtin_bit_cast(float, __builtin_amdgcn_update_dpp(0, __builtin_bit_cast(int, x), 0xB1, 0xf, 0xf, true));
  x += __builtin_bit_cast(float, __builtin_amdgcn_update_dpp(0, __builtin_bit_cast(int, x), 0x4E, 0xf, 0xf, true));
  x += __builtin_bit_cast(float, __builtin_amdgcn_update_dpp(0, __builtin_bit_cast(int, x), 0x141, 0xf, 0xf, true));
  x += __builtin_bit_cast(float, __builtin_amdgcn_update_dpp(0, __builtin_bit_cast(int, x), 0x140, 0xf, 0xf, true));
  const int xi = __builtin_bit_cast(int, x);
  return (__builtin_bit_cast(float, __builtin_amdgcn_readlane(xi, 0)) + __builtin_bit_cast(float, __builtin_amdgcn_readlane(xi, 16))) +
         (__builtin_bit_cast(float, __builtin_amdgcn_readlane(xi, 32)) + __builtin_bit_cast(float, __builtin_amdgcn_readlane(xi, 48)));
}
#define WAIT_V(n) asm volatile("s_waitcnt vmcnt(" #n ")" ::: "memory")
#define WAIT_L(n) asm volatile("s_waitcnt lgkmcnt(" #n ")" ::: "memory")
#define CFENCE() asm volatile("" ::: "memory")
#define STAMP(kid, i)
#define RSTAMP(kid, i)
#define STAMP_DECL
#define STAMP_FLUSH(kid)
#define K0STAMP(i)
#define SCHED0() __builtin_amdgcn_sched_barrier(0)
template <int... I, class Fn> DI void sfor_impl(std::integer_sequence<int, I...>, Fn&& f) { (f(std::integral_constant<int, I>{}), ...); }
template <int N, class Fn> DI void sfor(Fn&& f) { sfor_impl(std::make_integer_sequence<int, N>{}, f); }
#define ALWAYS_INLINE __attribute__((always_inline))
template <int N> DI void wait_vm() { asm volatile("s_waitcnt vmcnt(%0)" ::"i"(N) : "memory"); }

DI void glds4(const void* sbase, unsigned v0, unsigned v1, unsigned v2, unsigned v3, unsigned lds_dst) {
  unsigned keep;
  asm volatile("s_mov_b32 %0, m0\n\ts_mov_b32 m0, %6\n\ts_nop 0\n\t"
               "global_load_lds_dwordx4 %1, %5\n\t"
               "global_load_lds_dwordx4 %2, %5 offset:1024\n\t"
               "global_load_lds_dwordx4 %3, %5 offset:2048\n\t"
               "global_load_lds_dwordx4 %4, %5 offset:3072\n\t"
               "s_mov_b32 m0, %0"
               : "=&s"(keep) : "v"(v0), "v"(v1), "v"(v2), "v"(v3), "s"(sbase), "s"(lds_dst) : "memory");
}
DI void glds3(const void* sbase, unsigned v0, unsigned v1, unsigned v2, unsigned lds_dst) {
  unsigned keep;
  asm volatile("s_mov_b32 %0, m0\n\ts_mov_b32 m0, %5\n\ts_nop 0\n\t"
               "global_load_lds_dwordx4 %1, %4\n\t"
               "global_load_lds_dwordx4 %2, %4 offset:1024\n\t"
               "global_load_lds_dwordx4 %3, %4 offset:2048\n\t"
               "s_mov_b32 m0, %0"
               : "=&s"(keep) : "v"(v0), "v"(v1), "v"(v2), "s"(sbase), "s"(lds_dst) : "memory");
}
DI void glds2(const void* sbase, unsigned v0, unsigned v1, unsigned lds_dst) {
  unsigned keep;
  asm volatile("s_mov_b32 %0, m0\n\ts_mov_b32 m0, %4\n\ts_nop 0\n\t"
               "global_load_lds_dwordx4 %1, %3\n\t"
               "global_load_lds_dwordx4 %2, %3 offset:1024\n\t"
               "s_mov_b32 m0, %0"
               : "=&s"(keep) : "v"(v0), "v"(v1), "s"(sbase), "s"(lds_dst) : "memory");
}
DI void glds1(const void* sbase, unsigned v0, unsigned lds_dst) {
  unsigned keep;
  asm volatile("s_mov_b32 %0, m0\n\ts_mov_b32 m0, %3\n\ts_nop 0\n\t"
               "global_load_lds_dwordx4 %1, %2\n\t"
               "s_mov_b32 m0, %0"
               : "=&s"(keep) : "v"(v0), "s"(sbase), "s"(lds_dst) : "memory");
}

template <bool NT> DI void glds1p(const void* sbase, unsigned v0, unsigned lds_dst) {
  unsigned keep;
  if constexpr (NT)
    asm volatile("s_mov_b32 %0, m0\n\ts_mov_b32 m0, %3\n\ts_nop 0\n\tglobal_load_lds_dwordx4 %1, %2 nt\n\ts_mov_b32 m0, %0"
                 : "=&s"(keep) : "v"(v0), "s"(sbase), "s"(lds_dst) : "memory");
  else
    asm volatile("s_mov_b32 %0, m0\n\ts_mov_b32 m0, %3\n\ts_nop 0\n\tglobal_load_lds_dwordx4 %1, %2\n\ts_mov_b32 m0, %0"
                 : "=&s"(keep) : "v"(v0), "s"(sbase), "s"(lds_dst) : "memory");
}
DI void glds1_sc1(const void* sbase, unsigned v0, unsigned lds_dst) {
  unsigned keep;
  asm volatile("s_mov_b32 %0, m0\n\ts_mov_b32 m0, %3\n\ts_nop 0\n\tglobal_load_lds_dwordx4 %1, %2 sc1\n\ts_mov_b32 m0, %0"
               : "=&s"(keep) : "v"(v0), "s"(sbase), "s"(lds_dst) : "memory");
}
DI void touch_lines(const void* sbase, unsigned v0, unsigned lds_dst) {
  unsigned keep;
  asm volatile("s_mov_b32 %0, m0\n\ts_mov_b32 m0, %3\n\ts_nop 0\n\tglobal_load_lds_dword %1, %2\n\ts_mov_b32 m0, %0"
               : "=&s"(keep) : "v"(v0), "s"(sbase), "s"(lds_dst) : "memory");
}
template <int OFF> DI void gload16(bf16x8& dst, const void* sbase, unsigned voff) {
  asm volatile("global_load_dwordx4 %0, %1, %2 offset:%3" : "=v"(dst) : "v"(voff), "s"(sbase), "i"(OFF) : "memory");
}
template <int OFF> DI void gload16_sc1(bf16x8& dst, const void* sbase, unsigned voff) {
  asm volatile("global_load_dwordx4 %0, %1, %2 offset:%3 sc1" : "=v"(dst) : "v"(voff), "s"(sbase), "i"(OFF) : "memory");
}
DI void gload16u(u32x4& dst, const void* sbase, unsigned voff) {
  asm volatile("global_load_dwordx4 %0, %1, %2" : "=v"(dst) : "v"(voff), "s"(sbase) : "memory");
}
template <int OFF> DI void gstore16_sc1_s(const void* sbase, unsigned voff, const u32x4& v) {
  asm volatile("global_store_dwordx4 %0, %1, %2 offset:%3 sc1\n\ts_nop 1" ::"v"(voff), "v"(v), "s"(sbase), "i"(OFF) : "memory");
}
DI void gload4u(unsigned& dst, const void* sbase, unsigned voff) {
  asm volatile("global_load_dword %0, %1, %2" : "=v"(dst) : "v"(voff), "s"(sbase) : "memory");
}
DI void gstore16_sc1(void* p, const u32x4& v) { asm volatile("global_store_dwordx4 %0, %1, off sc1\n\ts_nop 1" ::"v"(p), "v"(v) : "memory"); }
DI void set_flag(u64* f, u64 tag) { __hip_atomic_store(f, tag, __ATOMIC_RELAXED, __HIP_MEMORY_SCOPE_AGENT); }
template <int PER> DI void poll_flags(u64* f, int n, u64 tag, int lane) {
  asm volatile("" : "+v"(lane));
  const int i = lane < n ? lane : 0;
  for (unsigned spins = 0; spins < (1u << 20); ++spins) {
    bool ok = true;
#pragma unroll
    for (int k = 0; k < PER; ++k) ok &= __hip_atomic_load(f + i + 64 * k, __ATOMIC_RELAXED, __HIP_MEMORY_SCOPE_AGENT) == tag;
    if (__all(ok)) break;
    __builtin_amdgcn_s_sleep(1);
  }
}

constexpr int K1_SLOT = 37120;
constexpr int K1_UOFF = 18432 + 128;
constexpr int K1_NSLOT = 4;
constexpr int K1_PF_OFF = K1_NSLOT * K1_SLOT;
constexpr int XR_OFF = K1_PF_OFF + 288, XR_STRIDE = 4608;
constexpr int HZ_OFF = XR_OFF + 3 * XR_STRIDE;
constexpr int K1_LDS = HZ_OFF + 288;
constexpr int PART_OFF = 65536;
constexpr int PARTC_OFF = PART_OFF + 65536;
constexpr int HCNT_OFF = K1_PF_OFF;
constexpr int XG_OFF = 3 * K1_SLOT;

DI bf16x8 tr_frag(LAS char* p, int off, int hi_off) {
  s16x4 lo = __builtin_amdgcn_ds_read_tr16_b64_v4i16((LAS s16x4*)(p + off));
  s16x4 hi = __builtin_amdgcn_ds_read_tr16_b64_v4i16((LAS s16x4*)(p + off + hi_off));
  return __builtin_shufflevector(lo, hi, 0, 1, 2, 3, 4, 5, 6, 7);
}

constexpr int K1_MID = 16;
constexpr int k1_xkey(int i) { return i < 9 ? -100 + i : 4 * (1 + 2 * (i - 9)) + 2; }
constexpr int k1_dstep(int d, int n) { return d == 2 ? 2 + 4 * n : d == 3 ? 24 + 4 * n : d == 4 ? 58 + 6 * n : d == 5 ? 94 + 6 * n : 36 * (d - 3) + K1_MID + 6 * n; }
constexpr int k1_dkey(int d, int n) { return d < 2 ? -400 + 10 * d + n : 4 * k1_dstep(d, n) + 1; }
constexpr int k1_cstep(int i) { return i < 7 ? 218 + 4 * i : (i < 14 ? 254 + 4 * (i - 7) : (i < 21 ? 289 + (i - 14) : 304 + 2 * (i - 21))); }
constexpr int k1_sstep(int c) { return c == 0 ? 143 : (c < 4 ? 144 + 6 * c : (c == 4 ? 182 : 302 + 9 * (c - 5))); }
constexpr int k1_astep(int i) { return i < 14 ? k1_cstep(i) + 28 : (i < 21 ? k1_cstep(i) + 20 : 321 + (i - 21)); }
constexpr int K1_TOUCH = 5 * 36 + K1_MID + 1;
constexpr int k1_younger(int okey, int wkey) {
  int n = 0;
  if (4 * K1_TOUCH + 1 > okey && 4 * K1_TOUCH + 1 < wkey) n += 1;
  if (-70 > okey && -70 < wkey) n += 2;
  for (int i = 0; i < 36; ++i) if (k1_xkey(i) > okey && k1_xkey(i) < wkey) n += 1;
  for (int d = 0; d < 9; ++d) for (int pc = 0; pc < 5; ++pc) if (k1_dkey(d, pc) > okey && k1_dkey(d, pc) < wkey) n += 1;
  for (int i = 0; i < 24; ++i) if (4 * k1_cstep(i) + 2 > okey && 4 * k1_cstep(i) + 2 < wkey) n += 2;
  for (int c = 0; c < 8; ++c) if (4 * k1_sstep(c) + 3 > okey && 4 * k1_sstep(c) + 3 < wkey) n += 1;
  return n;
}
#define HSTORE 1

constexpr int K2_HALF = 16384;
constexpr int K2_SLOT = 2 * K2_HALF;
constexpr int K2_NSLOT = 4;
constexpr int K2_OWN_OFF = K2_NSLOT * K2_SLOT;
constexpr int K2_WT_OFF = K2_OWN_OFF + 18432;
constexpr int k2_g0(int st) { return st <= 9 ? 16 * st : 153; }
constexpr int k2_stage(int g) { return g < 144 ? g / 16 : (g < 153 ? 9 : 10); }
constexpr int k2_mid(int st) { return st < 9 ? 6 : 4; }
constexpr int k2_unit(int st) { return st < 5 ? 2 * st : 2 * (st - 5) + 1; }
constexpr int k2_np(int d) { return d < 9 ? 4 : 3; }
constexpr int k2_dkey(int d, int n) { return d < 2 ? -300 + 10 * d + n : (d == 2 ? 4 * (1 + n) + 2 : 4 * (k2_g0(d - 3) + k2_mid(d - 3) + 4 * n) + 2); }
constexpr int k2_hkey(int j, int m) { return j < 3 ? -250 + 10 * j + m : 4 * (k2_g0(j - 3) + k2_mid(j - 3) + 2 + 4 * m) + 3; }
constexpr int k2_skey(int p) { return 4 * (k2_g0(9 + p) + k2_mid(9 + p)) + 1; }
constexpr int K2_WST = 7;
constexpr int k2_wkey() { return 4 * (k2_g0(K2_WST) + k2_mid(K2_WST)) + 1; }
constexpr int k2_younger(int okey, int wkey, bool stores) {
  int n = 0;
  if (k2_wkey() > okey && k2_wkey() < wkey) n += 2;
  if (-100 > okey && -100 < wkey) n += 1;
  for (int d = 0; d < 11; ++d) for (int pc = 0; pc < k2_np(d); ++pc) if (k2_dkey(d, pc) > okey && k2_dkey(d, pc) < wkey) n += 1;
  for (int j = 0; j < 9; ++j) for (int m = 0; m < 4; ++m) if (k2_hkey(j, m) > okey && k2_hkey(j, m) < wkey) n += 1;
  if (stores) for (int p = 0; p < 2; ++p) if (k2_skey(p) > okey && k2_skey(p) < wkey) n += 4;
  return n;
}
constexpr unsigned CODE_BYTES = 42 * 1024;
constexpr int cmax(int a, int b) { return a > b ? a : b; }

__global__ __launch_bounds__(512, 2) void k_fused(const u16* __restrict__ q, const u16* __restrict__ kk, const u16* __restrict__ vv,
                                                  const u16* __restrict__ shk, const u16* __restrict__ ssmk, const u16* __restrict__ ssmd,
                                                  const u16* __restrict__ wg, const u16* __restrict__ wu, const u16* __restrict__ wd,
                                                  char* ws, u16* __restrict__ out) {
  u16* xfrag = (u16*)(ws + WS_XFRAG);
  u64* stats = (u64*)(ws + WS_STATS);
  u16* kvr = (u16*)(ws + WS_KVR);
  u16* wT = (u16*)(ws + WS_WT);
  u16* hout = (u16*)(ws + WS_H);
  float* slab = (float*)(ws + WS_SLAB);
  u64* flg = (u64*)(ws + WS_FLG);
  extern __shared__ __attribute__((aligned(16))) char smem_g[];
  LAS char* smem = (LAS char*)smem_g;
  const unsigned lds0 = (unsigned)(uintptr_t)smem;
  const int bid = blockIdx.x, tid = threadIdx.x;
  const int w = __builtin_amdgcn_readfirstlane(tid >> 6), lane = tid & 63;
  const int xcd = bid & 7, idx = bid >> 3;
  STAMP_DECL; RSTAMP(1, 14); STAMP(1, 0);
  const u64 genv = __hip_atomic_load(flg + FL_GEN, __ATOMIC_RELAXED, __HIP_MEMORY_SCOPE_AGENT);
  u64 tag;
  auto make_tag = [&]() ALWAYS_INLINE {
    tag = (((u64)(unsigned)__builtin_amdgcn_readfirstlane((unsigned)(genv >> 32)) << 32) | (unsigned)__builtin_amdgcn_readfirstlane((unsigned)genv)) + 1ull;
  };
  const int ksl = xcd >> 1, mt = (xcd & 1) * 8 + (idx >> 2), nt = idx & 3;
  const int tq = w & 3, khf = w >> 2;
  const int kbase = ksl * 1152 + khf * 576;
  const int colA2 = 128 * nt, colQ2 = 512 + 16 * nt;
  const int mtile = (xcd & 1) * 4 + (idx & 3);
  const int cgrp = (xcd >> 1) * 2 + (idx >> 4), cq = (idx >> 2) & 3;
  const int colA = 64 * (9 * cgrp + 2 * cq), colQ = 64 * (9 * cgrp + 8) + 16 * cq;

  const int grp = bid >> 2, qd = bid & 3;
  const int chA = grp * 9 + 2 * qd, chC = grp * 9 + 8;
  float ddA, ddC;
  {
    const int c1 = chA + (w >> 2);
    const unsigned d1 = ((const unsigned*)ssmd)[c1 >> 1], d2 = ((const unsigned*)ssmd)[chC >> 1];
    ddA = (c1 & 1) ? bf_hi(d1) : bf_lo(d1);
    ddC = (chC & 1) ? bf_hi(d2) : bf_lo(d2);
  }

  const int wq = w & 3, part = w >> 2;
  const char* wbase = (const char*)(part ? wu : wg);
  unsigned voff[5];
#pragma unroll
  for (int n = 0; n < 5; ++n) {
    const int o = (n < 4) ? ((wq * 4 + n) * 1024 + lane * 16) : (16384 + wq * 512 + (lane & 31) * 16);
    const int k = o >> 7, pc = (o >> 4) & 7;
    const int j = (pc >> 1) ^ (((k >> 1) & 1) | (part << 1));
    voff[n] = (unsigned)(k * (F_ * 2) + (2 * j + (pc & 1)) * 16);
  }
  const unsigned ldst = lds0 + part * K1_UOFF + wq * 4096;
  const unsigned ldst4 = lds0 + part * K1_UOFF + 16384 + wq * 512;

  auto piece_load = [&](int st, int n) ALWAYS_INLINE {
    const int slot = st % K1_NSLOT;
    const unsigned dst = (n < 4 ? ldst + n * 1024 : ldst4) + slot * K1_SLOT;
    if (st != 4) {
      const int ks = st < 4 ? st : st - 5;
      const char* g = wbase + (size_t)ks * 144 * (F_ * 2) + (colA + (st > 4 ? 64 : 0)) * 2;
      if (n < 4 || lane < 32) glds1p<false>(g, voff[n], dst);
    } else {
      int l2 = lane; asm volatile("" : "+v"(l2));
      const int o = (n < 4) ? ((wq * 4 + n) * 1024 + l2 * 16) : (16384 + wq * 512 + (l2 & 31) * 16);
      const unsigned vq = (unsigned)((o >> 5) * (F_ * 2) + ((o >> 4) & 1) * 16);
      if (n < 4 || lane < 32) glds1p<false>(wbase + colQ * 2, vq, dst);
    }
  };

  bf16x8 xf[36];
  const char* xb = (const char*)xfrag + (size_t)(mtile * 8 + w) * 36 * 1024;
  const unsigned xv = lane * 16;
  auto x_load = [&](auto ic) ALWAYS_INLINE {
    constexpr int i = ic;
    gload16_sc1<(i & 3) * 1024>(xf[i], xb + (i >> 2) * 4096, xv);
  };
  {
    LAS u16* tile = (LAS u16*)(smem + XR_OFF);
    constexpr int KT_OFF = 2 * K1_SLOT, VT_OFF = KT_OFF + 9728, WT_OFF = VT_OFF + 9216;
    const int tt = bid >> 3, ct = bid & 7, t0 = tt * 64, c0 = ct * 72;
    const int tok0 = mtile * 256 + (cgrp * 4 + cq) * 8, token = tok0 + w;
    const bool act = lane < 48;
    u32x2 d[3];
    u32x4 kin, vin, ein;
#pragma unroll
    for (int i = 0; i < 3; ++i)
      asm volatile("global_load_dwordx2 %0, %1, %2 offset:%3" : "=v"(d[i]) : "v"((unsigned)(token * (D_ * 2) + (act ? lane : 0) * 8)), "s"(q), "i"(i * 384) : "memory");
    auto k_off = [&](int pc) ALWAYS_INLINE -> unsigned { const int r = pc / 9, cg = pc - r * 9, ts = t0 - 3 + r; return (unsigned)((ts < 0 ? 0 : ts) * (D_ * 2) + (c0 + 8 * cg) * 2); };
    auto v_off = [&](int pc) ALWAYS_INLINE -> unsigned { const int r = pc / 9, cg = pc - r * 9; return (unsigned)((t0 + r) * (D_ * 2) + (c0 + 8 * cg) * 2); };
    gload16u(kin, kk, k_off(tid));
    gload16u(vin, vv, v_off(tid));
    int edst;
    {
      const int kp = w == 0 ? 512 + lane : 576 + (lane < 27 ? lane : 0);
      const int wl = lane < 63 ? lane - 27 : 35;
      const bool isw = w == 1 && lane >= 27, isv = w >= 2;
      const char* src = isv ? (const char*)vv + v_off(512 + lane) : (isw ? (const char*)shk + c0 * 8 + wl * 16 : (const char*)kk + k_off(kp));
      edst = isv ? VT_OFF + (512 + lane) * 16 : (isw ? WT_OFF + wl * 16 : KT_OFF + kp * 16);
      asm volatile("global_load_dwordx4 %0, %1, off" : "=v"(ein) : "v"(src) : "memory");
    }
    {
      const int li = (w == 3 || w == 4) ? (w & 1) * 64 + lane : 0;
      const int ch = li < 32 ? chA : (li < 64 ? chA + 1 : chC);
      u64 pc; asm volatile("s_getpc_b64 %0" : "=s"(pc));
      const unsigned cl = (unsigned)((idx & 7) * 64 + lane) * 128u;
      const char* tb = w == 5 ? (const char*)(pc & ~127ull) : (const char*)ssmk;
      touch_lines(tb, w == 5 ? (cl < CODE_BYTES ? cl : 0u) : (unsigned)(ch * (S_ * 2) + (li & 31) * 128), lds0 + K1_PF_OFF + 16);
    }
    if (w >= 4) {
#pragma unroll
      for (int pt = 0; pt < 2; ++pt)
#pragma unroll
        for (int n = 0; n < 5; ++n) {
          const int o = (n < 4) ? ((wq * 4 + n) * 1024 + lane * 16) : (16384 + wq * 512 + (lane & 31) * 16);
          const int k = o >> 7, pc = (o >> 4) & 7;
          const int j = (pc >> 1) ^ (((k >> 1) & 1) | (pt << 1));
          const unsigned vo = (unsigned)(k * (F_ * 2) + (2 * j + (pc & 1)) * 16);
          const unsigned dst = lds0 + pt * K1_UOFF + (n < 4 ? wq * 4096 + n * 1024 : 16384 + wq * 512);
          const char* g = (const char*)(pt ? wu : wg) + colA * 2;
#pragma unroll
          for (int st = 0; st < 2; ++st)
            if (n < 4 || lane < 32) glds1p<false>(g + (size_t)st * 144 * (F_ * 2), vo, dst + st * K1_SLOT);
        }
      wait_vm<24>();
    } else wait_vm<4>();
    asm volatile("" : "+v"(d[0]), "+v"(d[1]), "+v"(d[2])::"memory");
    STAMP(1, 9);
    f32x2 xa[3], xb2[3];
#pragma unroll
    for (int i = 0; i < 3; ++i) { xa[i] = unpk(d[i].x); xb2[i] = unpk(d[i].y); }
    const f32x2 s2 = (xa[0] + xb2[0]) + (xa[1] + xb2[1]) + (xa[2] + xb2[2]);
    const float mean = wave_sum(act ? s2.x + s2.y : 0.f) * (1.0f / 576.0f);
    f32x2 v2 = {0.f, 0.f};
#pragma unroll
    for (int i = 0; i < 3; ++i) { const f32x2 a = xa[i] - mean, b = xb2[i] - mean; v2 += a * a + b * b; }
    const float vs = wave_sum(act ? v2.x + v2.y : 0.f);
    const float mb = bfr(mean), vb = bfr(vs * (1.0f / 576.0f));
    const float sd = bfr(sqrtf(bfr(vb + 1e-5f)));
    const float rsd = 1.0f / sd;
#pragma unroll
    for (int i = 0; i < 3; ++i) {
      const f32x2 a = bfr2(xa[i] - mb) * rsd, b = bfr2(xb2[i] - mb) * rsd;
      u32x2 o; o.x = pk_bf16(a.x, a.y); o.y = pk_bf16(b.x, b.y);
      const int p8 = lane + 48 * i;
      if (act) *(LAS u32x2*)(smem + XG_OFF + ((p8 >> 1) * 8 + w) * 16 + (p8 & 1) * 8) = o;
    }
    if (tid == 0) *(LAS unsigned*)(smem + HCNT_OFF + 4) = 0u;
    if (lane == 0) *(LAS u64*)(smem + XG_OFF + 9216 + w * 8) = (u64)__float_as_uint(mb) | ((u64)__float_as_uint(rsd) << 32);
    if (w >= 4) wait_vm<21>(); else wait_vm<1>();
    asm volatile("" : "+v"(kin), "+v"(vin), "+v"(ein)::"memory");
    if (t0 == 0 && tid < 27) kin = (u32x4){0u, 0u, 0u, 0u};
    *(LAS u32x4*)(smem + KT_OFF + tid * 16) = kin;
    *(LAS u32x4*)(smem + VT_OFF + tid * 16) = vin;
    if (w < 3) *(LAS u32x4*)(smem + edst) = ein;
    WAIT_L(0);
    __builtin_amdgcn_s_barrier();
    CFENCE();
    if (w < 4) {
      char* xo = (char*)xfrag + (size_t)((tok0 >> 5) * 36 * 64 + (tok0 & 31)) * 16;
#pragma unroll
      for (int it = 0; it < 3; ++it) {
        const int id = it * 256 + tid, p16 = id >> 3;
        if (it < 2 || w == 0) {
          const u32x4 v = *(LAS u32x4*)(smem + XG_OFF + id * 16);
          gstore16_sc1(xo + ((p16 >> 1) * 64 + (p16 & 1) * 32 + (id & 7)) * 16, v);
        }
      }
      if (tid < 8) __hip_atomic_store(stats + tok0 + tid, *(LAS u64*)(smem + XG_OFF + 9216 + tid * 8), __ATOMIC_RELAXED, __HIP_MEMORY_SCOPE_AGENT);
      STAMP(1, 10);
      make_tag();
      wait_vm<0>();
      STAMP(1, 11);
      if (lane == 0) {
        const unsigned old = __hip_atomic_fetch_add((LAS unsigned*)(smem + HCNT_OFF + 4), 1u, __ATOMIC_RELAXED, __HIP_MEMORY_SCOPE_WORKGROUP);
        if (old == 3u) set_flag(flg + FL_X + (tok0 >> 3), tag);
      }
    }
    auto kv_compute = [&](int item) ALWAYS_INLINE {
      const int tl = item / 9, cg = item - tl * 9;
      u32x4 kr[4], w4[4];
#pragma unroll
      for (int i = 0; i < 4; ++i) { kr[i] = *(LAS u32x4*)(smem + KT_OFF + (item + 9 * i) * 16); w4[i] = *(LAS u32x4*)(smem + WT_OFF + cg * 64 + i * 16); }
      const u32x4 vr = *(LAS u32x4*)(smem + VT_OFF + item * 16);
#pragma unroll
      for (int e2 = 0; e2 < 4; ++e2) {
        const f32x2 w0 = {bf_lo(w4[e2].x), bf_lo(w4[e2].z)}, w1 = {bf_hi(w4[e2].x), bf_hi(w4[e2].z)};
        const f32x2 w2 = {bf_lo(w4[e2].y), bf_lo(w4[e2].w)}, w3 = {bf_hi(w4[e2].y), bf_hi(w4[e2].w)};
        f32x2 ks = bfr2(unpk(kr[0][e2]) * w3);
        ks = bfr2(ks + bfr2(unpk(kr[1][e2]) * w2));
        ks = bfr2(ks + bfr2(unpk(kr[2][e2]) * w1));
        ks = bfr2(ks + bfr2(unpk(kr[3][e2]) * w0));
        const f32x2 kv = ks * unpk(vr[e2]);
        const unsigned pk = pk_bf16(kv.x, kv.y);
        tile[(8 * cg + 2 * e2) * 72 + (63 - tl)] = (u16)(pk & 0xffffu);
        tile[(8 * cg + 2 * e2 + 1) * 72 + (63 - tl)] = (u16)(pk >> 16);
      }
    };
    kv_compute(tid);
    if (w == 7) kv_compute(512 + lane);
    if (w >= 4) make_tag();
    STAMP(1, 7);
    poll_flags<1>(flg + FL_X + mtile * 32 + w * 4, 4, tag, lane);
    wait_vm<0>();
    STAMP(1, 8);
    sfor<9>([&](auto ic) ALWAYS_INLINE { x_load(ic); });
    WAIT_L(0);
    __builtin_amdgcn_s_barrier();
    CFENCE();
    {
      const int ch = tid >> 3, pp = tid & 7, ch2 = 64 + (lane >> 3);
      const u32x4 o1 = *(LAS u32x4*)(tile + ch * 72 + pp * 8), o2 = *(LAS u32x4*)(tile + ch2 * 72 + pp * 8);
      gstore16_sc1(kvr + (size_t)(c0 + ch) * S_ + (1984 - t0) + pp * 8, o1);
      gstore16_sc1(kvr + (size_t)(c0 + ch2) * S_ + (1984 - t0) + pp * 8, o2);
    }
  }

  const int kh = lane >> 5, prt = (lane >> 4) & 1, qq = (lane & 15) >> 2, p4 = lane & 3;
  const unsigned rbU = lds0 + (8 * kh + qq) * 128 + prt * K1_UOFF + ((((qq >> 1) & 1) | (prt << 1)) << 5) + p4 * 8;

  f32x16 acc[4];
  const f32x16 zero16 = {0.f, 0.f, 0.f, 0.f, 0.f, 0.f, 0.f, 0.f, 0.f, 0.f, 0.f, 0.f, 0.f, 0.f, 0.f, 0.f};

  const int token = mtile * 256 + w * 32 + (lane & 31);
  char* hb = (char*)hout + ((size_t)(token >> 4) * 144 * 64 + (lane >> 5) * 16 + (token & 15)) * 16;
  auto h_word = [&](const f32x16& a, int i) ALWAYS_INLINE -> unsigned {
    return pk_bf16(bfr(a[2 * i]) * bfr(a[2 * i + 8]), bfr(a[2 * i + 1]) * bfr(a[2 * i + 9]));
  };
  auto h_store = [&](const u32x4& o, int c16) ALWAYS_INLINE {
    gstore16_sc1(hb + ((size_t)(c16 >> 1) * 64 + (c16 & 1) * 32) * 16, o);
  };
  u32x4 ho;

  const char* hpA = (const char*)(ssmk + (size_t)(chA + (w >> 2)) * S_ + 64 * (w & 3));
  const char* hpC = (const char*)(ssmk + (size_t)chC * S_ + 32 * w);
  const bool zA = (w & 3) == 0, zC = w == 0;
  const char* pf_sk = (const char*)ssmk;
  const char* pf_wd = (const char*)wd + (size_t)(xcd >> 1) * 1152 * 1152;
  const char* kvb = (const char*)kvr;
  u32x4 caf[24];
  unsigned cax[24];
  bf16x8 cf[24];
  int cvo = 0, cvx = 0;
  auto conv_align = [&](auto ic) ALWAYS_INLINE {
    constexpr int i = ic;
    const unsigned sh = ((lane + 1) & 1) * 2;
    u32x4 r;
    r.x = __builtin_amdgcn_alignbyte(caf[i].y, caf[i].x, sh);
    r.y = __builtin_amdgcn_alignbyte(caf[i].z, caf[i].y, sh);
    r.z = __builtin_amdgcn_alignbyte(caf[i].w, caf[i].z, sh);
    r.w = __builtin_amdgcn_alignbyte(cax[i], caf[i].w, sh);
    cf[i] = __builtin_bit_cast(bf16x8, r);
    asm volatile("" : "+v"(cf[i]));
  };

  STAMP(1, 1);

  constexpr int PD = 3, MID = K1_MID;
  bf16x8 af[PD + 1];
  unsigned rb[4];
#pragma unroll
  for (int j = 0; j < 4; ++j) rb[j] = rbU ^ (j << 5);
#pragma unroll
  for (int g = 0; g < PD; ++g) af[g] = tr_frag((LAS char*)(uintptr_t)rb[g], 0, 512);
  sfor<324>([&](auto gc) ALWAYS_INLINE {
    constexpr int g = gc;
    constexpr int st = g / 36, f = g - st * 36;
    constexpr int s = st == 4 ? f : (st == 8 ? f % 9 : f / 4), c = st == 4 ? 0 : (st == 8 ? f / 9 : f % 4);
    constexpr int xi = st < 4 ? st * 9 + s : (st == 4 ? s : (st - 5) * 9 + s);
    constexpr int ai = st < 4 ? c : (st == 4 ? 0 : (c + 1) & 3);
    constexpr bool first = (st == 0 || st == 5) ? s == 0 : (st == 4 && f == 0);
    if constexpr (f == MID) {

      if constexpr (st < 8) wait_vm<k1_younger(k1_dkey(st + 1, 4), 4 * g)>();
      else { wait_vm<k1_younger(4 * (7 * 36 + MID + 1) + 1, 4 * g)>(); if (w <= 3) WAIT_L(0); }
      __builtin_amdgcn_s_barrier();
      CFENCE();

      if constexpr (st == 1) { if (tid == 0) set_flag(flg + FL_K + (bid & 7) * 32 + (bid >> 3), tag); }
      if constexpr (st == 6) {
        static_assert(k1_dkey(7, 4) > 4 * k1_sstep(4) + 3, "the stage 7 wait must cover the h stores of panels A and Q");
        if (tid == 0) set_flag(flg + FL_HE + (mtile * 4 + (cgrp >> 1)) * 8 + (cgrp & 1) * 4 + cq, tag);
      }
    }
    if constexpr (((f + 36 - MID) % 36) % 9 == 0) __builtin_amdgcn_s_setprio(3 - ((f + 36 - MID) % 36) / 9);
    sfor<35>([&](auto dc) ALWAYS_INLINE { if constexpr (k1_dkey(dc / 5 + 2, dc % 5) == 4 * g + 1) piece_load(dc / 5 + 2, dc % 5); });
    sfor<27>([&](auto ic) ALWAYS_INLINE { if constexpr (k1_xkey(ic + 9) == 4 * g + 2) x_load(std::integral_constant<int, ic + 9>{}); });
    if constexpr (g == 7 * 36 + MID + 1) {
      if (w < 3) {
        const int ch = (w == 2) ? chC : chA + w;
        const unsigned lv = lane * 16;
        poll_flags<1>(flg + FL_K + (grp >> 3) * 32, 32, tag, lane);
#pragma unroll
        for (int i = 0; i < 4; ++i) glds1_sc1(kvb + (size_t)ch * (S_ * 2) + i * 1024, lv, lds0 + XR_OFF + w * XR_STRIDE + i * 1024);
        if (lane < 4) glds1(pf_sk + (size_t)ch * (S_ * 2), lv, lds0 + HZ_OFF + w * 96 + 32);
      } else if (w == 3) {
        if (lane < 32) {
#pragma unroll
          for (int im = 0; im < 3; ++im) *(LAS u32x4*)(smem + XR_OFF + im * XR_STRIDE + 4096 + lane * 16) = (u32x4){0u, 0u, 0u, 0u};
        } else if (lane < 38) {
          const int im = (lane - 32) >> 1, hh = lane & 1;
          *(LAS u32x4*)(smem + HZ_OFF + im * 96 + hh * 16) = (u32x4){0u, 0u, 0u, 0u};
        } else if (lane == 38) *(LAS unsigned*)(smem + HCNT_OFF) = 0u;
      }
    }
    sfor<24>([&](auto ac) ALWAYS_INLINE {
      if constexpr (k1_astep(ac) == g) {
        asm volatile("s_waitcnt vmcnt(%2)" : "+v"(caf[ac]), "+v"(cax[ac]) : "i"(k1_younger(4 * k1_cstep(ac) + 2, 4 * g + 2)) : "memory");
        conv_align(ac);
      }
    });
    if constexpr (g == K1_TOUCH) {
      int l2 = lane; asm volatile("" : "+v"(l2));
      const unsigned line = (unsigned)(w * 64 + l2 < 324 ? idx * 324 + w * 64 + l2 : idx * 324);
      touch_lines(pf_wd, line * 128u, lds0 + K1_PF_OFF + 16);
    }
    sfor<24>([&](auto cc) ALWAYS_INLINE {
      constexpr int ci = cc;
      if constexpr (k1_cstep(ci) == g) {
        if constexpr (ci == 0) {
          int l2 = lane; asm volatile("" : "+v"(l2));
          const int e = (l2 & 15) - 15 + 8 * (l2 >> 4);
          cvo = (e & ~1) * 2; cvx = cvo + ((e & 1) ? 16 : 0);
        }
        constexpr int cb = ci < 16 ? 64 * (ci >> 3) + 512 * (ci & 7) : 512 * (ci - 16);
        const char* hp = (ci < 16 ? hpA : hpC) + cb + ((ci == 0 && zA) || (ci == 16 && zC) ? 32 : 0);
        gload16u(caf[ci], hp - 32, (unsigned)(cvo + 32));
        gload4u(cax[ci], hp - 32, (unsigned)(cvx + 32));
      }
    });
    if constexpr (g + PD < 324) {
      constexpr int g1 = g + PD, st1 = g1 / 36, f1 = g1 - st1 * 36;
      if constexpr (f1 == 0) {
        if constexpr (st1 != 4) {
          unsigned b2 = rbU; asm volatile("" : "+v"(b2));
#pragma unroll
          for (int j = 0; j < 4; ++j) { rb[j] = (b2 + (st1 % K1_NSLOT) * K1_SLOT) ^ (j << 5); asm volatile("" : "+v"(rb[j])); }
        } else {
          int l2 = lane; asm volatile("" : "+v"(l2));
          rb[0] = lds0 + (8 * (l2 >> 5) + ((l2 & 15) >> 2)) * 32 + ((l2 >> 4) & 1) * K1_UOFF + (l2 & 3) * 8 + (st1 % K1_NSLOT) * K1_SLOT;
          asm volatile("" : "+v"(rb[0]));
        }
      }
      if constexpr (st1 == 8) af[g1 % (PD + 1)] = tr_frag((LAS char*)(uintptr_t)rb[f1 / 9], (f1 % 9) * 2048, 512);
      else if constexpr (st1 != 4) af[g1 % (PD + 1)] = tr_frag((LAS char*)(uintptr_t)rb[f1 % 4], (f1 / 4) * 2048, 512);
      else af[g1 % (PD + 1)] = tr_frag((LAS char*)(uintptr_t)rb[0], f1 * 512, 128);
    }
    if constexpr (st < 4 && c == 0) {
      asm volatile("s_waitcnt vmcnt(%1)" : "+v"(xf[xi]) : "i"(k1_younger(k1_xkey(xi), 4 * g + 3)) : "memory");
    }
    SCHED0();
    acc[ai] = __builtin_amdgcn_mfma_f32_32x32x16_bf16(af[g % (PD + 1)], xf[xi], first ? zero16 : acc[ai], 0, 0, 0);
    SCHED0();
    if constexpr (g == 143) {
#pragma unroll
      for (int i = 0; i < 4; ++i) ho[i] = h_word(acc[0], i);
      h_store(ho, colA >> 4);
      SCHED0();
    }
    if constexpr (g > 144 && g <= 162) {
      constexpr int cc = (g - 145) / 6 + 1, pi = (g - 145) % 6;
      if constexpr (pi < 4) ho[pi] = h_word(acc[cc], pi);
      if constexpr (pi == 5) h_store(ho, (colA >> 4) + cc);
      SCHED0();
    }
    if constexpr (g >= 298 && (g - 298) % 9 < 5 && (g - 298) / 9 < 3) {
      constexpr int cc = (g - 298) / 9, pi = (g - 298) % 9;
      if constexpr (pi < 4) ho[pi] = h_word(acc[(cc + 1) & 3], pi);
      if constexpr (pi == 4) h_store(ho, (colA >> 4) + 4 + cc);
      SCHED0();
    }
    if constexpr (g >= 180 && g <= 182) {
      if constexpr (g == 180) { ho[0] = h_word(acc[0], 0); ho[1] = h_word(acc[0], 1); }
      if constexpr (g == 181) { ho[2] = h_word(acc[0], 2); ho[3] = h_word(acc[0], 3); }
      if constexpr (g == 182) h_store(ho, colQ >> 4);
      SCHED0();
    }
  });
  const char* wbase2 = (const char*)wd + (size_t)kbase * (D_ * 2);
  unsigned voff2[4];
  int lane2 = lane; asm volatile("" : "+v"(lane2));
#pragma unroll
  for (int n = 0; n < 4; ++n) {
    const int o = (tq * 4 + n) * 1024 + lane2 * 16;
    const int k = o >> 8, pc = (o >> 4) & 15;
    const int j = (pc >> 1) ^ (k & 7);
    voff2[n] = (unsigned)(k * (D_ * 2) + (2 * j + (pc & 1)) * 16);
  }
  const unsigned ldst2 = lds0 + khf * K2_HALF;
  auto piece_load2 = [&](int st, int n) ALWAYS_INLINE {
    const int slot = st % K2_NSLOT;
    if (st < 9) {
      const char* g = wbase2 + (size_t)k2_unit(st) * 64 * (D_ * 2) + colA2 * 2;
      glds1p<false>(g, voff2[n], ldst2 + slot * K2_SLOT + tq * 4096 + n * 1024);
    } else {
      int l2 = lane; asm volatile("" : "+v"(l2));
      const char* g = wbase2 + (size_t)(st - 9) * 288 * (D_ * 2) + colQ2 * 2;
      const int o = n < 2 ? tq * 2048 + n * 1024 + l2 * 16 : 8192 + tq * 256 + (l2 & 15) * 16;
      const unsigned v0 = (unsigned)((o >> 5) * (D_ * 2) + ((o >> 4) & 1) * 16);
      if (n < 2 || l2 < 16) glds1p<false>(g, v0, ldst2 + slot * K2_SLOT + (n < 2 ? tq * 2048 + n * 1024 : 8192 + tq * 256));
    }
  };
  const char* hbase = (const char*)hout + ((size_t)(mt * 8 + tq * 2) * 144 * 64 + (size_t)(kbase >> 5) * 64) * 16;
  const unsigned hv0 = lane2 * 16 + khf * (144 * 64 * 16), hv1 = lane2 * 16 + (1 - khf) * (144 * 64 * 16);
  bf16x8 hf[18][2];
  auto h_load = [&](auto lc) ALWAYS_INLINE {
    constexpr int k = 2 * k2_unit(lc / 4) + (lc / 2) % 2, t = lc % 2;
    gload16_sc1<(k & 3) * 1024>(hf[k][t], hbase + (k >> 2) * 4096, t ? hv1 : hv0);
  };
  STAMP(1, 2);
  __builtin_amdgcn_s_setprio(0);
  {
    u32x4 o;
#pragma unroll
    for (int i = 0; i < 4; ++i) o[i] = h_word(acc[0], i);
    h_store(o, (colA >> 4) + 7);
  }

  __builtin_amdgcn_s_barrier();
  CFENCE();
#pragma unroll
  for (int n = 0; n < 4; ++n) piece_load2(0, n);
#pragma unroll
  for (int n = 0; n < 4; ++n) piece_load2(1, n);

  STAMP(1, 3);
  const int ct0 = lane & 15, ckg = lane >> 4;
  {
    const int boff = (2032 - 16 * (lane & 15) + 8 * ckg) * 2;
    auto head_frag = [&](int im) ALWAYS_INLINE -> bf16x8 {
      bf16x8 r;
      LAS u16* hz = (LAS u16*)(smem + HZ_OFF + im * 96) + 16 + (ct0 - 15 + 8 * ckg);
#pragma unroll
      for (int j = 0; j < 8; ++j) r[j] = (short)hz[j];
      return r;
    };
    if (zA) cf[0] = head_frag(w >> 2);
    if (zC) cf[16] = head_frag(2);
    {
      const int im = w >> 2;
      LAS char* xr = smem + XR_OFF + im * XR_STRIDE + boff + 128 * (w & 3);
      f32x4 ac[8];
#pragma unroll
      for (int j = 0; j < 8; ++j) ac[j] = (f32x4){0.f, 0.f, 0.f, 0.f};
#pragma unroll
      for (int rr = 1; rr >= 0; --rr) {
        bf16x8 bfm[8];
#pragma unroll
        for (int b = 0; b < 8; ++b) bfm[b] = *(LAS bf16x8*)(xr - 512 * b + 64 * rr);
#pragma unroll
        for (int a = 7; a >= 0; --a)
#pragma unroll
          for (int j = a; j < 8; ++j) ac[j] = __builtin_amdgcn_mfma_f32_16x16x32_bf16(cf[rr * 8 + a], bfm[j - a], ac[j], 0, 0, 0);
        if (rr == 1) {
          SCHED0();
          wait_vm<8>();
          if (lane == 0) {
            const unsigned old = __hip_atomic_fetch_add((LAS unsigned*)(smem + HCNT_OFF), 1u, __ATOMIC_RELAXED, __HIP_MEMORY_SCOPE_WORKGROUP);
            if (old == 7u) set_flag(flg + FL_H + (mtile * 4 + (cgrp >> 1)) * 8 + (cgrp & 1) * 4 + cq, tag);
          }
          SCHED0();
        }
      }
#pragma unroll
      for (int j = 0; j < 8; ++j)
        *(LAS f32x4*)(smem + PART_OFF + w * 8192 + (j * 64 + lane) * 16) = ac[j];
    }
    poll_flags<1>(flg + FL_HE + ((mt >> 1) * 4 + ksl) * 8, 8, tag, lane);
    sfor<12>([&](auto kc) ALWAYS_INLINE { h_load(kc); });
    {
      LAS char* xr = smem + XR_OFF + 2 * XR_STRIDE + boff + 64 * w;
      const int jA = 7 - qd, jB = qd;
      bf16x8 b0[8], b1[4];
#pragma unroll
      for (int a = 0; a < 8; ++a) { const int bb = jA - a; b0[a] = *(LAS bf16x8*)(xr - 512 * (bb < 0 ? 0 : bb)); }
#pragma unroll
      for (int a = 0; a < 4; ++a) { const int bb = jB - a; b1[a] = *(LAS bf16x8*)(xr - 512 * (bb < 0 ? 0 : bb)); }
      f32x4 c0 = (f32x4){0.f, 0.f, 0.f, 0.f}, c1 = c0;
#pragma unroll
      for (int a = 7; a >= 0; --a) {
        if (a <= jA) c0 = __builtin_amdgcn_mfma_f32_16x16x32_bf16(cf[16 + a], b0[a], c0, 0, 0, 0);
        if (a < 4 && a <= jB) c1 = __builtin_amdgcn_mfma_f32_16x16x32_bf16(cf[16 + a], b1[a < 4 ? a : 0], c1, 0, 0, 0);
      }
      *(LAS f32x4*)(smem + PARTC_OFF + w * 2048 + lane * 16) = c0;
      *(LAS f32x4*)(smem + PARTC_OFF + w * 2048 + 1024 + lane * 16) = c1;
    }
  }
  STAMP(1, 4);
  WAIT_L(0);
  __builtin_amdgcn_s_barrier();
  CFENCE();
  STAMP(1, 5);
  auto finalize = [&](f32x4 s0, f32x4 s1, int im, int ch, int t8, float dd) ALWAYS_INLINE {
    const u32x4 kvv = *(LAS u32x4*)(smem + XR_OFF + im * XR_STRIDE + (2040 - t8) * 2);
    float y[8] = {s0[0], s0[1], s0[2], s0[3], s1[0], s1[1], s1[2], s1[3]};
    float o[8];
#pragma unroll
    for (int e = 0; e < 8; ++e) {
      const int ri = 7 - e;
      const float kvf = (ri & 1) ? bf_hi(kvv[ri >> 1]) : bf_lo(kvv[ri >> 1]);
      o[e] = bfr(y[e]) + bfr(kvf * dd);
    }
    u32x4 ov; ov.x = pk_bf16(o[0], o[1]); ov.y = pk_bf16(o[2], o[3]); ov.z = pk_bf16(o[4], o[5]); ov.w = pk_bf16(o[6], o[7]);
    gstore16_sc1(wT + (size_t)ch * S_ + t8, ov);
  };
  {
    const int im = tid >> 8, t8 = (tid & 255) * 8;
    const int po = ((t8 >> 8) * 64 + ((t8 >> 2) & 3) * 16 + ((t8 >> 4) & 15)) * 16;
    f32x4 s0 = (f32x4){0.f, 0.f, 0.f, 0.f}, s1 = s0;
#pragma unroll
    for (int ww = 0; ww < 4; ++ww) {
      s0 += *(LAS f32x4*)(smem + PART_OFF + (im * 4 + ww) * 8192 + po);
      s1 += *(LAS f32x4*)(smem + PART_OFF + (im * 4 + ww) * 8192 + po + 256);
    }
    finalize(s0, s1, im, chA + im, t8, ddA);
  }
  if (tid >= 448) {
    const int t2 = tid - 448;
    const int tl = t2 >> 5, l8 = (t2 & 31) * 8;
    const int jt = tl ? qd : 7 - qd, t8 = 256 * jt + l8;
    const int po = (((l8 >> 2) & 3) * 16 + (l8 >> 4)) * 16;
    f32x4 s0 = (f32x4){0.f, 0.f, 0.f, 0.f}, s1 = s0;
#pragma unroll
    for (int ww = 0; ww < 8; ++ww) {
      s0 += *(LAS f32x4*)(smem + PARTC_OFF + ww * 2048 + tl * 1024 + po);
      s1 += *(LAS f32x4*)(smem + PARTC_OFF + ww * 2048 + tl * 1024 + po + 256);
    }
    finalize(s0, s1, 2, chC, t8, ddC);
  }
  STAMP(1, 6);

  STAMP(2, 0);
  {
  const int kq = lane2 >> 4, qq = (lane2 & 15) >> 2, p4 = lane2 & 3;
  const unsigned rbU2 = lds0 + khf * K2_HALF + (16 * (kq >> 1) + 4 * (kq & 1) + qq) * 256 + ((4 * (kq & 1) + qq) << 5) + p4 * 8;

  const char* wtb = (const char*)(wT + mt * 128 + ksl * 32);
  f32x4 acc[2][8], accq[2];
  const f32x4 zero4 = {0.f, 0.f, 0.f, 0.f};

  const int tile = mt * 4 + nt;
  const __amdgpu_buffer_rsrc_t rs = __builtin_amdgcn_make_buffer_rsrc((char*)slab + (size_t)tile * (16 * 18432), 0, 16 * 18432, 0x00020000);
  const bool foreign = tq != ksl;
  auto give = [&](int xb, int i, const f32x4& a0, const f32x4& a1) ALWAYS_INLINE {
    *(LAS f32x4*)(smem + xb + ((w * 4 + i) * 64 + lane) * 16) = a1;
  };
  auto publish = [&](int xb, int i, const f32x4& a0, const f32x4& a1, int dtg) ALWAYS_INLINE {
    const f32x4 sm = a0 + *(LAS f32x4*)(smem + xb + (((w ^ 4) * 4 + i) * 64 + lane) * 16);
    if (foreign) __builtin_amdgcn_raw_buffer_store_b128(__builtin_bit_cast(u32x4, sm), rs, (unsigned)(((tq * 4 + ksl) * 18 + khf * 9 + dtg) * 1024 + lane * 16), 0, 16);
    else *(LAS f32x4*)(smem + K2_OWN_OFF + ((khf * 9 + dtg) * 64 + lane) * 16) = sm;
  };
  constexpr int XB1 = 3 * K2_SLOT, XB2 = 0, XB3 = 3 * K2_SLOT;

  asm volatile("s_waitcnt vmcnt(%4)" : "+v"(hf[0][0]), "+v"(hf[0][1]), "+v"(hf[1][0]), "+v"(hf[1][1])
               : "i"(k2_younger(k2_hkey(0, 3), 0, false)) : "memory");
  WAIT_L(0);
  __builtin_amdgcn_s_barrier();
  CFENCE();
  STAMP(2, 1);

  constexpr int PD = 2;
  bf16x8 af[PD + 1];
  unsigned sb = rbU2;
#pragma unroll
  for (int g = 0; g < PD; ++g) af[g] = tr_frag((LAS char*)(uintptr_t)(sb ^ (g << 5)), 0, 2048);
  sfor<162>([&](auto gc) ALWAYS_INLINE {
    constexpr int g = gc;
    constexpr int st = k2_stage(g), f = g - k2_g0(st);
    constexpr int ks = st < 9 ? k2_unit(st) * 2 + f / 8 : (st - 9) * 9 + f, dt = st < 9 ? f % 8 : 0;
    if constexpr (f == k2_mid(st)) {
      if constexpr (st < 8) { STAMP(0, 2 * st); }
      if constexpr (st == 2) { if (w == 0) poll_flags<1>(flg + FL_H + ((mt >> 1) * 4 + ksl) * 8, 8, tag, lane); }
      if constexpr (st == K2_WST) { if (w == 0) poll_flags<4>(flg + FL_W, 64, tag, lane); }
      if constexpr (st < 10) {
        constexpr int okey = cmax(k2_dkey(st + 1, k2_np(st + 1) - 1), st + 1 < 9 ? k2_hkey(st + 1, 3) : -1000);
        constexpr int n0 = k2_younger(okey, 4 * g, false);
        if constexpr (st == 0) { if (w == 7) wait_vm<n0 + 1>(); else wait_vm<n0>(); }
        else wait_vm<n0>();
        if constexpr (st + 1 < 9) {
          constexpr int k0 = 2 * k2_unit(st + 1);
          asm volatile("" : "+v"(hf[k0][0]), "+v"(hf[k0][1]), "+v"(hf[k0 + 1][0]), "+v"(hf[k0 + 1][1])::"memory");
        }
      }
      if constexpr (st >= 9) WAIT_L(0);
      __builtin_amdgcn_s_barrier();
      CFENCE();
      if constexpr (st < 8) { STAMP(0, 2 * st + 1); }
      if constexpr (st == 1) { if (tid == 0) set_flag(flg + FL_W + bid, tag); }
      if constexpr (st == K2_WST) {
        int l2 = lane; asm volatile("" : "+v"(l2));
        glds1_sc1(wtb, (unsigned)((colA2 + w * 16 + (l2 >> 2)) * (S_ * 2) + (l2 & 3) * 16), lds0 + K2_WT_OFF + w * 1024);
        glds1_sc1(wtb, (unsigned)((colQ2 + (l2 >> 2)) * (S_ * 2) + (l2 & 3) * 16), lds0 + K2_WT_OFF + 8192);
      }
      if constexpr (st == 9) {
#pragma unroll
        for (int i = 0; i < 4; ++i) publish(XB1, i, acc[0][i], acc[1][i], i);
#pragma unroll
        for (int i = 0; i < 4; ++i) give(XB2, i, acc[0][4 + i], acc[1][4 + i]);
      }
      if constexpr (st == 10) {
#pragma unroll
        for (int i = 0; i < 4; ++i) publish(XB2, i, acc[0][4 + i], acc[1][4 + i], 4 + i);
      }
    }
    if constexpr (st < 9 && ((f + 16 - 6) % 16) % 4 == 0) __builtin_amdgcn_s_setprio(3 - ((f + 16 - 6) % 16) / 4);
    sfor<36>([&](auto dc) ALWAYS_INLINE { if constexpr (dc % 4 < k2_np(dc / 4 + 2) && k2_dkey(dc / 4 + 2, dc % 4) == 4 * g + 2) piece_load2(dc / 4 + 2, dc % 4); });
    sfor<24>([&](auto hc) ALWAYS_INLINE { if constexpr (k2_hkey(hc / 4 + 3, hc % 4) == 4 * g + 3) h_load(std::integral_constant<int, hc + 12>{}); });
    if constexpr (g + PD < 162) {
      constexpr int g1 = g + PD, st1 = k2_stage(g1), f1 = g1 - k2_g0(st1);
      if constexpr (f1 == 0) {
        if constexpr (st1 < 9) {
          sb += (st1 % K2_NSLOT == 0) ? -3 * K2_SLOT : K2_SLOT;
        } else {
          int l2 = lane; asm volatile("" : "+v"(l2));
          const int kq2 = l2 >> 4;
          sb = lds0 + khf * K2_HALF + (16 * (kq2 >> 1) + 4 * (kq2 & 1) + ((l2 & 15) >> 2)) * 32 + (l2 & 3) * 8 + (st1 % K2_NSLOT) * K2_SLOT;
        }
      }
      asm volatile("" : "+v"(sb));
      if constexpr (st1 < 9) af[g1 % (PD + 1)] = tr_frag((LAS char*)(uintptr_t)(sb ^ ((f1 % 8) << 5)), (f1 / 8) * 8192, 2048);
      else af[g1 % (PD + 1)] = tr_frag((LAS char*)(uintptr_t)sb, f1 * 1024, 256);
    }
    SCHED0();
#pragma unroll
    for (int t = 0; t < 2; ++t) {
      if constexpr (st < 9) acc[t][dt] = __builtin_amdgcn_mfma_f32_16x16x32_bf16(af[g % (PD + 1)], hf[ks][t], g < 8 ? zero4 : acc[t][dt], 0, 0, 0);
      else accq[t] = __builtin_amdgcn_mfma_f32_16x16x32_bf16(af[g % (PD + 1)], hf[ks][t], g == 144 ? zero4 : accq[t], 0, 0, 0);
    }
    SCHED0();
    if constexpr (g == 143) {
#pragma unroll
      for (int i = 0; i < 4; ++i) give(XB1, i, acc[0][i], acc[1][i]);
      SCHED0();
    }
  });
  STAMP(2, 2);
  __builtin_amdgcn_s_setprio(0);
  give(XB3, 0, accq[0], accq[1]);

  int lane3 = lane; asm volatile("" : "+v"(lane3));
  u32x2 xq[3];
  float2 stt[2];
#pragma unroll
  for (int i = 0; i < 3; ++i) {
    const int t = i < 2 ? i : (w & 1);
    const int token = mt * 128 + ksl * 32 + t * 16 + (lane3 & 15);
    xq[i] = *(const u32x2*)(q + (size_t)token * D_ + (i < 2 ? colA2 + w * 16 : colQ2) + 4 * (lane3 >> 4));
    if (i < 2) {
      const u64 sv = __hip_atomic_load(stats + token, __ATOMIC_RELAXED, __HIP_MEMORY_SCOPE_AGENT);
      stt[i] = make_float2(__uint_as_float((unsigned)sv), __uint_as_float((unsigned)(sv >> 32)));
    }
  }
  WAIT_L(0);
  __builtin_amdgcn_s_barrier();
  CFENCE();
  publish(XB3, 0, accq[0], accq[1], 8);
  asm volatile("s_waitcnt vmcnt(0) lgkmcnt(0)" ::: "memory");
  __builtin_amdgcn_s_barrier();
  CFENCE();
  STAMP(2, 3);
  if (tid == 0) set_flag(flg + FL_S + tile * 4 + ksl, tag);
  poll_flags<1>(flg + FL_S + tile * 4, 4, tag, lane3);
  STAMP(2, 4);
  auto fetch = [&](f32x4 (&pv)[3], int fi) ALWAYS_INLINE {
#pragma unroll
    for (int k = 1; k < 4; ++k)
      pv[k - 1] = __builtin_bit_cast(f32x4, __builtin_amdgcn_raw_buffer_load_b128(rs, (unsigned)(((ksl * 4 + ((ksl + k) & 3)) * 18 + fi) * 1024 + lane3 * 16), 0, 16));
  };
  auto combine = [&](const f32x4 (&pv)[3], int t, int dt, const u32x2& xw2, const float2& st2) ALWAYS_INLINE {
    const int tl = t * 16 + (lane3 & 15), dl = dt * 16 + 4 * (lane3 >> 4);
    const f32x4 own = *(LAS f32x4*)(smem + K2_OWN_OFF + ((t * 9 + dt) * 64 + lane3) * 16);
    const f32x4 bs = ((own + pv[0]) + pv[1]) + pv[2];
    float o[4];
#pragma unroll
    for (int c = 0; c < 4; ++c) {
      const unsigned xw = (c < 2) ? xw2.x : xw2.y;
      const float x = (c & 1) ? bf_hi(xw) : bf_lo(xw);
      const float wy = bf2f(*(LAS u16*)(smem + K2_WT_OFF + (dl + c) * 64 + tl * 2));
      const float xn = bfr(bfr(x - st2.x) * st2.y);
      const float a = bfr(xn * wy);
      o[c] = bfr(x + a) + bfr(bs[c]);
    }
    u32x2 ov; ov.x = pk_bf16(o[0], o[1]); ov.y = pk_bf16(o[2], o[3]);
    *(u32x2*)(out + (size_t)(mt * 128 + ksl * 32 + tl) * D_ + (dt < 8 ? colA2 + dt * 16 : colQ2) + 4 * (lane3 >> 4)) = ov;
  };
  {
    f32x4 p0[3], p1[3], p2[3];
    fetch(p0, w); fetch(p1, 9 + w); fetch(p2, 9 * (w & 1) + 8);
    combine(p0, 0, w, xq[0], stt[0]);
    combine(p1, 1, w, xq[1], stt[1]);
    if (w < 2) combine(p2, w, 8, xq[2], w ? stt[1] : stt[0]);
  }
  if (bid == 0 && w == 0) {
    poll_flags<4>(flg + FL_K, 64, tag, lane);
    if (lane == 0) set_flag(flg + FL_GEN, tag);
  }
  STAMP(2, 5); RSTAMP(1, 15); STAMP_FLUSH(1); STAMP_FLUSH(2); STAMP_FLUSH(0);
  }
}

extern "C" void kernel_launch(void* const* d_in, const int* in_sizes, int n_in,
                              void* d_out, int out_size, void* d_ws, size_t ws_size,
                              hipStream_t stream) {
  const u16* q = (const u16*)d_in[0];
  const u16* k = (const u16*)d_in[1];
  const u16* v = (const u16*)d_in[2];
  const u16* shk = (const u16*)d_in[3];
  const u16* ssmk = (const u16*)d_in[4];
  const u16* ssmd = (const u16*)d_in[5];
  const u16* wg = (const u16*)d_in[7];
  const u16* wu = (const u16*)d_in[8];
  const u16* wd = (const u16*)d_in[9];
  (void)hipFuncSetAttribute((const void*)k_fused, hipFuncAttributeMaxDynamicSharedMemorySize, K1_LDS);
  k_fused<<<256, 512, K1_LDS, stream>>>(q, k, v, shk, ssmk, ssmd, wg, wu, wd, (char*)d_ws, (u16*)d_out);
}
```

```cpp
#include <hip/hip_runtime.h>
#include <hip/hip_bf16.h>
#include <stdint.h>
#include <utility>

typedef unsigned short u16;
typedef short bf16x8 __attribute__((ext_vector_type(8)));
typedef short s16x4 __attribute__((ext_vector_type(4)));
typedef float f32x4 __attribute__((ext_vector_type(4)));
typedef float f32x16 __attribute__((ext_vector_type(16)));
typedef unsigned u32x4 __attribute__((ext_vector_type(4)));
typedef unsigned u32x2 __attribute__((ext_vector_type(2)));
typedef __bf16 bf2_t __attribute__((ext_vector_type(2)));
#define LAS __attribute__((address_space(3)))
#define DI __device__ __forceinline__

constexpr int S_ = 2048, D_ = 576, F_ = 4608;

constexpr size_t WS_XFRAG = 0;
constexpr size_t WS_STATS = WS_XFRAG + (size_t)S_ * D_ * 2;
constexpr size_t WS_KVR   = WS_STATS + (size_t)S_ * 8;
constexpr size_t WS_WT    = WS_KVR + (size_t)S_ * D_ * 2;
constexpr size_t WS_H     = WS_WT + (size_t)S_ * D_ * 2;
constexpr size_t WS_SLAB  = WS_H + (size_t)S_ * F_ * 2;
constexpr size_t WS_FLG   = WS_SLAB + (size_t)4 * S_ * D_ * 4;
constexpr int FL_GEN = 0, FL_X = 32, FL_K = FL_X + 256, FL_H = FL_K + 256, FL_HE = FL_H + 256, FL_W = FL_HE + 256, FL_S = FL_W + 256, FL_END = FL_S + 256;
constexpr size_t WS_END   = WS_FLG + FL_END * 8;
typedef unsigned long long u64;

DI float bf_lo(unsigned w) { return __uint_as_float(w << 16); }
DI float bf_hi(unsigned w) { return __uint_as_float(w & 0xffff0000u); }
DI float bf2f(u16 b) { return __uint_as_float(((unsigned)b) << 16); }
DI float bfr(float x) { return (float)(__bf16)x; }
typedef float f32x2 __attribute__((ext_vector_type(2)));
DI unsigned pk_bf16(float lo, float hi);
DI f32x2 unpk(unsigned w) { return (f32x2){__uint_as_float(w << 16), __uint_as_float(w & 0xffff0000u)}; }
DI f32x2 bfr2(f32x2 v) { return unpk(pk_bf16(v.x, v.y)); }
DI unsigned pk_bf16(float lo, float hi) { bf2_t v = {(__bf16)lo, (__bf16)hi}; return __builtin_bit_cast(unsigned, v); }

DI float wave_sum(float x) {
  x += __builtin_bit_cast(float, __builtin_amdgcn_update_dpp(0, __builtin_bit_cast(int, x), 0xB1, 0xf, 0xf, true));
  x += __builtin_bit_cast(float, __builtin_amdgcn_update_dpp(0, __builtin_bit_cast(int, x), 0x4E, 0xf, 0xf, true));
  x += __builtin_bit_cast(float, __builtin_amdgcn_update_dpp(0, __builtin_bit_cast(int, x), 0x141, 0xf, 0xf, true));
  x += __builtin_bit_cast(float, __builtin_amdgcn_update_dpp(0, __builtin_bit_cast(int, x), 0x140, 0xf, 0xf, true));
  const int xi = __builtin_bit_cast(int, x);
  return (__builtin_bit_cast(float, __builtin_amdgcn_readlane(xi, 0)) + __builtin_bit_cast(float, __builtin_amdgcn_readlane(xi, 16))) +
         (__builtin_bit_cast(float, __builtin_amdgcn_readlane(xi, 32)) + __builtin_bit_cast(float, __builtin_amdgcn_readlane(xi, 48)));
}
#define WAIT_V(n) asm volatile("s_waitcnt vmcnt(" #n ")" ::: "memory")
#define WAIT_L(n) asm volatile("s_waitcnt lgkmcnt(" #n ")" ::: "memory")
#define CFENCE() asm volatile("" ::: "memory")
#define STAMP(kid, i)
#define RSTAMP(kid, i)
#define STAMP_DECL
#define STAMP_FLUSH(kid)
#define K0STAMP(i)
#define SCHED0() __builtin_amdgcn_sched_barrier(0)
template <int... I, class Fn> DI void sfor_impl(std::integer_sequence<int, I...>, Fn&& f) { (f(std::integral_constant<int, I>{}), ...); }
template <int N, class Fn> DI void sfor(Fn&& f) { sfor_impl(std::make_integer_sequence<int, N>{}, f); }
#define ALWAYS_INLINE __attribute__((always_inline))
template <int N> DI void wait_vm() { asm volatile("s_waitcnt vmcnt(%0)" ::"i"(N) : "memory"); }

DI void glds4(const void* sbase, unsigned v0, unsigned v1, unsigned v2, unsigned v3, unsigned lds_dst) {
  unsigned keep;
  asm volatile("s_mov_b32 %0, m0\n\ts_mov_b32 m0, %6\n\ts_nop 0\n\t"
               "global_load_lds_dwordx4 %1, %5\n\t"
               "global_load_lds_dwordx4 %2, %5 offset:1024\n\t"
               "global_load_lds_dwordx4 %3, %5 offset:2048\n\t"
               "global_load_lds_dwordx4 %4, %5 offset:3072\n\t"
               "s_mov_b32 m0, %0"
               : "=&s"(keep) : "v"(v0), "v"(v1), "v"(v2), "v"(v3), "s"(sbase), "s"(lds_dst) : "memory");
}
DI void glds3(const void* sbase, unsigned v0, unsigned v1, unsigned v2, unsigned lds_dst) {
  unsigned keep;
  asm volatile("s_mov_b32 %0, m0\n\ts_mov_b32 m0, %5\n\ts_nop 0\n\t"
               "global_load_lds_dwordx4 %1, %4\n\t"
               "global_load_lds_dwordx4 %2, %4 offset:1024\n\t"
               "global_load_lds_dwordx4 %3, %4 offset:2048\n\t"
               "s_mov_b32 m0, %0"
               : "=&s"(keep) : "v"(v0), "v"(v1), "v"(v2), "s"(sbase), "s"(lds_dst) : "memory");
}
DI void glds2(const void* sbase, unsigned v0, unsigned v1, unsigned lds_dst) {
  unsigned keep;
  asm volatile("s_mov_b32 %0, m0\n\ts_mov_b32 m0, %4\n\ts_nop 0\n\t"
               "global_load_lds_dwordx4 %1, %3\n\t"
               "global_load_lds_dwordx4 %2, %3 offset:1024\n\t"
               "s_mov_b32 m0, %0"
               : "=&s"(keep) : "v"(v0), "v"(v1), "s"(sbase), "s"(lds_dst) : "memory");
}
DI void glds1(const void* sbase, unsigned v0, unsigned lds_dst) {
  unsigned keep;
  asm volatile("s_mov_b32 %0, m0\n\ts_mov_b32 m0, %3\n\ts_nop 0\n\t"
               "global_load_lds_dwordx4 %1, %2\n\t"
               "s_mov_b32 m0, %0"
               : "=&s"(keep) : "v"(v0), "s"(sbase), "s"(lds_dst) : "memory");
}

template <bool NT> DI void glds1p(const void* sbase, unsigned v0, unsigned lds_dst) {
  unsigned keep;
  if constexpr (NT)
    asm volatile("s_mov_b32 %0, m0\n\ts_mov_b32 m0, %3\n\ts_nop 0\n\tglobal_load_lds_dwordx4 %1, %2 nt\n\ts_mov_b32 m0, %0"
                 : "=&s"(keep) : "v"(v0), "s"(sbase), "s"(lds_dst) : "memory");
  else
    asm volatile("s_mov_b32 %0, m0\n\ts_mov_b32 m0, %3\n\ts_nop 0\n\tglobal_load_lds_dwordx4 %1, %2\n\ts_mov_b32 m0, %0"
                 : "=&s"(keep) : "v"(v0), "s"(sbase), "s"(lds_dst) : "memory");
}
DI void glds1_sc1(const void* sbase, unsigned v0, unsigned lds_dst) {
  unsigned keep;
  asm volatile("s_mov_b32 %0, m0\n\ts_mov_b32 m0, %3\n\ts_nop 0\n\tglobal_load_lds_dwordx4 %1, %2 sc1\n\ts_mov_b32 m0, %0"
               : "=&s"(keep) : "v"(v0), "s"(sbase), "s"(lds_dst) : "memory");
}
DI void touch_lines(const void* sbase, unsigned v0, unsigned lds_dst) {
  unsigned keep;
  asm volatile("s_mov_b32 %0, m0\n\ts_mov_b32 m0, %3\n\ts_nop 0\n\tglobal_load_lds_dword %1, %2\n\ts_mov_b32 m0, %0"
               : "=&s"(keep) : "v"(v0), "s"(sbase), "s"(lds_dst) : "memory");
}
template <int OFF> DI void gload16(bf16x8& dst, const void* sbase, unsigned voff) {
  asm volatile("global_load_dwordx4 %0, %1, %2 offset:%3" : "=v"(dst) : "v"(voff), "s"(sbase), "i"(OFF) : "memory");
}
template <int OFF> DI void gload16_sc1(bf16x8& dst, const void* sbase, unsigned voff) {
  asm volatile("global_load_dwordx4 %0, %1, %2 offset:%3 sc1" : "=v"(dst) : "v"(voff), "s"(sbase), "i"(OFF) : "memory");
}
DI void gload16u(u32x4& dst, const void* sbase, unsigned voff) {
  asm volatile("global_load_dwordx4 %0, %1, %2" : "=v"(dst) : "v"(voff), "s"(sbase) : "memory");
}
template <int OFF> DI void gstore16_sc1_s(const void* sbase, unsigned voff, const u32x4& v) {
  asm volatile("global_store_dwordx4 %0, %1, %2 offset:%3 sc1\n\ts_nop 1" ::"v"(voff), "v"(v), "s"(sbase), "i"(OFF) : "memory");
}
DI void gload4u(unsigned& dst, const void* sbase, unsigned voff) {
  asm volatile("global_load_dword %0, %1, %2" : "=v"(dst) : "v"(voff), "s"(sbase) : "memory");
}
DI void gstore16_sc1(void* p, const u32x4& v) { asm volatile("global_store_dwordx4 %0, %1, off sc1\n\ts_nop 1" ::"v"(p), "v"(v) : "memory"); }
DI void set_flag(u64* f, u64 tag) { __hip_atomic_store(f, tag, __ATOMIC_RELAXED, __HIP_MEMORY_SCOPE_AGENT); }
template <int PER> DI void poll_flags(u64* f, int n, u64 tag, int lane) {
  asm volatile("" : "+v"(lane));
  const int i = lane < n ? lane : 0;
  for (unsigned spins = 0; spins < (1u << 20); ++spins) {
    bool ok = true;
#pragma unroll
    for (int k = 0; k < PER; ++k) ok &= __hip_atomic_load(f + i + 64 * k, __ATOMIC_RELAXED, __HIP_MEMORY_SCOPE_AGENT) == tag;
    if (__all(ok)) break;
    __builtin_amdgcn_s_sleep(1);
  }
}

constexpr int K1_SLOT = 37120;
constexpr int K1_UOFF = 18432 + 128;
constexpr int K1_NSLOT = 4;
constexpr int K1_PF_OFF = K1_NSLOT * K1_SLOT;
constexpr int XR_OFF = K1_PF_OFF + 288, XR_STRIDE = 4608;
constexpr int HZ_OFF = XR_OFF + 3 * XR_STRIDE;
constexpr int K1_LDS = HZ_OFF + 288;
constexpr int PART_OFF = 65536;
constexpr int PARTC_OFF = PART_OFF + 65536;
constexpr int HCNT_OFF = K1_PF_OFF;
constexpr int XG_OFF = 3 * K1_SLOT;

DI bf16x8 tr_frag(LAS char* p, int off, int hi_off) {
  s16x4 lo = __builtin_amdgcn_ds_read_tr16_b64_v4i16((LAS s16x4*)(p + off));
  s16x4 hi = __builtin_amdgcn_ds_read_tr16_b64_v4i16((LAS s16x4*)(p + off + hi_off));
  return __builtin_shufflevector(lo, hi, 0, 1, 2, 3, 4, 5, 6, 7);
}

constexpr int K1_MID = 16;
constexpr int k1_xkey(int i) { return i < 9 ? -100 + i : 4 * (1 + 2 * (i - 9)) + 2; }
constexpr int k1_dstep(int d, int n) { return d == 2 ? 2 + 4 * n : d == 3 ? 24 + 4 * n : d == 4 ? 58 + 6 * n : d == 5 ? 94 + 6 * n : 36 * (d - 3) + K1_MID + 6 * n; }
constexpr int k1_dkey(int d, int n) { return d < 2 ? -400 + 10 * d + n : 4 * k1_dstep(d, n) + 1; }
constexpr int k1_cstep(int i) { return i < 7 ? 218 + 4 * i : (i < 14 ? 254 + 4 * (i - 7) : (i < 21 ? 289 + (i - 14) : 304 + 2 * (i - 21))); }
constexpr int k1_sstep(int c) { return c == 0 ? 143 : (c < 4 ? 144 + 6 * c : (c == 4 ? 182 : 302 + 9 * (c - 5))); }
constexpr int k1_astep(int i) { return i < 14 ? k1_cstep(i) + 28 : (i < 21 ? k1_cstep(i) + 20 : 321 + (i - 21)); }
constexpr int K1_TOUCH = 5 * 36 + K1_MID + 1;
constexpr int k1_younger(int okey, int wkey) {
  int n = 0;
  if (4 * K1_TOUCH + 1 > okey && 4 * K1_TOUCH + 1 < wkey) n += 1;
  if (-70 > okey && -70 < wkey) n += 2;
  for (int i = 0; i < 36; ++i) if (k1_xkey(i) > okey && k1_xkey(i) < wkey) n += 1;
  for (int d = 0; d < 9; ++d) for (int pc = 0; pc < 5; ++pc) if (k1_dkey(d, pc) > okey && k1_dkey(d, pc) < wkey) n += 1;
  for (int i = 0; i < 24; ++i) if (4 * k1_cstep(i) + 2 > okey && 4 * k1_cstep(i) + 2 < wkey) n += 2;
  for (int c = 0; c < 8; ++c) if (4 * k1_sstep(c) + 3 > okey && 4 * k1_sstep(c) + 3 < wkey) n += 1;
  return n;
}
#define HSTORE 1

constexpr int K2_HALF = 16384;
constexpr int K2_SLOT = 2 * K2_HALF;
constexpr int K2_NSLOT = 4;
constexpr int K2_OWN_OFF = K2_NSLOT * K2_SLOT;
constexpr int K2_WT_OFF = K2_OWN_OFF + 18432;
constexpr int k2_g0(int st) { return st <= 9 ? 16 * st : 153; }
constexpr int k2_stage(int g) { return g < 144 ? g / 16 : (g < 153 ? 9 : 10); }
constexpr int k2_mid(int st) { return st < 9 ? 6 : 4; }
constexpr int k2_unit(int st) { return st < 5 ? 2 * st : 2 * (st - 5) + 1; }
constexpr int k2_np(int d) { return d < 9 ? 4 : 3; }
constexpr int k2_dkey(int d, int n) { return d < 2 ? -300 + 10 * d + n : (d == 2 ? 4 * (1 + n) + 2 : 4 * (k2_g0(d - 3) + k2_mid(d - 3) + 4 * n) + 2); }
constexpr int k2_hkey(int j, int m) { return j < 3 ? -250 + 10 * j + m : 4 * (k2_g0(j - 3) + k2_mid(j - 3) + 2 + 4 * m) + 3; }
constexpr int k2_skey(int p) { return 4 * (k2_g0(9 + p) + k2_mid(9 + p)) + 1; }
constexpr int K2_WST = 7;
constexpr int k2_wkey() { return 4 * (k2_g0(K2_WST) + k2_mid(K2_WST)) + 1; }
constexpr int k2_younger(int okey, int wkey, bool stores) {
  int n = 0;
  if (k2_wkey() > okey && k2_wkey() < wkey) n += 2;
  if (-100 > okey && -100 < wkey) n += 1;
  for (int d = 0; d < 11; ++d) for (int pc = 0; pc < k2_np(d); ++pc) if (k2_dkey(d, pc) > okey && k2_dkey(d, pc) < wkey) n += 1;
  for (int j = 0; j < 9; ++j) for (int m = 0; m < 4; ++m) if (k2_hkey(j, m) > okey && k2_hkey(j, m) < wkey) n += 1;
  if (stores) for (int p = 0; p < 2; ++p) if (k2_skey(p) > okey && k2_skey(p) < wkey) n += 4;
  return n;
}
constexpr unsigned CODE_BYTES = 42 * 1024;
constexpr int cmax(int a, int b) { return a > b ? a : b; }

__global__ __launch_bounds__(512, 2) void k_fused(const u16* __restrict__ q, const u16* __restrict__ kk, const u16* __restrict__ vv,
                                                  const u16* __restrict__ shk, const u16* __restrict__ ssmk, const u16* __restrict__ ssmd,
                                                  const u16* __restrict__ wg, const u16* __restrict__ wu, const u16* __restrict__ wd,
                                                  char* ws, u16* __restrict__ out) {
  u16* xfrag = (u16*)(ws + WS_XFRAG);
  u64* stats = (u64*)(ws + WS_STATS);
  u16* kvr = (u16*)(ws + WS_KVR);
  u16* wT = (u16*)(ws + WS_WT);
  u16* hout = (u16*)(ws + WS_H);
  float* slab = (float*)(ws + WS_SLAB);
  u64* flg = (u64*)(ws + WS_FLG);
  extern __shared__ __attribute__((aligned(16))) char smem_g[];
  LAS char* smem = (LAS char*)smem_g;
  const unsigned lds0 = (unsigned)(uintptr_t)smem;
  const int bid = blockIdx.x, tid = threadIdx.x;
  const int w = __builtin_amdgcn_readfirstlane(tid >> 6), lane = tid & 63;
  const int xcd = bid & 7, idx = bid >> 3;
  STAMP_DECL; RSTAMP(1, 14); STAMP(1, 0);
  const u64 genv = __hip_atomic_load(flg + FL_GEN, __ATOMIC_RELAXED, __HIP_MEMORY_SCOPE_AGENT);
  u64 tag;
  auto make_tag = [&]() ALWAYS_INLINE {
    tag = (((u64)(unsigned)__builtin_amdgcn_readfirstlane((unsigned)(genv >> 32)) << 32) | (unsigned)__builtin_amdgcn_readfirstlane((unsigned)genv)) + 1ull;
  };
  const int ksl = xcd >> 1, mt = (xcd & 1) * 8 + (idx >> 2), nt = idx & 3;
  const int tq = w & 3, khf = w >> 2;
  const int kbase = ksl * 1152 + khf * 576;
  const int colA2 = 128 * nt, colQ2 = 512 + 16 * nt;
  const int mtile = (xcd & 1) * 4 + (idx & 3);
  const int cgrp = (xcd >> 1) * 2 + (idx >> 4), cq = (idx >> 2) & 3;
  const int colA = 64 * (9 * cgrp + 2 * cq), colQ = 64 * (9 * cgrp + 8) + 16 * cq;

  const int grp = bid >> 2, qd = bid & 3;
  const int chA = grp * 9 + 2 * qd, chC = grp * 9 + 8;
  float ddA, ddC;
  {
    const int c1 = chA + (w >> 2);
    const unsigned d1 = ((const unsigned*)ssmd)[c1 >> 1], d2 = ((const unsigned*)ssmd)[chC >> 1];
    ddA = (c1 & 1) ? bf_hi(d1) : bf_lo(d1);
    ddC = (chC & 1) ? bf_hi(d2) : bf_lo(d2);
  }

  const int wq = w & 3, part = w >> 2;
  const char* wbase = (const char*)(part ? wu : wg);
  unsigned voff[5];
#pragma unroll
  for (int n = 0; n < 5; ++n) {
    const int o = (n < 4) ? ((wq * 4 + n) * 1024 + lane * 16) : (16384 + wq * 512 + (lane & 31) * 16);
    const int k = o >> 7, pc = (o >> 4) & 7;
    const int j = (pc >> 1) ^ (((k >> 1) & 1) | (part << 1));
    voff[n] = (unsigned)(k * (F_ * 2) + (2 * j + (pc & 1)) * 16);
  }
  const unsigned ldst = lds0 + part * K1_UOFF + wq * 4096;
  const unsigned ldst4 = lds0 + part * K1_UOFF + 16384 + wq * 512;

  auto piece_load = [&](int st, int n) ALWAYS_INLINE {
    const int slot = st % K1_NSLOT;
    const unsigned dst = (n < 4 ? ldst + n * 1024 : ldst4) + slot * K1_SLOT;
    if (st != 4) {
      const int ks = st < 4 ? st : st - 5;
      const char* g = wbase + (size_t)ks * 144 * (F_ * 2) + (colA + (st > 4 ? 64 : 0)) * 2;
      if (n < 4 || lane < 32) glds1p<false>(g, voff[n], dst);
    } else {
      int l2 = lane; asm volatile("" : "+v"(l2));
      const int o = (n < 4) ? ((wq * 4 + n) * 1024 + l2 * 16) : (16384 + wq * 512 + (l2 & 31) * 16);
      const unsigned vq = (unsigned)((o >> 5) * (F_ * 2) + ((o >> 4) & 1) * 16);
      if (n < 4 || lane < 32) glds1p<false>(wbase + colQ * 2, vq, dst);
    }
  };

  bf16x8 xf[36];
  const char* xb = (const char*)xfrag + (size_t)(mtile * 8 + w) * 36 * 1024;
  const unsigned xv = lane * 16;
  auto x_load = [&](auto ic) ALWAYS_INLINE {
    constexpr int i = ic;
    gload16_sc1<(i & 3) * 1024>(xf[i], xb + (i >> 2) * 4096, xv);
  };
  {
    LAS u16* tile = (LAS u16*)(smem + XR_OFF);
    constexpr int KT_OFF = 2 * K1_SLOT, VT_OFF = KT_OFF + 9728, WT_OFF = VT_OFF + 9216;
    const int tt = bid >> 3, ct = bid & 7, t0 = tt * 64, c0 = ct * 72;
    const int tok0 = mtile * 256 + (cgrp * 4 + cq) * 8, token = tok0 + w;
    const bool act = lane < 48;
    u32x2 d[3];
    u32x4 kin, vin, ein;
#pragma unroll
    for (int i = 0; i < 3; ++i)
      asm volatile("global_load_dwordx2 %0, %1, %2 offset:%3" : "=v"(d[i]) : "v"((unsigned)(token * (D_ * 2) + (act ? lane : 0) * 8)), "s"(q), "i"(i * 384) : "memory");
    auto k_off = [&](int pc) ALWAYS_INLINE -> unsigned { const int r = pc / 9, cg = pc - r * 9, ts = t0 - 3 + r; return (unsigned)((ts < 0 ? 0 : ts) * (D_ * 2) + (c0 + 8 * cg) * 2); };
    auto v_off = [&](int pc) ALWAYS_INLINE -> unsigned { const int r = pc / 9, cg = pc - r * 9; return (unsigned)((t0 + r) * (D_ * 2) + (c0 + 8 * cg) * 2); };
    gload16u(kin, kk, k_off(tid));
    gload16u(vin, vv, v_off(tid));
    int edst;
    {
      const int kp = w == 0 ? 512 + lane : 576 + (lane < 27 ? lane : 0);
      const int wl = lane < 63 ? lane - 27 : 35;
      const bool isw = w == 1 && lane >= 27, isv = w >= 2;
      const char* src = isv ? (const char*)vv + v_off(512 + lane) : (isw ? (const char*)shk + c0 * 8 + wl * 16 : (const char*)kk + k_off(kp));
      edst = isv ? VT_OFF + (512 + lane) * 16 : (isw ? WT_OFF + wl * 16 : KT_OFF + kp * 16);
      asm volatile("global_load_dwordx4 %0, %1, off" : "=v"(ein) : "v"(src) : "memory");
    }
    {
      const int li = (w == 3 || w == 4) ? (w & 1) * 64 + lane : 0;
      const int ch = li < 32 ? chA : (li < 64 ? chA + 1 : chC);
      u64 pc; asm volatile("s_getpc_b64 %0" : "=s"(pc));
      const unsigned cl = (unsigned)((idx & 7) * 64 + lane) * 128u;
      const char* tb = w == 5 ? (const char*)(pc & ~127ull) : (const char*)ssmk;
      touch_lines(tb, w == 5 ? (cl < CODE_BYTES ? cl : 0u) : (unsigned)(ch * (S_ * 2) + (li & 31) * 128), lds0 + K1_PF_OFF + 16);
    }
    asm volatile("s_waitcnt vmcnt(4)" : "+v"(d[0]), "+v"(d[1]), "+v"(d[2])::"memory");
    STAMP(1, 9);
    f32x2 xa[3], xb2[3];
#pragma unroll
    for (int i = 0; i < 3; ++i) { xa[i] = unpk(d[i].x); xb2[i] = unpk(d[i].y); }
    const f32x2 s2 = (xa[0] + xb2[0]) + (xa[1] + xb2[1]) + (xa[2] + xb2[2]);
    const float mean = wave_sum(act ? s2.x + s2.y : 0.f) * (1.0f / 576.0f);
    f32x2 v2 = {0.f, 0.f};
#pragma unroll
    for (int i = 0; i < 3; ++i) { const f32x2 a = xa[i] - mean, b = xb2[i] - mean; v2 += a * a + b * b; }
    const float vs = wave_sum(act ? v2.x + v2.y : 0.f);
    const float mb = bfr(mean), vb = bfr(vs * (1.0f / 576.0f));
    const float sd = bfr(sqrtf(bfr(vb + 1e-5f)));
    const float rsd = 1.0f / sd;
#pragma unroll
    for (int i = 0; i < 3; ++i) {
      const f32x2 a = bfr2(xa[i] - mb) * rsd, b = bfr2(xb2[i] - mb) * rsd;
      u32x2 o; o.x = pk_bf16(a.x, a.y); o.y = pk_bf16(b.x, b.y);
      const int p8 = lane + 48 * i;
      if (act) *(LAS u32x2*)(smem + XG_OFF + ((p8 >> 1) * 8 + w) * 16 + (p8 & 1) * 8) = o;
    }
    if (tid == 0) *(LAS unsigned*)(smem + HCNT_OFF + 4) = 0u;
    asm volatile("s_waitcnt vmcnt(0)" : "+v"(kin), "+v"(vin), "+v"(ein)::"memory");
    make_tag();
    if (t0 == 0 && tid < 27) kin = (u32x4){0u, 0u, 0u, 0u};
    *(LAS u32x4*)(smem + KT_OFF + tid * 16) = kin;
    *(LAS u32x4*)(smem + VT_OFF + tid * 16) = vin;
    if (w < 3) *(LAS u32x4*)(smem + edst) = ein;
    WAIT_L(0);
    __builtin_amdgcn_s_barrier();
    CFENCE();
    {
      char* xo = (char*)xfrag + (size_t)((tok0 >> 5) * 36 * 64 + (tok0 & 31)) * 16;
#pragma unroll
      for (int it = 0; it < 2; ++it) {
        const int id = it ? 512 + lane : tid, p16 = id >> 3;
        if (it == 0 || w == 0) {
          const u32x4 v = *(LAS u32x4*)(smem + XG_OFF + id * 16);
          gstore16_sc1(xo + ((p16 >> 1) * 64 + (p16 & 1) * 32 + (id & 7)) * 16, v);
        }
      }
      if (lane == 0) __hip_atomic_store(stats + token, (u64)__float_as_uint(mb) | ((u64)__float_as_uint(rsd) << 32), __ATOMIC_RELAXED, __HIP_MEMORY_SCOPE_AGENT);
    }
    STAMP(1, 10);
    CFENCE();
#pragma unroll
    for (int n = 0; n < 5; ++n) piece_load(0, n);
#pragma unroll
    for (int n = 0; n < 5; ++n) piece_load(1, n);
    wait_vm<10>();
    STAMP(1, 11);
    if (lane == 0) {
      const unsigned old = __hip_atomic_fetch_add((LAS unsigned*)(smem + HCNT_OFF + 4), 1u, __ATOMIC_RELAXED, __HIP_MEMORY_SCOPE_WORKGROUP);
      if (old == 7u) set_flag(flg + FL_X + (tok0 >> 3), tag);
    }
    auto kv_compute = [&](int item) ALWAYS_INLINE {
      const int tl = item / 9, cg = item - tl * 9;
      u32x4 kr[4], w4[4];
#pragma unroll
      for (int i = 0; i < 4; ++i) { kr[i] = *(LAS u32x4*)(smem + KT_OFF + (item + 9 * i) * 16); w4[i] = *(LAS u32x4*)(smem + WT_OFF + cg * 64 + i * 16); }
      const u32x4 vr = *(LAS u32x4*)(smem + VT_OFF + item * 16);
#pragma unroll
      for (int e2 = 0; e2 < 4; ++e2) {
        const f32x2 w0 = {bf_lo(w4[e2].x), bf_lo(w4[e2].z)}, w1 = {bf_hi(w4[e2].x), bf_hi(w4[e2].z)};
        const f32x2 w2 = {bf_lo(w4[e2].y), bf_lo(w4[e2].w)}, w3 = {bf_hi(w4[e2].y), bf_hi(w4[e2].w)};
        f32x2 ks = bfr2(unpk(kr[0][e2]) * w3);
        ks = bfr2(ks + bfr2(unpk(kr[1][e2]) * w2));
        ks = bfr2(ks + bfr2(unpk(kr[2][e2]) * w1));
        ks = bfr2(ks + bfr2(unpk(kr[3][e2]) * w0));
        const f32x2 kv = ks * unpk(vr[e2]);
        const unsigned pk = pk_bf16(kv.x, kv.y);
        tile[(8 * cg + 2 * e2) * 72 + (63 - tl)] = (u16)(pk & 0xffffu);
        tile[(8 * cg + 2 * e2 + 1) * 72 + (63 - tl)] = (u16)(pk >> 16);
      }
    };
    kv_compute(tid);
    STAMP(1, 7);
    poll_flags<1>(flg + FL_X + mtile * 32 + w * 4, 4, tag, lane);
    wait_vm<0>();
    STAMP(1, 8);
    sfor<9>([&](auto ic) ALWAYS_INLINE { x_load(ic); });
    if (w == 7) kv_compute(512 + lane);
    WAIT_L(0);
    __builtin_amdgcn_s_barrier();
    CFENCE();
    {
      const int ch = tid >> 3, pp = tid & 7, ch2 = 64 + (lane >> 3);
      const u32x4 o1 = *(LAS u32x4*)(tile + ch * 72 + pp * 8), o2 = *(LAS u32x4*)(tile + ch2 * 72 + pp * 8);
      gstore16_sc1(kvr + (size_t)(c0 + ch) * S_ + (1984 - t0) + pp * 8, o1);
      gstore16_sc1(kvr + (size_t)(c0 + ch2) * S_ + (1984 - t0) + pp * 8, o2);
    }
  }

  const int kh = lane >> 5, prt = (lane >> 4) & 1, qq = (lane & 15) >> 2, p4 = lane & 3;
  const unsigned rbU = lds0 + (8 * kh + qq) * 128 + prt * K1_UOFF + ((((qq >> 1) & 1) | (prt << 1)) << 5) + p4 * 8;

  f32x16 acc[4];
  const f32x16 zero16 = {0.f, 0.f, 0.f, 0.f, 0.f, 0.f, 0.f, 0.f, 0.f, 0.f, 0.f, 0.f, 0.f, 0.f, 0.f, 0.f};

  const int token = mtile * 256 + w * 32 + (lane & 31);
  char* hb = (char*)hout + ((size_t)(token >> 4) * 144 * 64 + (lane >> 5) * 16 + (token & 15)) * 16;
  auto h_word = [&](const f32x16& a, int i) ALWAYS_INLINE -> unsigned {
    return pk_bf16(bfr(a[2 * i]) * bfr(a[2 * i + 8]), bfr(a[2 * i + 1]) * bfr(a[2 * i + 9]));
  };
  auto h_store = [&](const u32x4& o, int c16) ALWAYS_INLINE {
    gstore16_sc1(hb + ((size_t)(c16 >> 1) * 64 + (c16 & 1) * 32) * 16, o);
  };
  u32x4 ho;

  const char* hpA = (const char*)(ssmk + (size_t)(chA + (w >> 2)) * S_ + 64 * (w & 3));
  const char* hpC = (const char*)(ssmk + (size_t)chC * S_ + 32 * w);
  const bool zA = (w & 3) == 0, zC = w == 0;
  const char* pf_sk = (const char*)ssmk;
  const char* pf_wd = (const char*)wd + (size_t)(xcd >> 1) * 1152 * 1152;
  const char* kvb = (const char*)kvr;
  u32x4 caf[24];
  unsigned cax[24];
  bf16x8 cf[24];
  int cvo = 0, cvx = 0;
  auto conv_align = [&](auto ic) ALWAYS_INLINE {
    constexpr int i = ic;
    const unsigned sh = ((lane + 1) & 1) * 2;
    u32x4 r;
    r.x = __builtin_amdgcn_alignbyte(caf[i].y, caf[i].x, sh);
    r.y = __builtin_amdgcn_alignbyte(caf[i].z, caf[i].y, sh);
    r.z = __builtin_amdgcn_alignbyte(caf[i].w, caf[i].z, sh);
    r.w = __builtin_amdgcn_alignbyte(cax[i], caf[i].w, sh);
    cf[i] = __builtin_bit_cast(bf16x8, r);
    asm volatile("" : "+v"(cf[i]));
  };

  STAMP(1, 1);

  constexpr int PD = 3, MID = K1_MID;
  bf16x8 af[PD + 1];
  unsigned rb[4];
#pragma unroll
  for (int j = 0; j < 4; ++j) rb[j] = rbU ^ (j << 5);
#pragma unroll
  for (int g = 0; g < PD; ++g) af[g] = tr_frag((LAS char*)(uintptr_t)rb[g], 0, 512);
  sfor<324>([&](auto gc) ALWAYS_INLINE {
    constexpr int g = gc;
    constexpr int st = g / 36, f = g - st * 36;
    constexpr int s = st == 4 ? f : (st == 8 ? f % 9 : f / 4), c = st == 4 ? 0 : (st == 8 ? f / 9 : f % 4);
    constexpr int xi = st < 4 ? st * 9 + s : (st == 4 ? s : (st - 5) * 9 + s);
    constexpr int ai = st < 4 ? c : (st == 4 ? 0 : (c + 1) & 3);
    constexpr bool first = (st == 0 || st == 5) ? s == 0 : (st == 4 && f == 0);
    if constexpr (f == MID) {

      if constexpr (st < 8) wait_vm<k1_younger(k1_dkey(st + 1, 4), 4 * g)>();
      else { wait_vm<k1_younger(4 * (7 * 36 + MID + 1) + 1, 4 * g)>(); if (w <= 3) WAIT_L(0); }
      __builtin_amdgcn_s_barrier();
      CFENCE();

      if constexpr (st == 1) { if (tid == 0) set_flag(flg + FL_K + (bid & 7) * 32 + (bid >> 3), tag); }
      if constexpr (st == 6) {
        static_assert(k1_dkey(7, 4) > 4 * k1_sstep(4) + 3, "the stage 7 wait must cover the h stores of panels A and Q");
        if (tid == 0) set_flag(flg + FL_HE + (mtile * 4 + (cgrp >> 1)) * 8 + (cgrp & 1) * 4 + cq, tag);
      }
    }
    if constexpr (((f + 36 - MID) % 36) % 9 == 0) __builtin_amdgcn_s_setprio(3 - ((f + 36 - MID) % 36) / 9);
    sfor<35>([&](auto dc) ALWAYS_INLINE { if constexpr (k1_dkey(dc / 5 + 2, dc % 5) == 4 * g + 1) piece_load(dc / 5 + 2, dc % 5); });
    sfor<27>([&](auto ic) ALWAYS_INLINE { if constexpr (k1_xkey(ic + 9) == 4 * g + 2) x_load(std::integral_constant<int, ic + 9>{}); });
    if constexpr (g == 7 * 36 + MID + 1) {
      if (w < 3) {
        const int ch = (w == 2) ? chC : chA + w;
        const unsigned lv = lane * 16;
        poll_flags<1>(flg + FL_K + (grp >> 3) * 32, 32, tag, lane);
#pragma unroll
        for (int i = 0; i < 4; ++i) glds1_sc1(kvb + (size_t)ch * (S_ * 2) + i * 1024, lv, lds0 + XR_OFF + w * XR_STRIDE + i * 1024);
        if (lane < 4) glds1(pf_sk + (size_t)ch * (S_ * 2), lv, lds0 + HZ_OFF + w * 96 + 32);
      } else if (w == 3) {
        if (lane < 32) {
#pragma unroll
          for (int im = 0; im < 3; ++im) *(LAS u32x4*)(smem + XR_OFF + im * XR_STRIDE + 4096 + lane * 16) = (u32x4){0u, 0u, 0u, 0u};
        } else if (lane < 38) {
          const int im = (lane - 32) >> 1, hh = lane & 1;
          *(LAS u32x4*)(smem + HZ_OFF + im * 96 + hh * 16) = (u32x4){0u, 0u, 0u, 0u};
        } else if (lane == 38) *(LAS unsigned*)(smem + HCNT_OFF) = 0u;
      }
    }
    sfor<24>([&](auto ac) ALWAYS_INLINE {
      if constexpr (k1_astep(ac) == g) {
        asm volatile("s_waitcnt vmcnt(%2)" : "+v"(caf[ac]), "+v"(cax[ac]) : "i"(k1_younger(4 * k1_cstep(ac) + 2, 4 * g + 2)) : "memory");
        conv_align(ac);
      }
    });
    if constexpr (g == K1_TOUCH) {
      int l2 = lane; asm volatile("" : "+v"(l2));
      const unsigned line = (unsigned)(w * 64 + l2 < 324 ? idx * 324 + w * 64 + l2 : idx * 324);
      touch_lines(pf_wd, line * 128u, lds0 + K1_PF_OFF + 16);
    }
    sfor<24>([&](auto cc) ALWAYS_INLINE {
      constexpr int ci = cc;
      if constexpr (k1_cstep(ci) == g) {
        if constexpr (ci == 0) {
          int l2 = lane; asm volatile("" : "+v"(l2));
          const int e = (l2 & 15) - 15 + 8 * (l2 >> 4);
          cvo = (e & ~1) * 2; cvx = cvo + ((e & 1) ? 16 : 0);
        }
        constexpr int cb = ci < 16 ? 64 * (ci >> 3) + 512 * (ci & 7) : 512 * (ci - 16);
        const char* hp = (ci < 16 ? hpA : hpC) + cb + ((ci == 0 && zA) || (ci == 16 && zC) ? 32 : 0);
        gload16u(caf[ci], hp - 32, (unsigned)(cvo + 32));
        gload4u(cax[ci], hp - 32, (unsigned)(cvx + 32));
      }
    });
    if constexpr (g + PD < 324) {
      constexpr int g1 = g + PD, st1 = g1 / 36, f1 = g1 - st1 * 36;
      if constexpr (f1 == 0) {
        if constexpr (st1 != 4) {
          unsigned b2 = rbU; asm volatile("" : "+v"(b2));
#pragma unroll
          for (int j = 0; j < 4; ++j) { rb[j] = (b2 + (st1 % K1_NSLOT) * K1_SLOT) ^ (j << 5); asm volatile("" : "+v"(rb[j])); }
        } else {
          int l2 = lane; asm volatile("" : "+v"(l2));
          rb[0] = lds0 + (8 * (l2 >> 5) + ((l2 & 15) >> 2)) * 32 + ((l2 >> 4) & 1) * K1_UOFF + (l2 & 3) * 8 + (st1 % K1_NSLOT) * K1_SLOT;
          asm volatile("" : "+v"(rb[0]));
        }
      }
      if constexpr (st1 == 8) af[g1 % (PD + 1)] = tr_frag((LAS char*)(uintptr_t)rb[f1 / 9], (f1 % 9) * 2048, 512);
      else if constexpr (st1 != 4) af[g1 % (PD + 1)] = tr_frag((LAS char*)(uintptr_t)rb[f1 % 4], (f1 / 4) * 2048, 512);
      else af[g1 % (PD + 1)] = tr_frag((LAS char*)(uintptr_t)rb[0], f1 * 512, 128);
    }
    if constexpr (st < 4 && c == 0) {
      asm volatile("s_waitcnt vmcnt(%1)" : "+v"(xf[xi]) : "i"(k1_younger(k1_xkey(xi), 4 * g + 3)) : "memory");
    }
    SCHED0();
    acc[ai] = __builtin_amdgcn_mfma_f32_32x32x16_bf16(af[g % (PD + 1)], xf[xi], first ? zero16 : acc[ai], 0, 0, 0);
    SCHED0();
    if constexpr (g == 143) {
#pragma unroll
      for (int i = 0; i < 4; ++i) ho[i] = h_word(acc[0], i);
      h_store(ho, colA >> 4);
      SCHED0();
    }
    if constexpr (g > 144 && g <= 162) {
      constexpr int cc = (g - 145) / 6 + 1, pi = (g - 145) % 6;
      if constexpr (pi < 4) ho[pi] = h_word(acc[cc], pi);
      if constexpr (pi == 5) h_store(ho, (colA >> 4) + cc);
      SCHED0();
    }
    if constexpr (g >= 298 && (g - 298) % 9 < 5 && (g - 298) / 9 < 3) {
      constexpr int cc = (g - 298) / 9, pi = (g - 298) % 9;
      if constexpr (pi < 4) ho[pi] = h_word(acc[(cc + 1) & 3], pi);
      if constexpr (pi == 4) h_store(ho, (colA >> 4) + 4 + cc);
      SCHED0();
    }
    if constexpr (g >= 180 && g <= 182) {
      if constexpr (g == 180) { ho[0] = h_word(acc[0], 0); ho[1] = h_word(acc[0], 1); }
      if constexpr (g == 181) { ho[2] = h_word(acc[0], 2); ho[3] = h_word(acc[0], 3); }
      if constexpr (g == 182) h_store(ho, colQ >> 4);
      SCHED0();
    }
  });
  const char* wbase2 = (const char*)wd + (size_t)kbase * (D_ * 2);
  unsigned voff2[4];
  int lane2 = lane; asm volatile("" : "+v"(lane2));
#pragma unroll
  for (int n = 0; n < 4; ++n) {
    const int o = (tq * 4 + n) * 1024 + lane2 * 16;
    const int k = o >> 8, pc = (o >> 4) & 15;
    const int j = (pc >> 1) ^ (k & 7);
    voff2[n] = (unsigned)(k * (D_ * 2) + (2 * j + (pc & 1)) * 16);
  }
  const unsigned ldst2 = lds0 + khf * K2_HALF;
  auto piece_load2 = [&](int st, int n) ALWAYS_INLINE {
    const int slot = st % K2_NSLOT;
    if (st < 9) {
      const char* g = wbase2 + (size_t)k2_unit(st) * 64 * (D_ * 2) + colA2 * 2;
      glds1p<false>(g, voff2[n], ldst2 + slot * K2_SLOT + tq * 4096 + n * 1024);
    } else {
      int l2 = lane; asm volatile("" : "+v"(l2));
      const char* g = wbase2 + (size_t)(st - 9) * 288 * (D_ * 2) + colQ2 * 2;
      const int o = n < 2 ? tq * 2048 + n * 1024 + l2 * 16 : 8192 + tq * 256 + (l2 & 15) * 16;
      const unsigned v0 = (unsigned)((o >> 5) * (D_ * 2) + ((o >> 4) & 1) * 16);
      if (n < 2 || l2 < 16) glds1p<false>(g, v0, ldst2 + slot * K2_SLOT + (n < 2 ? tq * 2048 + n * 1024 : 8192 + tq * 256));
    }
  };
  const char* hbase = (const char*)hout + ((size_t)(mt * 8 + tq * 2) * 144 * 64 + (size_t)(kbase >> 5) * 64) * 16;
  const unsigned hv0 = lane2 * 16 + khf * (144 * 64 * 16), hv1 = lane2 * 16 + (1 - khf) * (144 * 64 * 16);
  bf16x8 hf[18][2];
  auto h_load = [&](auto lc) ALWAYS_INLINE {
    constexpr int k = 2 * k2_unit(lc / 4) + (lc / 2) % 2, t = lc % 2;
    gload16_sc1<(k & 3) * 1024>(hf[k][t], hbase + (k >> 2) * 4096, t ? hv1 : hv0);
  };
  STAMP(1, 2);
  __builtin_amdgcn_s_setprio(0);
  {
    u32x4 o;
#pragma unroll
    for (int i = 0; i < 4; ++i) o[i] = h_word(acc[0], i);
    h_store(o, (colA >> 4) + 7);
  }

  __builtin_amdgcn_s_barrier();
  CFENCE();
#pragma unroll
  for (int n = 0; n < 4; ++n) piece_load2(0, n);
#pragma unroll
  for (int n = 0; n < 4; ++n) piece_load2(1, n);

  STAMP(1, 3);
  const int ct0 = lane & 15, ckg = lane >> 4;
  {
    const int boff = (2032 - 16 * (lane & 15) + 8 * ckg) * 2;
    auto head_frag = [&](int im) ALWAYS_INLINE -> bf16x8 {
      bf16x8 r;
      LAS u16* hz = (LAS u16*)(smem + HZ_OFF + im * 96) + 16 + (ct0 - 15 + 8 * ckg);
#pragma unroll
      for (int j = 0; j < 8; ++j) r[j] = (short)hz[j];
      return r;
    };
    if (zA) cf[0] = head_frag(w >> 2);
    if (zC) cf[16] = head_frag(2);
    {
      const int im = w >> 2;
      LAS char* xr = smem + XR_OFF + im * XR_STRIDE + boff + 128 * (w & 3);
      f32x4 ac[8];
#pragma unroll
      for (int j = 0; j < 8; ++j) ac[j] = (f32x4){0.f, 0.f, 0.f, 0.f};
#pragma unroll
      for (int rr = 1; rr >= 0; --rr) {
        bf16x8 bfm[8];
#pragma unroll
        for (int b = 0; b < 8; ++b) bfm[b] = *(LAS bf16x8*)(xr - 512 * b + 64 * rr);
#pragma unroll
        for (int a = 7; a >= 0; --a)
#pragma unroll
          for (int j = a; j < 8; ++j) ac[j] = __builtin_amdgcn_mfma_f32_16x16x32_bf16(cf[rr * 8 + a], bfm[j - a], ac[j], 0, 0, 0);
        if (rr == 1) {
          SCHED0();
          wait_vm<8>();
          if (lane == 0) {
            const unsigned old = __hip_atomic_fetch_add((LAS unsigned*)(smem + HCNT_OFF), 1u, __ATOMIC_RELAXED, __HIP_MEMORY_SCOPE_WORKGROUP);
            if (old == 7u) set_flag(flg + FL_H + (mtile * 4 + (cgrp >> 1)) * 8 + (cgrp & 1) * 4 + cq, tag);
          }
          SCHED0();
        }
      }
#pragma unroll
      for (int j = 0; j < 8; ++j)
        *(LAS f32x4*)(smem + PART_OFF + w * 8192 + (j * 64 + lane) * 16) = ac[j];
    }
    poll_flags<1>(flg + FL_HE + ((mt >> 1) * 4 + ksl) * 8, 8, tag, lane);
    sfor<12>([&](auto kc) ALWAYS_INLINE { h_load(kc); });
    {
      LAS char* xr = smem + XR_OFF + 2 * XR_STRIDE + boff + 64 * w;
      const int jA = 7 - qd, jB = qd;
      bf16x8 b0[8], b1[4];
#pragma unroll
      for (int a = 0; a < 8; ++a) { const int bb = jA - a; b0[a] = *(LAS bf16x8*)(xr - 512 * (bb < 0 ? 0 : bb)); }
#pragma unroll
      for (int a = 0; a < 4; ++a) { const int bb = jB - a; b1[a] = *(LAS bf16x8*)(xr - 512 * (bb < 0 ? 0 : bb)); }
      f32x4 c0 = (f32x4){0.f, 0.f, 0.f, 0.f}, c1 = c0;
#pragma unroll
      for (int a = 7; a >= 0; --a) {
        if (a <= jA) c0 = __builtin_amdgcn_mfma_f32_16x16x32_bf16(cf[16 + a], b0[a], c0, 0, 0, 0);
        if (a < 4 && a <= jB) c1 = __builtin_amdgcn_mfma_f32_16x16x32_bf16(cf[16 + a], b1[a < 4 ? a : 0], c1, 0, 0, 0);
      }
      *(LAS f32x4*)(smem + PARTC_OFF + w * 2048 + lane * 16) = c0;
      *(LAS f32x4*)(smem + PARTC_OFF + w * 2048 + 1024 + lane * 16) = c1;
    }
  }
  STAMP(1, 4);
  WAIT_L(0);
  __builtin_amdgcn_s_barrier();
  CFENCE();
  STAMP(1, 5);
  auto finalize = [&](f32x4 s0, f32x4 s1, int im, int ch, int t8, float dd) ALWAYS_INLINE {
    const u32x4 kvv = *(LAS u32x4*)(smem + XR_OFF + im * XR_STRIDE + (2040 - t8) * 2);
    float y[8] = {s0[0], s0[1], s0[2], s0[3], s1[0], s1[1], s1[2], s1[3]};
    float o[8];
#pragma unroll
    for (int e = 0; e < 8; ++e) {
      const int ri = 7 - e;
      const float kvf = (ri & 1) ? bf_hi(kvv[ri >> 1]) : bf_lo(kvv[ri >> 1]);
      o[e] = bfr(y[e]) + bfr(kvf * dd);
    }
    u32x4 ov; ov.x = pk_bf16(o[0], o[1]); ov.y = pk_bf16(o[2], o[3]); ov.z = pk_bf16(o[4], o[5]); ov.w = pk_bf16(o[6], o[7]);
    gstore16_sc1(wT + (size_t)ch * S_ + t8, ov);
  };
  {
    const int im = tid >> 8, t8 = (tid & 255) * 8;
    const int po = ((t8 >> 8) * 64 + ((t8 >> 2) & 3) * 16 + ((t8 >> 4) & 15)) * 16;
    f32x4 s0 = (f32x4){0.f, 0.f, 0.f, 0.f}, s1 = s0;
#pragma unroll
    for (int ww = 0; ww < 4; ++ww) {
      s0 += *(LAS f32x4*)(smem + PART_OFF + (im * 4 + ww) * 8192 + po);
      s1 += *(LAS f32x4*)(smem + PART_OFF + (im * 4 + ww) * 8192 + po + 256);
    }
    finalize(s0, s1, im, chA + im, t8, ddA);
  }
  if (tid >= 448) {
    const int t2 = tid - 448;
    const int tl = t2 >> 5, l8 = (t2 & 31) * 8;
    const int jt = tl ? qd : 7 - qd, t8 = 256 * jt + l8;
    const int po = (((l8 >> 2) & 3) * 16 + (l8 >> 4)) * 16;
    f32x4 s0 = (f32x4){0.f, 0.f, 0.f, 0.f}, s1 = s0;
#pragma unroll
    for (int ww = 0; ww < 8; ++ww) {
      s0 += *(LAS f32x4*)(smem + PARTC_OFF + ww * 2048 + tl * 1024 + po);
      s1 += *(LAS f32x4*)(smem + PARTC_OFF + ww * 2048 + tl * 1024 + po + 256);
    }
    finalize(s0, s1, 2, chC, t8, ddC);
  }
  STAMP(1, 6);

  STAMP(2, 0);
  {
  const int kq = lane2 >> 4, qq = (lane2 & 15) >> 2, p4 = lane2 & 3;
  const unsigned rbU2 = lds0 + khf * K2_HALF + (16 * (kq >> 1) + 4 * (kq & 1) + qq) * 256 + ((4 * (kq & 1) + qq) << 5) + p4 * 8;

  const char* wtb = (const char*)(wT + mt * 128 + ksl * 32);
  f32x4 acc[2][8], accq[2];
  const f32x4 zero4 = {0.f, 0.f, 0.f, 0.f};

  const int tile = mt * 4 + nt;
  const __amdgpu_buffer_rsrc_t rs = __builtin_amdgcn_make_buffer_rsrc((char*)slab + (size_t)tile * (16 * 18432), 0, 16 * 18432, 0x00020000);
  const bool foreign = tq != ksl;
  auto give = [&](int xb, int i, const f32x4& a0, const f32x4& a1) ALWAYS_INLINE {
    *(LAS f32x4*)(smem + xb + ((w * 4 + i) * 64 + lane) * 16) = a1;
  };
  auto publish = [&](int xb, int i, const f32x4& a0, const f32x4& a1, int dtg) ALWAYS_INLINE {
    const f32x4 sm = a0 + *(LAS f32x4*)(smem + xb + (((w ^ 4) * 4 + i) * 64 + lane) * 16);
    if (foreign) __builtin_amdgcn_raw_buffer_store_b128(__builtin_bit_cast(u32x4, sm), rs, (unsigned)(((tq * 4 + ksl) * 18 + khf * 9 + dtg) * 1024 + lane * 16), 0, 16);
    else *(LAS f32x4*)(smem + K2_OWN_OFF + ((khf * 9 + dtg) * 64 + lane) * 16) = sm;
  };
  constexpr int XB1 = 3 * K2_SLOT, XB2 = 0, XB3 = 3 * K2_SLOT;

  asm volatile("s_waitcnt vmcnt(%4)" : "+v"(hf[0][0]), "+v"(hf[0][1]), "+v"(hf[1][0]), "+v"(hf[1][1])
               : "i"(k2_younger(k2_hkey(0, 3), 0, false)) : "memory");
  WAIT_L(0);
  __builtin_amdgcn_s_barrier();
  CFENCE();
  STAMP(2, 1);

  constexpr int PD = 2;
  bf16x8 af[PD + 1];
  unsigned sb = rbU2;
#pragma unroll
  for (int g = 0; g < PD; ++g) af[g] = tr_frag((LAS char*)(uintptr_t)(sb ^ (g << 5)), 0, 2048);
  sfor<162>([&](auto gc) ALWAYS_INLINE {
    constexpr int g = gc;
    constexpr int st = k2_stage(g), f = g - k2_g0(st);
    constexpr int ks = st < 9 ? k2_unit(st) * 2 + f / 8 : (st - 9) * 9 + f, dt = st < 9 ? f % 8 : 0;
    if constexpr (f == k2_mid(st)) {
      if constexpr (st < 8) { STAMP(0, 2 * st); }
      if constexpr (st == 2) { if (w == 0) poll_flags<1>(flg + FL_H + ((mt >> 1) * 4 + ksl) * 8, 8, tag, lane); }
      if constexpr (st == K2_WST) { if (w == 0) poll_flags<4>(flg + FL_W, 64, tag, lane); }
      if constexpr (st < 10) {
        constexpr int okey = cmax(k2_dkey(st + 1, k2_np(st + 1) - 1), st + 1 < 9 ? k2_hkey(st + 1, 3) : -1000);
        constexpr int n0 = k2_younger(okey, 4 * g, false);
        if constexpr (st == 0) { if (w == 7) wait_vm<n0 + 1>(); else wait_vm<n0>(); }
        else wait_vm<n0>();
        if constexpr (st + 1 < 9) {
          constexpr int k0 = 2 * k2_unit(st + 1);
          asm volatile("" : "+v"(hf[k0][0]), "+v"(hf[k0][1]), "+v"(hf[k0 + 1][0]), "+v"(hf[k0 + 1][1])::"memory");
        }
      }
      if constexpr (st >= 9) WAIT_L(0);
      __builtin_amdgcn_s_barrier();
      CFENCE();
      if constexpr (st < 8) { STAMP(0, 2 * st + 1); }
      if constexpr (st == 1) { if (tid == 0) set_flag(flg + FL_W + bid, tag); }
      if constexpr (st == K2_WST) {
        int l2 = lane; asm volatile("" : "+v"(l2));
        glds1_sc1(wtb, (unsigned)((colA2 + w * 16 + (l2 >> 2)) * (S_ * 2) + (l2 & 3) * 16), lds0 + K2_WT_OFF + w * 1024);
        glds1_sc1(wtb, (unsigned)((colQ2 + (l2 >> 2)) * (S_ * 2) + (l2 & 3) * 16), lds0 + K2_WT_OFF + 8192);
      }
      if constexpr (st == 9) {
#pragma unroll
        for (int i = 0; i < 4; ++i) publish(XB1, i, acc[0][i], acc[1][i], i);
#pragma unroll
        for (int i = 0; i < 4; ++i) give(XB2, i, acc[0][4 + i], acc[1][4 + i]);
      }
      if constexpr (st == 10) {
#pragma unroll
        for (int i = 0; i < 4; ++i) publish(XB2, i, acc[0][4 + i], acc[1][4 + i], 4 + i);
      }
    }
    if constexpr (st < 9 && ((f + 16 - 6) % 16) % 4 == 0) __builtin_amdgcn_s_setprio(3 - ((f + 16 - 6) % 16) / 4);
    sfor<36>([&](auto dc) ALWAYS_INLINE { if constexpr (dc % 4 < k2_np(dc / 4 + 2) && k2_dkey(dc / 4 + 2, dc % 4) == 4 * g + 2) piece_load2(dc / 4 + 2, dc % 4); });
    sfor<24>([&](auto hc) ALWAYS_INLINE { if constexpr (k2_hkey(hc / 4 + 3, hc % 4) == 4 * g + 3) h_load(std::integral_constant<int, hc + 12>{}); });
    if constexpr (g + PD < 162) {
      constexpr int g1 = g + PD, st1 = k2_stage(g1), f1 = g1 - k2_g0(st1);
      if constexpr (f1 == 0) {
        if constexpr (st1 < 9) {
          sb += (st1 % K2_NSLOT == 0) ? -3 * K2_SLOT : K2_SLOT;
        } else {
          int l2 = lane; asm volatile("" : "+v"(l2));
          const int kq2 = l2 >> 4;
          sb = lds0 + khf * K2_HALF + (16 * (kq2 >> 1) + 4 * (kq2 & 1) + ((l2 & 15) >> 2)) * 32 + (l2 & 3) * 8 + (st1 % K2_NSLOT) * K2_SLOT;
        }
      }
      asm volatile("" : "+v"(sb));
      if constexpr (st1 < 9) af[g1 % (PD + 1)] = tr_frag((LAS char*)(uintptr_t)(sb ^ ((f1 % 8) << 5)), (f1 / 8) * 8192, 2048);
      else af[g1 % (PD + 1)] = tr_frag((LAS char*)(uintptr_t)sb, f1 * 1024, 256);
    }
    SCHED0();
#pragma unroll
    for (int t = 0; t < 2; ++t) {
      if constexpr (st < 9) acc[t][dt] = __builtin_amdgcn_mfma_f32_16x16x32_bf16(af[g % (PD + 1)], hf[ks][t], g < 8 ? zero4 : acc[t][dt], 0, 0, 0);
      else accq[t] = __builtin_amdgcn_mfma_f32_16x16x32_bf16(af[g % (PD + 1)], hf[ks][t], g == 144 ? zero4 : accq[t], 0, 0, 0);
    }
    SCHED0();
    if constexpr (g == 143) {
#pragma unroll
      for (int i = 0; i < 4; ++i) give(XB1, i, acc[0][i], acc[1][i]);
      SCHED0();
    }
  });
  STAMP(2, 2);
  __builtin_amdgcn_s_setprio(0);
  give(XB3, 0, accq[0], accq[1]);

  int lane3 = lane; asm volatile("" : "+v"(lane3));
  u32x2 xq[3];
  float2 stt[2];
#pragma unroll
  for (int i = 0; i < 3; ++i) {
    const int t = i < 2 ? i : (w & 1);
    const int token = mt * 128 + ksl * 32 + t * 16 + (lane3 & 15);
    xq[i] = *(const u32x2*)(q + (size_t)token * D_ + (i < 2 ? colA2 + w * 16 : colQ2) + 4 * (lane3 >> 4));
    if (i < 2) {
      const u64 sv = __hip_atomic_load(stats + token, __ATOMIC_RELAXED, __HIP_MEMORY_SCOPE_AGENT);
      stt[i] = make_float2(__uint_as_float((unsigned)sv), __uint_as_float((unsigned)(sv >> 32)));
    }
  }
  WAIT_L(0);
  __builtin_amdgcn_s_barrier();
  CFENCE();
  publish(XB3, 0, accq[0], accq[1], 8);
  asm volatile("s_waitcnt vmcnt(0) lgkmcnt(0)" ::: "memory");
  __builtin_amdgcn_s_barrier();
  CFENCE();
  STAMP(2, 3);
  if (tid == 0) set_flag(flg + FL_S + tile * 4 + ksl, tag);
  poll_flags<1>(flg + FL_S + tile * 4, 4, tag, lane3);
  STAMP(2, 4);
  auto fetch = [&](f32x4 (&pv)[3], int fi) ALWAYS_INLINE {
#pragma unroll
    for (int k = 1; k < 4; ++k)
      pv[k - 1] = __builtin_bit_cast(f32x4, __builtin_amdgcn_raw_buffer_load_b128(rs, (unsigned)(((ksl * 4 + ((ksl + k) & 3)) * 18 + fi) * 1024 + lane3 * 16), 0, 16));
  };
  auto combine = [&](const f32x4 (&pv)[3], int t, int dt, const u32x2& xw2, const float2& st2) ALWAYS_INLINE {
    const int tl = t * 16 + (lane3 & 15), dl = dt * 16 + 4 * (lane3 >> 4);
    const f32x4 own = *(LAS f32x4*)(smem + K2_OWN_OFF + ((t * 9 + dt) * 64 + lane3) * 16);
    const f32x4 bs = ((own + pv[0]) + pv[1]) + pv[2];
    float o[4];
#pragma unroll
    for (int c = 0; c < 4; ++c) {
      const unsigned xw = (c < 2) ? xw2.x : xw2.y;
      const float x = (c & 1) ? bf_hi(xw) : bf_lo(xw);
      const float wy = bf2f(*(LAS u16*)(smem + K2_WT_OFF + (dl + c) * 64 + tl * 2));
      const float xn = bfr(bfr(x - st2.x) * st2.y);
      const float a = bfr(xn * wy);
      o[c] = bfr(x + a) + bfr(bs[c]);
    }
    u32x2 ov; ov.x = pk_bf16(o[0], o[1]); ov.y = pk_bf16(o[2], o[3]);
    *(u32x2*)(out + (size_t)(mt * 128 + ksl * 32 + tl) * D_ + (dt < 8 ? colA2 + dt * 16 : colQ2) + 4 * (lane3 >> 4)) = ov;
  };
  {
    f32x4 p0[3], p1[3], p2[3];
    fetch(p0, w); fetch(p1, 9 + w); fetch(p2, 9 * (w & 1) + 8);
    combine(p0, 0, w, xq[0], stt[0]);
    combine(p1, 1, w, xq[1], stt[1]);
    if (w < 2) combine(p2, w, 8, xq[2], w ? stt[1] : stt[0]);
  }
  if (bid == 0 && w == 0) {
    poll_flags<4>(flg + FL_K, 64, tag, lane);
    if (lane == 0) set_flag(flg + FL_GEN, tag);
  }
  STAMP(2, 5); RSTAMP(1, 15); STAMP_FLUSH(1); STAMP_FLUSH(2); STAMP_FLUSH(0);
  }
}

extern "C" void kernel_launch(void* const* d_in, const int* in_sizes, int n_in,
                              void* d_out, int out_size, void* d_ws, size_t ws_size,
                              hipStream_t stream) {
  const u16* q = (const u16*)d_in[0];
  const u16* k = (const u16*)d_in[1];
  const u16* v = (const u16*)d_in[2];
  const u16* shk = (const u16*)d_in[3];
  const u16* ssmk = (const u16*)d_in[4];
  const u16* ssmd = (const u16*)d_in[5];
  const u16* wg = (const u16*)d_in[7];
  const u16* wu = (const u16*)d_in[8];
  const u16* wd = (const u16*)d_in[9];
  (void)hipFuncSetAttribute((const void*)k_fused, hipFuncAttributeMaxDynamicSharedMemorySize, K1_LDS);
  k_fused<<<256, 512, K1_LDS, stream>>>(q, k, v, shk, ssmk, ssmd, wg, wu, wd, (char*)d_ws + ((ws_size - WS_END) & ~(size_t)4095), (u16*)d_out);
}
```

```cpp
#include <hip/hip_runtime.h>
#include <hip/hip_bf16.h>
#include <stdint.h>
#include <utility>

typedef unsigned short u16;
typedef short bf16x8 __attribute__((ext_vector_type(8)));
typedef short s16x4 __attribute__((ext_vector_type(4)));
typedef float f32x4 __attribute__((ext_vector_type(4)));
typedef float f32x16 __attribute__((ext_vector_type(16)));
typedef unsigned u32x4 __attribute__((ext_vector_type(4)));
typedef unsigned u32x2 __attribute__((ext_vector_type(2)));
typedef __bf16 bf2_t __attribute__((ext_vector_type(2)));
#define LAS __attribute__((address_space(3)))
#define DI __device__ __forceinline__

constexpr int S_ = 2048, D_ = 576, F_ = 4608;

constexpr size_t WS_XFRAG = 0;
constexpr size_t WS_STATS = WS_XFRAG + (size_t)S_ * D_ * 2;
constexpr size_t WS_KVR   = WS_STATS + (size_t)S_ * 8;
constexpr size_t WS_WT    = WS_KVR + (size_t)S_ * D_ * 2;
constexpr size_t WS_H     = WS_WT + (size_t)S_ * D_ * 2;
constexpr size_t WS_SLAB  = WS_H + (size_t)S_ * F_ * 2;
constexpr size_t WS_FLG   = WS_SLAB + (size_t)4 * S_ * D_ * 4;
constexpr int FL_GEN = 0, FL_X = 32, FL_K = FL_X + 256, FL_H = FL_K + 256, FL_HE = FL_H + 256, FL_W = FL_HE + 256, FL_S = FL_W + 256, FL_END = FL_S + 256;
constexpr size_t WS_END   = WS_FLG + FL_END * 8;
typedef unsigned long long u64;

DI float bf_lo(unsigned w) { return __uint_as_float(w << 16); }
DI float bf_hi(unsigned w) { return __uint_as_float(w & 0xffff0000u); }
DI float bf2f(u16 b) { return __uint_as_float(((unsigned)b) << 16); }
DI float bfr(float x) { return (float)(__bf16)x; }
typedef float f32x2 __attribute__((ext_vector_type(2)));
DI unsigned pk_bf16(float lo, float hi);
DI f32x2 unpk(unsigned w) { return (f32x2){__uint_as_float(w << 16), __uint_as_float(w & 0xffff0000u)}; }
DI f32x2 bfr2(f32x2 v) { return unpk(pk_bf16(v.x, v.y)); }
DI unsigned pk_bf16(float lo, float hi) { bf2_t v = {(__bf16)lo, (__bf16)hi}; return __builtin_bit_cast(unsigned, v); }

DI float wave_sum(float x) {
  x += __builtin_bit_cast(float, __builtin_amdgcn_update_dpp(0, __builtin_bit_cast(int, x), 0xB1, 0xf, 0xf, true));
  x += __builtin_bit_cast(float, __builtin_amdgcn_update_dpp(0, __builtin_bit_cast(int, x), 0x4E, 0xf, 0xf, true));
  x += __builtin_bit_cast(float, __builtin_amdgcn_update_dpp(0, __builtin_bit_cast(int, x), 0x141, 0xf, 0xf, true));
  x += __builtin_bit_cast(float, __builtin_amdgcn_update_dpp(0, __builtin_bit_cast(int, x), 0x140, 0xf, 0xf, true));
  const int xi = __builtin_bit_cast(int, x);
  return (__builtin_bit_cast(float, __builtin_amdgcn_readlane(xi, 0)) + __builtin_bit_cast(float, __builtin_amdgcn_readlane(xi, 16))) +
         (__builtin_bit_cast(float, __builtin_amdgcn_readlane(xi, 32)) + __builtin_bit_cast(float, __builtin_amdgcn_readlane(xi, 48)));
}
#define WAIT_L(n) asm volatile("s_waitcnt lgkmcnt(" #n ")" ::: "memory")
#define CFENCE() asm volatile("" ::: "memory")
#define SCHED0() __builtin_amdgcn_sched_barrier(0)
template <int... I, class Fn> DI void sfor_impl(std::integer_sequence<int, I...>, Fn&& f) { (f(std::integral_constant<int, I>{}), ...); }
template <int N, class Fn> DI void sfor(Fn&& f) { sfor_impl(std::make_integer_sequence<int, N>{}, f); }
#define ALWAYS_INLINE __attribute__((always_inline))
template <int N> DI void wait_vm() { asm volatile("s_waitcnt vmcnt(%0)" ::"i"(N) : "memory"); }

DI void glds1(const void* sbase, unsigned v0, unsigned lds_dst) {
  unsigned keep;
  asm volatile("s_mov_b32 %0, m0\n\ts_mov_b32 m0, %3\n\ts_nop 0\n\t"
               "global_load_lds_dwordx4 %1, %2\n\t"
               "s_mov_b32 m0, %0"
               : "=&s"(keep) : "v"(v0), "s"(sbase), "s"(lds_dst) : "memory");
}

template <bool NT> DI void glds1p(const void* sbase, unsigned v0, unsigned lds_dst) {
  unsigned keep;
  if constexpr (NT)
    asm volatile("s_mov_b32 %0, m0\n\ts_mov_b32 m0, %3\n\ts_nop 0\n\tglobal_load_lds_dwordx4 %1, %2 nt\n\ts_mov_b32 m0, %0"
                 : "=&s"(keep) : "v"(v0), "s"(sbase), "s"(lds_dst) : "memory");
  else
    asm volatile("s_mov_b32 %0, m0\n\ts_mov_b32 m0, %3\n\ts_nop 0\n\tglobal_load_lds_dwordx4 %1, %2\n\ts_mov_b32 m0, %0"
                 : "=&s"(keep) : "v"(v0), "s"(sbase), "s"(lds_dst) : "memory");
}
DI void glds1_sc1(const void* sbase, unsigned v0, unsigned lds_dst) {
  unsigned keep;
  asm volatile("s_mov_b32 %0, m0\n\ts_mov_b32 m0, %3\n\ts_nop 0\n\tglobal_load_lds_dwordx4 %1, %2 sc1\n\ts_mov_b32 m0, %0"
               : "=&s"(keep) : "v"(v0), "s"(sbase), "s"(lds_dst) : "memory");
}
DI void touch_lines(const void* sbase, unsigned v0, unsigned lds_dst) {
  unsigned keep;
  asm volatile("s_mov_b32 %0, m0\n\ts_mov_b32 m0, %3\n\ts_nop 0\n\tglobal_load_lds_dword %1, %2\n\ts_mov_b32 m0, %0"
               : "=&s"(keep) : "v"(v0), "s"(sbase), "s"(lds_dst) : "memory");
}
template <int OFF> DI void gload16_sc1(bf16x8& dst, const void* sbase, unsigned voff) {
  asm volatile("global_load_dwordx4 %0, %1, %2 offset:%3 sc1" : "=v"(dst) : "v"(voff), "s"(sbase), "i"(OFF) : "memory");
}
DI void gload16u(u32x4& dst, const void* sbase, unsigned voff) {
  asm volatile("global_load_dwordx4 %0, %1, %2" : "=v"(dst) : "v"(voff), "s"(sbase) : "memory");
}
DI void gload4u(unsigned& dst, const void* sbase, unsigned voff) {
  asm volatile("global_load_dword %0, %1, %2" : "=v"(dst) : "v"(voff), "s"(sbase) : "memory");
}
DI void gstore16_sc1(void* p, const u32x4& v) { asm volatile("global_store_dwordx4 %0, %1, off sc1\n\ts_nop 1" ::"v"(p), "v"(v) : "memory"); }
DI void set_flag(u64* f, u64 tag) { __hip_atomic_store(f, tag, __ATOMIC_RELAXED, __HIP_MEMORY_SCOPE_AGENT); }
template <int PER> DI void poll_flags(u64* f, int n, u64 tag, int lane) {
  asm volatile("" : "+v"(lane));
  const int i = lane < n ? lane : 0;
  for (unsigned spins = 0; spins < (1u << 20); ++spins) {
    bool ok = true;
#pragma unroll
    for (int k = 0; k < PER; ++k) ok &= __hip_atomic_load(f + i + 64 * k, __ATOMIC_RELAXED, __HIP_MEMORY_SCOPE_AGENT) == tag;
    if (__all(ok)) break;
    __builtin_amdgcn_s_sleep(1);
  }
}

constexpr int K1_SLOT = 37120;
constexpr int K1_UOFF = 18432 + 128;
constexpr int K1_NSLOT = 4;
constexpr int K1_PF_OFF = K1_NSLOT * K1_SLOT;
constexpr int XR_OFF = K1_PF_OFF + 288, XR_STRIDE = 4608;
constexpr int HZ_OFF = XR_OFF + 3 * XR_STRIDE;
constexpr int K1_LDS = HZ_OFF + 288;
constexpr int PART_OFF = 65536;
constexpr int PARTC_OFF = PART_OFF + 65536;
constexpr int HCNT_OFF = K1_PF_OFF;
constexpr int XG_OFF = 3 * K1_SLOT;

DI bf16x8 tr_frag(LAS char* p, int off, int hi_off) {
  s16x4 lo = __builtin_amdgcn_ds_read_tr16_b64_v4i16((LAS s16x4*)(p + off));
  s16x4 hi = __builtin_amdgcn_ds_read_tr16_b64_v4i16((LAS s16x4*)(p + off + hi_off));
  return __builtin_shufflevector(lo, hi, 0, 1, 2, 3, 4, 5, 6, 7);
}

constexpr int K1_MID = 16;
constexpr int k1_xkey(int i) { return i < 9 ? -100 + i : 4 * (1 + 2 * (i - 9)) + 2; }
constexpr int k1_dstep(int d, int n) { return d == 2 ? 2 + 4 * n : d == 3 ? 24 + 4 * n : d == 4 ? 58 + 6 * n : d == 5 ? 94 + 6 * n : 36 * (d - 3) + K1_MID + 6 * n; }
constexpr int k1_dkey(int d, int n) { return d < 2 ? -400 + 10 * d + n : 4 * k1_dstep(d, n) + 1; }
constexpr int k1_cstep(int i) { return i < 7 ? 218 + 4 * i : (i < 14 ? 254 + 4 * (i - 7) : (i < 21 ? 289 + (i - 14) : 304 + 2 * (i - 21))); }
constexpr int k1_sstep(int c) { return c == 0 ? 143 : (c < 4 ? 144 + 6 * c : (c == 4 ? 182 : 302 + 9 * (c - 5))); }
constexpr int k1_astep(int i) { return i < 14 ? k1_cstep(i) + 28 : (i < 21 ? k1_cstep(i) + 20 : 321 + (i - 21)); }
constexpr int K1_TOUCH = 5 * 36 + K1_MID + 1;
constexpr int k1_younger(int okey, int wkey) {
  int n = 0;
  if (4 * K1_TOUCH + 1 > okey && 4 * K1_TOUCH + 1 < wkey) n += 1;
  if (-70 > okey && -70 < wkey) n += 2;
  for (int i = 0; i < 36; ++i) if (k1_xkey(i) > okey && k1_xkey(i) < wkey) n += 1;
  for (int d = 0; d < 9; ++d) for (int pc = 0; pc < 5; ++pc) if (k1_dkey(d, pc) > okey && k1_dkey(d, pc) < wkey) n += 1;
  for (int i = 0; i < 24; ++i) if (4 * k1_cstep(i) + 2 > okey && 4 * k1_cstep(i) + 2 < wkey) n += 2;
  for (int c = 0; c < 8; ++c) if (4 * k1_sstep(c) + 3 > okey && 4 * k1_sstep(c) + 3 < wkey) n += 1;
  return n;
}

constexpr int K2_HALF = 16384;
constexpr int K2_SLOT = 2 * K2_HALF;
constexpr int K2_NSLOT = 4;
constexpr int K2_OWN_OFF = K2_NSLOT * K2_SLOT;
constexpr int K2_WT_OFF = K2_OWN_OFF + 18432;
constexpr int k2_g0(int st) { return st <= 9 ? 16 * st : 153; }
constexpr int k2_stage(int g) { return g < 144 ? g / 16 : (g < 153 ? 9 : 10); }
constexpr int k2_mid(int st) { return st < 9 ? 6 : 4; }
constexpr int k2_unit(int st) { return st < 5 ? 2 * st : 2 * (st - 5) + 1; }
constexpr int k2_np(int d) { return d < 9 ? 4 : 3; }
constexpr int k2_dkey(int d, int n) { return d < 2 ? -300 + 10 * d + n : (d == 2 ? 4 * (1 + n) + 2 : 4 * (k2_g0(d - 3) + k2_mid(d - 3) + 4 * n) + 2); }
constexpr int k2_hkey(int j, int m) { return j < 3 ? -250 + 10 * j + m : 4 * (k2_g0(j - 3) + k2_mid(j - 3) + 2 + 4 * m) + 3; }
constexpr int k2_skey(int p) { return 4 * (k2_g0(9 + p) + k2_mid(9 + p)) + 1; }
constexpr int K2_WST = 7;
constexpr int k2_wkey() { return 4 * (k2_g0(K2_WST) + k2_mid(K2_WST)) + 1; }
constexpr int k2_younger(int okey, int wkey, bool stores) {
  int n = 0;
  if (k2_wkey() > okey && k2_wkey() < wkey) n += 2;
  if (-100 > okey && -100 < wkey) n += 1;
  for (int d = 0; d < 11; ++d) for (int pc = 0; pc < k2_np(d); ++pc) if (k2_dkey(d, pc) > okey && k2_dkey(d, pc) < wkey) n += 1;
  for (int j = 0; j < 9; ++j) for (int m = 0; m < 4; ++m) if (k2_hkey(j, m) > okey && k2_hkey(j, m) < wkey) n += 1;
  if (stores) for (int p = 0; p < 2; ++p) if (k2_skey(p) > okey && k2_skey(p) < wkey) n += 4;
  return n;
}
constexpr unsigned CODE_BYTES = 40 * 1024;
constexpr int cmax(int a, int b) { return a > b ? a : b; }

__global__ __launch_bounds__(512, 2) void k_fused(const u16* __restrict__ q, const u16* __restrict__ kk, const u16* __restrict__ vv,
                                                  const u16* __restrict__ shk, const u16* __restrict__ ssmk, const u16* __restrict__ ssmd,
                                                  const u16* __restrict__ wg, const u16* __restrict__ wu, const u16* __restrict__ wd,
                                                  char* ws, u16* __restrict__ out) {
  u16* xfrag = (u16*)(ws + WS_XFRAG);
  u64* stats = (u64*)(ws + WS_STATS);
  u16* kvr = (u16*)(ws + WS_KVR);
  u16* wT = (u16*)(ws + WS_WT);
  u16* hout = (u16*)(ws + WS_H);
  float* slab = (float*)(ws + WS_SLAB);
  u64* flg = (u64*)(ws + WS_FLG);
  extern __shared__ __attribute__((aligned(16))) char smem_g[];
  LAS char* smem = (LAS char*)smem_g;
  const unsigned lds0 = (unsigned)(uintptr_t)smem;
  const int bid = blockIdx.x, tid = threadIdx.x;
  const int w = __builtin_amdgcn_readfirstlane(tid >> 6), lane = tid & 63;
  const int xcd = bid & 7, idx = bid >> 3;
  const u64 genv = __hip_atomic_load(flg + FL_GEN, __ATOMIC_RELAXED, __HIP_MEMORY_SCOPE_AGENT);
  u64 tag;
  auto make_tag = [&]() ALWAYS_INLINE {
    tag = (((u64)(unsigned)__builtin_amdgcn_readfirstlane((unsigned)(genv >> 32)) << 32) | (unsigned)__builtin_amdgcn_readfirstlane((unsigned)genv)) + 1ull;
  };
  const int ksl = xcd >> 1, mt = (xcd & 1) * 8 + (idx >> 2), nt = idx & 3;
  const int tq = w & 3, khf = w >> 2;
  const int kbase = ksl * 1152 + khf * 576;
  const int colA2 = 128 * nt, colQ2 = 512 + 16 * nt;
  const int mtile = (xcd & 1) * 4 + (idx & 3);
  const int cgrp = (xcd >> 1) * 2 + (idx >> 4), cq = (idx >> 2) & 3;
  const int colA = 64 * (9 * cgrp + 2 * cq), colQ = 64 * (9 * cgrp + 8) + 16 * cq;

  const int grp = bid >> 2, qd = bid & 3;
  const int chA = grp * 9 + 2 * qd, chC = grp * 9 + 8;
  float ddA, ddC;
  {
    const int c1 = chA + (w >> 2);
    const unsigned d1 = ((const unsigned*)ssmd)[c1 >> 1], d2 = ((const unsigned*)ssmd)[chC >> 1];
    ddA = (c1 & 1) ? bf_hi(d1) : bf_lo(d1);
    ddC = (chC & 1) ? bf_hi(d2) : bf_lo(d2);
  }

  const int wq = w & 3, part = w >> 2;
  const char* wbase = (const char*)(part ? wu : wg);
  unsigned voff[5];
#pragma unroll
  for (int n = 0; n < 5; ++n) {
    const int o = (n < 4) ? ((wq * 4 + n) * 1024 + lane * 16) : (16384 + wq * 512 + (lane & 31) * 16);
    const int k = o >> 7, pc = (o >> 4) & 7;
    const int j = (pc >> 1) ^ (((k >> 1) & 1) | (part << 1));
    voff[n] = (unsigned)(k * (F_ * 2) + (2 * j + (pc & 1)) * 16);
  }
  const unsigned ldst = lds0 + part * K1_UOFF + wq * 4096;
  const unsigned ldst4 = lds0 + part * K1_UOFF + 16384 + wq * 512;

  auto piece_load = [&](int st, int n) ALWAYS_INLINE {
    const int slot = st % K1_NSLOT;
    const unsigned dst = (n < 4 ? ldst + n * 1024 : ldst4) + slot * K1_SLOT;
    if (st != 4) {
      const int ks = st < 4 ? st : st - 5;
      const char* g = wbase + (size_t)ks * 144 * (F_ * 2) + (colA + (st > 4 ? 64 : 0)) * 2;
      if (n < 4 || lane < 32) glds1p<false>(g, voff[n], dst);
    } else {
      int l2 = lane; asm volatile("" : "+v"(l2));
      const int o = (n < 4) ? ((wq * 4 + n) * 1024 + l2 * 16) : (16384 + wq * 512 + (l2 & 31) * 16);
      const unsigned vq = (unsigned)((o >> 5) * (F_ * 2) + ((o >> 4) & 1) * 16);
      if (n < 4 || lane < 32) glds1p<false>(wbase + colQ * 2, vq, dst);
    }
  };

  bf16x8 xf[36];
  const char* xb = (const char*)xfrag + (size_t)(mtile * 8 + w) * 36 * 1024;
  const unsigned xv = lane * 16;
  auto x_load = [&](auto ic) ALWAYS_INLINE {
    constexpr int i = ic;
    gload16_sc1<(i & 3) * 1024>(xf[i], xb + (i >> 2) * 4096, xv);
  };
  {
    LAS u16* tile = (LAS u16*)(smem + XR_OFF);
    constexpr int KT_OFF = 2 * K1_SLOT, VT_OFF = KT_OFF + 9728, WT_OFF = VT_OFF + 9216;
    const int tt = bid >> 3, ct = bid & 7, t0 = tt * 64, c0 = ct * 72;
    const int tok0 = mtile * 256 + (cgrp * 4 + cq) * 8, token = tok0 + w;
    const bool act = lane < 48;
    u32x2 d[3];
    u32x4 kin, vin, ein;
#pragma unroll
    for (int i = 0; i < 3; ++i)
      asm volatile("global_load_dwordx2 %0, %1, %2 offset:%3" : "=v"(d[i]) : "v"((unsigned)(token * (D_ * 2) + (act ? lane : 0) * 8)), "s"(q), "i"(i * 384) : "memory");
    auto k_off = [&](int pc) ALWAYS_INLINE -> unsigned { const int r = pc / 9, cg = pc - r * 9, ts = t0 - 3 + r; return (unsigned)((ts < 0 ? 0 : ts) * (D_ * 2) + (c0 + 8 * cg) * 2); };
    auto v_off = [&](int pc) ALWAYS_INLINE -> unsigned { const int r = pc / 9, cg = pc - r * 9; return (unsigned)((t0 + r) * (D_ * 2) + (c0 + 8 * cg) * 2); };
    gload16u(kin, kk, k_off(tid));
    gload16u(vin, vv, v_off(tid));
    int edst;
    {
      const int kp = w == 0 ? 512 + lane : 576 + (lane < 27 ? lane : 0);
      const int wl = lane < 63 ? lane - 27 : 35;
      const bool isw = w == 1 && lane >= 27, isv = w >= 2;
      const char* src = isv ? (const char*)vv + v_off(512 + lane) : (isw ? (const char*)shk + c0 * 8 + wl * 16 : (const char*)kk + k_off(kp));
      edst = isv ? VT_OFF + (512 + lane) * 16 : (isw ? WT_OFF + wl * 16 : KT_OFF + kp * 16);
      asm volatile("global_load_dwordx4 %0, %1, off" : "=v"(ein) : "v"(src) : "memory");
    }
    {
      const int li = (w == 3 || w == 4) ? (w & 1) * 64 + lane : 0;
      const int ch = li < 32 ? chA : (li < 64 ? chA + 1 : chC);
      u64 pc; asm volatile("s_getpc_b64 %0" : "=s"(pc));
      const unsigned cl = (unsigned)((idx & 7) * 64 + lane) * 128u;
      const char* tb = w == 5 ? (const char*)(pc & ~127ull) : (const char*)ssmk;
      touch_lines(tb, w == 5 ? (cl < CODE_BYTES ? cl : 0u) : (unsigned)(ch * (S_ * 2) + (li & 31) * 128), lds0 + K1_PF_OFF + 16);
    }
    asm volatile("s_waitcnt vmcnt(4)" : "+v"(d[0]), "+v"(d[1]), "+v"(d[2])::"memory");
    f32x2 xa[3], xb2[3];
#pragma unroll
    for (int i = 0; i < 3; ++i) { xa[i] = unpk(d[i].x); xb2[i] = unpk(d[i].y); }
    const f32x2 s2 = (xa[0] + xb2[0]) + (xa[1] + xb2[1]) + (xa[2] + xb2[2]);
    const float mean = wave_sum(act ? s2.x + s2.y : 0.f) * (1.0f / 576.0f);
    f32x2 v2 = {0.f, 0.f};
#pragma unroll
    for (int i = 0; i < 3; ++i) { const f32x2 a = xa[i] - mean, b = xb2[i] - mean; v2 += a * a + b * b; }
    const float vs = wave_sum(act ? v2.x + v2.y : 0.f);
    const float mb = bfr(mean), vb = bfr(vs * (1.0f / 576.0f));
    const float sd = bfr(sqrtf(bfr(vb + 1e-5f)));
    const float rsd = 1.0f / sd;
#pragma unroll
    for (int i = 0; i < 3; ++i) {
      const f32x2 a = bfr2(xa[i] - mb) * rsd, b = bfr2(xb2[i] - mb) * rsd;
      u32x2 o; o.x = pk_bf16(a.x, a.y); o.y = pk_bf16(b.x, b.y);
      const int p8 = lane + 48 * i;
      if (act) *(LAS u32x2*)(smem + XG_OFF + ((p8 >> 1) * 8 + w) * 16 + (p8 & 1) * 8) = o;
    }
    if (tid == 0) *(LAS unsigned*)(smem + HCNT_OFF + 4) = 0u;
    asm volatile("s_waitcnt vmcnt(0)" : "+v"(kin), "+v"(vin), "+v"(ein)::"memory");
    make_tag();
    if (t0 == 0 && tid < 27) kin = (u32x4){0u, 0u, 0u, 0u};
    *(LAS u32x4*)(smem + KT_OFF + tid * 16) = kin;
    *(LAS u32x4*)(smem + VT_OFF + tid * 16) = vin;
    if (w < 3) *(LAS u32x4*)(smem + edst) = ein;
    WAIT_L(0);
    __builtin_amdgcn_s_barrier();
    CFENCE();
    {
      char* xo = (char*)xfrag + (size_t)((tok0 >> 5) * 36 * 64 + (tok0 & 31)) * 16;
#pragma unroll
      for (int it = 0; it < 2; ++it) {
        const int id = it ? 512 + lane : tid, p16 = id >> 3;
        if (it == 0 || w == 0) {
          const u32x4 v = *(LAS u32x4*)(smem + XG_OFF + id * 16);
          gstore16_sc1(xo + ((p16 >> 1) * 64 + (p16 & 1) * 32 + (id & 7)) * 16, v);
        }
      }
      if (lane == 0) __hip_atomic_store(stats + token, (u64)__float_as_uint(mb) | ((u64)__float_as_uint(rsd) << 32), __ATOMIC_RELAXED, __HIP_MEMORY_SCOPE_AGENT);
    }
    CFENCE();
#pragma unroll
    for (int n = 0; n < 5; ++n) piece_load(0, n);
#pragma unroll
    for (int n = 0; n < 5; ++n) piece_load(1, n);
    wait_vm<10>();
    if (lane == 0) {
      const unsigned old = __hip_atomic_fetch_add((LAS unsigned*)(smem + HCNT_OFF + 4), 1u, __ATOMIC_RELAXED, __HIP_MEMORY_SCOPE_WORKGROUP);
      if (old == 7u) set_flag(flg + FL_X + (tok0 >> 3), tag);
    }
    auto kv_compute = [&](int item) ALWAYS_INLINE {
      const int tl = item / 9, cg = item - tl * 9;
      u32x4 kr[4], w4[4];
#pragma unroll
      for (int i = 0; i < 4; ++i) { kr[i] = *(LAS u32x4*)(smem + KT_OFF + (item + 9 * i) * 16); w4[i] = *(LAS u32x4*)(smem + WT_OFF + cg * 64 + i * 16); }
      const u32x4 vr = *(LAS u32x4*)(smem + VT_OFF + item * 16);
#pragma unroll
      for (int e2 = 0; e2 < 4; ++e2) {
        const f32x2 w0 = {bf_lo(w4[e2].x), bf_lo(w4[e2].z)}, w1 = {bf_hi(w4[e2].x), bf_hi(w4[e2].z)};
        const f32x2 w2 = {bf_lo(w4[e2].y), bf_lo(w4[e2].w)}, w3 = {bf_hi(w4[e2].y), bf_hi(w4[e2].w)};
        f32x2 ks = bfr2(unpk(kr[0][e2]) * w3);
        ks = bfr2(ks + bfr2(unpk(kr[1][e2]) * w2));
        ks = bfr2(ks + bfr2(unpk(kr[2][e2]) * w1));
        ks = bfr2(ks + bfr2(unpk(kr[3][e2]) * w0));
        const f32x2 kv = ks * unpk(vr[e2]);
        const unsigned pk = pk_bf16(kv.x, kv.y);
        tile[(8 * cg + 2 * e2) * 72 + (63 - tl)] = (u16)(pk & 0xffffu);
        tile[(8 * cg + 2 * e2 + 1) * 72 + (63 - tl)] = (u16)(pk >> 16);
      }
    };
    kv_compute(tid);
    poll_flags<1>(flg + FL_X + mtile * 32 + w * 4, 4, tag, lane);
    wait_vm<0>();
    sfor<9>([&](auto ic) ALWAYS_INLINE { x_load(ic); });
    if (w == 7) kv_compute(512 + lane);
    WAIT_L(0);
    __builtin_amdgcn_s_barrier();
    CFENCE();
    {
      const int ch = tid >> 3, pp = tid & 7, ch2 = 64 + (lane >> 3);
      const u32x4 o1 = *(LAS u32x4*)(tile + ch * 72 + pp * 8), o2 = *(LAS u32x4*)(tile + ch2 * 72 + pp * 8);
      gstore16_sc1(kvr + (size_t)(c0 + ch) * S_ + (1984 - t0) + pp * 8, o1);
      gstore16_sc1(kvr + (size_t)(c0 + ch2) * S_ + (1984 - t0) + pp * 8, o2);
    }
  }

  const int kh = lane >> 5, prt = (lane >> 4) & 1, qq = (lane & 15) >> 2, p4 = lane & 3;
  const unsigned rbU = lds0 + (8 * kh + qq) * 128 + prt * K1_UOFF + ((((qq >> 1) & 1) | (prt << 1)) << 5) + p4 * 8;

  f32x16 acc[4];
  const f32x16 zero16 = {0.f, 0.f, 0.f, 0.f, 0.f, 0.f, 0.f, 0.f, 0.f, 0.f, 0.f, 0.f, 0.f, 0.f, 0.f, 0.f};

  const int token = mtile * 256 + w * 32 + (lane & 31);
  char* hb = (char*)hout + ((size_t)(token >> 4) * 144 * 64 + (lane >> 5) * 16 + (token & 15)) * 16;
  auto h_word = [&](const f32x16& a, int i) ALWAYS_INLINE -> unsigned {
    return pk_bf16(bfr(a[2 * i]) * bfr(a[2 * i + 8]), bfr(a[2 * i + 1]) * bfr(a[2 * i + 9]));
  };
  auto h_store = [&](const u32x4& o, int c16) ALWAYS_INLINE {
    gstore16_sc1(hb + ((size_t)(c16 >> 1) * 64 + (c16 & 1) * 32) * 16, o);
  };
  u32x4 ho;

  const char* hpA = (const char*)(ssmk + (size_t)(chA + (w >> 2)) * S_ + 64 * (w & 3));
  const char* hpC = (const char*)(ssmk + (size_t)chC * S_ + 32 * w);
  const bool zA = (w & 3) == 0, zC = w == 0;
  const char* pf_sk = (const char*)ssmk;
  const char* pf_wd = (const char*)wd + (size_t)(xcd >> 1) * 1152 * 1152;
  const char* kvb = (const char*)kvr;
  u32x4 caf[24];
  unsigned cax[24];
  bf16x8 cf[24];
  int cvo = 0, cvx = 0;
  auto conv_align = [&](auto ic) ALWAYS_INLINE {
    constexpr int i = ic;
    const unsigned sh = ((lane + 1) & 1) * 2;
    u32x4 r;
    r.x = __builtin_amdgcn_alignbyte(caf[i].y, caf[i].x, sh);
    r.y = __builtin_amdgcn_alignbyte(caf[i].z, caf[i].y, sh);
    r.z = __builtin_amdgcn_alignbyte(caf[i].w, caf[i].z, sh);
    r.w = __builtin_amdgcn_alignbyte(cax[i], caf[i].w, sh);
    cf[i] = __builtin_bit_cast(bf16x8, r);
    asm volatile("" : "+v"(cf[i]));
  };


  constexpr int PD = 3, MID = K1_MID;
  bf16x8 af[PD + 1];
  unsigned rb[4];
#pragma unroll
  for (int j = 0; j < 4; ++j) rb[j] = rbU ^ (j << 5);
#pragma unroll
  for (int g = 0; g < PD; ++g) af[g] = tr_frag((LAS char*)(uintptr_t)rb[g], 0, 512);
  sfor<324>([&](auto gc) ALWAYS_INLINE {
    constexpr int g = gc;
    constexpr int st = g / 36, f = g - st * 36;
    constexpr int s = st == 4 ? f : (st == 8 ? f % 9 : f / 4), c = st == 4 ? 0 : (st == 8 ? f / 9 : f % 4);
    constexpr int xi = st < 4 ? st * 9 + s : (st == 4 ? s : (st - 5) * 9 + s);
    constexpr int ai = st < 4 ? c : (st == 4 ? 0 : (c + 1) & 3);
    constexpr bool first = (st == 0 || st == 5) ? s == 0 : (st == 4 && f == 0);
    if constexpr (f == MID) {

      if constexpr (st < 8) wait_vm<k1_younger(k1_dkey(st + 1, 4), 4 * g)>();
      else { wait_vm<k1_younger(4 * (7 * 36 + MID + 1) + 1, 4 * g)>(); if (w <= 3) WAIT_L(0); }
      __builtin_amdgcn_s_barrier();
      CFENCE();

      if constexpr (st == 1) { if (tid == 0) set_flag(flg + FL_K + (bid & 7) * 32 + (bid >> 3), tag); }
      if constexpr (st == 6) {
        static_assert(k1_dkey(7, 4) > 4 * k1_sstep(4) + 3, "the stage 7 wait must cover the h stores of panels A and Q");
        if (tid == 0) set_flag(flg + FL_HE + (mtile * 4 + (cgrp >> 1)) * 8 + (cgrp & 1) * 4 + cq, tag);
      }
    }
    if constexpr (((f + 36 - MID) % 36) % 9 == 0) __builtin_amdgcn_s_setprio(3 - ((f + 36 - MID) % 36) / 9);
    sfor<35>([&](auto dc) ALWAYS_INLINE { if constexpr (k1_dkey(dc / 5 + 2, dc % 5) == 4 * g + 1) piece_load(dc / 5 + 2, dc % 5); });
    sfor<27>([&](auto ic) ALWAYS_INLINE { if constexpr (k1_xkey(ic + 9) == 4 * g + 2) x_load(std::integral_constant<int, ic + 9>{}); });
    if constexpr (g == 7 * 36 + MID + 1) {
      if (w < 3) {
        const int ch = (w == 2) ? chC : chA + w;
        const unsigned lv = lane * 16;
        poll_flags<1>(flg + FL_K + (grp >> 3) * 32, 32, tag, lane);
#pragma unroll
        for (int i = 0; i < 4; ++i) glds1_sc1(kvb + (size_t)ch * (S_ * 2) + i * 1024, lv, lds0 + XR_OFF + w * XR_STRIDE + i * 1024);
        if (lane < 4) glds1(pf_sk + (size_t)ch * (S_ * 2), lv, lds0 + HZ_OFF + w * 96 + 32);
      } else if (w == 3) {
        if (lane < 32) {
#pragma unroll
          for (int im = 0; im < 3; ++im) *(LAS u32x4*)(smem + XR_OFF + im * XR_STRIDE + 4096 + lane * 16) = (u32x4){0u, 0u, 0u, 0u};
        } else if (lane < 38) {
          const int im = (lane - 32) >> 1, hh = lane & 1;
          *(LAS u32x4*)(smem + HZ_OFF + im * 96 + hh * 16) = (u32x4){0u, 0u, 0u, 0u};
        } else if (lane == 38) *(LAS unsigned*)(smem + HCNT_OFF) = 0u;
      }
    }
    sfor<24>([&](auto ac) ALWAYS_INLINE {
      if constexpr (k1_astep(ac) == g) {
        asm volatile("s_waitcnt vmcnt(%2)" : "+v"(caf[ac]), "+v"(cax[ac]) : "i"(k1_younger(4 * k1_cstep(ac) + 2, 4 * g + 2)) : "memory");
        conv_align(ac);
      }
    });
    if constexpr (g == K1_TOUCH) {
      int l2 = lane; asm volatile("" : "+v"(l2));
      const unsigned line = (unsigned)(w * 64 + l2 < 324 ? idx * 324 + w * 64 + l2 : idx * 324);
      touch_lines(pf_wd, line * 128u, lds0 + K1_PF_OFF + 16);
    }
    sfor<24>([&](auto cc) ALWAYS_INLINE {
      constexpr int ci = cc;
      if constexpr (k1_cstep(ci) == g) {
        if constexpr (ci == 0) {
          int l2 = lane; asm volatile("" : "+v"(l2));
          const int e = (l2 & 15) - 15 + 8 * (l2 >> 4);
          cvo = (e & ~1) * 2; cvx = cvo + ((e & 1) ? 16 : 0);
        }
        constexpr int cb = ci < 16 ? 64 * (ci >> 3) + 512 * (ci & 7) : 512 * (ci - 16);
        const char* hp = (ci < 16 ? hpA : hpC) + cb + ((ci == 0 && zA) || (ci == 16 && zC) ? 32 : 0);
        gload16u(caf[ci], hp - 32, (unsigned)(cvo + 32));
        gload4u(cax[ci], hp - 32, (unsigned)(cvx + 32));
      }
    });
    if constexpr (g + PD < 324) {
      constexpr int g1 = g + PD, st1 = g1 / 36, f1 = g1 - st1 * 36;
      if constexpr (f1 == 0) {
        if constexpr (st1 != 4) {
          unsigned b2 = rbU; asm volatile("" : "+v"(b2));
#pragma unroll
          for (int j = 0; j < 4; ++j) { rb[j] = (b2 + (st1 % K1_NSLOT) * K1_SLOT) ^ (j << 5); asm volatile("" : "+v"(rb[j])); }
        } else {
          int l2 = lane; asm volatile("" : "+v"(l2));
          rb[0] = lds0 + (8 * (l2 >> 5) + ((l2 & 15) >> 2)) * 32 + ((l2 >> 4) & 1) * K1_UOFF + (l2 & 3) * 8 + (st1 % K1_NSLOT) * K1_SLOT;
          asm volatile("" : "+v"(rb[0]));
        }
      }
      if constexpr (st1 == 8) af[g1 % (PD + 1)] = tr_frag((LAS char*)(uintptr_t)rb[f1 / 9], (f1 % 9) * 2048, 512);
      else if constexpr (st1 != 4) af[g1 % (PD + 1)] = tr_frag((LAS char*)(uintptr_t)rb[f1 % 4], (f1 / 4) * 2048, 512);
      else af[g1 % (PD + 1)] = tr_frag((LAS char*)(uintptr_t)rb[0], f1 * 512, 128);
    }
    if constexpr (st < 4 && c == 0) {
      asm volatile("s_waitcnt vmcnt(%1)" : "+v"(xf[xi]) : "i"(k1_younger(k1_xkey(xi), 4 * g + 3)) : "memory");
    }
    SCHED0();
    acc[ai] = __builtin_amdgcn_mfma_f32_32x32x16_bf16(af[g % (PD + 1)], xf[xi], first ? zero16 : acc[ai], 0, 0, 0);
    SCHED0();
    if constexpr (g == 143) {
#pragma unroll
      for (int i = 0; i < 4; ++i) ho[i] = h_word(acc[0], i);
      h_store(ho, colA >> 4);
      SCHED0();
    }
    if constexpr (g > 144 && g <= 162) {
      constexpr int cc = (g - 145) / 6 + 1, pi = (g - 145) % 6;
      if constexpr (pi < 4) ho[pi] = h_word(acc[cc], pi);
      if constexpr (pi == 5) h_store(ho, (colA >> 4) + cc);
      SCHED0();
    }
    if constexpr (g >= 298 && (g - 298) % 9 < 5 && (g - 298) / 9 < 3) {
      constexpr int cc = (g - 298) / 9, pi = (g - 298) % 9;
      if constexpr (pi < 4) ho[pi] = h_word(acc[(cc + 1) & 3], pi);
      if constexpr (pi == 4) h_store(ho, (colA >> 4) + 4 + cc);
      SCHED0();
    }
    if constexpr (g >= 180 && g <= 182) {
      if constexpr (g == 180) { ho[0] = h_word(acc[0], 0); ho[1] = h_word(acc[0], 1); }
      if constexpr (g == 181) { ho[2] = h_word(acc[0], 2); ho[3] = h_word(acc[0], 3); }
      if constexpr (g == 182) h_store(ho, colQ >> 4);
      SCHED0();
    }
  });
  const char* wbase2 = (const char*)wd + (size_t)kbase * (D_ * 2);
  unsigned voff2[4];
  int lane2 = lane; asm volatile("" : "+v"(lane2));
#pragma unroll
  for (int n = 0; n < 4; ++n) {
    const int o = (tq * 4 + n) * 1024 + lane2 * 16;
    const int k = o >> 8, pc = (o >> 4) & 15;
    const int j = (pc >> 1) ^ (k & 7);
    voff2[n] = (unsigned)(k * (D_ * 2) + (2 * j + (pc & 1)) * 16);
  }
  const unsigned ldst2 = lds0 + khf * K2_HALF;
  auto piece_load2 = [&](int st, int n) ALWAYS_INLINE {
    const int slot = st % K2_NSLOT;
    if (st < 9) {
      const char* g = wbase2 + (size_t)k2_unit(st) * 64 * (D_ * 2) + colA2 * 2;
      glds1p<false>(g, voff2[n], ldst2 + slot * K2_SLOT + tq * 4096 + n * 1024);
    } else {
      int l2 = lane; asm volatile("" : "+v"(l2));
      const char* g = wbase2 + (size_t)(st - 9) * 288 * (D_ * 2) + colQ2 * 2;
      const int o = n < 2 ? tq * 2048 + n * 1024 + l2 * 16 : 8192 + tq * 256 + (l2 & 15) * 16;
      const unsigned v0 = (unsigned)((o >> 5) * (D_ * 2) + ((o >> 4) & 1) * 16);
      if (n < 2 || l2 < 16) glds1p<false>(g, v0, ldst2 + slot * K2_SLOT + (n < 2 ? tq * 2048 + n * 1024 : 8192 + tq * 256));
    }
  };
  const char* hbase = (const char*)hout + ((size_t)(mt * 8 + tq * 2) * 144 * 64 + (size_t)(kbase >> 5) * 64) * 16;
  const unsigned hv0 = lane2 * 16 + khf * (144 * 64 * 16), hv1 = lane2 * 16 + (1 - khf) * (144 * 64 * 16);
  bf16x8 hf[18][2];
  auto h_load = [&](auto lc) ALWAYS_INLINE {
    constexpr int k = 2 * k2_unit(lc / 4) + (lc / 2) % 2, t = lc % 2;
    gload16_sc1<(k & 3) * 1024>(hf[k][t], hbase + (k >> 2) * 4096, t ? hv1 : hv0);
  };
  __builtin_amdgcn_s_setprio(0);
  {
    u32x4 o;
#pragma unroll
    for (int i = 0; i < 4; ++i) o[i] = h_word(acc[0], i);
    h_store(o, (colA >> 4) + 7);
  }

  __builtin_amdgcn_s_barrier();
  CFENCE();
#pragma unroll
  for (int n = 0; n < 4; ++n) piece_load2(0, n);
#pragma unroll
  for (int n = 0; n < 4; ++n) piece_load2(1, n);

  const int ct0 = lane & 15, ckg = lane >> 4;
  {
    const int boff = (2032 - 16 * (lane & 15) + 8 * ckg) * 2;
    auto head_frag = [&](int im) ALWAYS_INLINE -> bf16x8 {
      bf16x8 r;
      LAS u16* hz = (LAS u16*)(smem + HZ_OFF + im * 96) + 16 + (ct0 - 15 + 8 * ckg);
#pragma unroll
      for (int j = 0; j < 8; ++j) r[j] = (short)hz[j];
      return r;
    };
    if (zA) cf[0] = head_frag(w >> 2);
    if (zC) cf[16] = head_frag(2);
    {
      const int im = w >> 2;
      LAS char* xr = smem + XR_OFF + im * XR_STRIDE + boff + 128 * (w & 3);
      f32x4 ac[8];
#pragma unroll
      for (int j = 0; j < 8; ++j) ac[j] = (f32x4){0.f, 0.f, 0.f, 0.f};
#pragma unroll
      for (int rr = 1; rr >= 0; --rr) {
        bf16x8 bfm[8];
#pragma unroll
        for (int b = 0; b < 8; ++b) bfm[b] = *(LAS bf16x8*)(xr - 512 * b + 64 * rr);
#pragma unroll
        for (int a = 7; a >= 0; --a)
#pragma unroll
          for (int j = a; j < 8; ++j) ac[j] = __builtin_amdgcn_mfma_f32_16x16x32_bf16(cf[rr * 8 + a], bfm[j - a], ac[j], 0, 0, 0);
        if (rr == 1) {
          SCHED0();
          wait_vm<8>();
          if (lane == 0) {
            const unsigned old = __hip_atomic_fetch_add((LAS unsigned*)(smem + HCNT_OFF), 1u, __ATOMIC_RELAXED, __HIP_MEMORY_SCOPE_WORKGROUP);
            if (old == 7u) set_flag(flg + FL_H + (mtile * 4 + (cgrp >> 1)) * 8 + (cgrp & 1) * 4 + cq, tag);
          }
          SCHED0();
        }
      }
#pragma unroll
      for (int j = 0; j < 8; ++j)
        *(LAS f32x4*)(smem + PART_OFF + w * 8192 + (j * 64 + lane) * 16) = ac[j];
    }
    poll_flags<1>(flg + FL_HE + ((mt >> 1) * 4 + ksl) * 8, 8, tag, lane);
    sfor<12>([&](auto kc) ALWAYS_INLINE { h_load(kc); });
    {
      LAS char* xr = smem + XR_OFF + 2 * XR_STRIDE + boff + 64 * w;
      const int jA = 7 - qd, jB = qd;
      bf16x8 b0[8], b1[4];
#pragma unroll
      for (int a = 0; a < 8; ++a) { const int bb = jA - a; b0[a] = *(LAS bf16x8*)(xr - 512 * (bb < 0 ? 0 : bb)); }
#pragma unroll
      for (int a = 0; a < 4; ++a) { const int bb = jB - a; b1[a] = *(LAS bf16x8*)(xr - 512 * (bb < 0 ? 0 : bb)); }
      f32x4 c0 = (f32x4){0.f, 0.f, 0.f, 0.f}, c1 = c0;
#pragma unroll
      for (int a = 7; a >= 0; --a) {
        if (a <= jA) c0 = __builtin_amdgcn_mfma_f32_16x16x32_bf16(cf[16 + a], b0[a], c0, 0, 0, 0);
        if (a < 4 && a <= jB) c1 = __builtin_amdgcn_mfma_f32_16x16x32_bf16(cf[16 + a], b1[a < 4 ? a : 0], c1, 0, 0, 0);
      }
      *(LAS f32x4*)(smem + PARTC_OFF + w * 2048 + lane * 16) = c0;
      *(LAS f32x4*)(smem + PARTC_OFF + w * 2048 + 1024 + lane * 16) = c1;
    }
  }
  WAIT_L(0);
  __builtin_amdgcn_s_barrier();
  CFENCE();
  auto finalize = [&](f32x4 s0, f32x4 s1, int im, int ch, int t8, float dd) ALWAYS_INLINE {
    const u32x4 kvv = *(LAS u32x4*)(smem + XR_OFF + im * XR_STRIDE + (2040 - t8) * 2);
    float y[8] = {s0[0], s0[1], s0[2], s0[3], s1[0], s1[1], s1[2], s1[3]};
    float o[8];
#pragma unroll
    for (int e = 0; e < 8; ++e) {
      const int ri = 7 - e;
      const float kvf = (ri & 1) ? bf_hi(kvv[ri >> 1]) : bf_lo(kvv[ri >> 1]);
      o[e] = bfr(y[e]) + bfr(kvf * dd);
    }
    u32x4 ov; ov.x = pk_bf16(o[0], o[1]); ov.y = pk_bf16(o[2], o[3]); ov.z = pk_bf16(o[4], o[5]); ov.w = pk_bf16(o[6], o[7]);
    gstore16_sc1(wT + (size_t)ch * S_ + t8, ov);
  };
  {
    const int im = tid >> 8, t8 = (tid & 255) * 8;
    const int po = ((t8 >> 8) * 64 + ((t8 >> 2) & 3) * 16 + ((t8 >> 4) & 15)) * 16;
    f32x4 s0 = (f32x4){0.f, 0.f, 0.f, 0.f}, s1 = s0;
#pragma unroll
    for (int ww = 0; ww < 4; ++ww) {
      s0 += *(LAS f32x4*)(smem + PART_OFF + (im * 4 + ww) * 8192 + po);
      s1 += *(LAS f32x4*)(smem + PART_OFF + (im * 4 + ww) * 8192 + po + 256);
    }
    finalize(s0, s1, im, chA + im, t8, ddA);
  }
  if (tid >= 448) {
    const int t2 = tid - 448;
    const int tl = t2 >> 5, l8 = (t2 & 31) * 8;
    const int jt = tl ? qd : 7 - qd, t8 = 256 * jt + l8;
    const int po = (((l8 >> 2) & 3) * 16 + (l8 >> 4)) * 16;
    f32x4 s0 = (f32x4){0.f, 0.f, 0.f, 0.f}, s1 = s0;
#pragma unroll
    for (int ww = 0; ww < 8; ++ww) {
      s0 += *(LAS f32x4*)(smem + PARTC_OFF + ww * 2048 + tl * 1024 + po);
      s1 += *(LAS f32x4*)(smem + PARTC_OFF + ww * 2048 + tl * 1024 + po + 256);
    }
    finalize(s0, s1, 2, chC, t8, ddC);
  }

  {
  const int kq = lane2 >> 4, qq = (lane2 & 15) >> 2, p4 = lane2 & 3;
  const unsigned rbU2 = lds0 + khf * K2_HALF + (16 * (kq >> 1) + 4 * (kq & 1) + qq) * 256 + ((4 * (kq & 1) + qq) << 5) + p4 * 8;

  const char* wtb = (const char*)(wT + mt * 128 + ksl * 32);
  f32x4 acc[2][8], accq[2];
  const f32x4 zero4 = {0.f, 0.f, 0.f, 0.f};

  const int tile = mt * 4 + nt;
  const __amdgpu_buffer_rsrc_t rs = __builtin_amdgcn_make_buffer_rsrc((char*)slab + (size_t)tile * (16 * 18432), 0, 16 * 18432, 0x00020000);
  const bool foreign = tq != ksl;
  auto give = [&](int xb, int i, const f32x4& a0, const f32x4& a1) ALWAYS_INLINE {
    *(LAS f32x4*)(smem + xb + ((w * 4 + i) * 64 + lane) * 16) = a1;
  };
  auto publish = [&](int xb, int i, const f32x4& a0, const f32x4& a1, int dtg) ALWAYS_INLINE {
    const f32x4 sm = a0 + *(LAS f32x4*)(smem + xb + (((w ^ 4) * 4 + i) * 64 + lane) * 16);
    if (foreign) __builtin_amdgcn_raw_buffer_store_b128(__builtin_bit_cast(u32x4, sm), rs, (unsigned)(((tq * 4 + ksl) * 18 + khf * 9 + dtg) * 1024 + lane * 16), 0, 16);
    else *(LAS f32x4*)(smem + K2_OWN_OFF + ((khf * 9 + dtg) * 64 + lane) * 16) = sm;
  };
  constexpr int XB1 = 3 * K2_SLOT, XB2 = 0, XB3 = 3 * K2_SLOT;

  asm volatile("s_waitcnt vmcnt(%4)" : "+v"(hf[0][0]), "+v"(hf[0][1]), "+v"(hf[1][0]), "+v"(hf[1][1])
               : "i"(k2_younger(k2_hkey(0, 3), 0, false)) : "memory");
  WAIT_L(0);
  __builtin_amdgcn_s_barrier();
  CFENCE();

  constexpr int PD = 2;
  bf16x8 af[PD + 1];
  unsigned sb = rbU2;
#pragma unroll
  for (int g = 0; g < PD; ++g) af[g] = tr_frag((LAS char*)(uintptr_t)(sb ^ (g << 5)), 0, 2048);
  sfor<162>([&](auto gc) ALWAYS_INLINE {
    constexpr int g = gc;
    constexpr int st = k2_stage(g), f = g - k2_g0(st);
    constexpr int ks = st < 9 ? k2_unit(st) * 2 + f / 8 : (st - 9) * 9 + f, dt = st < 9 ? f % 8 : 0;
    if constexpr (f == k2_mid(st)) {
      if constexpr (st == 2) { if (w == 0) poll_flags<1>(flg + FL_H + ((mt >> 1) * 4 + ksl) * 8, 8, tag, lane); }
      if constexpr (st == K2_WST) { if (w == 0) poll_flags<4>(flg + FL_W, 64, tag, lane); }
      if constexpr (st < 10) {
        constexpr int okey = cmax(k2_dkey(st + 1, k2_np(st + 1) - 1), st + 1 < 9 ? k2_hkey(st + 1, 3) : -1000);
        constexpr int n0 = k2_younger(okey, 4 * g, false);
        if constexpr (st == 0) { if (w == 7) wait_vm<n0 + 1>(); else wait_vm<n0>(); }
        else wait_vm<n0>();
        if constexpr (st + 1 < 9) {
          constexpr int k0 = 2 * k2_unit(st + 1);
          asm volatile("" : "+v"(hf[k0][0]), "+v"(hf[k0][1]), "+v"(hf[k0 + 1][0]), "+v"(hf[k0 + 1][1])::"memory");
        }
      }
      if constexpr (st >= 9) WAIT_L(0);
      __builtin_amdgcn_s_barrier();
      CFENCE();
      if constexpr (st == 1) { if (tid == 0) set_flag(flg + FL_W + bid, tag); }
      if constexpr (st == K2_WST) {
        int l2 = lane; asm volatile("" : "+v"(l2));
        glds1_sc1(wtb, (unsigned)((colA2 + w * 16 + (l2 >> 2)) * (S_ * 2) + (l2 & 3) * 16), lds0 + K2_WT_OFF + w * 1024);
        glds1_sc1(wtb, (unsigned)((colQ2 + (l2 >> 2)) * (S_ * 2) + (l2 & 3) * 16), lds0 + K2_WT_OFF + 8192);
      }
      if constexpr (st == 9) {
#pragma unroll
        for (int i = 0; i < 4; ++i) publish(XB1, i, acc[0][i], acc[1][i], i);
#pragma unroll
        for (int i = 0; i < 4; ++i) give(XB2, i, acc[0][4 + i], acc[1][4 + i]);
      }
      if constexpr (st == 10) {
#pragma unroll
        for (int i = 0; i < 4; ++i) publish(XB2, i, acc[0][4 + i], acc[1][4 + i], 4 + i);
      }
    }
    if constexpr (st < 9 && ((f + 16 - 6) % 16) % 4 == 0) __builtin_amdgcn_s_setprio(3 - ((f + 16 - 6) % 16) / 4);
    sfor<36>([&](auto dc) ALWAYS_INLINE { if constexpr (dc % 4 < k2_np(dc / 4 + 2) && k2_dkey(dc / 4 + 2, dc % 4) == 4 * g + 2) piece_load2(dc / 4 + 2, dc % 4); });
    sfor<24>([&](auto hc) ALWAYS_INLINE { if constexpr (k2_hkey(hc / 4 + 3, hc % 4) == 4 * g + 3) h_load(std::integral_constant<int, hc + 12>{}); });
    if constexpr (g + PD < 162) {
      constexpr int g1 = g + PD, st1 = k2_stage(g1), f1 = g1 - k2_g0(st1);
      if constexpr (f1 == 0) {
        if constexpr (st1 < 9) {
          sb += (st1 % K2_NSLOT == 0) ? -3 * K2_SLOT : K2_SLOT;
        } else {
          int l2 = lane; asm volatile("" : "+v"(l2));
          const int kq2 = l2 >> 4;
          sb = lds0 + khf * K2_HALF + (16 * (kq2 >> 1) + 4 * (kq2 & 1) + ((l2 & 15) >> 2)) * 32 + (l2 & 3) * 8 + (st1 % K2_NSLOT) * K2_SLOT;
        }
      }
      asm volatile("" : "+v"(sb));
      if constexpr (st1 < 9) af[g1 % (PD + 1)] = tr_frag((LAS char*)(uintptr_t)(sb ^ ((f1 % 8) << 5)), (f1 / 8) * 8192, 2048);
      else af[g1 % (PD + 1)] = tr_frag((LAS char*)(uintptr_t)sb, f1 * 1024, 256);
    }
    SCHED0();
#pragma unroll
    for (int t = 0; t < 2; ++t) {
      if constexpr (st < 9) acc[t][dt] = __builtin_amdgcn_mfma_f32_16x16x32_bf16(af[g % (PD + 1)], hf[ks][t], g < 8 ? zero4 : acc[t][dt], 0, 0, 0);
      else accq[t] = __builtin_amdgcn_mfma_f32_16x16x32_bf16(af[g % (PD + 1)], hf[ks][t], g == 144 ? zero4 : accq[t], 0, 0, 0);
    }
    SCHED0();
    if constexpr (g == 143) {
#pragma unroll
      for (int i = 0; i < 4; ++i) give(XB1, i, acc[0][i], acc[1][i]);
      SCHED0();
    }
  });
  __builtin_amdgcn_s_setprio(0);
  give(XB3, 0, accq[0], accq[1]);

  int lane3 = lane; asm volatile("" : "+v"(lane3));
  u32x2 xq[3];
  float2 stt[2];
#pragma unroll
  for (int i = 0; i < 3; ++i) {
    const int t = i < 2 ? i : (w & 1);
    const int token = mt * 128 + ksl * 32 + t * 16 + (lane3 & 15);
    xq[i] = *(const u32x2*)(q + (size_t)token * D_ + (i < 2 ? colA2 + w * 16 : colQ2) + 4 * (lane3 >> 4));
    if (i < 2) {
      const u64 sv = __hip_atomic_load(stats + token, __ATOMIC_RELAXED, __HIP_MEMORY_SCOPE_AGENT);
      stt[i] = make_float2(__uint_as_float((unsigned)sv), __uint_as_float((unsigned)(sv >> 32)));
    }
  }
  WAIT_L(0);
  __builtin_amdgcn_s_barrier();
  CFENCE();
  publish(XB3, 0, accq[0], accq[1], 8);
  asm volatile("s_waitcnt vmcnt(0) lgkmcnt(0)" ::: "memory");
  __builtin_amdgcn_s_barrier();
  CFENCE();
  if (tid == 0) set_flag(flg + FL_S + tile * 4 + ksl, tag);
  poll_flags<1>(flg + FL_S + tile * 4, 4, tag, lane3);
  auto fetch = [&](f32x4 (&pv)[3], int fi) ALWAYS_INLINE {
#pragma unroll
    for (int k = 1; k < 4; ++k)
      pv[k - 1] = __builtin_bit_cast(f32x4, __builtin_amdgcn_raw_buffer_load_b128(rs, (unsigned)(((ksl * 4 + ((ksl + k) & 3)) * 18 + fi) * 1024 + lane3 * 16), 0, 16));
  };
  auto combine = [&](const f32x4 (&pv)[3], int t, int dt, const u32x2& xw2, const float2& st2) ALWAYS_INLINE {
    const int tl = t * 16 + (lane3 & 15), dl = dt * 16 + 4 * (lane3 >> 4);
    const f32x4 own = *(LAS f32x4*)(smem + K2_OWN_OFF + ((t * 9 + dt) * 64 + lane3) * 16);
    const f32x4 bs = ((own + pv[0]) + pv[1]) + pv[2];
    float o[4];
#pragma unroll
    for (int c = 0; c < 4; ++c) {
      const unsigned xw = (c < 2) ? xw2.x : xw2.y;
      const float x = (c & 1) ? bf_hi(xw) : bf_lo(xw);
      const float wy = bf2f(*(LAS u16*)(smem + K2_WT_OFF + (dl + c) * 64 + tl * 2));
      const float xn = bfr(bfr(x - st2.x) * st2.y);
      const float a = bfr(xn * wy);
      o[c] = bfr(x + a) + bfr(bs[c]);
    }
    u32x2 ov; ov.x = pk_bf16(o[0], o[1]); ov.y = pk_bf16(o[2], o[3]);
    *(u32x2*)(out + (size_t)(mt * 128 + ksl * 32 + tl) * D_ + (dt < 8 ? colA2 + dt * 16 : colQ2) + 4 * (lane3 >> 4)) = ov;
  };
  {
    f32x4 p0[3], p1[3], p2[3];
    fetch(p0, w); fetch(p1, 9 + w); fetch(p2, 9 * (w & 1) + 8);
    combine(p0, 0, w, xq[0], stt[0]);
    combine(p1, 1, w, xq[1], stt[1]);
    if (w < 2) combine(p2, w, 8, xq[2], w ? stt[1] : stt[0]);
  }
  if (bid == 0 && w == 0) {
    poll_flags<4>(flg + FL_K, 64, tag, lane);
    if (lane == 0) set_flag(flg + FL_GEN, tag);
  }
  }
}

extern "C" void kernel_launch(void* const* d_in, const int* in_sizes, int n_in,
                              void* d_out, int out_size, void* d_ws, size_t ws_size,
                              hipStream_t stream) {
  const u16* q = (const u16*)d_in[0];
  const u16* k = (const u16*)d_in[1];
  const u16* v = (const u16*)d_in[2];
  const u16* shk = (const u16*)d_in[3];
  const u16* ssmk = (const u16*)d_in[4];
  const u16* ssmd = (const u16*)d_in[5];
  const u16* wg = (const u16*)d_in[7];
  const u16* wu = (const u16*)d_in[8];
  const u16* wd = (const u16*)d_in[9];
  (void)hipFuncSetAttribute((const void*)k_fused, hipFuncAttributeMaxDynamicSharedMemorySize, K1_LDS);
  k_fused<<<256, 512, K1_LDS, stream>>>(q, k, v, shk, ssmk, ssmd, wg, wu, wd, (char*)d_ws, (u16*)d_out);
}
```

```cpp
#include <hip/hip_runtime.h>
#include <hip/hip_bf16.h>
#include <stdint.h>
#include <utility>

typedef unsigned short u16;
typedef short bf16x8 __attribute__((ext_vector_type(8)));
typedef short s16x4 __attribute__((ext_vector_type(4)));
typedef float f32x4 __attribute__((ext_vector_type(4)));
typedef float f32x16 __attribute__((ext_vector_type(16)));
typedef unsigned u32x4 __attribute__((ext_vector_type(4)));
typedef unsigned u32x2 __attribute__((ext_vector_type(2)));
typedef __bf16 bf2_t __attribute__((ext_vector_type(2)));
#define LAS __attribute__((address_space(3)))
#define DI __device__ __forceinline__

constexpr int S_ = 2048, D_ = 576, F_ = 4608;

constexpr size_t WS_XFRAG = 0;
constexpr size_t WS_STATS = WS_XFRAG + (size_t)S_ * D_ * 2;
constexpr size_t WS_KVR   = WS_STATS + (size_t)S_ * 8;
constexpr size_t WS_WT    = WS_KVR + (size_t)S_ * D_ * 2;
constexpr size_t WS_H     = WS_WT + (size_t)S_ * D_ * 2;
constexpr size_t WS_SLAB  = WS_H + (size_t)S_ * F_ * 2;
constexpr size_t WS_FLG   = WS_SLAB + (size_t)4 * S_ * D_ * 4;
constexpr int FL_GEN = 0, FL_X = 32, FL_K = FL_X + 256, FL_H = FL_K + 256, FL_HE = FL_H + 256, FL_W = FL_HE + 256, FL_S = FL_W + 256, FL_END = FL_S + 256;
constexpr size_t WS_END   = WS_FLG + FL_END * 8;
typedef unsigned long long u64;

DI float bf_lo(unsigned w) { return __uint_as_float(w << 16); }
DI float bf_hi(unsigned w) { return __uint_as_float(w & 0xffff0000u); }
DI float bf2f(u16 b) { return __uint_as_float(((unsigned)b) << 16); }
DI float bfr(float x) { return (float)(__bf16)x; }
typedef float f32x2 __attribute__((ext_vector_type(2)));
DI unsigned pk_bf16(float lo, float hi);
DI f32x2 unpk(unsigned w) { return (f32x2){__uint_as_float(w << 16), __uint_as_float(w & 0xffff0000u)}; }
DI f32x2 bfr2(f32x2 v) { return unpk(pk_bf16(v.x, v.y)); }
DI unsigned pk_bf16(float lo, float hi) { bf2_t v = {(__bf16)lo, (__bf16)hi}; return __builtin_bit_cast(unsigned, v); }

DI float wave_sum(float x) {
  x += __builtin_bit_cast(float, __builtin_amdgcn_update_dpp(0, __builtin_bit_cast(int, x), 0xB1, 0xf, 0xf, true));
  x += __builtin_bit_cast(float, __builtin_amdgcn_update_dpp(0, __builtin_bit_cast(int, x), 0x4E, 0xf, 0xf, true));
  x += __builtin_bit_cast(float, __builtin_amdgcn_update_dpp(0, __builtin_bit_cast(int, x), 0x141, 0xf, 0xf, true));
  x += __builtin_bit_cast(float, __builtin_amdgcn_update_dpp(0, __builtin_bit_cast(int, x), 0x140, 0xf, 0xf, true));
  const int xi = __builtin_bit_cast(int, x);
  return (__builtin_bit_cast(float, __builtin_amdgcn_readlane(xi, 0)) + __builtin_bit_cast(float, __builtin_amdgcn_readlane(xi, 16))) +
         (__builtin_bit_cast(float, __builtin_amdgcn_readlane(xi, 32)) + __builtin_bit_cast(float, __builtin_amdgcn_readlane(xi, 48)));
}
#define WAIT_L(n) asm volatile("s_waitcnt lgkmcnt(" #n ")" ::: "memory")
#define CFENCE() asm volatile("" ::: "memory")
#define SCHED0() __builtin_amdgcn_sched_barrier(0)
template <int... I, class Fn> DI void sfor_impl(std::integer_sequence<int, I...>, Fn&& f) { (f(std::integral_constant<int, I>{}), ...); }
template <int N, class Fn> DI void sfor(Fn&& f) { sfor_impl(std::make_integer_sequence<int, N>{}, f); }
#define ALWAYS_INLINE __attribute__((always_inline))
template <int N> DI void wait_vm() { asm volatile("s_waitcnt vmcnt(%0)" ::"i"(N) : "memory"); }

DI void glds1(const void* sbase, unsigned v0, unsigned lds_dst) {
  unsigned keep;
  asm volatile("s_mov_b32 %0, m0\n\ts_mov_b32 m0, %3\n\ts_nop 0\n\t"
               "global_load_lds_dwordx4 %1, %2\n\t"
               "s_mov_b32 m0, %0"
               : "=&s"(keep) : "v"(v0), "s"(sbase), "s"(lds_dst) : "memory");
}

template <bool NT> DI void glds1p(const void* sbase, unsigned v0, unsigned lds_dst) {
  unsigned keep;
  if constexpr (NT)
    asm volatile("s_mov_b32 %0, m0\n\ts_mov_b32 m0, %3\n\ts_nop 0\n\tglobal_load_lds_dwordx4 %1, %2 nt\n\ts_mov_b32 m0, %0"
                 : "=&s"(keep) : "v"(v0), "s"(sbase), "s"(lds_dst) : "memory");
  else
    asm volatile("s_mov_b32 %0, m0\n\ts_mov_b32 m0, %3\n\ts_nop 0\n\tglobal_load_lds_dwordx4 %1, %2\n\ts_mov_b32 m0, %0"
                 : "=&s"(keep) : "v"(v0), "s"(sbase), "s"(lds_dst) : "memory");
}
DI void glds1_sc1(const void* sbase, unsigned v0, unsigned lds_dst) {
  unsigned keep;
  asm volatile("s_mov_b32 %0, m0\n\ts_mov_b32 m0, %3\n\ts_nop 0\n\tglobal_load_lds_dwordx4 %1, %2 sc1\n\ts_mov_b32 m0, %0"
               : "=&s"(keep) : "v"(v0), "s"(sbase), "s"(lds_dst) : "memory");
}
DI void touch_lines(const void* sbase, unsigned v0, unsigned lds_dst) {
  unsigned keep;
  asm volatile("s_mov_b32 %0, m0\n\ts_mov_b32 m0, %3\n\ts_nop 0\n\tglobal_load_lds_dword %1, %2\n\ts_mov_b32 m0, %0"
               : "=&s"(keep) : "v"(v0), "s"(sbase), "s"(lds_dst) : "memory");
}
template <int OFF> DI void gload16_sc1(bf16x8& dst, const void* sbase, unsigned voff) {
  asm volatile("global_load_dwordx4 %0, %1, %2 offset:%3 sc1" : "=v"(dst) : "v"(voff), "s"(sbase), "i"(OFF) : "memory");
}
DI void gload16u(u32x4& dst, const void* sbase, unsigned voff) {
  asm volatile("global_load_dwordx4 %0, %1, %2" : "=v"(dst) : "v"(voff), "s"(sbase) : "memory");
}
DI void gload4u(unsigned& dst, const void* sbase, unsigned voff) {
  asm volatile("global_load_dword %0, %1, %2" : "=v"(dst) : "v"(voff), "s"(sbase) : "memory");
}
DI void gstore16_sc1(void* p, const u32x4& v) { asm volatile("global_store_dwordx4 %0, %1, off sc1\n\ts_nop 1" ::"v"(p), "v"(v) : "memory"); }
DI void set_flag(u64* f, u64 tag) { __hip_atomic_store(f, tag, __ATOMIC_RELAXED, __HIP_MEMORY_SCOPE_AGENT); }
DI void peek_flag(u64& dst, const u64* f, int i) {
  asm volatile("global_load_dwordx2 %0, %1, %2 sc1" : "=v"(dst) : "v"((unsigned)i * 8u), "s"(f) : "memory");
}
template <int PER> DI void poll_flags(u64* f, int n, u64 tag, int lane) {
  asm volatile("" : "+v"(lane));
  const int i = lane < n ? lane : 0;
#pragma nounroll
  for (unsigned spins = 0; spins < (1u << 20); ++spins) {
    bool ok = true;
#pragma unroll
    for (int k = 0; k < PER; ++k) ok &= __hip_atomic_load(f + i + 64 * k, __ATOMIC_RELAXED, __HIP_MEMORY_SCOPE_AGENT) == tag;
    if (__all(ok)) break;
    __builtin_amdgcn_s_sleep(1);
  }
}

constexpr int K1_SLOT = 37120;
constexpr int K1_UOFF = 18432 + 128;
constexpr int K1_NSLOT = 4;
constexpr int K1_PF_OFF = K1_NSLOT * K1_SLOT;
constexpr int XR_OFF = K1_PF_OFF + 288, XR_STRIDE = 4608;
constexpr int HZ_OFF = XR_OFF + 3 * XR_STRIDE;
constexpr int K1_LDS = HZ_OFF + 288;
constexpr int PART_OFF = 65536;
constexpr int PARTC_OFF = PART_OFF + 65536;
constexpr int HCNT_OFF = K1_PF_OFF;
constexpr int XG_OFF = 3 * K1_SLOT;

DI bf16x8 tr_frag(LAS char* p, int off, int hi_off) {
  s16x4 lo = __builtin_amdgcn_ds_read_tr16_b64_v4i16((LAS s16x4*)(p + off));
  s16x4 hi = __builtin_amdgcn_ds_read_tr16_b64_v4i16((LAS s16x4*)(p + off + hi_off));
  return __builtin_shufflevector(lo, hi, 0, 1, 2, 3, 4, 5, 6, 7);
}

constexpr int K1_MID = 16;
constexpr int k1_xkey(int i) { return i < 9 ? -100 + i : 4 * (1 + (i - 9) + (i - 9) / 2) + 2; }
constexpr int k1_dstep(int d, int n) { return d == 2 ? 2 + 4 * n : d == 3 ? 42 + 3 * n : d == 4 ? 58 + 6 * n : d == 5 ? 94 + 6 * n : 36 * (d - 3) + K1_MID + 6 * n; }
constexpr int k1_dkey(int d, int n) { return d < 3 ? -400 + 10 * d + n : 4 * k1_dstep(d, n) + 1; }
constexpr int k1_cstep(int i) { return i < 7 ? 218 + 4 * i : (i < 14 ? 254 + 4 * (i - 7) : (i < 21 ? 289 + (i - 14) : 304 + 2 * (i - 21))); }
constexpr int k1_sstep(int c) { return c == 0 ? 143 : (c < 4 ? 144 + 6 * c : (c == 4 ? 182 : 302 + 9 * (c - 5))); }
constexpr int k1_astep(int i) { return i < 14 ? k1_cstep(i) + 28 : (i < 21 ? k1_cstep(i) + 20 : 321 + (i - 21)); }
constexpr int K1_PEEK = 251;
constexpr int K1_TOUCH = 5 * 36 + K1_MID + 1;
constexpr int k1_younger(int okey, int wkey) {
  int n = 0;
  if (4 * K1_TOUCH + 1 > okey && 4 * K1_TOUCH + 1 < wkey) n += 1;
  if (4 * K1_PEEK + 1 > okey && 4 * K1_PEEK + 1 < wkey) n += 1;
  if (-70 > okey && -70 < wkey) n += 2;
  for (int i = 0; i < 36; ++i) if (k1_xkey(i) > okey && k1_xkey(i) < wkey) n += 1;
  for (int d = 0; d < 9; ++d) for (int pc = 0; pc < 5; ++pc) if (k1_dkey(d, pc) > okey && k1_dkey(d, pc) < wkey) n += 1;
  for (int i = 0; i < 24; ++i) if (4 * k1_cstep(i) + 2 > okey && 4 * k1_cstep(i) + 2 < wkey) n += 2;
  for (int c = 0; c < 8; ++c) if (4 * k1_sstep(c) + 3 > okey && 4 * k1_sstep(c) + 3 < wkey) n += 1;
  return n;
}

constexpr int K2_HALF = 16384;
constexpr int K2_SLOT = 2 * K2_HALF;
constexpr int K2_NSLOT = 4;
constexpr int K2_OWN_OFF = K2_NSLOT * K2_SLOT;
constexpr int K2_WT_OFF = K2_OWN_OFF + 18432;
constexpr int k2_g0(int st) { return st <= 9 ? 16 * st : 153; }
constexpr int k2_stage(int g) { return g < 144 ? g / 16 : (g < 153 ? 9 : 10); }
constexpr int k2_mid(int st) { return st < 9 ? 6 : 4; }
constexpr int k2_unit(int st) { return st < 5 ? 2 * st : 2 * (st - 5) + 1; }
constexpr int k2_np(int d) { return d < 9 ? 4 : 3; }
constexpr int k2_dkey(int d, int n) { return d < 2 ? -300 + 10 * d + n : (d == 2 ? 4 * (1 + n) + 2 : 4 * (k2_g0(d - 3) + k2_mid(d - 3) + 4 * n) + 2); }
constexpr int k2_hkey(int j, int m) { return j < 3 ? -250 + 10 * j + m : 4 * (k2_g0(j - 3) + k2_mid(j - 3) + 2 + 4 * m) + 3; }
constexpr int k2_skey(int p) { return 4 * (k2_g0(9 + p) + k2_mid(9 + p)) + 1; }
constexpr int K2_WST = 7;
constexpr int k2_wkey() { return 4 * (k2_g0(K2_WST) + k2_mid(K2_WST)) + 1; }
constexpr int K2_PEEKH = 25, K2_PEEKW = 16 * (K2_WST - 1) + 9;
constexpr int k2_younger(int okey, int wkey, bool stores) {
  int n = 0;
  if (4 * K2_PEEKH + 1 > okey && 4 * K2_PEEKH + 1 < wkey) n += 1;
  if (4 * K2_PEEKW + 1 > okey && 4 * K2_PEEKW + 1 < wkey) n += 2;
  if (k2_wkey() > okey && k2_wkey() < wkey) n += 2;
  if (-100 > okey && -100 < wkey) n += 1;
  for (int d = 0; d < 11; ++d) for (int pc = 0; pc < k2_np(d); ++pc) if (k2_dkey(d, pc) > okey && k2_dkey(d, pc) < wkey) n += 1;
  for (int j = 0; j < 9; ++j) for (int m = 0; m < 4; ++m) if (k2_hkey(j, m) > okey && k2_hkey(j, m) < wkey) n += 1;
  if (stores) for (int p = 0; p < 2; ++p) if (k2_skey(p) > okey && k2_skey(p) < wkey) n += 4;
  return n;
}
constexpr unsigned CODE_BYTES = 40 * 1024;
constexpr int cmax(int a, int b) { return a > b ? a : b; }

__global__ __launch_bounds__(512, 2) void k_fused(const u16* __restrict__ q, const u16* __restrict__ kk, const u16* __restrict__ vv,
                                                  const u16* __restrict__ shk, const u16* __restrict__ ssmk, const u16* __restrict__ ssmd,
                                                  const u16* __restrict__ wg, const u16* __restrict__ wu, const u16* __restrict__ wd,
                                                  char* ws, u16* __restrict__ out) {
  u16* xfrag = (u16*)(ws + WS_XFRAG);
  u64* stats = (u64*)(ws + WS_STATS);
  u16* kvr = (u16*)(ws + WS_KVR);
  u16* wT = (u16*)(ws + WS_WT);
  u16* hout = (u16*)(ws + WS_H);
  float* slab = (float*)(ws + WS_SLAB);
  u64* flg = (u64*)(ws + WS_FLG);
  extern __shared__ __attribute__((aligned(16))) char smem_g[];
  LAS char* smem = (LAS char*)smem_g;
  const unsigned lds0 = (unsigned)(uintptr_t)smem;
  const int bid = blockIdx.x, tid = threadIdx.x;
  const int w = __builtin_amdgcn_readfirstlane(tid >> 6), lane = tid & 63;
  const int xcd = bid & 7, idx = bid >> 3;
  const u64 genv = __hip_atomic_load(flg + FL_GEN, __ATOMIC_RELAXED, __HIP_MEMORY_SCOPE_AGENT);
  u64 tag;
  auto make_tag = [&]() ALWAYS_INLINE {
    tag = (((u64)(unsigned)__builtin_amdgcn_readfirstlane((unsigned)(genv >> 32)) << 32) | (unsigned)__builtin_amdgcn_readfirstlane((unsigned)genv)) + 1ull;
  };
  const int ksl = xcd >> 1, mt = (xcd & 1) * 8 + (idx >> 2), nt = idx & 3;
  const int tq = w & 3, khf = w >> 2;
  const int kbase = ksl * 1152 + khf * 576;
  const int colA2 = 128 * nt, colQ2 = 512 + 16 * nt;
  const int mtile = (xcd & 1) * 4 + (idx & 3);
  const int cgrp = (xcd >> 1) * 2 + (idx >> 4), cq = (idx >> 2) & 3;
  const int colA = 64 * (9 * cgrp + 2 * cq), colQ = 64 * (9 * cgrp + 8) + 16 * cq;

  const int grp = bid >> 2, qd = bid & 3;
  const int chA = grp * 9 + 2 * qd, chC = grp * 9 + 8;
  float ddA, ddC;
  {
    const int c1 = chA + (w >> 2);
    const unsigned d1 = ((const unsigned*)ssmd)[c1 >> 1], d2 = ((const unsigned*)ssmd)[chC >> 1];
    ddA = (c1 & 1) ? bf_hi(d1) : bf_lo(d1);
    ddC = (chC & 1) ? bf_hi(d2) : bf_lo(d2);
  }

  const int wq = w & 3, part = w >> 2;
  const char* wbase = (const char*)(part ? wu : wg);
  unsigned voff[5];
#pragma unroll
  for (int n = 0; n < 5; ++n) {
    const int o = (n < 4) ? ((wq * 4 + n) * 1024 + lane * 16) : (16384 + wq * 512 + (lane & 31) * 16);
    const int k = o >> 7, pc = (o >> 4) & 7;
    const int j = (pc >> 1) ^ (((k >> 1) & 1) | (part << 1));
    voff[n] = (unsigned)(k * (F_ * 2) + (2 * j + (pc & 1)) * 16);
  }
  const unsigned ldst = lds0 + part * K1_UOFF + wq * 4096;
  const unsigned ldst4 = lds0 + part * K1_UOFF + 16384 + wq * 512;

  auto piece_load = [&](int st, int n) ALWAYS_INLINE {
    const int slot = st % K1_NSLOT;
    const unsigned dst = (n < 4 ? ldst + n * 1024 : ldst4) + slot * K1_SLOT;
    if (st != 4) {
      const int ks = st < 4 ? st : st - 5;
      const char* g = wbase + (size_t)ks * 144 * (F_ * 2) + (colA + (st > 4 ? 64 : 0)) * 2;
      if (n < 4 || lane < 32) glds1p<false>(g, voff[n], dst);
    } else {
      int l2 = lane; asm volatile("" : "+v"(l2));
      const int o = (n < 4) ? ((wq * 4 + n) * 1024 + l2 * 16) : (16384 + wq * 512 + (l2 & 31) * 16);
      const unsigned vq = (unsigned)((o >> 5) * (F_ * 2) + ((o >> 4) & 1) * 16);
      if (n < 4 || lane < 32) glds1p<false>(wbase + colQ * 2, vq, dst);
    }
  };

  bf16x8 xf[36];
  const char* xb = (const char*)xfrag + (size_t)(mtile * 8 + w) * 36 * 1024;
  const unsigned xv = lane * 16;
  auto x_load = [&](auto ic) ALWAYS_INLINE {
    constexpr int i = ic;
    gload16_sc1<(i & 3) * 1024>(xf[i], xb + (i >> 2) * 4096, xv);
  };
  {
    LAS u16* tile = (LAS u16*)(smem + XR_OFF);
    constexpr int KT_OFF = XG_OFF + 9472, VT_OFF = KT_OFF + 9728, WT_OFF = VT_OFF + 9216;
    const int tt = bid >> 3, ct = bid & 7, t0 = tt * 64, c0 = ct * 72;
    const int tok0 = mtile * 256 + (cgrp * 4 + cq) * 8, token = tok0 + w;
    const bool act = lane < 48;
    u32x2 d[3];
    u32x4 kin, vin, ein;
#pragma unroll
    for (int i = 0; i < 3; ++i)
      asm volatile("global_load_dwordx2 %0, %1, %2 offset:%3" : "=v"(d[i]) : "v"((unsigned)(token * (D_ * 2) + (act ? lane : 0) * 8)), "s"(q), "i"(i * 384) : "memory");
    auto k_off = [&](int pc) ALWAYS_INLINE -> unsigned { const int r = pc / 9, cg = pc - r * 9, ts = t0 - 3 + r; return (unsigned)((ts < 0 ? 0 : ts) * (D_ * 2) + (c0 + 8 * cg) * 2); };
    auto v_off = [&](int pc) ALWAYS_INLINE -> unsigned { const int r = pc / 9, cg = pc - r * 9; return (unsigned)((t0 + r) * (D_ * 2) + (c0 + 8 * cg) * 2); };
    gload16u(kin, kk, k_off(tid));
    gload16u(vin, vv, v_off(tid));
    int edst;
    {
      const int kp = w == 0 ? 512 + lane : 576 + (lane < 27 ? lane : 0);
      const int wl = lane < 63 ? lane - 27 : 35;
      const bool isw = w == 1 && lane >= 27, isv = w >= 2;
      const char* src = isv ? (const char*)vv + v_off(512 + lane) : (isw ? (const char*)shk + c0 * 8 + wl * 16 : (const char*)kk + k_off(kp));
      edst = isv ? VT_OFF + (512 + lane) * 16 : (isw ? WT_OFF + wl * 16 : KT_OFF + kp * 16);
      asm volatile("global_load_dwordx4 %0, %1, off" : "=v"(ein) : "v"(src) : "memory");
    }
    {
      const int li = (w == 3 || w == 4) ? (w & 1) * 64 + lane : 0;
      const int ch = li < 32 ? chA : (li < 64 ? chA + 1 : chC);
      u64 pc; asm volatile("s_getpc_b64 %0" : "=s"(pc));
      const unsigned cl = (unsigned)((idx & 7) * 64 + lane) * 128u;
      const char* tb = w == 5 ? (const char*)(pc & ~127ull) : (const char*)ssmk;
      touch_lines(tb, w == 5 ? (cl < CODE_BYTES ? cl : 0u) : (unsigned)(ch * (S_ * 2) + (li & 31) * 128), lds0 + K1_PF_OFF + 16);
    }
    if (w >= 4) {
#pragma unroll
      for (int pt = 0; pt < 2; ++pt)
#pragma unroll
        for (int n = 0; n < 5; ++n) {
          const int o = (n < 4) ? ((wq * 4 + n) * 1024 + lane * 16) : (16384 + wq * 512 + (lane & 31) * 16);
          const int k = o >> 7, pc = (o >> 4) & 7;
          const int j = (pc >> 1) ^ (((k >> 1) & 1) | (pt << 1));
          const unsigned vo = (unsigned)(k * (F_ * 2) + (2 * j + (pc & 1)) * 16);
          const unsigned dst = lds0 + pt * K1_UOFF + (n < 4 ? wq * 4096 + n * 1024 : 16384 + wq * 512);
          const char* g = (const char*)(pt ? wu : wg) + colA * 2;
#pragma unroll
          for (int st = 0; st < 3; ++st)
            if (n < 4 || lane < 32) glds1p<false>(g + (size_t)st * 144 * (F_ * 2), vo, dst + st * K1_SLOT);
        }
      wait_vm<34>();
    } else wait_vm<4>();
    asm volatile("" : "+v"(d[0]), "+v"(d[1]), "+v"(d[2])::"memory");
    f32x2 xa[3], xb2[3];
#pragma unroll
    for (int i = 0; i < 3; ++i) { xa[i] = unpk(d[i].x); xb2[i] = unpk(d[i].y); }
    const f32x2 s2 = (xa[0] + xb2[0]) + (xa[1] + xb2[1]) + (xa[2] + xb2[2]);
    const float mean = wave_sum(act ? s2.x + s2.y : 0.f) * (1.0f / 576.0f);
    f32x2 v2 = {0.f, 0.f};
#pragma unroll
    for (int i = 0; i < 3; ++i) { const f32x2 a = xa[i] - mean, b = xb2[i] - mean; v2 += a * a + b * b; }
    const float vs = wave_sum(act ? v2.x + v2.y : 0.f);
    const float mb = bfr(mean), vb = bfr(vs * (1.0f / 576.0f));
    const float sd = bfr(sqrtf(bfr(vb + 1e-5f)));
    const float rsd = 1.0f / sd;
#pragma unroll
    for (int i = 0; i < 3; ++i) {
      const f32x2 a = bfr2(xa[i] - mb) * rsd, b = bfr2(xb2[i] - mb) * rsd;
      u32x2 o; o.x = pk_bf16(a.x, a.y); o.y = pk_bf16(b.x, b.y);
      const int p8 = lane + 48 * i;
      if (act) *(LAS u32x2*)(smem + XG_OFF + ((p8 >> 1) * 8 + w) * 16 + (p8 & 1) * 8) = o;
    }
    if (tid == 0) *(LAS unsigned*)(smem + HCNT_OFF + 4) = 0u;
    if (lane == 0) *(LAS u64*)(smem + XG_OFF + 9216 + w * 8) = (u64)__float_as_uint(mb) | ((u64)__float_as_uint(rsd) << 32);
    if (w >= 4) wait_vm<31>(); else wait_vm<1>();
    asm volatile("" : "+v"(kin), "+v"(vin), "+v"(ein)::"memory");
    if (t0 == 0 && tid < 27) kin = (u32x4){0u, 0u, 0u, 0u};
    *(LAS u32x4*)(smem + KT_OFF + tid * 16) = kin;
    *(LAS u32x4*)(smem + VT_OFF + tid * 16) = vin;
    if (w < 3) *(LAS u32x4*)(smem + edst) = ein;
    WAIT_L(0);
    __builtin_amdgcn_s_barrier();
    CFENCE();
    if (w < 4) {
      char* xo = (char*)xfrag + (size_t)((tok0 >> 5) * 36 * 64 + (tok0 & 31)) * 16;
#pragma unroll
      for (int it = 0; it < 3; ++it) {
        const int id = it * 256 + tid, p16 = id >> 3;
        if (it < 2 || w == 0) {
          const u32x4 v = *(LAS u32x4*)(smem + XG_OFF + id * 16);
          gstore16_sc1(xo + ((p16 >> 1) * 64 + (p16 & 1) * 32 + (id & 7)) * 16, v);
        }
      }
      if (tid < 8) __hip_atomic_store(stats + tok0 + tid, *(LAS u64*)(smem + XG_OFF + 9216 + tid * 8), __ATOMIC_RELAXED, __HIP_MEMORY_SCOPE_AGENT);
      make_tag();
      wait_vm<0>();
      if (lane == 0) {
        const unsigned old = __hip_atomic_fetch_add((LAS unsigned*)(smem + HCNT_OFF + 4), 1u, __ATOMIC_RELAXED, __HIP_MEMORY_SCOPE_WORKGROUP);
        if (old == 3u) set_flag(flg + FL_X + (tok0 >> 3), tag);
      }
    }
    auto kv_compute = [&](int item) ALWAYS_INLINE {
      const int tl = item / 9, cg = item - tl * 9;
      u32x4 kr[4], w4[4];
#pragma unroll
      for (int i = 0; i < 4; ++i) { kr[i] = *(LAS u32x4*)(smem + KT_OFF + (item + 9 * i) * 16); w4[i] = *(LAS u32x4*)(smem + WT_OFF + cg * 64 + i * 16); }
      const u32x4 vr = *(LAS u32x4*)(smem + VT_OFF + item * 16);
#pragma unroll
      for (int e2 = 0; e2 < 4; ++e2) {
        const f32x2 w0 = {bf_lo(w4[e2].x), bf_lo(w4[e2].z)}, w1 = {bf_hi(w4[e2].x), bf_hi(w4[e2].z)};
        const f32x2 w2 = {bf_lo(w4[e2].y), bf_lo(w4[e2].w)}, w3 = {bf_hi(w4[e2].y), bf_hi(w4[e2].w)};
        f32x2 ks = bfr2(unpk(kr[0][e2]) * w3);
        ks = bfr2(ks + bfr2(unpk(kr[1][e2]) * w2));
        ks = bfr2(ks + bfr2(unpk(kr[2][e2]) * w1));
        ks = bfr2(ks + bfr2(unpk(kr[3][e2]) * w0));
        const f32x2 kv = ks * unpk(vr[e2]);
        const unsigned pk = pk_bf16(kv.x, kv.y);
        tile[(8 * cg + 2 * e2) * 72 + (63 - tl)] = (u16)(pk & 0xffffu);
        tile[(8 * cg + 2 * e2 + 1) * 72 + (63 - tl)] = (u16)(pk >> 16);
      }
    };
    kv_compute(tid);
    if (w == 7) kv_compute(512 + lane);
    if (w >= 4) make_tag();
    poll_flags<1>(flg + FL_X + mtile * 32 + w * 4, 4, tag, lane);
    wait_vm<0>();
    sfor<9>([&](auto ic) ALWAYS_INLINE { x_load(ic); });
    WAIT_L(0);
    __builtin_amdgcn_s_barrier();
    CFENCE();
    {
      const int ch = tid >> 3, pp = tid & 7, ch2 = 64 + (lane >> 3);
      const u32x4 o1 = *(LAS u32x4*)(tile + ch * 72 + pp * 8), o2 = *(LAS u32x4*)(tile + ch2 * 72 + pp * 8);
      gstore16_sc1(kvr + (size_t)(c0 + ch) * S_ + (1984 - t0) + pp * 8, o1);
      gstore16_sc1(kvr + (size_t)(c0 + ch2) * S_ + (1984 - t0) + pp * 8, o2);
    }
  }

  const int kh = lane >> 5, prt = (lane >> 4) & 1, qq = (lane & 15) >> 2, p4 = lane & 3;
  const unsigned rbU = lds0 + (8 * kh + qq) * 128 + prt * K1_UOFF + ((((qq >> 1) & 1) | (prt << 1)) << 5) + p4 * 8;

  f32x16 acc[4];
  const f32x16 zero16 = {0.f, 0.f, 0.f, 0.f, 0.f, 0.f, 0.f, 0.f, 0.f, 0.f, 0.f, 0.f, 0.f, 0.f, 0.f, 0.f};

  const int token = mtile * 256 + w * 32 + (lane & 31);
  char* hb = (char*)hout + ((size_t)(token >> 4) * 144 * 64 + (lane >> 5) * 16 + (token & 15)) * 16;
  auto h_word = [&](const f32x16& a, int i) ALWAYS_INLINE -> unsigned {
    return pk_bf16(bfr(a[2 * i]) * bfr(a[2 * i + 8]), bfr(a[2 * i + 1]) * bfr(a[2 * i + 9]));
  };
  auto h_store = [&](const u32x4& o, int c16) ALWAYS_INLINE {
    gstore16_sc1(hb + ((size_t)(c16 >> 1) * 64 + (c16 & 1) * 32) * 16, o);
  };
  u32x4 ho;

  const char* hpA = (const char*)(ssmk + (size_t)(chA + (w >> 2)) * S_ + 64 * (w & 3));
  const char* hpC = (const char*)(ssmk + (size_t)chC * S_ + 32 * w);
  const bool zA = (w & 3) == 0, zC = w == 0;
  const char* pf_sk = (const char*)ssmk;
  const char* pf_wd = (const char*)wd + (size_t)(xcd >> 1) * 1152 * 1152;
  const char* kvb = (const char*)kvr;
  u32x4 caf[24];
  unsigned cax[24];
  bf16x8 cf[24];
  int cvo = 0, cvx = 0;
  u64 pk1;
  auto conv_align = [&](auto ic) ALWAYS_INLINE {
    constexpr int i = ic;
    const unsigned sh = ((lane + 1) & 1) * 2;
    u32x4 r;
    r.x = __builtin_amdgcn_alignbyte(caf[i].y, caf[i].x, sh);
    r.y = __builtin_amdgcn_alignbyte(caf[i].z, caf[i].y, sh);
    r.z = __builtin_amdgcn_alignbyte(caf[i].w, caf[i].z, sh);
    r.w = __builtin_amdgcn_alignbyte(cax[i], caf[i].w, sh);
    cf[i] = __builtin_bit_cast(bf16x8, r);
    asm volatile("" : "+v"(cf[i]));
  };


  constexpr int PD = 3, MID = K1_MID;
  bf16x8 af[PD + 1];
  unsigned rb[4];
#pragma unroll
  for (int j = 0; j < 4; ++j) rb[j] = rbU ^ (j << 5);
#pragma unroll
  for (int g = 0; g < PD; ++g) af[g] = tr_frag((LAS char*)(uintptr_t)rb[g], 0, 512);
  sfor<324>([&](auto gc) ALWAYS_INLINE {
    constexpr int g = gc;
    constexpr int st = g / 36, f = g - st * 36;
    constexpr int s = st == 4 ? f : (st == 8 ? f % 9 : f / 4), c = st == 4 ? 0 : (st == 8 ? f / 9 : f % 4);
    constexpr int xi = st < 4 ? st * 9 + s : (st == 4 ? s : (st - 5) * 9 + s);
    constexpr int ai = st < 4 ? c : (st == 4 ? 0 : (c + 1) & 3);
    constexpr bool first = (st == 0 || st == 5) ? s == 0 : (st == 4 && f == 0);
    if constexpr (f == MID) {

      if constexpr (st < 8) wait_vm<k1_younger(k1_dkey(st + 1, 4), 4 * g)>();
      else { wait_vm<k1_younger(4 * (7 * 36 + MID + 1) + 1, 4 * g)>(); if (w <= 3) WAIT_L(0); }
      __builtin_amdgcn_s_barrier();
      CFENCE();

      if constexpr (st == 2) { if (tid == 0) set_flag(flg + FL_K + (bid & 7) * 32 + (bid >> 3), tag); }
      if constexpr (st == 6) {
        static_assert(k1_dkey(7, 4) > 4 * k1_sstep(4) + 3, "the stage 7 wait must cover the h stores of panels A and Q");
        if (tid == 0) set_flag(flg + FL_HE + (mtile * 4 + (cgrp >> 1)) * 8 + (cgrp & 1) * 4 + cq, tag);
      }
    }
    if constexpr (((f + 36 - MID) % 36) % 9 == 0) __builtin_amdgcn_s_setprio(3 - ((f + 36 - MID) % 36) / 9);
    sfor<35>([&](auto dc) ALWAYS_INLINE { if constexpr (k1_dkey(dc / 5 + 2, dc % 5) == 4 * g + 1) piece_load(dc / 5 + 2, dc % 5); });
    sfor<27>([&](auto ic) ALWAYS_INLINE { if constexpr (k1_xkey(ic + 9) == 4 * g + 2) x_load(std::integral_constant<int, ic + 9>{}); });
    if constexpr (g == K1_PEEK) { int l2 = lane; asm volatile("" : "+v"(l2)); peek_flag(pk1, flg + FL_K + (grp >> 3) * 32, l2 & 31); }
    if constexpr (g == 7 * 36 + MID + 1) {
      asm volatile("s_waitcnt vmcnt(%1)" : "+v"(pk1) : "i"(k1_younger(4 * K1_PEEK + 1, 4 * g + 1)) : "memory");
      if (w < 3) {
        const int ch = (w == 2) ? chC : chA + w;
        const unsigned lv = lane * 16;
        if (!__all(pk1 == tag)) poll_flags<1>(flg + FL_K + (grp >> 3) * 32, 32, tag, lane);
#pragma unroll
        for (int i = 0; i < 4; ++i) glds1_sc1(kvb + (size_t)ch * (S_ * 2) + i * 1024, lv, lds0 + XR_OFF + w * XR_STRIDE + i * 1024);
        if (lane < 4) glds1(pf_sk + (size_t)ch * (S_ * 2), lv, lds0 + HZ_OFF + w * 96 + 32);
      } else if (w == 3) {
        if (lane < 32) {
#pragma unroll
          for (int im = 0; im < 3; ++im) *(LAS u32x4*)(smem + XR_OFF + im * XR_STRIDE + 4096 + lane * 16) = (u32x4){0u, 0u, 0u, 0u};
        } else if (lane < 38) {
          const int im = (lane - 32) >> 1, hh = lane & 1;
          *(LAS u32x4*)(smem + HZ_OFF + im * 96 + hh * 16) = (u32x4){0u, 0u, 0u, 0u};
        } else if (lane == 38) *(LAS unsigned*)(smem + HCNT_OFF) = 0u;
      }
    }
    sfor<24>([&](auto ac) ALWAYS_INLINE {
      if constexpr (k1_astep(ac) == g) {
        asm volatile("s_waitcnt vmcnt(%2)" : "+v"(caf[ac]), "+v"(cax[ac]) : "i"(k1_younger(4 * k1_cstep(ac) + 2, 4 * g + 2)) : "memory");
        conv_align(ac);
      }
    });
    if constexpr (g == K1_TOUCH) {
      int l2 = lane; asm volatile("" : "+v"(l2));
      const unsigned line = (unsigned)(w * 64 + l2 < 324 ? idx * 324 + w * 64 + l2 : idx * 324);
      touch_lines(pf_wd, line * 128u, lds0 + K1_PF_OFF + 16);
    }
    sfor<24>([&](auto cc) ALWAYS_INLINE {
      constexpr int ci = cc;
      if constexpr (k1_cstep(ci) == g) {
        if constexpr (ci == 0) {
          int l2 = lane; asm volatile("" : "+v"(l2));
          const int e = (l2 & 15) - 15 + 8 * (l2 >> 4);
          cvo = (e & ~1) * 2; cvx = cvo + ((e & 1) ? 16 : 0);
        }
        constexpr int cb = ci < 16 ? 64 * (ci >> 3) + 512 * (ci & 7) : 512 * (ci - 16);
        const char* hp = (ci < 16 ? hpA : hpC) + cb + ((ci == 0 && zA) || (ci == 16 && zC) ? 32 : 0);
        gload16u(caf[ci], hp - 32, (unsigned)(cvo + 32));
        gload4u(cax[ci], hp - 32, (unsigned)(cvx + 32));
      }
    });
    if constexpr (g + PD < 324) {
      constexpr int g1 = g + PD, st1 = g1 / 36, f1 = g1 - st1 * 36;
      if constexpr (f1 == 0) {
        if constexpr (st1 != 4) {
          unsigned b2 = rbU; asm volatile("" : "+v"(b2));
#pragma unroll
          for (int j = 0; j < 4; ++j) { rb[j] = (b2 + (st1 % K1_NSLOT) * K1_SLOT) ^ (j << 5); asm volatile("" : "+v"(rb[j])); }
        } else {
          int l2 = lane; asm volatile("" : "+v"(l2));
          rb[0] = lds0 + (8 * (l2 >> 5) + ((l2 & 15) >> 2)) * 32 + ((l2 >> 4) & 1) * K1_UOFF + (l2 & 3) * 8 + (st1 % K1_NSLOT) * K1_SLOT;
          asm volatile("" : "+v"(rb[0]));
        }
      }
      if constexpr (st1 == 8) af[g1 % (PD + 1)] = tr_frag((LAS char*)(uintptr_t)rb[f1 / 9], (f1 % 9) * 2048, 512);
      else if constexpr (st1 != 4) af[g1 % (PD + 1)] = tr_frag((LAS char*)(uintptr_t)rb[f1 % 4], (f1 / 4) * 2048, 512);
      else af[g1 % (PD + 1)] = tr_frag((LAS char*)(uintptr_t)rb[0], f1 * 512, 128);
    }
    if constexpr (st < 4 && c == 0) {
      asm volatile("s_waitcnt vmcnt(%1)" : "+v"(xf[xi]) : "i"(k1_younger(k1_xkey(xi), 4 * g + 3)) : "memory");
    }
    SCHED0();
    acc[ai] = __builtin_amdgcn_mfma_f32_32x32x16_bf16(af[g % (PD + 1)], xf[xi], first ? zero16 : acc[ai], 0, 0, 0);
    SCHED0();
    if constexpr (g == 143) {
#pragma unroll
      for (int i = 0; i < 4; ++i) ho[i] = h_word(acc[0], i);
      h_store(ho, colA >> 4);
      SCHED0();
    }
    if constexpr (g > 144 && g <= 162) {
      constexpr int cc = (g - 145) / 6 + 1, pi = (g - 145) % 6;
      if constexpr (pi < 4) ho[pi] = h_word(acc[cc], pi);
      if constexpr (pi == 5) h_store(ho, (colA >> 4) + cc);
      SCHED0();
    }
    if constexpr (g >= 298 && (g - 298) % 9 < 5 && (g - 298) / 9 < 3) {
      constexpr int cc = (g - 298) / 9, pi = (g - 298) % 9;
      if constexpr (pi < 4) ho[pi] = h_word(acc[(cc + 1) & 3], pi);
      if constexpr (pi == 4) h_store(ho, (colA >> 4) + 4 + cc);
      SCHED0();
    }
    if constexpr (g >= 180 && g <= 182) {
      if constexpr (g == 180) { ho[0] = h_word(acc[0], 0); ho[1] = h_word(acc[0], 1); }
      if constexpr (g == 181) { ho[2] = h_word(acc[0], 2); ho[3] = h_word(acc[0], 3); }
      if constexpr (g == 182) h_store(ho, colQ >> 4);
      SCHED0();
    }
  });
  const char* wbase2 = (const char*)wd + (size_t)kbase * (D_ * 2);
  unsigned voff2[4];
  int lane2 = lane; asm volatile("" : "+v"(lane2));
#pragma unroll
  for (int n = 0; n < 4; ++n) {
    const int o = (tq * 4 + n) * 1024 + lane2 * 16;
    const int k = o >> 8, pc = (o >> 4) & 15;
    const int j = (pc >> 1) ^ (k & 7);
    voff2[n] = (unsigned)(k * (D_ * 2) + (2 * j + (pc & 1)) * 16);
  }
  const unsigned ldst2 = lds0 + khf * K2_HALF;
  auto piece_load2 = [&](int st, int n) ALWAYS_INLINE {
    const int slot = st % K2_NSLOT;
    if (st < 9) {
      const char* g = wbase2 + (size_t)k2_unit(st) * 64 * (D_ * 2) + colA2 * 2;
      glds1p<false>(g, voff2[n], ldst2 + slot * K2_SLOT + tq * 4096 + n * 1024);
    } else {
      int l2 = lane; asm volatile("" : "+v"(l2));
      const char* g = wbase2 + (size_t)(st - 9) * 288 * (D_ * 2) + colQ2 * 2;
      const int o = n < 2 ? tq * 2048 + n * 1024 + l2 * 16 : 8192 + tq * 256 + (l2 & 15) * 16;
      const unsigned v0 = (unsigned)((o >> 5) * (D_ * 2) + ((o >> 4) & 1) * 16);
      if (n < 2 || l2 < 16) glds1p<false>(g, v0, ldst2 + slot * K2_SLOT + (n < 2 ? tq * 2048 + n * 1024 : 8192 + tq * 256));
    }
  };
  const char* hbase = (const char*)hout + ((size_t)(mt * 8 + tq * 2) * 144 * 64 + (size_t)(kbase >> 5) * 64) * 16;
  const unsigned hv0 = lane2 * 16 + khf * (144 * 64 * 16), hv1 = lane2 * 16 + (1 - khf) * (144 * 64 * 16);
  bf16x8 hf[18][2];
  auto h_load = [&](auto lc) ALWAYS_INLINE {
    constexpr int k = 2 * k2_unit(lc / 4) + (lc / 2) % 2, t = lc % 2;
    gload16_sc1<(k & 3) * 1024>(hf[k][t], hbase + (k >> 2) * 4096, t ? hv1 : hv0);
  };
  __builtin_amdgcn_s_setprio(0);
  {
    u32x4 o;
#pragma unroll
    for (int i = 0; i < 4; ++i) o[i] = h_word(acc[0], i);
    h_store(o, (colA >> 4) + 7);
  }

  __builtin_amdgcn_s_barrier();
  CFENCE();
#pragma unroll
  for (int n = 0; n < 4; ++n) piece_load2(0, n);
#pragma unroll
  for (int n = 0; n < 4; ++n) piece_load2(1, n);

  const int ct0 = lane & 15, ckg = lane >> 4;
  {
    const int boff = (2032 - 16 * (lane & 15) + 8 * ckg) * 2;
    auto head_frag = [&](int im) ALWAYS_INLINE -> bf16x8 {
      bf16x8 r;
      LAS u16* hz = (LAS u16*)(smem + HZ_OFF + im * 96) + 16 + (ct0 - 15 + 8 * ckg);
#pragma unroll
      for (int j = 0; j < 8; ++j) r[j] = (short)hz[j];
      return r;
    };
    if (zA) cf[0] = head_frag(w >> 2);
    if (zC) cf[16] = head_frag(2);
    {
      const int im = w >> 2;
      LAS char* xr = smem + XR_OFF + im * XR_STRIDE + boff + 128 * (w & 3);
      f32x4 ac[8];
#pragma unroll
      for (int j = 0; j < 8; ++j) ac[j] = (f32x4){0.f, 0.f, 0.f, 0.f};
#pragma unroll
      for (int rr = 1; rr >= 0; --rr) {
        bf16x8 bfm[8];
#pragma unroll
        for (int b = 0; b < 8; ++b) bfm[b] = *(LAS bf16x8*)(xr - 512 * b + 64 * rr);
#pragma unroll
        for (int a = 7; a >= 0; --a)
#pragma unroll
          for (int j = a; j < 8; ++j) ac[j] = __builtin_amdgcn_mfma_f32_16x16x32_bf16(cf[rr * 8 + a], bfm[j - a], ac[j], 0, 0, 0);
        if (rr == 1) {
          SCHED0();
          wait_vm<8>();
          if (lane == 0) {
            const unsigned old = __hip_atomic_fetch_add((LAS unsigned*)(smem + HCNT_OFF), 1u, __ATOMIC_RELAXED, __HIP_MEMORY_SCOPE_WORKGROUP);
            if (old == 7u) set_flag(flg + FL_H + (mtile * 4 + (cgrp >> 1)) * 8 + (cgrp & 1) * 4 + cq, tag);
          }
          SCHED0();
        }
      }
#pragma unroll
      for (int j = 0; j < 8; ++j)
        *(LAS f32x4*)(smem + PART_OFF + w * 8192 + (j * 64 + lane) * 16) = ac[j];
    }
    poll_flags<1>(flg + FL_HE + ((mt >> 1) * 4 + ksl) * 8, 8, tag, lane);
    sfor<12>([&](auto kc) ALWAYS_INLINE { h_load(kc); });
    {
      LAS char* xr = smem + XR_OFF + 2 * XR_STRIDE + boff + 64 * w;
      const int jA = 7 - qd, jB = qd;
      bf16x8 b0[8], b1[4];
#pragma unroll
      for (int a = 0; a < 8; ++a) { const int bb = jA - a; b0[a] = *(LAS bf16x8*)(xr - 512 * (bb < 0 ? 0 : bb)); }
#pragma unroll
      for (int a = 0; a < 4; ++a) { const int bb = jB - a; b1[a] = *(LAS bf16x8*)(xr - 512 * (bb < 0 ? 0 : bb)); }
      f32x4 c0 = (f32x4){0.f, 0.f, 0.f, 0.f}, c1 = c0;
#pragma unroll
      for (int a = 7; a >= 0; --a) {
        if (a <= jA) c0 = __builtin_amdgcn_mfma_f32_16x16x32_bf16(cf[16 + a], b0[a], c0, 0, 0, 0);
        if (a < 4 && a <= jB) c1 = __builtin_amdgcn_mfma_f32_16x16x32_bf16(cf[16 + a], b1[a < 4 ? a : 0], c1, 0, 0, 0);
      }
      *(LAS f32x4*)(smem + PARTC_OFF + w * 2048 + lane * 16) = c0;
      *(LAS f32x4*)(smem + PARTC_OFF + w * 2048 + 1024 + lane * 16) = c1;
    }
  }
  WAIT_L(0);
  __builtin_amdgcn_s_barrier();
  CFENCE();
  auto finalize = [&](f32x4 s0, f32x4 s1, int im, int ch, int t8, float dd) ALWAYS_INLINE {
    const u32x4 kvv = *(LAS u32x4*)(smem + XR_OFF + im * XR_STRIDE + (2040 - t8) * 2);
    float y[8] = {s0[0], s0[1], s0[2], s0[3], s1[0], s1[1], s1[2], s1[3]};
    float o[8];
#pragma unroll
    for (int e = 0; e < 8; ++e) {
      const int ri = 7 - e;
      const float kvf = (ri & 1) ? bf_hi(kvv[ri >> 1]) : bf_lo(kvv[ri >> 1]);
      o[e] = bfr(y[e]) + bfr(kvf * dd);
    }
    u32x4 ov; ov.x = pk_bf16(o[0], o[1]); ov.y = pk_bf16(o[2], o[3]); ov.z = pk_bf16(o[4], o[5]); ov.w = pk_bf16(o[6], o[7]);
    gstore16_sc1(wT + (size_t)ch * S_ + t8, ov);
  };
  {
    const int im = tid >> 8, t8 = (tid & 255) * 8;
    const int po = ((t8 >> 8) * 64 + ((t8 >> 2) & 3) * 16 + ((t8 >> 4) & 15)) * 16;
    f32x4 s0 = (f32x4){0.f, 0.f, 0.f, 0.f}, s1 = s0;
#pragma unroll
    for (int ww = 0; ww < 4; ++ww) {
      s0 += *(LAS f32x4*)(smem + PART_OFF + (im * 4 + ww) * 8192 + po);
      s1 += *(LAS f32x4*)(smem + PART_OFF + (im * 4 + ww) * 8192 + po + 256);
    }
    finalize(s0, s1, im, chA + im, t8, ddA);
  }
  if (tid >= 448) {
    const int t2 = tid - 448;
    const int tl = t2 >> 5, l8 = (t2 & 31) * 8;
    const int jt = tl ? qd : 7 - qd, t8 = 256 * jt + l8;
    const int po = (((l8 >> 2) & 3) * 16 + (l8 >> 4)) * 16;
    f32x4 s0 = (f32x4){0.f, 0.f, 0.f, 0.f}, s1 = s0;
#pragma unroll
    for (int ww = 0; ww < 8; ++ww) {
      s0 += *(LAS f32x4*)(smem + PARTC_OFF + ww * 2048 + tl * 1024 + po);
      s1 += *(LAS f32x4*)(smem + PARTC_OFF + ww * 2048 + tl * 1024 + po + 256);
    }
    finalize(s0, s1, 2, chC, t8, ddC);
  }

  {
  const int kq = lane2 >> 4, qq = (lane2 & 15) >> 2, p4 = lane2 & 3;
  const unsigned rbU2 = lds0 + khf * K2_HALF + (16 * (kq >> 1) + 4 * (kq & 1) + qq) * 256 + ((4 * (kq & 1) + qq) << 5) + p4 * 8;

  const char* wtb = (const char*)(wT + mt * 128 + ksl * 32);
  u64 pk2, pk3;
  f32x4 acc[2][8], accq[2];
  const f32x4 zero4 = {0.f, 0.f, 0.f, 0.f};

  const int tile = mt * 4 + nt;
  const __amdgpu_buffer_rsrc_t rs = __builtin_amdgcn_make_buffer_rsrc((char*)slab + (size_t)tile * (16 * 18432), 0, 16 * 18432, 0x00020000);
  const bool foreign = tq != ksl;
  auto give = [&](int xb, int i, const f32x4& a0, const f32x4& a1) ALWAYS_INLINE {
    *(LAS f32x4*)(smem + xb + ((w * 4 + i) * 64 + lane) * 16) = a1;
  };
  auto publish = [&](int xb, int i, const f32x4& a0, const f32x4& a1, int dtg) ALWAYS_INLINE {
    const f32x4 sm = a0 + *(LAS f32x4*)(smem + xb + (((w ^ 4) * 4 + i) * 64 + lane) * 16);
    if (foreign) __builtin_amdgcn_raw_buffer_store_b128(__builtin_bit_cast(u32x4, sm), rs, (unsigned)(((tq * 4 + ksl) * 18 + khf * 9 + dtg) * 1024 + lane * 16), 0, 16);
    else *(LAS f32x4*)(smem + K2_OWN_OFF + ((khf * 9 + dtg) * 64 + lane) * 16) = sm;
  };
  constexpr int XB1 = 3 * K2_SLOT, XB2 = 0, XB3 = 3 * K2_SLOT;

  asm volatile("s_waitcnt vmcnt(%4)" : "+v"(hf[0][0]), "+v"(hf[0][1]), "+v"(hf[1][0]), "+v"(hf[1][1])
               : "i"(k2_younger(k2_hkey(0, 3), 0, false)) : "memory");
  WAIT_L(0);
  __builtin_amdgcn_s_barrier();
  CFENCE();

  constexpr int PD = 2;
  bf16x8 af[PD + 1];
  unsigned sb = rbU2;
#pragma unroll
  for (int g = 0; g < PD; ++g) af[g] = tr_frag((LAS char*)(uintptr_t)(sb ^ (g << 5)), 0, 2048);
  sfor<162>([&](auto gc) ALWAYS_INLINE {
    constexpr int g = gc;
    constexpr int st = k2_stage(g), f = g - k2_g0(st);
    constexpr int ks = st < 9 ? k2_unit(st) * 2 + f / 8 : (st - 9) * 9 + f, dt = st < 9 ? f % 8 : 0;
    if constexpr (f == k2_mid(st)) {
      if constexpr (st == 2) {
        asm volatile("s_waitcnt vmcnt(%1)" : "+v"(pk2) : "i"(k2_younger(4 * K2_PEEKH + 1, 4 * g, false)) : "memory");
        if (!__all(pk2 == tag)) poll_flags<1>(flg + FL_H + ((mt >> 1) * 4 + ksl) * 8, 8, tag, lane);
      }
      if constexpr (st == K2_WST) {
        asm volatile("s_waitcnt vmcnt(%2)" : "+v"(pk2), "+v"(pk3) : "i"(k2_younger(4 * K2_PEEKW + 1, 4 * g, false)) : "memory");
        if (!__all((pk2 == tag) & (pk3 == tag))) poll_flags<4>(flg + FL_W, 64, tag, lane);
      }
      if constexpr (st < 10) {
        constexpr int okey = cmax(k2_dkey(st + 1, k2_np(st + 1) - 1), st + 1 < 9 ? k2_hkey(st + 1, 3) : -1000);
        constexpr int n0 = k2_younger(okey, 4 * g, false);
        if constexpr (st == 0) { if (w == 7) wait_vm<n0 + 1>(); else wait_vm<n0>(); }
        else wait_vm<n0>();
        if constexpr (st + 1 < 9) {
          constexpr int k0 = 2 * k2_unit(st + 1);
          asm volatile("" : "+v"(hf[k0][0]), "+v"(hf[k0][1]), "+v"(hf[k0 + 1][0]), "+v"(hf[k0 + 1][1])::"memory");
        }
      }
      if constexpr (st >= 9) WAIT_L(0);
      __builtin_amdgcn_s_barrier();
      CFENCE();
      if constexpr (st == 1) { if (tid == 0) set_flag(flg + FL_W + bid, tag); }
      if constexpr (st == K2_WST) {
        int l2 = lane; asm volatile("" : "+v"(l2));
        glds1_sc1(wtb, (unsigned)((colA2 + w * 16 + (l2 >> 2)) * (S_ * 2) + (l2 & 3) * 16), lds0 + K2_WT_OFF + w * 1024);
        glds1_sc1(wtb, (unsigned)((colQ2 + (l2 >> 2)) * (S_ * 2) + (l2 & 3) * 16), lds0 + K2_WT_OFF + 8192);
      }
      if constexpr (st == 9) {
#pragma unroll
        for (int i = 0; i < 4; ++i) publish(XB1, i, acc[0][i], acc[1][i], i);
#pragma unroll
        for (int i = 0; i < 4; ++i) give(XB2, i, acc[0][4 + i], acc[1][4 + i]);
      }
      if constexpr (st == 10) {
#pragma unroll
        for (int i = 0; i < 4; ++i) publish(XB2, i, acc[0][4 + i], acc[1][4 + i], 4 + i);
      }
    }
    if constexpr (g == K2_PEEKH) { int l2 = lane; asm volatile("" : "+v"(l2)); peek_flag(pk2, flg + FL_H + ((mt >> 1) * 4 + ksl) * 8, l2 & 7); }
    if constexpr (g == K2_PEEKW) {
      int l2 = lane; asm volatile("" : "+v"(l2));
      const int b0 = 4 * (colA2 / 9) + l2, b1 = 4 * (colQ2 / 9) + (l2 < 12 ? l2 : 0);
      peek_flag(pk2, flg + FL_W, b0 < 256 ? b0 : 255);
      peek_flag(pk3, flg + FL_W, b1 < 256 ? b1 : 255);
    }
    if constexpr (st < 9 && ((f + 16 - 6) % 16) % 4 == 0) __builtin_amdgcn_s_setprio(3 - ((f + 16 - 6) % 16) / 4);
    sfor<36>([&](auto dc) ALWAYS_INLINE { if constexpr (dc % 4 < k2_np(dc / 4 + 2) && k2_dkey(dc / 4 + 2, dc % 4) == 4 * g + 2) piece_load2(dc / 4 + 2, dc % 4); });
    sfor<24>([&](auto hc) ALWAYS_INLINE { if constexpr (k2_hkey(hc / 4 + 3, hc % 4) == 4 * g + 3) h_load(std::integral_constant<int, hc + 12>{}); });
    if constexpr (g + PD < 162) {
      constexpr int g1 = g + PD, st1 = k2_stage(g1), f1 = g1 - k2_g0(st1);
      if constexpr (f1 == 0) {
        if constexpr (st1 < 9) {
          sb += (st1 % K2_NSLOT == 0) ? -3 * K2_SLOT : K2_SLOT;
        } else {
          int l2 = lane; asm volatile("" : "+v"(l2));
          const int kq2 = l2 >> 4;
          sb = lds0 + khf * K2_HALF + (16 * (kq2 >> 1) + 4 * (kq2 & 1) + ((l2 & 15) >> 2)) * 32 + (l2 & 3) * 8 + (st1 % K2_NSLOT) * K2_SLOT;
        }
      }
      asm volatile("" : "+v"(sb));
      if constexpr (st1 < 9) af[g1 % (PD + 1)] = tr_frag((LAS char*)(uintptr_t)(sb ^ ((f1 % 8) << 5)), (f1 / 8) * 8192, 2048);
      else af[g1 % (PD + 1)] = tr_frag((LAS char*)(uintptr_t)sb, f1 * 1024, 256);
    }
    SCHED0();
#pragma unroll
    for (int t = 0; t < 2; ++t) {
      if constexpr (st < 9) acc[t][dt] = __builtin_amdgcn_mfma_f32_16x16x32_bf16(af[g % (PD + 1)], hf[ks][t], g < 8 ? zero4 : acc[t][dt], 0, 0, 0);
      else accq[t] = __builtin_amdgcn_mfma_f32_16x16x32_bf16(af[g % (PD + 1)], hf[ks][t], g == 144 ? zero4 : accq[t], 0, 0, 0);
    }
    SCHED0();
    if constexpr (g == 143) {
#pragma unroll
      for (int i = 0; i < 4; ++i) give(XB1, i, acc[0][i], acc[1][i]);
      SCHED0();
    }
  });
  __builtin_amdgcn_s_setprio(0);
  give(XB3, 0, accq[0], accq[1]);

  int lane3 = lane; asm volatile("" : "+v"(lane3));
  u32x2 xq[3];
  float2 stt[2];
#pragma unroll
  for (int i = 0; i < 3; ++i) {
    const int t = i < 2 ? i : (w & 1);
    const int token = mt * 128 + ksl * 32 + t * 16 + (lane3 & 15);
    xq[i] = *(const u32x2*)(q + (size_t)token * D_ + (i < 2 ? colA2 + w * 16 : colQ2) + 4 * (lane3 >> 4));
    if (i < 2) {
      const u64 sv = __hip_atomic_load(stats + token, __ATOMIC_RELAXED, __HIP_MEMORY_SCOPE_AGENT);
      stt[i] = make_float2(__uint_as_float((unsigned)sv), __uint_as_float((unsigned)(sv >> 32)));
    }
  }
  WAIT_L(0);
  __builtin_amdgcn_s_barrier();
  CFENCE();
  publish(XB3, 0, accq[0], accq[1], 8);
  asm volatile("s_waitcnt vmcnt(0) lgkmcnt(0)" ::: "memory");
  __builtin_amdgcn_s_barrier();
  CFENCE();
  if (tid == 0) set_flag(flg + FL_S + tile * 4 + ksl, tag);
  poll_flags<1>(flg + FL_S + tile * 4, 4, tag, lane3);
  auto fetch = [&](f32x4 (&pv)[3], int fi) ALWAYS_INLINE {
#pragma unroll
    for (int k = 1; k < 4; ++k)
      pv[k - 1] = __builtin_bit_cast(f32x4, __builtin_amdgcn_raw_buffer_load_b128(rs, (unsigned)(((ksl * 4 + ((ksl + k) & 3)) * 18 + fi) * 1024 + lane3 * 16), 0, 16));
  };
  auto combine = [&](const f32x4 (&pv)[3], int t, int dt, const u32x2& xw2, const float2& st2) ALWAYS_INLINE {
    const int tl = t * 16 + (lane3 & 15), dl = dt * 16 + 4 * (lane3 >> 4);
    const f32x4 own = *(LAS f32x4*)(smem + K2_OWN_OFF + ((t * 9 + dt) * 64 + lane3) * 16);
    const f32x4 bs = ((own + pv[0]) + pv[1]) + pv[2];
    float o[4];
#pragma unroll
    for (int c = 0; c < 4; ++c) {
      const unsigned xw = (c < 2) ? xw2.x : xw2.y;
      const float x = (c & 1) ? bf_hi(xw) : bf_lo(xw);
      const float wy = bf2f(*(LAS u16*)(smem + K2_WT_OFF + (dl + c) * 64 + tl * 2));
      const float xn = bfr(bfr(x - st2.x) * st2.y);
      const float a = bfr(xn * wy);
      o[c] = bfr(x + a) + bfr(bs[c]);
    }
    u32x2 ov; ov.x = pk_bf16(o[0], o[1]); ov.y = pk_bf16(o[2], o[3]);
    *(u32x2*)(out + (size_t)(mt * 128 + ksl * 32 + tl) * D_ + (dt < 8 ? colA2 + dt * 16 : colQ2) + 4 * (lane3 >> 4)) = ov;
  };
  {
    f32x4 p0[3], p1[3], p2[3];
    fetch(p0, w); fetch(p1, 9 + w); fetch(p2, 9 * (w & 1) + 8);
    combine(p0, 0, w, xq[0], stt[0]);
    combine(p1, 1, w, xq[1], stt[1]);
    if (w < 2) combine(p2, w, 8, xq[2], w ? stt[1] : stt[0]);
  }
  if (bid == 0 && w == 0) {
    poll_flags<4>(flg + FL_K, 64, tag, lane);
    if (lane == 0) set_flag(flg + FL_GEN, tag);
  }
  }
}

extern "C" void kernel_launch(void* const* d_in, const int* in_sizes, int n_in,
                              void* d_out, int out_size, void* d_ws, size_t ws_size,
                              hipStream_t stream) {
  const u16* q = (const u16*)d_in[0];
  const u16* k = (const u16*)d_in[1];
  const u16* v = (const u16*)d_in[2];
  const u16* shk = (const u16*)d_in[3];
  const u16* ssmk = (const u16*)d_in[4];
  const u16* ssmd = (const u16*)d_in[5];
  const u16* wg = (const u16*)d_in[7];
  const u16* wu = (const u16*)d_in[8];
  const u16* wd = (const u16*)d_in[9];
  (void)hipFuncSetAttribute((const void*)k_fused, hipFuncAttributeMaxDynamicSharedMemorySize, K1_LDS);
  k_fused<<<256, 512, K1_LDS, stream>>>(q, k, v, shk, ssmk, ssmd, wg, wu, wd, (char*)d_ws, (u16*)d_out);
}
```
